```python
import torch

def _softmax(x, axis=-1):
    return torch.softmax(x, dim=axis)
def _gelu(x):
    return torch.nn.functional.gelu(x, approximate="tanh")
def _silu(x):
    return torch.nn.functional.silu(x)
def _sigmoid(x):
    return torch.sigmoid(x)
def _layernorm(x, axis=-1, eps=1e-5):
    m = x.mean(dim=axis, keepdim=True)
    v = x.var(dim=axis, keepdim=True, unbiased=False)
    return (x - m) / torch.sqrt(v + eps)
def _scatter_add(tgt, idx, src, axis=0):
    return tgt.clone().index_add_(axis, idx, src)
def _segment_sum(src, seg, n):
    out = torch.zeros((n,) + src.shape[1:], dtype=src.dtype)
    return out.index_add_(0, seg, src)
def _pad(x, pw):
    flat = tuple(v for pair in reversed(pw) for v in pair)
    return torch.nn.functional.pad(x, flat)

kernel_name = 'gated_mix__sliding_local_dilated__glu_gated_glu__prompt'

def setup_inputs():

    _w = (lambda a: (lambda t: t.to(torch.bfloat16) if t.is_floating_point() else t)(torch.as_tensor(a)))
    def _A_setup():
        B, S, D = 1, 2048, 576
        return {
            "x": _w((lambda *s: torch.randn(*s, dtype=torch.bfloat16) * 0.05)(B, S, D)),
            "wqkv": _w((lambda *s: torch.randn(*s, dtype=torch.bfloat16) * 0.8)(D, 3 * D)),
            "wo": _w((lambda *s: torch.randn(*s, dtype=torch.bfloat16) * 0.05)(D, D)),
        }
    _A_in = _A_setup()

    def _B_setup():
        B, S, D, F = 1, 2048, 576, 4608
        return {
            "x": _w((lambda *s: torch.randn(*s, dtype=torch.bfloat16) * 0.05)(B, S, D)),
            "w_gate": _w((lambda *s: torch.randn(*s, dtype=torch.bfloat16) * 1.6)(D, F)),
            "w_up": _w((lambda *s: torch.randn(*s, dtype=torch.bfloat16) * 0.1)(D, F)),
            "w_down": _w((lambda *s: torch.randn(*s, dtype=torch.bfloat16) * 0.1)(F, D)),
        }
    _B_in = _B_setup()

    return {
        "A_x": _A_in["x"],
        "A_wqkv": _A_in["wqkv"],
        "A_wo": _A_in["wo"],
        "B_x": _B_in["x"],
        "B_w_gate": _B_in["w_gate"],
        "B_w_up": _B_in["w_up"],
        "B_w_down": _B_in["w_down"],
        "gate_w": _w((lambda *s: torch.randn(*s, dtype=torch.bfloat16) * 0.05)(576) * 16.0),
    }

def reference(A_x, A_wqkv, A_wo, B_x, B_w_gate, B_w_up, B_w_down, gate_w):
    def _A_ref(x, wqkv, wo):
        B, S, D = x.shape
        H = 32; Dh = D // H
        W, dil = 8, 2
        qkv = (lambda x, *d: x.permute(*d))((x @ wqkv).reshape(B, S, 3, H, Dh), 2, 0, 3, 1, 4)
        q, k, v = qkv[0], qkv[1], qkv[2]
        scale = Dh ** -0.5
        pos = torch.arange(S)
        off = torch.arange(W) * dil
        idx = pos[:, None] - off[None, :]
        mask = idx >= 0
        idx_c = torch.where(mask, idx, 0).reshape(-1)
        kg = k[:, :, idx_c, :].reshape(B, H, S, W, Dh)
        vg = v[:, :, idx_c, :].reshape(B, H, S, W, Dh)
        logits = torch.einsum("bhsd,bhswd->bhsw", q, kg) * scale
        logits = torch.where(mask[None, None, :, :], logits, -1e9)
        attn = _softmax(logits, axis=-1)
        o = torch.einsum("bhsw,bhswd->bhsd", attn, vg)
        return (lambda x, *d: x.permute(*d))(o, 0, 2, 1, 3).reshape(B, S, D) @ wo

    def _B_ref(x, w_gate, w_up, w_down):
        g = torch.einsum("bsd,df->bsf", x, w_gate)
        u = torch.einsum("bsd,df->bsf", x, w_up)
        h = _sigmoid(g) * u
        return torch.einsum("bsf,fd->bsd", h, w_down)

    x = A_x
    g = _sigmoid((x * gate_w).sum(-1, keepdims=True))
    a = _A_ref(x, A_wqkv, A_wo)
    b = _B_ref(x, B_w_gate, B_w_up, B_w_down)
    return g * a + (1 - g) * b
```

```cpp
#include <hip/hip_runtime.h>
#include <hip/hip_bf16.h>
typedef unsigned short u16;
__device__ inline float bf2f(u16 b) { return __uint_as_float(((unsigned)b) << 16); }
__device__ inline u16 f2bf(float f) { __hip_bfloat16 h = __float2bfloat16(f); return *(u16*)&h; }
constexpr int S=2048, D=576, F=4608, H=32, DH=18;

__global__ void k_gemm(const u16* A, const u16* B, u16* C, int M, int N, int K) {
  int n = blockIdx.x*blockDim.x+threadIdx.x, m = blockIdx.y;
  if (n>=N) return;
  float acc=0;
  for (int k=0;k<K;++k) acc += bf2f(A[m*K+k])*bf2f(B[(size_t)k*N+n]);
  C[(size_t)m*N+n]=f2bf(acc);
}
__global__ void k_attn(const u16* qkv, u16* o) {
  int idx = blockIdx.x*blockDim.x+threadIdx.x;
  if (idx>=S*H) return;
  int s=idx/H, h=idx%H;
  float q[DH], l[8];
  for (int d=0;d<DH;++d) q[d]=bf2f(qkv[s*3*D+h*DH+d]);
  float scale = bf2f(f2bf(0.23570226f));
  scale = 0.23570226039551584f;
  float mx=-1e30f;
  for (int w=0;w<8;++w){ int j=s-2*w; float a;
    if (j>=0){ a=0; for(int d=0;d<DH;++d) a+=q[d]*bf2f(qkv[j*3*D+D+h*DH+d]); a=bf2f(f2bf(a)); a=bf2f(f2bf(a*scale)); } else a=-1e9f;
    l[w]=a; mx=fmaxf(mx,a);}
  float sum=0; for(int w=0;w<8;++w){l[w]=__expf(l[w]-mx); sum+=l[w];}
  for(int w=0;w<8;++w) l[w]=bf2f(f2bf(l[w]/sum));
  for(int d=0;d<DH;++d){ float a=0; for(int w=0;w<8;++w){int j=s-2*w; if(j<0) j=0; a+=l[w]*bf2f(qkv[j*3*D+2*D+h*DH+d]);} o[s*D+h*DH+d]=f2bf(a);}
}
__global__ void k_h(const u16* g, const u16* u, u16* h, int n) {
  int i=blockIdx.x*blockDim.x+threadIdx.x; if(i>=n) return;
  float gv=bf2f(g[i]); float sg=bf2f(f2bf(1.f/(1.f+__expf(-gv))));
  h[i]=f2bf(sg*bf2f(u[i]));
}
__global__ void k_out(const u16* x, const u16* gw, const u16* a, const u16* b, u16* out) {
  int m=blockIdx.x; __shared__ float red[64];
  float s=0; for(int k=threadIdx.x;k<D;k+=64) s+=bf2f(f2bf(bf2f(x[m*D+k])*bf2f(gw[k])));
  red[threadIdx.x]=s; __syncthreads();
  float t=0; for(int i=0;i<64;++i) t+=red[i];
  t=bf2f(f2bf(t));
  float g=bf2f(f2bf(1.f/(1.f+__expf(-t))));
  float og=bf2f(f2bf(1.f-g));
  for(int k=threadIdx.x;k<D;k+=64){ float ga=bf2f(f2bf(g*bf2f(a[m*D+k]))); float gb=bf2f(f2bf(og*bf2f(b[m*D+k]))); out[m*D+k]=f2bf(ga+gb);}
}
extern "C" void kernel_launch(void* const* d_in, const int* in_sizes, int n_in, void* d_out, int out_size, void* d_ws, size_t ws_size, hipStream_t stream) {
  const u16* x=(const u16*)d_in[0]; const u16* wqkv=(const u16*)d_in[1]; const u16* wo=(const u16*)d_in[2];
  const u16* wg=(const u16*)d_in[4]; const u16* wu=(const u16*)d_in[5]; const u16* wd=(const u16*)d_in[6]; const u16* gw=(const u16*)d_in[7];
  u16* ws=(u16*)d_ws;
  u16* qkv=ws; u16* o=qkv+S*3*D; u16* a=o+S*D; u16* g=a+S*D; u16* u=g+S*F; u16* h=u+S*F; u16* b=h+S*F;
  k_gemm<<<dim3((3*D+255)/256,S),256,0,stream>>>(x,wqkv,qkv,S,3*D,D);
  k_attn<<<S*H/256,256,0,stream>>>(qkv,o);
  k_gemm<<<dim3((D+255)/256,S),256,0,stream>>>(o,wo,a,S,D,D);
  k_gemm<<<dim3((F+255)/256,S),256,0,stream>>>(x,wg,g,S,F,D);
  k_gemm<<<dim3((F+255)/256,S),256,0,stream>>>(x,wu,u,S,F,D);
  k_h<<<S*F/256,256,0,stream>>>(g,u,h,S*F);
  k_gemm<<<dim3((D+255)/256,S),256,0,stream>>>(h,wd,b,S,D,F);
  k_out<<<S,64,0,stream>>>(x,gw,a,b,(u16*)d_out);
}
```

```cpp
#include <hip/hip_runtime.h>
#include <utility>

typedef unsigned short u16;
typedef short s16x4 __attribute__((ext_vector_type(4)));
typedef __bf16 bf16x8 __attribute__((ext_vector_type(8)));
typedef __bf16 bf16x2 __attribute__((ext_vector_type(2)));
typedef float f32x2 __attribute__((ext_vector_type(2)));
typedef float f32x16 __attribute__((ext_vector_type(16)));
typedef unsigned u32x2 __attribute__((ext_vector_type(2)));
typedef unsigned u32x4 __attribute__((ext_vector_type(4)));
typedef __amdgpu_buffer_rsrc_t rsrc_t;
#define LAS __attribute__((address_space(3)))
#define DI __device__ __forceinline__
#define SBAR() __builtin_amdgcn_sched_barrier(0)
#define MFMA(a, b, c) __builtin_amdgcn_mfma_f32_32x32x16_bf16((a), (b), (c), 0, 0, 0)

constexpr int SEQ = 2048, DM = 576, FF = 4608;
constexpr unsigned SLOT = 16384;
constexpr unsigned HB = 65536;
constexpr unsigned XSLOT = 18432;
constexpr unsigned QKVB = HB + 8192, QP = 248;
constexpr unsigned OB = HB + 73728, OP = 104;
constexpr unsigned GB = OB + 128 * OP;
constexpr unsigned GWB = GB + 512;
constexpr unsigned LDS_BYTES = GWB + 2048;
static_assert(LDS_BYTES <= 163840, "lds");

template <int N> DI void wait_bar() { asm volatile("s_waitcnt vmcnt(%0) lgkmcnt(0)\n\ts_barrier" ::"i"(N) : "memory"); }
DI void dma16(unsigned lds_addr, unsigned voff, rsrc_t srd, unsigned soff) {
  asm volatile("s_mov_b32 m0, %0\n\ts_nop 0\n\tbuffer_load_dwordx4 %1, %2, %3 offen lds" ::"s"(lds_addr), "v"(voff), "s"(srd), "s"(soff) : "memory");
}
DI void dma16m(unsigned lds_addr, unsigned voff, rsrc_t srd, unsigned soff, unsigned long long mask) {
  asm volatile("s_mov_b32 m0, %0\n\ts_mov_b64 exec, %4\n\tbuffer_load_dwordx4 %1, %2, %3 offen lds\n\ts_mov_b64 exec, -1" ::"s"(lds_addr), "v"(voff), "s"(srd), "s"(soff), "s"(mask) : "memory");
}
DI s16x4 trd(unsigned a) { return __builtin_amdgcn_ds_read_tr16_b64_v4i16((LAS s16x4*)a); }
DI bf16x8 cat(s16x4 lo, s16x4 hi) { return __builtin_bit_cast(bf16x8, __builtin_shufflevector(lo, hi, 0, 1, 2, 3, 4, 5, 6, 7)); }
DI unsigned cvtpk(float lo, float hi) { f32x2 v = {lo, hi}; return __builtin_bit_cast(unsigned, __builtin_convertvector(v, bf16x2)); }
DI float dot2(unsigned a, unsigned b, float c) { return __builtin_amdgcn_fdot2_f32_bf16(__builtin_bit_cast(bf16x2, a), __builtin_bit_cast(bf16x2, b), c, false); }
DI float sigm(float x) { return __builtin_amdgcn_rcpf(1.0f + __builtin_amdgcn_exp2f(-1.4426950408889634f * x)); }
template <class T> DI T ldsr(unsigned a) { return *(const LAS T*)a; }
template <class T> DI void ldsw(unsigned a, T v) { *(LAS T*)a = v; }

enum { K_P0 = 0, K_PH = 1, K_P = 2, K_Q = 3, K_Q4 = 4 };

struct Ctx {
  rsrc_t rx, rqkv, rwo, rwg, rwu, rwd, rgw, rpart;
  unsigned wid, rb, half, I, J, m0;
  unsigned n;
  unsigned lane, r, h, blk, q, p;
  bf16x8 af[3][2];
  unsigned rbase[2];
};

DI void fresh_lane(Ctx& c) {
  unsigned l;
  asm volatile("v_mbcnt_lo_u32_b32 %0, -1, 0\n\tv_mbcnt_hi_u32_b32 %0, -1, %0" : "=v"(l));
  c.lane = l; c.r = l & 31; c.h = l >> 5; c.blk = (l >> 4) & 1; c.q = (l & 15) >> 2; c.p = l & 3;
}
template <int KIND> DI unsigned lane_off(const Ctx& c, int i) {
  if constexpr (KIND == K_P0) return (8 * c.h + c.q) * 256 + 64 * ((2 * c.half + i) ^ c.q) + 32 * c.blk + 8 * c.p;
  if constexpr (KIND == K_PH) return 2048 * i + c.blk * 1024 + (8 * c.h + c.q) * 64 + 32 * (c.half ^ c.blk) + 8 * c.p;
  if constexpr (KIND == K_P) return c.h * 2048 + c.blk * 1024 + c.q * 128 + 32 * ((2 * c.half + i) ^ (c.q >> 1) ^ (2 * c.blk)) + 8 * c.p;
  if constexpr (KIND == K_Q) return (4 * c.h + c.q) * 256 + 64 * ((2 * c.half + i) ^ c.q) + 32 * c.blk + 8 * c.p;
  return 2048 * i + (4 * c.h + c.q) * 128 + 64 * (c.half ^ (c.q >> 1)) + 32 * c.blk + 8 * c.p;
}
template <int KIND> constexpr unsigned sec_off() { return KIND == K_P0 ? 1024 : KIND == K_PH ? 256 : KIND == K_P ? 512 : KIND == K_Q ? 2048 : 1024; }
template <int KIND> DI void set_bases(Ctx& c) {
  const unsigned so = (c.n & 3) * SLOT;
  c.rbase[0] = lane_off<KIND>(c, 0) + so; c.rbase[1] = lane_off<KIND>(c, 1) + so;
}
template <int KIND, int SIC> DI bf16x8 rd_frag(const Ctx& c, int i) {
  return cat(trd(c.rbase[i] + 4096 * SIC), trd(c.rbase[i] + 4096 * SIC + sec_off<KIND>()));
}

DI void dma_p0(const Ctx& c, unsigned n, unsigned ch) {
  const unsigned l = c.lane, w = c.wid, so = (n & 3) * SLOT, xo = HB + (n & 3) * XSLOT;
  const unsigned rowp = l >> 4, lc = (l & 15) ^ (rowp << 2);
  const unsigned seg = lc / 5, qseg = (72 * c.J) & ~15u;
  const unsigned sb = (seg == 0 ? 1152u : seg == 1 ? 2304u : 0u) + qseg + 16 * (lc - 5 * seg);
  const unsigned vw = (8 * w + rowp) * 3456 + sb;
  constexpr unsigned long long MW = ~((1ull << 15) | (1ull << 27) | (1ull << 39) | (1ull << 51));
  dma16m(so + (2 * w) * 1024, vw, c.rqkv, ch * 64 * 3456, MW);
  dma16m(so + (2 * w + 1) * 1024, vw, c.rqkv, ch * 64 * 3456 + 4 * 3456, MW);
  const unsigned vx0 = (c.m0 + 16 * w + (l >> 3)) * 1152 + 16 * ((l & 7) ^ (l >> 4));
  const unsigned vx1 = (c.m0 + 16 * w + 8 + (l >> 3)) * 1152 + 16 * ((l & 7) ^ (4 + (l >> 4)));
  dma16(xo + (2 * w) * 1024, vx0, c.rx, ch * 128);
  dma16(xo + (2 * w + 1) * 1024, vx1, c.rx, ch * 128);
  const int hr = (int)c.m0 - 16 + 2 * (int)w + (int)(l >> 3);
  const unsigned vh = (unsigned)(hr < 0 ? 0 : hr) * 1152 + 16 * ((l & 7) ^ w);
  dma16m(xo + (128 + 2 * w) * 128, vh, c.rx, ch * 128, 0xFFFFull);
}
DI void gate_col(const Ctx& c, unsigned n, unsigned ch) {
  if (c.wid == 0) {
    const u16 g = ldsr<u16>(GWB + 2 * (64 * ch + c.lane));
    ldsw<u16>((n & 3) * SLOT + c.lane * 256 + 16 * (15 ^ ((c.lane & 3) << 2)), g);
  }
}
DI unsigned col_half(const Ctx& c) { return 288 * c.J + ((c.J & 1) ? 0 : 256); }
DI unsigned col_full(const Ctx& c, unsigned p) { return 288 * c.J + ((c.J & 1) ? 32 : 0) + 64 * p; }
DI void dma_ph(const Ctx& c, unsigned n, unsigned ch) {
  const unsigned l = c.lane, w = c.wid, so = (n & 3) * SLOT;
  const unsigned vg = (l >> 2) * 9216 + 16 * (l & 3), vu = vg ^ 32;
  const unsigned cb = col_half(c) * 2;
  if (ch < 4) {
    const unsigned soff = (128 * ch + 16 * w) * 9216 + cb;
    dma16(so + 2048 * w, vg, c.rwg, soff); dma16(so + 2048 * w + 1024, vu, c.rwu, soff);
  } else {
    const unsigned soff = (512 + 8 * w) * 9216 + cb, d = so + 2048 * (w >> 1) + 512 * (w & 1);
    dma16m(d, vg, c.rwg, soff, 0xFFFFFFFFull); dma16m(d + 1024, vu, c.rwu, soff, 0xFFFFFFFFull);
  }
}
DI void dma_p(const Ctx& c, unsigned n, unsigned p, unsigned ch) {
  const unsigned l = c.lane, w = c.wid, so = (n & 3) * SLOT;
  const unsigned vg = (l >> 3) * 9216 + 16 * ((l & 7) ^ (((l >> 4) & 1) << 1)), vu = vg ^ 64;
  const unsigned soff = (64 * ch + 8 * w) * 9216 + col_full(c, p) * 2;
  dma16(so + 2048 * w, vg, c.rwg, soff); dma16(so + 2048 * w + 1024, vu, c.rwu, soff);
}
template <int CH> DI void dma_q(const Ctx& c, unsigned n, unsigned p) {
  const unsigned l = c.lane, w = c.wid, so = (n & 3) * SLOT;
  const unsigned v = (l >> 4) * 1152 + 16 * ((l & 15) ^ ((l >> 4) << 2));
  if constexpr (CH < 3) {
    const unsigned soff = (288 * c.J + 64 * CH + 8 * w) * 1152 + 256 * p;
    dma16(so + 2048 * w, v, c.rwd, soff); dma16(so + 2048 * w + 1024, v, c.rwd, soff + 4 * 1152);
  } else {
    const unsigned r0 = CH == 5 ? 36 * c.J : 288 * c.J + 192 + 48 * (CH - 3);
    const rsrc_t rs = CH == 5 ? c.rwo : c.rwd;
    dma16(so + 1024 * w, v, rs, (r0 + 4 * w) * 1152 + 256 * p);
    dma16m(so + 8192 + 1024 * (w >> 1), v, rs, (r0 + 32 + 4 * (w >> 1)) * 1152 + 256 * p, 0xFFFFFFFFull << (32 * (w & 1)));
  }
}
template <int CH> DI void dma_q4(const Ctx& c, unsigned n) {
  const unsigned l = c.lane, w = c.wid, so = (n & 3) * SLOT;
  const unsigned v = (l >> 3) * 1152 + 16 * ((l & 7) ^ (((l >> 4) & 1) << 2));
  if constexpr (CH < 2) {
    const unsigned soff = (288 * c.J + 128 * CH + 16 * w) * 1152 + 1024;
    dma16(so + 2048 * w, v, c.rwd, soff); dma16(so + 2048 * w + 1024, v, c.rwd, soff + 8 * 1152);
  } else {
    dma16m(so + 1024 * (w >> 1), v, c.rwd, (288 * c.J + 256 + 8 * (w >> 1)) * 1152 + 1024, 0xFFFFFFFFull << (32 * (w & 1)));
    if (w < 6) dma16(so + 4096 + 1024 * w, v, c.rwo, (36 * c.J + 8 * w) * 1152 + 1024);
  }
}

DI void glu_epi(const Ctx& c, const f32x16& a, float omg, unsigned ks) {
  float hv[8];
#pragma unroll
  for (int i = 0; i < 8; ++i) hv[i] = a[i + 8] * omg * sigm(a[i]);
  u32x4 w = {cvtpk(hv[0], hv[1]), cvtpk(hv[2], hv[3]), cvtpk(hv[4], hv[5]), cvtpk(hv[6], hv[7])};
  ldsw<u32x4>(HB + (ks * 4 + c.rb) * 1024 + c.lane * 16, w);
}
DI void qkv_epi(const Ctx& c, const f32x16& a, unsigned row, unsigned t, int ngroups) {
#pragma unroll
  for (int g = 0; g < 4; ++g) if (g < ngroups) {
    u32x2 w = {cvtpk(a[4 * g], a[4 * g + 1]), cvtpk(a[4 * g + 2], a[4 * g + 3])};
    ldsw<u32x2>(QKVB + row * QP + 64 * t + 16 * g + 8 * c.h, w);
  }
}
DI void part_epi(const Ctx& c, const f32x16& a, unsigned tile) {
  u32x4 w0 = {cvtpk(a[0], a[1]), cvtpk(a[2], a[3]), cvtpk(a[4], a[5]), cvtpk(a[6], a[7])};
  u32x4 w1 = {cvtpk(a[8], a[9]), cvtpk(a[10], a[11]), cvtpk(a[12], a[13]), cvtpk(a[14], a[15])};
  const unsigned so = ((c.I * 16 + c.J) * 72 + tile * 4 + c.rb) * 2048;
  __builtin_amdgcn_raw_buffer_store_b128(w0, c.rpart, c.lane * 16, so, 0);
  __builtin_amdgcn_raw_buffer_store_b128(w1, c.rpart, c.lane * 16, so + 1024, 0);
}
DI void attention(const Ctx& c) {
  const unsigned R = 32 * c.rb + c.r, head = c.half;
  const unsigned cb = QKVB + 8 * (c.J & 1) + 32 * head;
  unsigned qv[10];
#pragma unroll
  for (int i = 0; i < 5; ++i) { u32x2 t = ldsr<u32x2>(cb + (R + 16) * QP + 160 + 8 * i); qv[2 * i] = t[0]; qv[2 * i + 1] = t[1]; }
  if (head == 0) qv[9] = 0; else qv[0] = 0;
  const unsigned kb = cb + (R + 16 - 8 * c.h) * QP;
  float lg[4];
#pragma unroll
  for (int w = 0; w < 4; ++w) {
    float a = 0.f;
#pragma unroll
    for (int i = 0; i < 5; ++i) { u32x2 t = ldsr<u32x2>(kb - 2 * w * QP + 8 * i); a = dot2(qv[2 * i], t[0], a); a = dot2(qv[2 * i + 1], t[1], a); }
    const int key = (int)(c.m0 + R) - 2 * (4 * (int)c.h + w);
    lg[w] = key >= 0 ? a * (0.23570226039551584f * 1.4426950408889634f) : -1e30f;
  }
  float m = fmaxf(fmaxf(lg[0], lg[1]), fmaxf(lg[2], lg[3]));
  { auto rr = __builtin_amdgcn_permlane32_swap(__float_as_uint(m), __float_as_uint(m), false, false); m = fmaxf(__uint_as_float(rr[0]), __uint_as_float(rr[1])); }
  float s = 0.f;
#pragma unroll
  for (int w = 0; w < 4; ++w) { lg[w] = __builtin_amdgcn_exp2f(lg[w] - m); s += lg[w]; }
  { auto rr = __builtin_amdgcn_permlane32_swap(__float_as_uint(s), __float_as_uint(s), false, false); s = __uint_as_float(rr[0]) + __uint_as_float(rr[1]); }
  const float inv = __builtin_amdgcn_rcpf(s);
  float ol[10], oh[10];
#pragma unroll
  for (int i = 0; i < 10; ++i) { ol[i] = 0.f; oh[i] = 0.f; }
#pragma unroll
  for (int w = 0; w < 4; ++w) {
    const unsigned pl = cvtpk(lg[w] * inv, 0.f), ph = pl << 16;
#pragma unroll
    for (int i = 0; i < 5; ++i) {
      u32x2 t = ldsr<u32x2>(kb - 2 * w * QP + 80 + 8 * i);
      ol[2 * i] = dot2(t[0], pl, ol[2 * i]); oh[2 * i] = dot2(t[0], ph, oh[2 * i]);
      ol[2 * i + 1] = dot2(t[1], pl, ol[2 * i + 1]); oh[2 * i + 1] = dot2(t[1], ph, oh[2 * i + 1]);
    }
  }
  const float g = ldsr<float>(GB + 4 * R);
#pragma unroll
  for (int i = 0; i < 5; ++i) {
    auto a = __builtin_amdgcn_permlane32_swap(__float_as_uint(ol[i]), __float_as_uint(ol[i + 5]), false, false);
    auto b = __builtin_amdgcn_permlane32_swap(__float_as_uint(oh[i]), __float_as_uint(oh[i + 5]), false, false);
    const float lo = (__uint_as_float(a[0]) + __uint_as_float(a[1])) * g, hi = (__uint_as_float(b[0]) + __uint_as_float(b[1])) * g;
    const unsigned d2 = 5 * c.h + i;
    const bool bad = head == 0 ? d2 == 9 : d2 == 0;
    ldsw<unsigned>(OB + R * OP + (bad ? 96u : 32 * head + 4 * d2), cvtpk(lo, hi));
  }
}

template <int... Is> using iseq = std::integer_sequence<int, Is...>;
template <int N> using mkseq = std::make_integer_sequence<int, N>;
template <class Fn, int... Is> DI void static_for_(Fn&& f, iseq<Is...>) { (f(std::integral_constant<int, Is>{}), ...); }
template <int N, class Fn> DI void static_for(Fn&& f) { static_for_(f, mkseq<N>{}); }

__global__ __launch_bounds__(512, 2) void fused_kernel(const u16* x, const u16* wqkv, const u16* wo, const u16* wg, const u16* wu, const u16* wd,
                                                        const u16* gw, u16* part) {
  __shared__ __attribute__((aligned(16))) char smem[LDS_BYTES];
  asm volatile("" ::"v"((unsigned)(size_t)smem));
  Ctx c;
  c.rx = __builtin_amdgcn_make_buffer_rsrc((void*)x, (short)0, SEQ * DM * 2, 0x00020000);
  c.rqkv = __builtin_amdgcn_make_buffer_rsrc((void*)wqkv, (short)0, DM * 3 * DM * 2, 0x00020000);
  c.rwo = __builtin_amdgcn_make_buffer_rsrc((void*)wo, (short)0, DM * DM * 2, 0x00020000);
  c.rwg = __builtin_amdgcn_make_buffer_rsrc((void*)wg, (short)0, DM * FF * 2, 0x00020000);
  c.rwu = __builtin_amdgcn_make_buffer_rsrc((void*)wu, (short)0, DM * FF * 2, 0x00020000);
  c.rwd = __builtin_amdgcn_make_buffer_rsrc((void*)wd, (short)0, DM * FF * 2, 0x00020000);
  c.rgw = __builtin_amdgcn_make_buffer_rsrc((void*)gw, (short)0, DM * 2, 0x00020000);
  c.rpart = __builtin_amdgcn_make_buffer_rsrc((void*)part, (short)0, 256 * 72 * 2048, 0x00020000);
  const unsigned tid = threadIdx.x, bid = blockIdx.x;
  c.wid = __builtin_amdgcn_readfirstlane(tid >> 6); c.rb = c.wid & 3; c.half = c.wid >> 2;
  c.J = 2 * (bid & 7) + ((bid >> 3) & 1); c.I = bid >> 4; c.m0 = 128 * c.I;
  fresh_lane(c);
  const f32x16 Z = {};

  if (c.wid == 0) { dma16(GWB, c.lane * 16, c.rgw, 0); dma16(GWB + 1024, c.lane * 16, c.rgw, 1024); }
  dma_p0(c, 0, 0); dma_p0(c, 1, 1); dma_p0(c, 2, 2);
  if ((tid & 3) < 3) ldsw<u32x2>(OB + (tid >> 2) * OP + 72 + 8 * (tid & 3), u32x2{0u, 0u});
  asm volatile("s_waitcnt vmcnt(15)" ::: "memory");
  gate_col(c, 0, 0); gate_col(c, 1, 1); gate_col(c, 2, 2);
  bf16x8 xf[36];
  f32x16 acc0, acc1, acch = Z;
  unsigned xrow = 32 * c.rb + c.r;
  const unsigned xl = xrow * 128 + 16 * (c.h ^ ((xrow >> 1) & 7));
  const unsigned hrow = 128 + (c.r & 15), hl = hrow * 128 + 16 * (c.h ^ ((hrow >> 1) & 7));
  const bool do_halo = c.wid == 0 || c.wid == 1 || c.wid == 6;
  bf16x8 hf3[3];
  unsigned xbase = 0;
  auto rd_x = [&](auto S_) {
    constexpr int S = decltype(S_)::value;
    xf[S] = ldsr<bf16x8>(xbase + (xl ^ (32 * (S & 3))));
    hf3[S % 3] = ldsr<bf16x8>(xbase + (hl ^ (32 * (S & 3))));
  };
  c.n = 0;
  wait_bar<10>();
  set_bases<K_P0>(c); xbase = HB;
  dma_p0(c, 3, 3); gate_col(c, 3, 3);
  c.af[0][0] = rd_frag<K_P0, 0>(c, 0); c.af[0][1] = rd_frag<K_P0, 0>(c, 1); rd_x(std::integral_constant<int, 0>{});
  c.af[1][0] = rd_frag<K_P0, 1>(c, 0); c.af[1][1] = rd_frag<K_P0, 1>(c, 1); rd_x(std::integral_constant<int, 1>{});
  static_for<36>([&](auto S_) {
    constexpr int S = decltype(S_)::value, T = S + 2;
    if constexpr (T % 4 == 0) {
      constexpr int CH = T / 4;
      constexpr int D1 = CH + 1 <= 8 ? 5 : 2, D2 = CH + 2 <= 8 ? 5 : 2;
      c.n = CH;
      wait_bar<D1 + D2>();
      if constexpr (CH == 6 || CH == 9) fresh_lane(c);
      if constexpr (CH <= 8) { set_bases<K_P0>(c); xbase = HB + (CH & 3) * XSLOT; } else set_bases<K_PH>(c);
      if constexpr (CH + 3 <= 8) { dma_p0(c, CH + 3, CH + 3); gate_col(c, CH + 3, CH + 3); } else dma_ph(c, CH + 3, CH + 3 - 9);
    }
    acc0 = MFMA(c.af[S % 3][0], xf[S], S == 0 ? Z : acc0);
    if (c.wid == 0 || c.wid == 6) acch = MFMA(c.af[S % 3][0], hf3[S % 3], S == 0 ? Z : acch);
    if constexpr (T < 36) { c.af[T % 3][0] = rd_frag<K_P0, T & 3>(c, 0); } else c.af[T % 3][0] = rd_frag<K_PH, T & 3>(c, 0);
    SBAR();
    acc1 = MFMA(c.af[S % 3][1], xf[S], S == 0 ? Z : acc1);
    if (c.wid == 1) acch = MFMA(c.af[S % 3][1], hf3[S % 3], S == 0 ? Z : acch);
    if constexpr (T < 36) { c.af[T % 3][1] = rd_frag<K_P0, T & 3>(c, 1); rd_x(std::integral_constant<int, T>{}); } else c.af[T % 3][1] = rd_frag<K_PH, T & 3>(c, 1);
    SBAR();
  });
  fresh_lane(c); xrow = 32 * c.rb + c.r;
  qkv_epi(c, acc0, 16 + xrow, 2 * c.half, 4);
  if (c.half == 0) qkv_epi(c, acc1, 16 + xrow, 1, 4);
  else { qkv_epi(c, acc1, 16 + xrow, 3, 3); if (c.h == 0) ldsw<float>(GB + 4 * xrow, sigm(acc1[12])); }
  if (do_halo && c.r < 16) qkv_epi(c, acch, c.r, c.wid == 6 ? 2 : c.wid, 4);
  float omg = 0.f;
  static_for<18>([&](auto S_) {
    constexpr int S = decltype(S_)::value, T = S + 2;
    if constexpr (T % 4 == 0 || T == 18) {
      constexpr int CH = T == 18 ? 5 : T / 4;
      c.n = 9 + CH;
      wait_bar<4>();
      if constexpr (CH == 1 || CH == 2) { fresh_lane(c); xrow = 32 * c.rb + c.r; }
      if constexpr (CH <= 4) set_bases<K_PH>(c); else set_bases<K_P>(c);
      if constexpr (CH + 3 <= 4) dma_ph(c, 9 + CH + 3, CH + 3); else dma_p(c, 9 + CH + 3, 0, CH + 3 - 5);
      if constexpr (CH == 1) { attention(c); omg = 1.0f - ldsr<float>(GB + 4 * xrow); SBAR(); }
    }
    acc0 = MFMA(c.af[S % 3][0], xf[2 * S], S == 0 ? Z : acc0);
    if constexpr (T < 18) c.af[T % 3][0] = rd_frag<K_PH, T & 3>(c, 0); else c.af[T % 3][0] = rd_frag<K_P, T - 18>(c, 0);
    SBAR();
    acc0 = MFMA(c.af[S % 3][1], xf[2 * S + 1], acc0);
    if constexpr (T < 18) c.af[T % 3][1] = rd_frag<K_PH, T & 3>(c, 1); else c.af[T % 3][1] = rd_frag<K_P, T - 18>(c, 1);
    SBAR();
  });
  glu_epi(c, acc0, omg, ((c.J & 1) ? 0 : 16) + c.half);
  for (unsigned p = 0; p < 4; ++p) {
    const unsigned nb = 14 + 9 * p;
    const bool last = p == 3;
    static_for<36>([&](auto S_) {
      constexpr int S = decltype(S_)::value, T = S + 2;
      if constexpr (T % 4 == 0) {
        constexpr int CH = T / 4;
        c.n = nb + CH;
        wait_bar<4>();
        if constexpr (CH == 6) fresh_lane(c);
        if constexpr (CH <= 8) set_bases<K_P>(c); else { if (last) set_bases<K_Q>(c); else set_bases<K_P>(c); }
        if constexpr (CH + 3 <= 8) dma_p(c, nb + CH + 3, p, CH + 3);
        else { if (last) dma_q<CH + 3 - 9>(c, nb + CH + 3, 0); else dma_p(c, nb + CH + 3, p + 1, CH + 3 - 9); }
      }
      acc0 = MFMA(c.af[S % 3][0], xf[S], S == 0 ? Z : acc0);
      if constexpr (T < 36) c.af[T % 3][0] = rd_frag<K_P, T & 3>(c, 0);
      else { if (last) c.af[T % 3][0] = rd_frag<K_Q, T & 3>(c, 0); else c.af[T % 3][0] = rd_frag<K_P, T & 3>(c, 0); }
      SBAR();
      acc1 = MFMA(c.af[S % 3][1], xf[S], S == 0 ? Z : acc1);
      if constexpr (T < 36) c.af[T % 3][1] = rd_frag<K_P, T & 3>(c, 1);
      else { if (last) c.af[T % 3][1] = rd_frag<K_Q, T & 3>(c, 1); else c.af[T % 3][1] = rd_frag<K_P, T & 3>(c, 1); }
      SBAR();
    });
    const unsigned ks = ((c.J & 1) ? 2 : 0) + 4 * p + 2 * c.half;
    glu_epi(c, acc0, omg, ks); glu_epi(c, acc1, omg, ks + 1);
  }
  asm volatile("s_waitcnt lgkmcnt(0)\n\ts_barrier" ::: "memory");
  fresh_lane(c); xrow = 32 * c.rb + c.r;
  bf16x8 hf[21];
#pragma unroll
  for (int s = 0; s < 18; ++s) hf[s] = ldsr<bf16x8>(HB + (s * 4 + c.rb) * 1024 + c.lane * 16);
#pragma unroll
  for (int s = 0; s < 3; ++s) hf[18 + s] = cat(ldsr<s16x4>(OB + xrow * OP + 32 * s + 8 * c.h), ldsr<s16x4>(OB + xrow * OP + 32 * s + 16 + 8 * c.h));
  for (unsigned p = 0; p < 4; ++p) {
    const unsigned nb = 50 + 6 * p;
    const bool last = p == 3;
    static_for<21>([&](auto S_) {
      constexpr int S = decltype(S_)::value, T = S + 2;
      constexpr int CHT = T < 12 ? T / 4 : T < 21 ? 3 + (T - 12) / 3 : 6;
      constexpr int SIC = T < 12 ? T & 3 : T < 21 ? (T - 12) % 3 : T - 21;
      if constexpr (SIC == 0) {
        c.n = nb + CHT;
        if constexpr (CHT <= 3) { if (p == 0) wait_bar<4>(); else wait_bar<8>(); }
        else if constexpr (CHT == 6) { if (last) wait_bar<3>(); else wait_bar<4>(); }
        else wait_bar<4>();
        if constexpr (CHT <= 5) set_bases<K_Q>(c); else { if (last) set_bases<K_Q4>(c); else set_bases<K_Q>(c); }
        if constexpr (CHT + 3 <= 5) dma_q<CHT + 3>(c, nb + CHT + 3, p);
        else { if (last) { if constexpr (CHT + 3 - 6 <= 2) dma_q4<CHT + 3 - 6>(c, nb + CHT + 3); } else dma_q<CHT + 3 - 6>(c, nb + CHT + 3, p + 1); }
      }
      acc0 = MFMA(c.af[S % 3][0], hf[S], S == 0 ? Z : acc0);
      if constexpr (T < 21) c.af[T % 3][0] = rd_frag<K_Q, SIC>(c, 0);
      else { if (last) c.af[T % 3][0] = rd_frag<K_Q4, SIC>(c, 0); else c.af[T % 3][0] = rd_frag<K_Q, SIC>(c, 0); }
      SBAR();
      acc1 = MFMA(c.af[S % 3][1], hf[S], S == 0 ? Z : acc1);
      if constexpr (T < 21) c.af[T % 3][1] = rd_frag<K_Q, SIC>(c, 1);
      else { if (last) c.af[T % 3][1] = rd_frag<K_Q4, SIC>(c, 1); else c.af[T % 3][1] = rd_frag<K_Q, SIC>(c, 1); }
      SBAR();
    });
    part_epi(c, acc0, 4 * p + 2 * c.half); part_epi(c, acc1, 4 * p + 2 * c.half + 1);
  }
  static_for<11>([&](auto S_) {
    constexpr int S = decltype(S_)::value, T = S + 2;
    if constexpr (T % 4 == 0 && T < 11) {
      c.n = 74 + T / 4;
      if constexpr (T == 4) wait_bar<5>(); else wait_bar<4>();
      set_bases<K_Q4>(c);
    }
    acc0 = MFMA(c.af[S % 3][0], hf[2 * S], S == 0 ? Z : acc0);
    if constexpr (T < 11) c.af[T % 3][0] = rd_frag<K_Q4, T & 3>(c, 0);
    SBAR();
    if constexpr (S < 10) acc0 = MFMA(c.af[S % 3][1], hf[2 * S + 1], acc0);
    if constexpr (T < 10) c.af[T % 3][1] = rd_frag<K_Q4, T & 3>(c, 1);
    SBAR();
  });
  part_epi(c, acc0, 16 + c.half);
}

__global__ __launch_bounds__(256) void reduce_kernel(const u32x4* part, u16* out) {
  const unsigned gid = blockIdx.x * 256 + threadIdx.x;
  const unsigned I = gid / (72 * 128), rem = gid % (72 * 128);
  const unsigned lane = rem & 63, hp = (rem >> 6) & 1, rb = (rem >> 7) & 3, tile = rem >> 9;
  float s[8] = {};
#pragma unroll
  for (int j = 0; j < 16; ++j) {
    const u32x4 v = part[(size_t)(I * 16 + j) * (72 * 128) + rem];
#pragma unroll
    for (int e = 0; e < 4; ++e) { s[2 * e] += __uint_as_float(v[e] << 16); s[2 * e + 1] += __uint_as_float(v[e] & 0xffff0000u); }
  }
  const unsigned row = 128 * I + 32 * rb + (lane & 31), col = 32 * tile + 16 * hp + 4 * (lane >> 5);
  *(u32x2*)(out + row * DM + col) = u32x2{cvtpk(s[0], s[1]), cvtpk(s[2], s[3])};
  *(u32x2*)(out + row * DM + col + 8) = u32x2{cvtpk(s[4], s[5]), cvtpk(s[6], s[7])};
}

extern "C" void kernel_launch(void* const* d_in, const int* in_sizes, int n_in, void* d_out, int out_size, void* d_ws, size_t ws_size,
                              hipStream_t stream) {
  fused_kernel<<<256, 512, 0, stream>>>((const u16*)d_in[0], (const u16*)d_in[1], (const u16*)d_in[2], (const u16*)d_in[4], (const u16*)d_in[5],
                                        (const u16*)d_in[6], (const u16*)d_in[7], (u16*)d_ws);
  reduce_kernel<<<16 * 72 * 128 / 256, 256, 0, stream>>>((const u32x4*)d_ws, (u16*)d_out);
}
```

```cpp
#include <hip/hip_runtime.h>
#include <utility>

typedef unsigned short u16;
typedef unsigned long long u64;
typedef short s16x4 __attribute__((ext_vector_type(4)));
typedef __bf16 bf16x8 __attribute__((ext_vector_type(8)));
typedef __bf16 bf16x2 __attribute__((ext_vector_type(2)));
typedef float f32x2 __attribute__((ext_vector_type(2)));
typedef float f32x4 __attribute__((ext_vector_type(4)));
typedef unsigned u32x2 __attribute__((ext_vector_type(2)));
typedef unsigned u32x4 __attribute__((ext_vector_type(4)));
typedef __amdgpu_buffer_rsrc_t rsrc_t;
#define LAS __attribute__((address_space(3)))
#define DI __device__ __forceinline__
#define SBAR() __builtin_amdgcn_sched_barrier(0)
#define IC(x) std::integral_constant<int, (x)>{}
#define CI(x) decltype(x)::value

constexpr int SEQ = 2048, DM = 576, FF = 4608;
constexpr unsigned WS_H = 0, H_BYTES = 128u * 4 * 42 * 1024;
constexpr unsigned WS_PART = 24u << 20;
constexpr unsigned WS_G = 40u << 20;
constexpr unsigned WS_F1 = 41u << 20;
constexpr unsigned WS_F2 = WS_F1 + 8192;
constexpr unsigned WS_E = WS_F2 + 65536;
constexpr unsigned WS_BYTES = 42u << 20;
constexpr unsigned LDS_BYTES = 163840;
constexpr unsigned S1 = 16384;
constexpr unsigned S2 = 18432;
constexpr unsigned AP = 152, APL = 5248, APL2 = 2 * 5248 + 32;
constexpr unsigned A_K = 4 * S2, A_V = A_K + 4 * APL2, A_Q = 0;
constexpr unsigned A_GW = A_V + 4 * APL2, A_G = A_GW + 2048;
constexpr unsigned A_S2 = 45056;
static_assert(A_G + 1024 <= LDS_BYTES - 1024 && 4 * APL2 <= A_S2 && A_S2 + 4 * (S2 + 8192) <= LDS_BYTES - 1024, "lds");

template <int... Is> using iseq = std::integer_sequence<int, Is...>;
template <class Fn, int... Is> DI void static_for_(Fn&& f, iseq<Is...>) { (f(std::integral_constant<int, Is>{}), ...); }
template <int N, class Fn> DI void static_for(Fn&& f) { static_for_(f, std::make_integer_sequence<int, N>{}); }

template <int N> DI void wait_bar() { asm volatile("s_waitcnt vmcnt(%0) lgkmcnt(0)\n\ts_barrier" ::"i"(N) : "memory"); }
template <class T> DI void PIN(T& x) { asm volatile("" : "+v"(x)); }
DI void tieA(bf16x8& a, bf16x8& b, bf16x8& c, bf16x8& d) { asm volatile("" : "+a"(a), "+a"(b), "+a"(c), "+a"(d)); }
DI void tieV(bf16x8& a, bf16x8& b, bf16x8& c, bf16x8& d) { asm volatile("" : "+v"(a), "+v"(b), "+v"(c), "+v"(d)); }
template <unsigned LO, unsigned SO> DI void dma(unsigned lb, unsigned voff, rsrc_t srd, unsigned sb) {
  unsigned t;
  asm volatile("s_add_u32 m0, %1, %5\n\ts_add_u32 %0, %4, %6\n\tbuffer_load_dwordx4 %2, %3, %0 offen lds"
               : "=&s"(t) : "s"(lb), "v"(voff), "s"(srd), "s"(sb), "i"(LO), "i"(SO) : "memory", "scc");
}
template <unsigned LO, unsigned SO> DI void dma_m(unsigned lb, unsigned voff, rsrc_t srd, unsigned sb, u64 mask) {
  unsigned t;
  asm volatile("s_add_u32 m0, %1, %5\n\ts_add_u32 %0, %4, %6\n\ts_mov_b64 exec, %7\n\tbuffer_load_dwordx4 %2, %3, %0 offen lds\n\ts_mov_b64 exec, -1"
               : "=&s"(t) : "s"(lb), "v"(voff), "s"(srd), "s"(sb), "i"(LO), "i"(SO), "s"(mask) : "memory", "scc");
}
template <bool AG, unsigned IMM> DI void ldx(bf16x8& d, unsigned voff, rsrc_t srd, unsigned so) {
  if constexpr (AG) asm volatile("buffer_load_dwordx4 %0, %1, %2, %3 offen offset:%4" : "=a"(d) : "v"(voff), "s"(srd), "s"(so), "i"(IMM) : "memory");
  else asm volatile("buffer_load_dwordx4 %0, %1, %2, %3 offen offset:%4" : "=v"(d) : "v"(voff), "s"(srd), "s"(so), "i"(IMM) : "memory");
}
template <unsigned SO> DI void ldh(bf16x8& d, unsigned voff, rsrc_t srd, unsigned sb) {
  unsigned t;
  asm volatile("s_add_u32 %1, %4, %5\n\tbuffer_load_dwordx4 %0, %2, %3, %1 offen sc1" : "=a"(d), "=&s"(t) : "v"(voff), "s"(srd), "s"(sb), "i"(SO) : "memory", "scc");
}
DI void ldg(f32x4& d, unsigned voff, rsrc_t srd, unsigned so) { asm volatile("buffer_load_dwordx4 %0, %1, %2, %3 offen sc1" : "=v"(d) : "v"(voff), "s"(srd), "s"(so) : "memory"); }
template <bool AG, bool ZERO> DI void mf(f32x4& c, const bf16x8& a, const bf16x8& b) {
  if constexpr (ZERO) {
    if constexpr (AG) asm volatile("v_mfma_f32_16x16x32_bf16 %0, %1, %2, 0" : "=&v"(c) : "v"(a), "a"(b));
    else asm volatile("v_mfma_f32_16x16x32_bf16 %0, %1, %2, 0" : "=&v"(c) : "v"(a), "v"(b));
  } else {
    if constexpr (AG) asm volatile("v_mfma_f32_16x16x32_bf16 %0, %1, %2, %0" : "+v"(c) : "v"(a), "a"(b));
    else asm volatile("v_mfma_f32_16x16x32_bf16 %0, %1, %2, %0" : "+v"(c) : "v"(a), "v"(b));
  }
}
DI s16x4 trd(unsigned a) { return __builtin_amdgcn_ds_read_tr16_b64_v4i16((LAS s16x4*)a); }
DI bf16x8 cat(s16x4 lo, s16x4 hi) { return __builtin_bit_cast(bf16x8, __builtin_shufflevector(lo, hi, 0, 1, 2, 3, 4, 5, 6, 7)); }
DI unsigned cvtpk(float lo, float hi) { f32x2 v = {lo, hi}; return __builtin_bit_cast(unsigned, __builtin_convertvector(v, bf16x2)); }
DI unsigned pkh(float lo, float hi) { return __builtin_bit_cast(unsigned, __builtin_amdgcn_cvt_pkrtz(lo, hi)); }
DI float mixlo(float u, unsigned s) { float r; asm("v_fma_mix_f32 %0, %1, %2, 0 op_sel_hi:[0,1,0]" : "=v"(r) : "v"(u), "v"(s)); return r; }
DI float mixhi(float u, unsigned s) { float r; asm("v_fma_mix_f32 %0, %1, %2, 0 op_sel:[0,1,0] op_sel_hi:[0,1,0]" : "=v"(r) : "v"(u), "v"(s)); return r; }
DI float dot2(unsigned a, unsigned b, float c) { return __builtin_amdgcn_fdot2_f32_bf16(__builtin_bit_cast(bf16x2, a), __builtin_bit_cast(bf16x2, b), c, false); }
DI float sigm(float x) { return __builtin_amdgcn_rcpf(1.0f + __builtin_amdgcn_exp2f(-1.4426950408889634f * x)); }
DI float bflo(unsigned v) { return __uint_as_float(v << 16); }
DI float bfhi(unsigned v) { return __uint_as_float(v & 0xffff0000u); }
template <class T> DI T ldsr(unsigned a) { return *(const LAS T*)a; }
template <class T> DI void ldsw(unsigned a, T v) { *(LAS T*)a = v; }
DI unsigned lane_id() { unsigned l; asm volatile("v_mbcnt_lo_u32_b32 %0, -1, 0\n\tv_mbcnt_hi_u32_b32 %0, -1, %0" : "=v"(l)); return l; }

#define STAMP(i)

struct Ctx {
  rsrc_t rx, rqkv, rwo, rwg, rwu, rwd, rgw, rws, rout;
  unsigned w, a, T, m;
  u64 E;
};

struct Sched {
  int cnt[64][12] = {}, need[64][12] = {}, ng[64] = {};
  constexpr void add(int r, int c, int n) { cnt[r + 1][ng[r + 1]] = c; need[r + 1][ng[r + 1]] = n; ++ng[r + 1]; }
  constexpr int vm(int n) const {
    int s = 0;
    for (int r = n; r >= 0; --r)
      for (int g = ng[r] - 1; g >= 0; --g) { if (need[r][g] <= n) return s > 63 ? 63 : s; s += cnt[r][g]; }
    return s > 63 ? 63 : s;
  }
};
constexpr void sched_p2(Sched& s, int B, int AD, int ND) {
  for (int u = 0; u <= 20; ++u) {
    if (u + AD <= 20) { s.add(B + u, ND, B + u + AD); s.add(B + u, 4, B + u + AD); }
    if (u + AD == 18) s.add(B + u, 1, B + 17);
    if (u + AD + 2 == 6 || u + AD + 2 == 12 || u + AD + 2 == 18) s.add(B + u, 1, B + u + 2);
  }
}
constexpr int MLP_AD = 5, ATT_AD = 3;
constexpr Sched make_mlp() {
  Sched s;
  for (int c = 0; c < 3; ++c) { s.add(-1, 4, c); s.add(-1, 16, c); }
  s.add(0, 4, 3); s.add(0, 16, 3);
  s.add(1, 4, 4); s.add(1, 8, 4); s.add(1, 4, 5);
  s.add(2, 4, 6); s.add(2, 4, 7); s.add(3, 4, 8); s.add(3, 4, 9);
  for (int r = 4; r <= 26; ++r) {
    if (r + 6 <= 26) s.add(r, 4, r + 6);
    if (r == 9 || r == 10 || r == 18 || r == 19) s.add(r, 4, r < 18 ? 12 : 21);
    if (r == 22) s.add(r, 1, 24);
    if (r == 24) { s.add(r, 4, 27); s.add(r, 4, 27); s.add(r, 4, 28); s.add(r, 4, 28); }
    if (r == 25) { s.add(r, 4, 29); s.add(r, 4, 29); s.add(r, 4, 30); s.add(r, 4, 30); }
    if (r == 26) { s.add(r, 4, 31); s.add(r, 4, 31); s.add(r, 1, 28); }
  }
  s.add(27, 8, 29);
  sched_p2(s, 27, MLP_AD, 4);
  return s;
}
constexpr Sched make_att1() {
  Sched s;
  for (int c = 0; c < 3; ++c) { s.add(-1, 4, c); s.add(-1, 8, c); s.add(-1, 2, c); }
  for (int r = 0; r + 3 <= 17; ++r) { s.add(r, 4, r + 3); if (r + 3 <= 8) s.add(r, 8, r + 3); s.add(r, 2, r + 3); }
  return s;
}
constexpr Sched make_att2() {
  Sched s;
  for (int c = 0; c < ATT_AD; ++c) s.add(-1, 6, c);
  for (int c = 0; c < ATT_AD; ++c) s.add(-1, 4, c);
  sched_p2(s, 0, ATT_AD, 6);
  return s;
}
constexpr Sched SC_MLP = make_mlp(), SC_ATT1 = make_att1(), SC_ATT2 = make_att2();
template <int WHICH, int N> constexpr int VM = WHICH == 0 ? SC_MLP.vm(N) : WHICH == 1 ? SC_ATT1.vm(N) : SC_ATT2.vm(N);

DI void flag_store(const Ctx& c, unsigned off, unsigned val) {
  const u64 v = c.E + val;
  __builtin_amdgcn_raw_buffer_store_b64(u32x2{(unsigned)v, (unsigned)(v >> 32)}, c.rws, 0, off, 16);
}
DI void poll_issue(const Ctx& c, u32x2& f, unsigned voff, unsigned off) {
  asm volatile("buffer_load_dwordx2 %0, %1, %2, %3 offen sc1" : "=v"(f) : "v"(voff), "s"(c.rws), "s"(off) : "memory");
}
DI bool flag_ok(const Ctx& c, u32x2 f, unsigned need) {
  const u64 d = (((u64)f[1] << 32) | f[0]) - c.E - need;
  return __builtin_amdgcn_ballot_w64(d >= 8) == 0;
}
DI void poll_check(const Ctx& c, u32x2& f, unsigned voff, unsigned off, unsigned need) {
  asm volatile("" : "+v"(f));
  if (__builtin_expect(!flag_ok(c, f, need), 0)) {
    for (unsigned spin = 0; spin < (1u << 16); ++spin) {
      __builtin_amdgcn_s_sleep(2);
      asm volatile("buffer_load_dwordx2 %0, %1, %2, %3 offen sc1\n\ts_waitcnt vmcnt(0)" : "=&v"(f) : "v"(voff), "s"(c.rws), "s"(off) : "memory");
      if (flag_ok(c, f, need)) break;
    }
  }
}

template <int NT> struct P2 {
  static constexpr int AD = NT == 8 ? MLP_AD : ATT_AD, NS = NT == 8 ? 8 : 4, B = NT == 8 ? 27 : 0;
  static constexpr unsigned SB = NT == 8 ? 0 : A_S2, SS = NT == 8 ? S2 : S2 + 8192;
  static constexpr unsigned slot(int u) { return SB + (u % NS) * SS; }
  static constexpr int WN = NT == 8 ? 3 : 2;
  static constexpr unsigned win(int u) { return SB + ((u % NS) / WN * WN) * SS; }
  unsigned v128, v64, vl16, vf;
  unsigned lw, lw64, sd, so, sh[2][2];
  bf16x8 bf[21][4];
  u32x2 pf;
  f32x4 omg;
  DI void init(const Ctx& c) {
    const unsigned l = lane_id(), colb = NT == 8 ? 256 * (c.m >> 1) : 768;
    v128 = (l >> 4) * 1152 + 16 * ((l & 15) ^ ((l >> 4) << 1) ^ (4 * c.a));
    v64 = (l >> 3) * 1152 + 16 * ((l & 7) ^ (((l >> 4) & 3) << 1) ^ (2 * c.a)) + 256;
    vl16 = l * 16; vf = 8 * (l & 7);
    lw = c.w * 4608; lw64 = c.w * 2048 + S2;
    sd = (1152 * c.a + 16 * c.w) * 1152 + colb;
    so = (144 * c.a + 72 * (c.w >> 1) + 16 * (c.w & 1)) * 1152 + colb;
    const unsigned kh = c.w >> 1, s0 = WS_H + (((16 * c.T + 8 * (c.m & 1) + 4 * (c.w & 1)) * 4 + c.a) * 42) * 1024;
    sh[0][0] = s0 + kh * (337 * 1024); sh[0][1] = s0 + (1 - kh) * (336 * 1024) + kh * 1024;
    sh[1][0] = sh[0][0] + kh * 2048; sh[1][1] = sh[0][1] + kh * 2048;
  }
  template <int U, int I> DI void dma1(const Ctx& c) {
    constexpr unsigned row = U < 18 ? 192 * (U % 6) + 64 * (U / 6) : 32 * (U - 18);
    if constexpr (I < 4) dma<slot(U) + I * 1152, (row + 4 * I) * 1152>(lw, v128, U < 18 ? c.rwd : c.rwo, U < 18 ? sd : so);
    else dma<slot(U) + (I - 4) * 1024, (row + 8 * (I - 4)) * 1152>(lw64, v64, U < 18 ? c.rwd : c.rwo, U < 18 ? sd : so);
  }
  template <int U, int RT> DI void ldb(const Ctx& c) { ldh<((RT & 1) * 168 + (U < 18 ? 2 * U : 18 + U)) * 1024>(bf[U][RT], vl16, c.rws, sh[U >= 18][RT >> 1]); }
};

template <int NT, class Hook> DI void phase2(const Ctx& c, P2<NT>& p, bf16x8 (&f)[16], Hook&& hook) {
  using P = P2<NT>;
  constexpr int PD = 3, AD = P::AD, ND = NT == 8 ? 4 : 6;
  const unsigned l = lane_id(), g = l >> 4, q = (l & 15) >> 2, pp = l & 3, kh = c.w >> 1;
  unsigned rb[4], rb64[4];
#pragma unroll
  for (int t = 0; t < 4; ++t) {
    rb[t] = P::win(0) + (8 * kh + g) * 1152 + q * 256 + 32 * (t ^ q) + 8 * pp; PIN(rb[t]);
    if (NT != 8) { rb64[t] = P::win(0) + S2 + (32 * kh + 4 * g + q) * 128 + 32 * (t ^ ((2 * g + (q >> 1)) & 3)) + 8 * pp; PIN(rb64[t]); }
  }
  const unsigned f1 = WS_F1 + (c.T * 4 + c.a) * 128;
  f32x4 acc[NT][4];
  s16x4 flo;
  auto rdlo = [&](auto U_, auto T_) { constexpr int t = CI(T_); constexpr unsigned o = P::slot(CI(U_)) - P::win(CI(U_)); return t < 8 ? trd(rb[t & 3] + o + 128 * (t >> 2)) : trd(rb64[t & 3] + o); };
  auto rdhi = [&](auto U_, auto T_) { constexpr int t = CI(T_); constexpr unsigned o = P::slot(CI(U_)) - P::win(CI(U_)); return t < 8 ? trd(rb[t & 3] + o + 128 * (t >> 2) + 4608) : trd(rb64[t & 3] + o + 2048); };
  if constexpr (NT != 8) {
    wait_bar<VM<2, 0>>();
    tieA(p.bf[0][0], p.bf[0][1], p.bf[0][2], p.bf[0][3]);
    static_for<PD>([&](auto T_) { f[CI(T_)] = cat(rdlo(IC(0), T_), rdhi(IC(0), T_)); });
  }
  static_for<21>([&](auto U_) {
    static_for<NT>([&](auto T_) {
      constexpr int U = CI(U_), t = CI(T_), N = U * NT + t + PD, UN = N / NT, tn = N % NT;
      constexpr int RG = UN, pos = tn;
      const bf16x8 a = f[t];
      static_for<4>([&](auto R_) {
        constexpr int R = CI(R_);
        mf<true, U == 0>(acc[t][R], a, p.bf[U][R]);
        if constexpr (R == 0 && UN <= 20) {
          if constexpr (tn == 0) {
            wait_bar<VM<(NT == 8 ? 0 : 2), P::B + UN>>();
            STAMP(P::B + UN + (NT == 8 ? 1 : 24));
            tieA(p.bf[UN][0], p.bf[UN][1], p.bf[UN][2], p.bf[UN][3]);
            if constexpr (P::win(UN) != P::win(UN - 1)) {
#pragma unroll
              for (int i = 0; i < 4; ++i) { rb[i] += P::win(UN) - P::win(UN - 1); PIN(rb[i]); if (NT != 8) { rb64[i] += P::win(UN) - P::win(UN - 1); PIN(rb64[i]); } }
            }
            if constexpr (UN + AD == 6 || UN + AD == 12) poll_check(c, p.pf, p.vf < 48 ? p.vf : 0, f1, (UN + AD) / 6 + 1);
            if constexpr (UN + AD == 18) poll_check(c, p.pf, 48 + (p.vf & 8), f1, 3);
            if constexpr (UN == 17) { PIN(p.omg); p.omg = 1.0f - (kh ? f32x4{p.omg[2], p.omg[3], p.omg[0], p.omg[1]} : p.omg); }
            hook(IC(UN));
          }
          flo = rdlo(IC(UN), IC(tn));
        }
        if constexpr (R == 1 && UN <= 20) f[tn] = cat(flo, rdhi(IC(UN), IC(tn)));
        constexpr int k = 2 * (pos - (RG == 0 ? PD : 0)) + (R - 2);
        if constexpr (R >= 2 && RG <= 20) {
          if constexpr (RG + AD <= 20) {
            if constexpr (k >= 0 && k < ND) p.template dma1<RG + AD, k>(c);
            if constexpr (k >= ND && k < ND + 4) p.template ldb<RG + AD, k - ND>(c);
          }
          if constexpr (k == ND + 4 && RG + AD == 18)
            ldg(p.omg, (l & 15) * 16, c.rws, WS_G + (c.a * 32 + 4 * c.T + 2 * (c.m & 1) + (c.w & 1)) * 256);
          if constexpr (k == ND + 5 && (RG + AD + 2 == 6 || RG + AD + 2 == 12)) poll_issue(c, p.pf, p.vf < 48 ? p.vf : 0, f1);
          if constexpr (k == ND + 5 && RG + AD + 2 == 18) poll_issue(c, p.pf, 48 + (p.vf & 8), f1);
        }
        constexpr int ts = U * NT + t - 2 - 17 * NT;
        if constexpr (ts >= 0 && ts < NT) { acc[ts][R] *= p.omg[R]; PIN(acc[ts][R]); }
        SBAR();
      });
    });
  });
  asm volatile("s_nop 7\n\ts_waitcnt lgkmcnt(0)\n\ts_barrier" ::: "memory");
  STAMP(NT == 8 ? 49 : 45);
  const unsigned l2 = lane_id();
  f32x4 val[NT][2];
  {
    const unsigned wa = c.w * (NT * 2048) + l2 * 16, ra = (c.w ^ 2) * (NT * 2048) + l2 * 16;
#pragma unroll
    for (int t = 0; t < NT; ++t) { ldsw<f32x4>(wa + t * 2048, acc[t][2]); ldsw<f32x4>(wa + t * 2048 + 1024, acc[t][3]); val[t][0] = acc[t][0]; val[t][1] = acc[t][1]; }
    asm volatile("s_waitcnt lgkmcnt(0)\n\ts_barrier" ::: "memory");
#pragma unroll
    for (int t = 0; t < NT; ++t) { val[t][0] += ldsr<f32x4>(ra + t * 2048); val[t][1] += ldsr<f32x4>(ra + t * 2048 + 1024); }
  }
  const unsigned pb = WS_PART + ((c.T * 8 + c.m) * 64 + c.w) * 3072;
#pragma unroll
  for (int t = 0; t < NT; ++t) {
    const int dq = t < 8 ? t >> 1 : t - 8, idx = t < 8 ? t & 1 : 2;
    if (dq != 0) {
      const u32x4 v = {cvtpk(val[t][0][0], val[t][0][1]), cvtpk(val[t][0][2], val[t][0][3]), cvtpk(val[t][1][0], val[t][1][1]), cvtpk(val[t][1][2], val[t][1][3])};
      __builtin_amdgcn_raw_buffer_store_b128(v, c.rws, l2 * 16, pb + ((dq ^ c.a) * 4 + c.a) * 12288 + idx * 1024, 16);
    }
  }
  asm volatile("s_waitcnt vmcnt(0)" ::: "memory");
  const unsigned f2 = WS_F2 + ((c.T * 8 + c.m) * 4 + c.w) * 128;
  if (l2 == 0) flag_store(c, f2 + 8 * c.a, 1);
  STAMP(NT == 8 ? 50 : 46);
  u32x2 pf = {0u, 0u};
  asm volatile("buffer_load_dwordx2 %0, %1, %2, %3 offen sc1\n\ts_waitcnt vmcnt(0)" : "=&v"(pf) : "v"(8 * (l2 & 3)), "s"(c.rws), "s"(f2) : "memory");
  poll_check(c, pf, 8 * (l2 & 3), f2, 1);
  STAMP(NT == 8 ? 51 : 47);
  constexpr int NI = NT / 4;
  u32x4 in[3][NI];
#pragma unroll
  for (int s = 1; s < 4; ++s)
#pragma unroll
    for (int i = 0; i < NI; ++i) in[s - 1][i] = __builtin_amdgcn_raw_buffer_load_b128(c.rws, l2 * 16, pb + (c.a * 4 + (s ^ c.a)) * 12288 + i * 1024, 16);
  const unsigned row0 = 256 * c.T + 128 * (c.m & 1) + 64 * (c.w & 1) + 32 * kh;
  const unsigned vo = (l2 & 15) * 1152 + 8 * (l2 >> 4), colb = NT == 8 ? 256 * (c.m >> 1) : 768;
#pragma unroll
  for (int i = 0; i < NI; ++i) {
    f32x4 s0 = val[i < 2 ? i : 8][0], s1 = val[i < 2 ? i : 8][1];
#pragma unroll
    for (int s = 0; s < 3; ++s) {
      s0 += f32x4{bflo(in[s][i][0]), bfhi(in[s][i][0]), bflo(in[s][i][1]), bfhi(in[s][i][1])};
      s1 += f32x4{bflo(in[s][i][2]), bfhi(in[s][i][2]), bflo(in[s][i][3]), bfhi(in[s][i][3])};
    }
    const unsigned cb = colb + (i < 2 ? 64 * c.a + 32 * i : 256 + 32 * c.a);
    __builtin_amdgcn_raw_buffer_store_b64(u32x2{cvtpk(s0[0], s0[1]), cvtpk(s0[2], s0[3])}, c.rout, vo, row0 * 1152 + cb, 0);
    __builtin_amdgcn_raw_buffer_store_b64(u32x2{cvtpk(s1[0], s1[1]), cvtpk(s1[2], s1[3])}, c.rout, vo, (row0 + 16) * 1152 + cb, 0);
  }
  if (NT == 8 && c.a == 0 && c.m == 0 && c.w == 0 && l2 == 0) flag_store(c, WS_E + c.T * 128, 16);
  STAMP(NT == 8 ? 52 : 48);
}

DI void role_mlp(const Ctx& c) {
  constexpr int PD = 3;
  const unsigned l = lane_id(), g = l >> 4, q = (l & 15) >> 2, pp = l & 3, r8 = l >> 3;
  const unsigned vW = (8 * (r8 >> 2) + (r8 & 3)) * 9216 + 16 * ((l & 7) ^ (((r8 >> 1) & 3) << 1));
  const unsigned vX = (l & 15) * 1152 + 16 * g;
  const unsigned lw = c.w * 1024, sW = (16 * (c.w & 1) + 4 * (c.w >> 1)) * 9216 + (1152 * c.a + 192 * c.m) * 2;
  const unsigned sX = (256 * c.T + 64 * c.w) * 1152;
  const unsigned sx[4] = {sX, sX + 18432, sX + 2 * 18432, sX + 3 * 18432};
  const unsigned sH = WS_H + (((16 * c.T + 4 * c.w) * 4 + c.a) * 42 + 2 * c.m) * 1024;
  const unsigned f1 = WS_F1 + (c.T * 4 + c.a) * 128;
  bf16x8 xf[18][4];
  auto dmaw = [&](auto N_, auto I_) {
    constexpr int N = CI(N_), I = CI(I_), pn = N / 9, ks = (N % 9) * 4 + I;
    dma<(N % 9) * S1 + I * 4096, (ks % 18) * 32 * 9216 + pn * 128>(lw, vW, ks < 18 ? c.rwg : c.rwu, sW);
  };
  auto ldxi = [&](auto C_, auto I_) {
    constexpr int C = CI(C_), I = CI(I_), ks = 4 * C + 2 * (I >> 3) + (I & 1), rt = (I >> 1) & 3;
    ldx<(ks < 16), ks * 64>(xf[ks][rt], vX, c.rx, sx[rt]);
  };
  static_for<3>([&](auto C_) { static_for<4>([&](auto I_) { dmaw(C_, I_); }); static_for<16>([&](auto I_) { ldxi(C_, I_); }); });
  unsigned rb[4];
#pragma unroll
  for (int t = 0; t < 4; ++t) rb[t] = (4 * g + q) * 128 + 32 * (t ^ ((2 * g + (q >> 1)) & 3)) + 8 * pp;
  f32x4 ag[4][4], au[4][4];
  unsigned sp[4][4][2];
  float e0 = 0.f, e1 = 0.f, e2 = 0.f;
  bf16x8 f[16];
  s16x4 flo;
  P2<8> p2;
  unsigned rb2[3];
  auto tiex = [&](auto C_) {
    constexpr int C = CI(C_);
    static_for<(C < 4 ? 4 : 2)>([&](auto K_) { constexpr int ks = 4 * C + CI(K_);
      if constexpr (ks < 16) tieA(xf[ks][0], xf[ks][1], xf[ks][2], xf[ks][3]); else tieV(xf[ks][0], xf[ks][1], xf[ks][2], xf[ks][3]); });
  };
  auto fin = [&](auto PN_, auto E_, auto R_) {
    constexpr int PN = CI(PN_), e = CI(E_), rt = CI(R_);
    u32x4 v;
#pragma unroll
    for (int i = 0; i < 4; ++i) { const int t = 2 * e + (i >> 1), k = 2 * (i & 1); v[i] = cvtpk(mixlo(au[t][rt][k], sp[t][rt][i & 1]), mixhi(au[t][rt][k + 1], sp[t][rt][i & 1])); }
    __builtin_amdgcn_raw_buffer_store_b128(v, c.rws, l * 16, sH + (rt * 168 + 12 * PN + e) * 1024, 16);
    asm volatile("" ::: "memory");
  };
  asm volatile("" ::"s"(c.E));
  wait_bar<VM<0, 0>>();
  STAMP(1);
  tiex(IC(0));
  static_for<PD>([&](auto I_) { f[CI(I_)] = cat(trd(rb[CI(I_)]), trd(rb[CI(I_)] + 2048)); });
  static_for<27>([&](auto N_) {
    static_for<16>([&](auto I_) {
      constexpr int N = CI(N_), I = CI(I_), F = N * 16 + I, pn = F / 144, FP = F % 144, ps = FP / 72, FF_ = FP % 72, ks = FF_ / 4, t = FF_ % 4;
      constexpr int FN = F + PD, RG = FN / 16, pos = FN % 16;
      const bf16x8 a = f[I];
      static_for<4>([&](auto R_) {
        constexpr int R = CI(R_);
        if constexpr (ps == 0) mf<(ks < 16), ks == 0>(ag[t][R], a, xf[ks][R]); else mf<(ks < 16), ks == 0>(au[t][R], a, xf[ks][R]);
        if constexpr (R == 0) {
          if constexpr (pos == 0) {
            wait_bar<VM<0, RG>>();
            STAMP(1 + RG);
            if constexpr (RG <= 4) tiex(IC(RG));
            if constexpr (RG < 27) {
              if constexpr (RG % 9 % 4 == 0) {
#pragma unroll
                for (int i = 0; i < 4; ++i) { rb[i] += RG % 9 == 0 ? 0u - 8 * S1 : 4 * S1; PIN(rb[i]); }
              }
            } else {
              tieA(p2.bf[0][0], p2.bf[0][1], p2.bf[0][2], p2.bf[0][3]);
              const unsigned l4 = lane_id();
#pragma unroll
              for (int i = 0; i < 3; ++i) rb2[i] = (8 * (c.w >> 1) + (l4 >> 4)) * 1152 + ((l4 >> 2) & 3) * 256 + 32 * (i ^ ((l4 >> 2) & 3)) + 8 * (l4 & 3);
            }
            if constexpr (RG == 12 || RG == 21) { if (threadIdx.x == 0) flag_store(c, f1 + 8 * c.m, RG / 9); }
            if constexpr (RG == 22) p2.init(c);
            if constexpr (RG == 24) poll_check(c, p2.pf, p2.vf < 48 ? p2.vf : 0, f1, 1);
          }
          if constexpr (RG < 27) flo = trd(rb[pos & 3] + (RG % 9 % 4) * S1 + (pos >> 2) * 4096); else flo = trd(rb2[pos]);
        }
        if constexpr (R == 1) { if constexpr (RG < 27) f[pos] = cat(flo, trd(rb[pos & 3] + (RG % 9 % 4) * S1 + (pos >> 2) * 4096 + 2048)); else f[pos] = cat(flo, trd(rb2[pos] + 4608)); }
        constexpr int k = 2 * (pos - (RG == 0 ? PD : 0)) + (R - 2);
        if constexpr (R >= 2) {
          if constexpr (RG == 0) { if constexpr (k >= 0 && k < 4) dmaw(IC(3), IC(k)); if constexpr (k >= 4 && k < 20) ldxi(IC(3), IC(k - 4)); }
          if constexpr (RG == 1) { if constexpr (k < 4) dmaw(IC(4), IC(k)); if constexpr (k >= 4 && k < 12) ldxi(IC(4), IC(k - 4)); if constexpr (k >= 12 && k < 16) dmaw(IC(5), IC(k - 12)); }
          if constexpr (RG == 2 || RG == 3) { if constexpr (R == 3 && pos < 8) dmaw(IC(2 * RG + 2 + pos / 4), IC(pos % 4)); }
          if constexpr (RG >= 4 && RG + 6 <= 26) { if constexpr (R == 3 && pos % 2 == 0 && pos < 8) dmaw(IC(RG + 6), IC(pos / 2)); }
          if constexpr (RG == 9 || RG == 10 || RG == 18 || RG == 19) { if constexpr (R == 2 && pos >= 8 && pos % 2 == 0) fin(IC(RG / 9 - 1), IC(RG % 9), IC((pos - 8) / 2)); }
          if constexpr (RG == 22 && k == 8) poll_issue(c, p2.pf, p2.vf < 48 ? p2.vf : 0, f1);
          if constexpr (RG >= 24 && RG <= 26 && R == 3) {
            constexpr int U = 2 * (RG - 24) + pos / 8, i = pos % 8;
            if constexpr (U <= 4) { if constexpr (i < 4) p2.template dma1<U, i>(c); else p2.template ldb<U, i - 4>(c); }
            if constexpr (RG == 26 && pos == 9) poll_issue(c, p2.pf, p2.vf < 48 ? p2.vf : 0, f1);
          }
        }
        if constexpr (ps == 1 && FF_ >= 4 && FF_ <= 68) {
          constexpr int e = FF_ - 4, d = e - 1;
          if constexpr (R == 0 && e < 64) { e0 = ag[e / 16][(e / 4) % 4][e % 4] * -1.4426950408889634f; PIN(e0); }
          if constexpr (R == 0 && e > 0) { e1 = 1.0f + e1; PIN(e1); }
          if constexpr (R == 1 && e > 0) { if constexpr (d % 2 == 0) { e2 = __builtin_amdgcn_rcpf(e1); PIN(e2); } else { e1 = __builtin_amdgcn_rcpf(e1); PIN(e1); } }
          if constexpr (R == 2 && e > 0 && d % 2 == 1) { sp[d / 16][(d / 4) % 4][(d % 4) / 2] = pkh(e2, e1); PIN(sp[d / 16][(d / 4) % 4][(d % 4) / 2]); }
          if constexpr (R == 3 && e < 64) { e1 = __builtin_amdgcn_exp2f(e0); PIN(e1); }
        }
        SBAR();
      });
    });
  });
  asm volatile("s_nop 7" ::: "memory");
  static_for<8>([&](auto J_) { fin(IC(2), IC(CI(J_) / 4), IC(CI(J_) % 4)); });
  SBAR();
  phase2<8>(c, p2, f, [&](auto U_) { if constexpr (CI(U_) == 2) { if (threadIdx.x == 0) flag_store(c, f1 + 8 * c.m, 3); } });
}

DI unsigned arow(unsigned r) { return ((r & 7) >> 1) * APL2 + (r & 1) * APL + (r >> 3) * AP; }

DI void role_att(const Ctx& c) {
  constexpr int PD = 3;
  const unsigned mp = c.m & 1;
  {
    const unsigned l = lane_id(), g = l >> 4, q = (l & 15) >> 2, pp = l & 3, j = l & 15;
    const unsigned lc = (l & 15) ^ (g << 1), hb = 144 * (2 * c.a + mp);
    const unsigned vA = g * 3456 + hb + (lc < 9 ? 1152 + 16 * lc : 2304 + 16 * (lc - 9));
    const unsigned vB = g * 3456 + hb + (lc < 2 ? 2304 + 16 * (7 + lc) : 16 * (lc - 2));
    const u64 maskB = __builtin_amdgcn_ballot_w64(lc < 11);
    const unsigned vX = j * 1152 + 16 * g;
    const int hr = 256 * (int)c.T - 16 + (int)j;
    const unsigned vHl = (unsigned)(hr < 0 ? 0 : hr) * 1152 + 16 * g;
    const unsigned lw = c.w * 1152, sW = 8 * c.w * 3456;
    const unsigned sX = (256 * c.T + 64 * c.w) * 1152;
    const unsigned sx[4] = {sX, sX + 18432, sX + 2 * 18432, sX + 3 * 18432};
    bf16x8 xf[18][4], hf[36];
    auto dmaw = [&](auto N_, auto I_) {
      constexpr int N = CI(N_), I = CI(I_);
      constexpr unsigned LO = (N % 4) * S2 + I * 4608, SO = (64 * (N % 9) + 32 * (I >> 1) + 4 * (I & 1)) * 3456;
      if constexpr (N < 9) dma<LO, SO>(lw, vA, c.rqkv, sW); else dma_m<LO, SO>(lw, vB, c.rqkv, sW, maskB);
    };
    auto ldxi = [&](auto C_, auto I_) { constexpr int ks = 2 * CI(C_) + (CI(I_) & 1), rt = CI(I_) >> 1; ldx<(ks < 16), ks * 64>(xf[ks][rt], vX, c.rx, sx[rt]); };
    auto ldhl = [&](auto C_, auto I_) { constexpr int s = 2 * CI(C_) + CI(I_); ldx<false, (s % 18) * 64>(hf[s], vHl, c.rx, 0); };
    if (c.w == 0) { dma<A_GW, 0>(0, l * 16, c.rgw, 0); dma<A_GW + 1024, 1024>(0, l * 16, c.rgw, 0); }
    static_for<3>([&](auto C_) {
      static_for<4>([&](auto I_) { dmaw(C_, I_); }); static_for<8>([&](auto I_) { ldxi(C_, I_); }); static_for<2>([&](auto I_) { ldhl(C_, I_); }); });
    unsigned rb[4];
#pragma unroll
    for (int t = 0; t < 4; ++t) rb[t] = g * 1152 + q * 256 + 32 * (t ^ c.w ^ q) + 8 * pp;
    const unsigned gsrc = A_GW + 2 * (32 * (l >> 5) + 8 * ((l >> 2) & 3) + 4 * ((l >> 4) & 1) + (l & 3));
    const unsigned gdst = (l >> 2) * 1152 + (l & 3) * 256 + 16 * (11 ^ ((l & 3) << 1));
    u16 gwv = 0;
    f32x4 acc[8][4], ah[2];
    bf16x8 f[16];
    s16x4 flo;
    auto tie = [&](auto C_) {
      constexpr int C = CI(C_);
      if constexpr (C <= 8) static_for<2>([&](auto K_) { constexpr int ks = 2 * C + CI(K_);
        if constexpr (ks < 16) tieA(xf[ks][0], xf[ks][1], xf[ks][2], xf[ks][3]); else tieV(xf[ks][0], xf[ks][1], xf[ks][2], xf[ks][3]); });
      PIN(hf[2 * C]); PIN(hf[2 * C + 1]);
    };
    auto put = [&](const f32x4& v, unsigned brow, unsigned ic) {
      const unsigned mat = ic >= 144 ? 2 : ic >= 72 ? 1 : 0;
      const unsigned base = mat == 2 ? A_Q - 288 - 2 * AP : mat == 1 ? A_V - 144 : A_K;
      ldsw<u32x2>(base + arow(brow) + 2 * ic, u32x2{cvtpk(v[0], v[1]), cvtpk(v[2], v[3])});
    };
    asm volatile("" ::"s"(c.E));
    wait_bar<VM<1, 0>>();
    STAMP(1);
    tie(IC(0));
    static_for<PD>([&](auto I_) { f[CI(I_)] = cat(trd(rb[CI(I_)]), trd(rb[CI(I_)] + 4608)); });
    auto pass = [&](auto PS_) {
      constexpr int PS = CI(PS_), NT = PS == 0 ? 8 : 6, FC = 2 * NT;
      static_for<9>([&](auto N_) {
        static_for<FC>([&](auto I_) {
          constexpr int N = CI(N_), I = CI(I_), ks = 2 * N + I / NT, t = I % NT;
          constexpr int IN = I + PD, RGL = N + IN / FC, pos = IN % FC, RG = 9 * PS + RGL;
          constexpr int NTN = RG < 9 ? 8 : 6, tn = pos % NTN;
          constexpr unsigned ro = (RG % 2) * S2 + (pos / NTN) * 9216 + 128 * (tn >> 2);
          const bf16x8 a = f[I];
          static_for<5>([&](auto R_) {
            constexpr int R = CI(R_);
            if constexpr (R < 4) mf<(ks < 16), ks == 0>(acc[t][R], a, xf[ks][R]);
            else if constexpr (t == 0 || (PS == 0 && t == 4)) mf<false, ks == 0>(ah[t >> 2], a, hf[18 * PS + ks]);
            if constexpr (R == 0 && RG < 18) {
              if constexpr (pos == 0) {
                wait_bar<VM<1, RG>>();
                STAMP(1 + RG);
                tie(IC(RG));
                if constexpr (RG % 2 == 0 || RG == 9) {
#pragma unroll
                  for (int i = 0; i < 4; ++i) { if (RG == 9) rb[i] ^= 32 * c.w; else rb[i] += RG % 4 == 0 ? 0u - 2 * S2 : 2 * S2; PIN(rb[i]); }
                }
              }
              flo = trd(rb[tn & 3] + ro);
            }
            if constexpr (R == 1 && RG < 18) f[pos] = cat(flo, trd(rb[tn & 3] + ro + 4608));
            constexpr int k = 2 * (pos - (RG == 0 ? PD : 0)) + (R - 2), C = RG + 3;
            if constexpr ((R == 2 || R == 3) && C <= 17) {
              constexpr int NX = C <= 8 ? 8 : 0;
              if constexpr (k >= 0 && k < 4) dmaw(IC(C), IC(k));
              if constexpr (k >= 4 && k < 4 + NX) ldxi(IC(C), IC(k - 4));
              if constexpr (k >= 4 + NX && k < 6 + NX) ldhl(IC(C), IC(k - 4 - NX));
              if constexpr (C >= 9 && k == 8) gwv = ldsr<u16>(gsrc + 128 * (C - 9));
              if constexpr (C >= 9 && k == 14) ldsw<u16>(gdst + (C % 4) * S2, gwv);
            }
            SBAR();
          });
        });
      });
    };
    pass(IC(0));
    asm volatile("s_nop 7" ::: "memory");
    {
      const unsigned l3 = lane_id(), g3 = l3 >> 4, j3 = l3 & 15;
#pragma unroll
      for (int t = 0; t < 8; ++t) {
        const unsigned ic = 16 * (t ^ c.w) + 4 * g3;
#pragma unroll
        for (int rt = 0; rt < 4; ++rt) put(acc[t][rt], 16 + 64 * c.w + 16 * rt + j3, ic);
        if (t % 4 == 0) put(ah[t >> 2], j3, ic);
      }
    }
    SBAR();
    pass(IC(1));
    asm volatile("s_nop 7\n\ts_waitcnt lgkmcnt(0)\n\ts_barrier" ::: "memory");
    STAMP(19);
    {
      const unsigned l3 = lane_id(), g3 = l3 >> 4, j3 = l3 & 15;
#pragma unroll
      for (int t = 0; t < 6; ++t) {
        const unsigned ic = 128 + 16 * t + 4 * g3;
        if (t < 5 || g3 < 2) {
#pragma unroll
          for (int rt = 0; rt < 4; ++rt) put(acc[t][rt], 16 + 64 * c.w + 16 * rt + j3, ic);
        }
      }
      put(ah[0], j3, 128 + 4 * g3);
      if (g3 == 2) {
        const f32x4 gv = {sigm(acc[5][0][0]), sigm(acc[5][1][0]), sigm(acc[5][2][0]), sigm(acc[5][3][0])};
#pragma unroll
        for (int rt = 0; rt < 4; ++rt) ldsw<float>(A_G + 4 * (64 * c.w + 16 * rt + j3), gv[rt]);
        if (mp == 0) __builtin_amdgcn_raw_buffer_store_b128(__builtin_bit_cast(u32x4, gv), c.rws, j3 * 16, WS_G + (c.a * 32 + 4 * c.T + c.w) * 256, 16);
      }
    }
    asm volatile("s_waitcnt lgkmcnt(0)\n\ts_barrier" ::: "memory");
    STAMP(20);
  }
  {
    const unsigned l = lane_id(), hd = c.w;
    const unsigned lr = (l & 1) * APL + (l >> 1) * AP;
    const unsigned b8 = lr + 36 * hd + 4 * (hd & 1), b4 = lr + 36 * hd + ((hd & 1) ? 0 : 32);
    const int R0 = 8 * (int)(l >> 1) + (int)(l & 1);
    auto rd9 = [&](unsigned (&d)[9], unsigned base, int rr) {
      const unsigned o = base + ((rr & 7) >> 1) * APL2 + (rr >> 3) * AP;
#pragma unroll
      for (int i = 0; i < 4; ++i) { const u32x2 t = ldsr<u32x2>(o + b8 + 8 * i); d[2 * i] = t[0]; d[2 * i + 1] = t[1]; }
      d[8] = ldsr<unsigned>(o + b4);
    };
    unsigned qv[4][9];
#pragma unroll
    for (int i = 0; i < 4; ++i) rd9(qv[i], A_Q, 2 * i);
    float lg[4][8];
#pragma unroll
    for (int kk = 0; kk < 11; ++kk) {
      unsigned kv[9];
      rd9(kv, A_K, 2 + 2 * kk);
      const float bias = 256 * (int)c.T + R0 - 14 + 2 * kk >= 0 ? 0.f : -1e30f;
#pragma unroll
      for (int i = 0; i < 4; ++i) if (kk >= i && kk <= i + 7) {
        float s = bias;
#pragma unroll
        for (int d = 0; d < 9; ++d) s = dot2(qv[i][d], kv[d], s);
        lg[i][kk - i] = s;
      }
    }
    constexpr float SC2 = 0.23570226039551584f * 1.4426950408889634f;
#pragma unroll
    for (int i = 0; i < 4; ++i) {
      float mx = fmaxf(fmaxf(fmaxf(lg[i][0], lg[i][1]), fmaxf(lg[i][2], lg[i][3])), fmaxf(fmaxf(lg[i][4], lg[i][5]), fmaxf(lg[i][6], lg[i][7])));
      const float nm = -mx * SC2;
      float s = 0.f;
#pragma unroll
      for (int k = 0; k < 8; ++k) { lg[i][k] = __builtin_amdgcn_exp2f(__builtin_fmaf(lg[i][k], SC2, nm)); s += lg[i][k]; }
      const float sc = ldsr<float>(A_G + 4 * (R0 + 2 * i)) * __builtin_amdgcn_rcpf(s);
#pragma unroll
      for (int k = 0; k < 8; ++k) lg[i][k] *= sc;
    }
    float ov[4][18];
#pragma unroll
    for (int i = 0; i < 4; ++i)
#pragma unroll
      for (int d = 0; d < 18; ++d) ov[i][d] = 0.f;
#pragma unroll
    for (int mm = 0; mm < 6; ++mm) {
      unsigned v0[9], v1[9];
      rd9(v0, A_V, 2 + 4 * mm);
      rd9(v1, A_V, mm < 5 ? 4 + 4 * mm : 2 + 4 * mm);
      unsigned pl[9], ph[9];
#pragma unroll
      for (int d = 0; d < 9; ++d) { pl[d] = __builtin_amdgcn_perm(v1[d], v0[d], 0x05040100u); ph[d] = __builtin_amdgcn_perm(v1[d], v0[d], 0x07060302u); }
#pragma unroll
      for (int i = 0; i < 4; ++i) {
        const int k0 = 2 * mm - i, k1 = k0 + 1;
        if (k1 >= 0 && k0 <= 7) {
          const unsigned pp = cvtpk(k0 >= 0 ? lg[i][k0 >= 0 ? k0 : 0] : 0.f, k1 <= 7 ? lg[i][k1 <= 7 ? k1 : 0] : 0.f);
#pragma unroll
          for (int d = 0; d < 9; ++d) { ov[i][2 * d] = dot2(pl[d], pp, ov[i][2 * d]); ov[i][2 * d + 1] = dot2(ph[d], pp, ov[i][2 * d + 1]); }
        }
      }
    }
#pragma unroll
    for (int i = 0; i < 4; ++i) {
      const unsigned o = A_Q + i * APL2;
#pragma unroll
      for (int d = 0; d < 4; ++d) ldsw<u32x2>(o + b8 + 8 * d, u32x2{cvtpk(ov[i][4 * d], ov[i][4 * d + 1]), cvtpk(ov[i][4 * d + 2], ov[i][4 * d + 3])});
      ldsw<unsigned>(o + b4, cvtpk(ov[i][16], ov[i][17]));
    }
  }
  P2<12> p2;
  p2.init(c);
  const unsigned f1 = WS_F1 + (c.T * 4 + c.a) * 128;
  poll_issue(c, p2.pf, p2.vf < 48 ? p2.vf : 0, f1);
  asm volatile("s_waitcnt vmcnt(0) lgkmcnt(0)\n\ts_barrier" ::: "memory");
  STAMP(21);
  {
    const unsigned l = lane_id(), g = l >> 4, j = l & 15;
    const unsigned sH = WS_H + (((16 * c.T + 4 * c.w) * 4 + c.a) * 42 + 36 + 3 * mp) * 1024;
#pragma unroll
    for (int rt = 0; rt < 4; ++rt) {
      const unsigned ra = A_Q + arow(64 * c.w + 16 * rt + j) + 8 * g;
#pragma unroll
      for (int i = 0; i < 3; ++i) {
        u32x2 lo = ldsr<u32x2>(ra + 64 * i), hi = {0u, 0u};
        if (i < 2) hi = ldsr<u32x2>(ra + 64 * i + 32); else if (g >= 2) lo = u32x2{0u, 0u};
        __builtin_amdgcn_raw_buffer_store_b128(u32x4{lo[0], lo[1], hi[0], hi[1]}, c.rws, l * 16, sH + (rt * 168 + i) * 1024, 16);
      }
    }
    asm volatile("" ::: "memory");
  }
  static_for<ATT_AD>([&](auto U_) { static_for<6>([&](auto I_) { p2.template dma1<CI(U_), CI(I_)>(c); }); });
  poll_check(c, p2.pf, p2.vf < 48 ? p2.vf : 0, f1, 1);
  static_for<ATT_AD>([&](auto U_) { static_for<4>([&](auto I_) { p2.template ldb<CI(U_), CI(I_)>(c); }); });
  STAMP(22);
  bf16x8 f[16];
  phase2<12>(c, p2, f, [&](auto U_) { if constexpr (CI(U_) == 1) { if (threadIdx.x == 0) flag_store(c, f1 + 8 * c.m, 3); } });
}

__global__ __launch_bounds__(256, 1) void fused_kernel(const u16* x, const u16* wqkv, const u16* wo, const u16* wg, const u16* wu, const u16* wd,
                                                        const u16* gw, char* ws, u16* out) {
  __shared__ __attribute__((aligned(16))) char smem[LDS_BYTES];
  asm volatile("" ::"v"((unsigned)(size_t)smem));
  Ctx c;
  c.rx = __builtin_amdgcn_make_buffer_rsrc((void*)x, (short)0, SEQ * DM * 2, 0x00020000);
  c.rqkv = __builtin_amdgcn_make_buffer_rsrc((void*)wqkv, (short)0, DM * 3 * DM * 2, 0x00020000);
  c.rwo = __builtin_amdgcn_make_buffer_rsrc((void*)wo, (short)0, DM * DM * 2, 0x00020000);
  c.rwg = __builtin_amdgcn_make_buffer_rsrc((void*)wg, (short)0, DM * FF * 2, 0x00020000);
  c.rwu = __builtin_amdgcn_make_buffer_rsrc((void*)wu, (short)0, DM * FF * 2, 0x00020000);
  c.rwd = __builtin_amdgcn_make_buffer_rsrc((void*)wd, (short)0, DM * FF * 2, 0x00020000);
  c.rgw = __builtin_amdgcn_make_buffer_rsrc((void*)gw, (short)0, DM * 2, 0x00020000);
  c.rws = __builtin_amdgcn_make_buffer_rsrc((void*)ws, (short)0, WS_BYTES, 0x00020000);
  c.rout = __builtin_amdgcn_make_buffer_rsrc((void*)out, (short)0, SEQ * DM * 2, 0x00020000);
  const unsigned bid = blockIdx.x, xcd = bid & 7, idx = bid >> 3;
  c.w = __builtin_amdgcn_readfirstlane(threadIdx.x >> 6);
  c.a = xcd & 3; c.T = 4 * (xcd >> 2) + (idx >> 3); c.m = idx & 7;
  c.E = *(const __attribute__((address_space(4))) u64*)(ws + WS_E + c.T * 128);
  STAMP(0);
  if (c.m < 6) role_mlp(c); else role_att(c);
}

extern "C" void kernel_launch(void* const* d_in, const int* in_sizes, int n_in, void* d_out, int out_size, void* d_ws, size_t ws_size,
                              hipStream_t stream) {
  fused_kernel<<<256, 256, 0, stream>>>((const u16*)d_in[0], (const u16*)d_in[1], (const u16*)d_in[2], (const u16*)d_in[4], (const u16*)d_in[5],
                                        (const u16*)d_in[6], (const u16*)d_in[7], (char*)d_ws, (u16*)d_out);
}
```

```cpp
#include <hip/hip_runtime.h>
#include <utility>

typedef unsigned short u16;
typedef unsigned long long u64;
typedef short s16x4 __attribute__((ext_vector_type(4)));
typedef __bf16 bf16x8 __attribute__((ext_vector_type(8)));
typedef __bf16 bf16x2 __attribute__((ext_vector_type(2)));
typedef float f32x2 __attribute__((ext_vector_type(2)));
typedef float f32x4 __attribute__((ext_vector_type(4)));
typedef unsigned u32x2 __attribute__((ext_vector_type(2)));
typedef unsigned u32x4 __attribute__((ext_vector_type(4)));
typedef unsigned u32x6 __attribute__((ext_vector_type(6)));
typedef unsigned u32x10 __attribute__((ext_vector_type(10)));
typedef __amdgpu_buffer_rsrc_t rsrc_t;
#define LAS __attribute__((address_space(3)))
#define DI __device__ __forceinline__
#define SBAR() __builtin_amdgcn_sched_barrier(0)
#define IC(x) std::integral_constant<int, (x)>{}
#define CI(x) decltype(x)::value

constexpr int SEQ = 2048, DM = 576, FF = 4608;
constexpr unsigned WS_H = 0;
constexpr unsigned WS_PART = 24u << 20;
constexpr unsigned WS_G = 40u << 20;
constexpr unsigned WS_F1 = 41u << 20;
constexpr unsigned WS_F2 = WS_F1 + 8192;
constexpr unsigned WS_E = WS_F2 + 65536;
constexpr unsigned WS_BYTES = 42u << 20;
constexpr unsigned LDS_BYTES = 163840;
constexpr unsigned S1 = 16384;
constexpr unsigned S2 = 18432;
constexpr unsigned S3 = S2 + 9216;
constexpr unsigned M_X = 65536;
constexpr unsigned A_X = 4 * S2;
constexpr unsigned A_HX = A_X + 65536;
constexpr unsigned A_GW = A_HX + 18432;
constexpr unsigned A_KX = 83968;
constexpr unsigned A_VX = A_KX + 18 * 2560;
static_assert(A_GW + 2048 <= LDS_BYTES && A_VX <= A_HX && A_VX + 20480 <= LDS_BYTES && 3 * S3 <= A_KX, "lds");

template <int... Is> using iseq = std::integer_sequence<int, Is...>;
template <class Fn, int... Is> DI void static_for_(Fn&& f, iseq<Is...>) { (f(std::integral_constant<int, Is>{}), ...); }
template <int N, class Fn> DI void static_for(Fn&& f) { static_for_(f, std::make_integer_sequence<int, N>{}); }

template <int N> DI void wait_bar() { asm volatile("s_waitcnt vmcnt(%0) lgkmcnt(0)\n\ts_barrier" ::"i"(N) : "memory"); }
template <int N> DI void wait_vm() { asm volatile("s_waitcnt vmcnt(%0)" ::"i"(N) : "memory"); }
template <class T> DI void PIN(T& x) { asm volatile("" : "+v"(x)); }
DI void tieA(bf16x8& a, bf16x8& b, bf16x8& c, bf16x8& d) { asm volatile("" : "+a"(a), "+a"(b), "+a"(c), "+a"(d)); }
template <unsigned LO, unsigned SO> DI unsigned dma(unsigned lb, unsigned voff, rsrc_t srd, unsigned sb) {
  unsigned t;
  asm volatile("s_add_u32 m0, %1, %5\n\ts_add_u32 %0, %4, %6\n\tbuffer_load_dwordx4 %2, %3, %0 offen lds"
               : "=&s"(t) : "s"(lb), "v"(voff), "s"(srd), "s"(sb), "i"(LO), "i"(SO) : "memory", "scc");
  return t;
}
template <unsigned IMM> DI void dma_n(unsigned voff, rsrc_t srd, unsigned so) {
  asm volatile("buffer_load_dwordx4 %0, %1, %2 offen offset:%3 lds" ::"v"(voff), "s"(srd), "s"(so), "i"(IMM) : "memory");
}
template <unsigned LO> DI void dma_l(unsigned lb, unsigned voff, rsrc_t srd, unsigned so) {
  asm volatile("s_add_u32 m0, %0, %4\n\ts_nop 0\n\tbuffer_load_dwordx4 %1, %2, %3 offen lds" ::"s"(lb), "v"(voff), "s"(srd), "s"(so), "i"(LO) : "memory", "scc");
}
template <unsigned LO, unsigned SO> DI void dma_m(unsigned lb, unsigned voff, rsrc_t srd, unsigned sb, u64 mask) {
  unsigned t;
  asm volatile("s_add_u32 m0, %1, %5\n\ts_add_u32 %0, %4, %6\n\ts_mov_b64 exec, %7\n\tbuffer_load_dwordx4 %2, %3, %0 offen lds\n\ts_mov_b64 exec, -1"
               : "=&s"(t) : "s"(lb), "v"(voff), "s"(srd), "s"(sb), "i"(LO), "i"(SO), "s"(mask) : "memory", "scc");
}
template <unsigned IMM> DI void ldh(bf16x8& d, unsigned voff, rsrc_t srd, unsigned so) {
  asm volatile("buffer_load_dwordx4 %0, %1, %2, %3 offen offset:%4 sc1" : "=a"(d) : "v"(voff), "s"(srd), "s"(so), "i"(IMM) : "memory");
}
DI void ldg(f32x4& d, unsigned voff, rsrc_t srd, unsigned so) { asm volatile("buffer_load_dwordx4 %0, %1, %2, %3 offen sc1" : "=v"(d) : "v"(voff), "s"(srd), "s"(so) : "memory"); }
DI void touch(unsigned& d, unsigned voff, rsrc_t srd, unsigned so) {
  asm volatile("buffer_load_dword %0, %1, %2, %3 offen sc1" : "+v"(d) : "v"(voff), "s"(srd), "s"(so) : "memory");
}
template <bool AG, bool ZERO> DI void mf(f32x4& c, const bf16x8& a, const bf16x8& b) {
  if constexpr (ZERO) {
    if constexpr (AG) asm volatile("v_mfma_f32_16x16x32_bf16 %0, %1, %2, 0" : "=&v"(c) : "v"(a), "a"(b));
    else asm volatile("v_mfma_f32_16x16x32_bf16 %0, %1, %2, 0" : "=&v"(c) : "v"(a), "v"(b));
  } else {
    if constexpr (AG) asm volatile("v_mfma_f32_16x16x32_bf16 %0, %1, %2, %0" : "+v"(c) : "v"(a), "a"(b));
    else asm volatile("v_mfma_f32_16x16x32_bf16 %0, %1, %2, %0" : "+v"(c) : "v"(a), "v"(b));
  }
}
template <bool AG, bool ZERO> DI void mfs(f32x4& c, const bf16x8& a, const bf16x8& b) {
  if constexpr (ZERO) {
    if constexpr (AG) asm volatile("v_mfma_f32_16x16x32_bf16 %0, %2, %1, 0" : "=&v"(c) : "v"(a), "a"(b));
    else asm volatile("v_mfma_f32_16x16x32_bf16 %0, %2, %1, 0" : "=&v"(c) : "v"(a), "v"(b));
  } else {
    if constexpr (AG) asm volatile("v_mfma_f32_16x16x32_bf16 %0, %2, %1, %0" : "+v"(c) : "v"(a), "a"(b));
    else asm volatile("v_mfma_f32_16x16x32_bf16 %0, %2, %1, %0" : "+v"(c) : "v"(a), "v"(b));
  }
}
DI f32x4 mfc(const u32x4& a, const u32x4& b, const f32x4& c) { f32x4 d; asm volatile("v_mfma_f32_16x16x32_bf16 %0, %1, %2, %3" : "=&v"(d) : "v"(a), "v"(b), "v"(c)); return d; }
DI f32x4 mfz(const u32x4& a, const u32x4& b) { f32x4 d; asm volatile("v_mfma_f32_16x16x32_bf16 %0, %1, %2, 0" : "=&v"(d) : "v"(a), "v"(b)); return d; }
DI float max2(float a, float b) { float r; asm("v_max_f32 %0, %1, %2" : "=v"(r) : "v"(a), "v"(b)); return r; }
DI float max3(float a, float b, float c) { float r; asm("v_max3_f32 %0, %1, %2, %3" : "=v"(r) : "v"(a), "v"(b), "v"(c)); return r; }
DI s16x4 trd(unsigned a) { return __builtin_amdgcn_ds_read_tr16_b64_v4i16((LAS s16x4*)a); }
DI bf16x8 cat(s16x4 lo, s16x4 hi) { return __builtin_bit_cast(bf16x8, __builtin_shufflevector(lo, hi, 0, 1, 2, 3, 4, 5, 6, 7)); }
DI unsigned cvtpk(float lo, float hi) { f32x2 v = {lo, hi}; return __builtin_bit_cast(unsigned, __builtin_convertvector(v, bf16x2)); }
DI float w_mul(float a) { float r; asm volatile("v_mul_f32 %0, 0xbfb8aa3b, %1" : "=v"(r) : "v"(a)); return r; }
DI float w_exp(float a) { float r; asm volatile("v_exp_f32 %0, %1" : "=v"(r) : "v"(a)); return r; }
DI unsigned w_add1(float a) { unsigned r; asm volatile("v_fma_mixlo_f16 %0, %1, 1.0, 1.0" : "=v"(r) : "v"(a)); return r; }
DI unsigned w_rcplo(unsigned a) { unsigned r; asm volatile("v_rcp_f16 %0, %1" : "=v"(r) : "v"(a)); return r; }
DI void w_rcphi(unsigned& d, unsigned a) { asm volatile("v_rcp_f16_sdwa %0, %1 dst_sel:WORD_1 dst_unused:UNUSED_PRESERVE src0_sel:WORD_0" : "+v"(d) : "v"(a)); }
DI float w_mixlo(float u, unsigned s) { float r; asm volatile("v_fma_mix_f32 %0, %1, %2, 0 op_sel_hi:[0,1,0]" : "=v"(r) : "v"(u), "v"(s)); return r; }
DI float w_mixhi(float u, unsigned s) { float r; asm volatile("v_fma_mix_f32 %0, %1, %2, 0 op_sel:[0,1,0] op_sel_hi:[0,1,0]" : "=v"(r) : "v"(u), "v"(s)); return r; }
DI unsigned w_cvt(float lo, float hi) { unsigned r; asm volatile("v_cvt_pk_bf16_f32 %0, %1, %2" : "=v"(r) : "v"(lo), "v"(hi)); return r; }
DI float w_scale(float a, float s) { float r; asm volatile("v_mul_f32 %0, %1, %2" : "=v"(r) : "v"(a), "v"(s)); return r; }
DI float sigm(float x) { return __builtin_amdgcn_rcpf(1.0f + __builtin_amdgcn_exp2f(-1.4426950408889634f * x)); }
DI float bflo(unsigned v) { return __uint_as_float(v << 16); }
DI float bfhi(unsigned v) { return __uint_as_float(v & 0xffff0000u); }
template <class T> DI T ldsr(unsigned a) { return *(const LAS T*)a; }
template <class T> DI void ldsw(unsigned a, T v) { *(LAS T*)a = v; }
DI unsigned lane_id() { unsigned l; asm volatile("v_mbcnt_lo_u32_b32 %0, -1, 0\n\tv_mbcnt_hi_u32_b32 %0, -1, %0" : "=v"(l)); return l; }

#define STAMP(i)

struct Ctx {
  rsrc_t rx, rqkv, rwo, rwg, rwu, rwd, rgw, rws, rout;
  unsigned w, a, T, m;
  unsigned kg[9];
  u64 E;
};

enum { O_NONE, O_W, O_X, O_HX, O_FIN, O_POLL, O_D2, O_B2, O_GATE, O_ST, O_TW, O_TH };
constexpr int NEVER = 1 << 20;
struct Op { int kind, a, b, need; };
constexpr int PD = 3;
constexpr int MLP_AD = 5, ATT_AD = 3;
constexpr Op NOP_ = {O_NONE, 0, 0, 0};
constexpr Op p2_op(int N, int R, int NT, int AD, int FB) {
  const int ND = NT == 8 ? 4 : 6, RG = (N + PD) / NT, pos = (N + PD) % NT, k = 2 * (pos - (RG == 0 ? PD : 1)) + (R - 2), U = RG + AD;
  if (RG > 20 || k < 0) return NOP_;
  if (U <= 20 && k < ND) return {O_D2, U, k, FB + U * NT - PD};
  if (U <= 20 && k < ND + 4) return {O_B2, U, k - ND, FB + U * NT - PD};
  if (U == 18 && k == ND + 4) return {O_GATE, 0, 0, FB + 17 * NT - PD};
  if ((U + 2 == 6 || U + 2 == 12 || U + 2 == 18) && k == ND + 5) return {O_POLL, (U + 2) / 6, 0, FB + (RG + 2) * NT - PD};
  return NOP_;
}
constexpr int bar1(int n) { return n == 0 ? -1 : 16 * n - PD; }
constexpr Op opW(int n, int i) { return {O_W, n, i, bar1(n)}; }
constexpr Op opX(int c, int i) { return {O_X, c, i, c == 0 ? -1 : 8 * c - 5}; }
constexpr Op mlp_op(int F, int R) {
  if (F < 0) {
    const int i = F + 22;
    if (R != 2 || i < 0 || i >= 20) return NOP_;
    return i < 4 ? opW(0, i) : opX((i - 4) / 8, (i - 4) % 8);
  }
  if (F >= 440) return p2_op(F - 440, R, 8, MLP_AD, 440);
  if (F >= 432) return R == 2 ? Op{O_ST, 0, 0, 440 + 2 * 8 - PD} : NOP_;
  const int RG = (F + PD) / 16, pos = (F + PD) % 16, r = R - 2;
  if (pos == 0) return NOP_;
  if (F < 61) {
    constexpr int tab[13][2] = {{0, 2}, {4, -1}, {6, 3}, {10, -2}, {14, 4}, {18, 5}, {22, -3}, {24, 6}, {30, 7}, {34, -4}, {38, 8}, {42, -5}, {46, -6}};
    for (int i = 0; i < 13; ++i) {
      const int d = F - tab[i][0];
      if (d >= 0 && d < (tab[i][1] > 0 ? 4 : 2)) return tab[i][1] > 0 ? opX(tab[i][1], 2 * d + r) : opW(-tab[i][1], 2 * d + r);
    }
    return NOP_;
  }
  if (R == 2) {
    if (F >= 144 && F % 144 >= 4 && F % 144 < 36 && F % 4 == 3) return {O_FIN, F / 144 - 1, (F % 144 - 4) / 4, bar1(F < 288 ? 13 : 22)};
    return NOP_;
  }
  if (RG >= 5 && RG <= 8) return pos % 4 == 1 ? opW(RG + 4, pos / 4) : NOP_;
  constexpr int first[27] = {0, 0, 0, 0, 7, 0, 0, 0, 0, 13, 15, 17, 19, 21, 0, 0, 0, 0, 22, 24, 26, 0, 0, 0, 0, 0, 0};
  constexpr int cnt[27] = {0, 0, 0, 0, 2, 0, 0, 0, 0, 2, 2, 2, 2, 1, 0, 0, 0, 0, 2, 2, 1, 0, 0, 0, 0, 0, 0};
  if (RG < 27 && pos >= 3 && pos - 3 < 4 * cnt[RG]) return opW(first[RG] + (pos - 3) / 4, (pos - 3) % 4);
  if ((RG == 13 || RG == 22) && pos == 9) return {O_TH, RG / 9 - 1, 0, RG == 22 ? bar1(24) : NEVER};
  if ((RG == 11 || RG == 12) && pos == 13) return {O_TW, RG - 11, 0, NEVER};
  if (RG == 22 && pos == 7) return {O_POLL, 0, 0, bar1(24)};
  if (RG == 20 && pos >= 8 && pos < 12) return {O_D2, 0, pos - 8, bar1(27)};
  if ((RG == 21 || RG == 22) && pos >= 2 && pos < 6) return {O_D2, RG - 20, pos - 2, 440 + (RG - 20) * 8 - PD};
  if (RG >= 24 && RG <= 26) {
    const int k = (RG - 24) * 15 + pos - 1;
    if (k < 12) { const int U = k / 4; return {O_B2, U, k % 4, U == 0 ? bar1(27) : 440 + U * 8 - PD}; }
    if (k >= 16 && k < 24) { const int i = k - 16; return i < 4 ? Op{O_D2, 3, i, 440 + 24 - PD} : Op{O_B2, 3, i - 4, 440 + 24 - PD}; }
    if (k >= 31 && k < 39) { const int i = k - 31; return i < 4 ? Op{O_D2, 4, i, 440 + 32 - PD} : Op{O_B2, 4, i - 4, 440 + 32 - PD}; }
    if (k == 40) return {O_POLL, 1, 0, 440 + 8 - PD};
  }
  return NOP_;
}
constexpr int barA(int n) { return n == 0 ? -1 : n <= 9 ? 16 * n - PD : 144 + 14 * (n - 9) - PD; }
constexpr Op att1_op(int F, int R) {
  if (F < 0) {
    const int i = F + 23;
    if (R != 2 || i < 0 || i >= 21) return NOP_;
    if (i < 4) return {O_W, 0, i, -1};
    if (i < 12) return {O_X, 0, i - 4, -1};
    if (i == 12) return {O_HX, 0, 0, -1};
    return {O_X, 1, i - 13, 6};
  }
  const int FP = F + PD, RG = FP < 144 ? FP / 16 : 9 + (FP - 144) / 14, pos = FP < 144 ? FP % 16 : (FP - 144) % 14, r = R - 2;
  if (pos == 0) return NOP_;
  if (F < 2) return {O_W, 1, 2 * F + r, barA(1)};
  if (F < 6) return {O_X, 2, 2 * (F - 2) + r, 16 * 2 - 10};
  if (F < 8) return {O_W, 2, 2 * (F - 6) + r, barA(2)};
  if (F == 8) return {O_HX, 1 + r, 0, barA(2 + 2 * r)};
  if (F < 144 && F % 16 >= 9 && F % 16 < 13 && F / 16 + 3 <= 8) { const int c = F / 16 + 3; return {O_X, c, 2 * (F % 16 - 9) + r, 16 * c - 10}; }
  if (RG == 1 && pos <= 2) return {O_W, 3, 2 * (pos - 1) + r, barA(3)};
  if (RG == 1 && pos == 3) return {O_HX, 3 + r, 0, barA(5)};
  if (R == 2) return NOP_;
  if (RG == 1 && pos >= 4 && pos < 8) return {O_W, 4, pos - 4, barA(4)};
  if (RG >= 2 && RG + 3 <= 17 && pos <= 4) return {O_W, RG + 3, pos - 1, barA(RG + 3)};
  if ((RG == 10 || RG == 11) && pos == 8) return {O_TW, RG - 10, 0, barA(15)};
  return NOP_;
}
constexpr Op att2_op(int F, int R) {
  if (F < 0) {
    const int i = F + 50;
    if (R != 2 || i < 0 || i >= 42) return NOP_;
    constexpr int n1 = 12 - PD, n2 = 24 - PD;
    if (i < 6) return {O_D2, 0, i, -1};
    if (i < 10) return {O_B2, 0, i - 6, -1};
    if (i < 16) return {O_D2, 1, i - 10, n1};
    if (i < 19) return {O_ST, 0, 0, n2};
    if (i < 23) return {O_B2, 1, i - 19, n1};
    if (i < 29) return {O_D2, 2, i - 23, n2};
    if (i < 32) return {O_ST, 0, 0, n2};
    if (i < 36) return {O_B2, 2, i - 32, n2};
    return {O_ST, 0, 0, n2};
  }
  return p2_op(F, R, 12, ATT_AD, 0);
}
template <int WHICH> constexpr Op op_at(int F, int R) { return WHICH == 0 ? mlp_op(F, R) : WHICH == 1 ? att1_op(F, R) : att2_op(F, R); }
template <int WHICH> constexpr int vm_at(int tau) {
  int s = 0;
  for (int F = tau - 1; F >= -64; --F)
    for (int R = 3; R >= 2; --R) {
      const Op o = op_at<WHICH>(F, R);
      if (o.kind == O_NONE) continue;
      if (o.need <= tau || s >= 63) return s;
      ++s;
    }
  return s;
}
template <int WHICH, int TAU> constexpr int VM = vm_at<WHICH>(TAU);

DI void flag_store(const Ctx& c, unsigned off, unsigned val) {
  const u64 v = c.E + val;
  __builtin_amdgcn_raw_buffer_store_b64(u32x2{(unsigned)v, (unsigned)(v >> 32)}, c.rws, 0, off, 16);
}
DI void poll_issue(const Ctx& c, u32x2& f, unsigned voff, unsigned off) {
  asm volatile("buffer_load_dwordx2 %0, %1, %2, %3 offen sc1" : "=v"(f) : "v"(voff), "s"(c.rws), "s"(off) : "memory");
}
DI bool flag_ok(const Ctx& c, u32x2 f, unsigned need) {
  const u64 d = (((u64)f[1] << 32) | f[0]) - c.E - need;
  return __builtin_amdgcn_ballot_w64(d >= 8) == 0;
}
DI void poll_check(const Ctx& c, u32x2& f, unsigned voff, unsigned off, unsigned need) {
  asm volatile("" : "+v"(f));
  if (__builtin_expect(!flag_ok(c, f, need), 0)) {
    for (unsigned spin = 0; spin < (1u << 16); ++spin) {
      __builtin_amdgcn_s_sleep(1);
      asm volatile("buffer_load_dwordx2 %0, %1, %2, %3 offen sc1\n\ts_waitcnt vmcnt(0)" : "=&v"(f) : "v"(voff), "s"(c.rws), "s"(off) : "memory");
      if (flag_ok(c, f, need)) break;
    }
  }
}

template <int NT> struct P2 {
  static constexpr int AD = NT == 8 ? MLP_AD : ATT_AD, NS = NT == 8 ? 8 : 4, FB = NT == 8 ? 440 : 0, ROT = NT == 8 ? 5 : 0;
  static constexpr unsigned SS = NT == 8 ? S2 : S3;
  static constexpr unsigned slot(int u) { return ((u + ROT) % NS) * SS; }
  static constexpr int WN = NT == 8 ? 3 : 2;
  static constexpr unsigned win(int u) { return (((u + ROT) % NS) / WN * WN) * SS; }
  unsigned v128[2], v64, vh[2], vf;
  unsigned lw, lw64, sd, so, sh[2];
  unsigned f1, cur, curh;
  bf16x8 bf[21][4];
  u32x2 pf;
  f32x4 omg;
  DI void init(const Ctx& c) {
    const unsigned l = lane_id(), colb = NT == 8 ? 256 * (c.m >> 1) : 768, kh = c.w >> 1;
    const unsigned lo = 16 * ((l & 15) ^ ((l >> 3) & 2) ^ (4 * c.a));
    v128[0] = (l >> 4) * 4608 + lo; v128[1] = (l >> 4) * 4608 + (lo ^ 64);
    v64 = (l >> 3) * 2304 + 16 * ((l & 7) ^ (((l >> 1) & 4) | ((l >> 3) & 2)) ^ (2 * c.a)) + 256;
    vh[0] = l * 16 + kh * 2048; vh[1] = l * 16 + (1 - kh) * 2048;
    vf = 8 * (l & 7);
    lw = c.w * 4608; lw64 = c.w * 2304 + S2;
    sd = (1152 * c.a + 16 * c.w) * 1152 + colb;
    so = (144 * c.a + 72 * kh + 16 * (c.w & 1)) * 1152 + colb;
    sh[0] = WS_H + (((4 * c.T + 2 * (c.m & 1) + (c.w & 1)) * 4 + c.a) * 42 + kh) * 4096; sh[1] = sh[0] + kh * 8192;
    f1 = WS_F1 + (c.T * 4 + c.a) * 128;
  }
  template <int S> DI unsigned pvo() const { return S < 3 ? (vf < 48 ? vf : 0) : 48 + (vf & 8); }
  template <int S> DI void poll(const Ctx& c) { poll_issue(c, pf, pvo<S>(), f1); }
  template <int S> DI void check(const Ctx& c) { poll_check(c, pf, pvo<S>(), f1, S < 3 ? S + 1 : 3); }
  template <int KIND, int U, int I> DI void issue(const Ctx& c) {
    if constexpr (KIND == O_D2) {
      constexpr unsigned row = U < 18 ? 192 * (U % 6) + 64 * (U / 6) : 32 * (U - 18);
      const rsrc_t rs = U < 18 ? c.rwd : c.rwo;
      if constexpr (I == 0) cur = dma<slot(U), row * 1152>(lw, v128[0], rs, U < 18 ? sd : so);
      else if constexpr (I < 4) dma_n<I * 1152>(v128[I >> 1], rs, cur);
      else if constexpr (I == 4) dma_l<slot(U)>(lw64, v64, rs, cur);
      else dma_n<1152>(v64, rs, cur);
    }
    if constexpr (KIND == O_B2) {
      if constexpr (I == 0) curh = sh[U >= 18] + (U < 18 ? 2 * U : 18 + U) * 4096;
      ldh<(I & 1) * 1024>(bf[U][I], vh[I >> 1], c.rws, curh);
    }
    if constexpr (KIND == O_POLL) poll<U>(c);
    if constexpr (KIND == O_GATE) ldg(omg, vh[0] & 255, c.rws, WS_G + (c.a * 32 + 4 * c.T + 2 * (c.m & 1) + (c.w & 1)) * 256);
  }
  static DI unsigned rbase(const Ctx& c, unsigned l, int t) {
    const unsigned g = l >> 4, q = (l >> 2) & 3;
    return (c.w >> 1) * 9216 + q * 1152 + g * 256 + 32 * (t ^ ((q & 2) | (g & 1))) + 8 * (l & 3);
  }
};

template <int NT> DI void phase2(Ctx& c, P2<NT>& p, bf16x8 (&f)[16]) {
  using P = P2<NT>;
  constexpr int AD = P::AD, WH = NT == 8 ? 0 : 2;
  const unsigned l = lane_id(), g = l >> 4, q = (l >> 2) & 3, kh = c.w >> 1;
  unsigned rb[4], rb64[4];
#pragma unroll
  for (int t = 0; t < 4; ++t) {
    rb[t] = P::win(0) + P::rbase(c, l, t); PIN(rb[t]);
    if (NT != 8) { rb64[t] = P::win(0) + S2 + kh * 4608 + (q & 1) * 1152 + (2 * g + (q >> 1)) * 128 + 32 * (t ^ ((q & 2) | (g & 1))) + 8 * (l & 3); PIN(rb64[t]); }
  }
  f32x4 acc[NT][4];
  s16x4 flo;
  auto rdlo = [&](auto U_, auto T_) { constexpr int t = CI(T_); constexpr unsigned o = P::slot(CI(U_)) - P::win(CI(U_)); return t < 8 ? trd(rb[t & 3] + o + 128 * (t >> 2)) : trd(rb64[t & 3] + o); };
  auto rdhi = [&](auto U_, auto T_) { constexpr int t = CI(T_); constexpr unsigned o = P::slot(CI(U_)) - P::win(CI(U_)); return t < 8 ? trd(rb[t & 3] + o + 128 * (t >> 2) + 4608) : trd(rb64[t & 3] + o + 2304); };
  if constexpr (NT != 8) {
    wait_bar<VM<2, -1>>();
    STAMP(24);
    tieA(p.bf[0][0], p.bf[0][1], p.bf[0][2], p.bf[0][3]);
    static_for<PD>([&](auto T_) { f[CI(T_)] = cat(rdlo(IC(0), T_), rdhi(IC(0), T_)); });
  }
  static_for<21>([&](auto U_) {
    static_for<NT>([&](auto T_) {
      constexpr int U = CI(U_), t = CI(T_), N = U * NT + t, UN = (N + PD) / NT, tn = (N + PD) % NT;
      constexpr int RR = tn == 0 ? 2 : 0;
      const bf16x8 a = f[t];
      static_for<4>([&](auto R_) {
        constexpr int R = CI(R_);
        mf<true, U == 0>(acc[t][R], a, p.bf[U][R]);
        SBAR();
        if constexpr (tn == 0 && UN <= 20) {
          if constexpr (R < 2 && P::win(UN) != P::win(UN - 1)) {
#pragma unroll
            for (int i = 2 * R; i < 2 * R + 2; ++i) { rb[i] += P::win(UN) - P::win(UN - 1); PIN(rb[i]); if (NT != 8) { rb64[i] += P::win(UN) - P::win(UN - 1); PIN(rb64[i]); } }
          }
          if constexpr (R == 2) {
            wait_bar<VM<WH, P::FB + N>>();
            STAMP(UN + (NT == 8 ? 28 : 24));
            tieA(p.bf[UN][0], p.bf[UN][1], p.bf[UN][2], p.bf[UN][3]);
          }
        }
        if constexpr (R == RR && UN <= 20) flo = rdlo(IC(UN), IC(tn));
        if constexpr (R == RR + 1 && UN <= 20) f[tn] = cat(flo, rdhi(IC(UN), IC(tn)));
        SBAR();
        if constexpr (tn == 0 && R == 3 && UN <= 20) {
          if constexpr (UN == 2) { if (threadIdx.x == 0) flag_store(c, p.f1 + 8 * c.m, 3); }
          if constexpr ((UN + AD) % 6 == 0 && UN + AD <= 18) p.template check<(UN + AD) / 6>(c);
          if constexpr (UN == 17) { PIN(p.omg); p.omg = 1.0f - (kh ? f32x4{p.omg[2], p.omg[3], p.omg[0], p.omg[1]} : p.omg); PIN(p.omg); }
        }
        if constexpr (R >= 2) { constexpr Op o = op_at<WH>(P::FB + N, R); p.template issue<o.kind, o.a, o.b>(c); }
        constexpr int ts = N - 2 - 17 * NT;
        if constexpr (ts >= 0 && ts < NT) { acc[ts][R] *= p.omg[R]; PIN(acc[ts][R]); }
        SBAR();
      });
    });
  });
  asm volatile("s_nop 7\n\ts_waitcnt lgkmcnt(0)\n\ts_barrier" ::: "memory");
  STAMP(NT == 8 ? 49 : 45);
  const unsigned l2 = lane_id();
  f32x4 val[NT][2];
  {
    const unsigned wa = c.w * (NT * 2048) + l2 * 16, ra = (c.w ^ 2) * (NT * 2048) + l2 * 16;
#pragma unroll
    for (int t = 0; t < NT; ++t) { ldsw<f32x4>(wa + t * 2048, acc[t][2]); ldsw<f32x4>(wa + t * 2048 + 1024, acc[t][3]); val[t][0] = acc[t][0]; val[t][1] = acc[t][1]; }
    asm volatile("s_waitcnt lgkmcnt(0)\n\ts_barrier" ::: "memory");
#pragma unroll
    for (int t = 0; t < NT; ++t) { val[t][0] += ldsr<f32x4>(ra + t * 2048); val[t][1] += ldsr<f32x4>(ra + t * 2048 + 1024); }
  }
  const unsigned pb = WS_PART + ((c.T * 8 + c.m) * 64 + c.w) * 3072;
#pragma unroll
  for (int t = 0; t < NT; ++t) {
    const int dq = t < 8 ? t >> 1 : t - 8, idx = t < 8 ? t & 1 : 2;
    if (dq != 0) {
      const u32x4 v = {cvtpk(val[t][0][0], val[t][0][1]), cvtpk(val[t][0][2], val[t][0][3]), cvtpk(val[t][1][0], val[t][1][1]), cvtpk(val[t][1][2], val[t][1][3])};
      __builtin_amdgcn_raw_buffer_store_b128(v, c.rws, l2 * 16, pb + ((dq ^ c.a) * 4 + c.a) * 12288 + idx * 1024, 16);
    }
  }
  asm volatile("s_waitcnt vmcnt(0)" ::: "memory");
  const unsigned f2 = WS_F2 + ((c.T * 8 + c.m) * 4 + c.w) * 128;
  if (l2 == 0) flag_store(c, f2 + 8 * c.a, 1);
  STAMP(NT == 8 ? 50 : 46);
  u32x2 pf = {0u, 0u};
  asm volatile("buffer_load_dwordx2 %0, %1, %2, %3 offen sc1\n\ts_waitcnt vmcnt(0)" : "=&v"(pf) : "v"(8 * (l2 & 3)), "s"(c.rws), "s"(f2) : "memory");
  poll_check(c, pf, 8 * (l2 & 3), f2, 1);
  STAMP(NT == 8 ? 51 : 47);
  constexpr int NI = NT / 4;
  u32x4 in[3][NI];
#pragma unroll
  for (int s = 1; s < 4; ++s)
#pragma unroll
    for (int i = 0; i < NI; ++i) in[s - 1][i] = __builtin_amdgcn_raw_buffer_load_b128(c.rws, l2 * 16, pb + (c.a * 4 + (s ^ c.a)) * 12288 + i * 1024, 16);
  const unsigned row0 = 256 * c.T + 128 * (c.m & 1) + 64 * (c.w & 1) + 32 * kh;
  const unsigned vo = (l2 & 15) * 2304 + 8 * (l2 >> 4), colb = NT == 8 ? 256 * (c.m >> 1) : 768;
#pragma unroll
  for (int i = 0; i < NI; ++i) {
    f32x4 s0 = val[i < 2 ? i : 8][0], s1 = val[i < 2 ? i : 8][1];
#pragma unroll
    for (int s = 0; s < 3; ++s) {
      s0 += f32x4{bflo(in[s][i][0]), bfhi(in[s][i][0]), bflo(in[s][i][1]), bfhi(in[s][i][1])};
      s1 += f32x4{bflo(in[s][i][2]), bfhi(in[s][i][2]), bflo(in[s][i][3]), bfhi(in[s][i][3])};
    }
    const unsigned cb = colb + (i < 2 ? 64 * c.a + 32 * i : 256 + 32 * c.a);
    __builtin_amdgcn_raw_buffer_store_b64(u32x2{cvtpk(s0[0], s0[1]), cvtpk(s0[2], s0[3])}, c.rout, vo, row0 * 1152 + cb, 0);
    __builtin_amdgcn_raw_buffer_store_b64(u32x2{cvtpk(s1[0], s1[1]), cvtpk(s1[2], s1[3])}, c.rout, vo, (row0 + 1) * 1152 + cb, 0);
  }
  if (NT == 8 && c.a == 0 && c.m == 0 && c.w == 0 && l2 == 0) flag_store(c, WS_E + c.T * 128, 16);
  STAMP(NT == 8 ? 52 : 48);
}

struct XS {
  unsigned v[2], lw, sx, rd[2], cur;
  DI void init(const Ctx& c, unsigned base) {
    const unsigned l = lane_id(), j = l & 15, g = l >> 4;
    v[0] = (l >> 3) * 2304 + 16 * ((l & 7) ^ g); v[1] = (l >> 3) * 2304 + 16 * ((l & 7) ^ (4 + g));
    lw = base + c.w * 8192; sx = (256 * c.T + 64 * c.w) * 1152;
    rd[0] = lw + j * 128 + 16 * (g ^ (j >> 1)); rd[1] = rd[0] ^ 64;
    PIN(rd[0]); PIN(rd[1]);
  }
  template <int NSL, int C, int I> DI void dmax(const Ctx& c) {
    if constexpr (I == 0) cur = sx + c.kg[C] * 128;
    dma<(C % NSL) * 32768 + I * 1024, (32 * (I >> 2) + 16 * (I & 1) + ((I >> 1) & 1)) * 1152>(lw, v[I & 1], c.rx, cur);
  }
};
template <int I> DI void touch_w2(const Ctx& c, unsigned& junk) {
  const unsigned l = lane_id(), idx = (c.T & 3) * 8 + c.m;
  if constexpr (I == 0) touch(junk, l * 128, c.rwd, (1152 * c.a + 36 * idx) * 1152 + c.w * 8192);
  else touch(junk, l * 128, c.w < 2 ? c.rwd : c.rwo, c.w < 2 ? (1152 * c.a + 36 * idx) * 1152 + 32768 + c.w * 512 : 144 * c.a * 1152 + idx * 6144);
}

constexpr int slot1(int n) { return n < 8 ? n % 4 : (n - 4) % 9; }
constexpr int sig_idx(int F) {
  if ((F + PD) % 16 == 0) return -1;
  int e = 0;
  for (int G = F - F % 72; G < F; ++G) if ((G + PD) % 16 != 0) ++e;
  return e <= 65 ? e : -1;
}
DI void role_mlp(Ctx& c) {
  const unsigned l = lane_id(), g = l >> 4, q = (l & 15) >> 2, pp = l & 3, r8 = l >> 3;
  const unsigned vW = (8 * (r8 >> 2) + (r8 & 3)) * 9216 + 16 * ((l & 7) ^ (((r8 >> 1) & 3) << 1));
  const unsigned lw = c.w * 1024, sW = (16 * (c.w & 1) + 4 * (c.w >> 1)) * 9216 + (1152 * c.a + 192 * c.m) * 2;
  unsigned sWk[9];
#pragma unroll
  for (int i = 0; i < 9; ++i) sWk[i] = sW + c.kg[i] * (64 * 9216);
  const unsigned sH = WS_H + (((4 * c.T + c.w) * 4 + c.a) * 42 + 2 * c.m) * 4096;
  const unsigned f1 = WS_F1 + (c.T * 4 + c.a) * 128;
  XS xs; xs.init(c, M_X);
  bf16x8 xf[18][4];
  f32x4 ag[4][4], au[4][4];
  unsigned sp[4][4][2];
  P2<8> p2;
  unsigned junk = 0;
  u32x4 hv;
  unsigned vl16 = l * 16;
  auto issue = [&](auto F_, auto R_) {
    constexpr Op o = mlp_op(CI(F_), CI(R_));
    if constexpr (o.kind == O_W) {
      constexpr int pn = o.a / 9, ks = (o.a % 9) * 4 + o.b, k = ks % 18;
      dma<slot1(o.a) * S1 + o.b * 4096, (k & 1) * 32 * 9216 + pn * 128>(lw, vW, ks < 18 ? c.rwg : c.rwu, sWk[k >> 1]);
    } else if constexpr (o.kind == O_X) {
      if constexpr (o.a >= 3 && o.b == 0) asm volatile("s_waitcnt lgkmcnt(4)" ::: "memory");
      xs.template dmax<3, o.a, o.b>(c);
    } else if constexpr (o.kind == O_FIN) {
      __builtin_amdgcn_raw_buffer_store_b128(hv, c.rws, vl16 + (o.b & 3) * 1024, sH + (12 * o.a + (o.b >> 2)) * 4096, 16);
      asm volatile("" ::: "memory");
    } else if constexpr (o.kind == O_TW) touch_w2<o.a>(c, junk);
    else if constexpr (o.kind == O_TH) touch(junk, vl16 * 8, c.rws, sH + 12 * o.a * 4096);
    else p2.template issue<o.kind, o.a, o.b>(c);
  };
  static_for<20>([&](auto I_) { issue(IC(CI(I_) - 22), IC(2)); });
  unsigned rdx2[2] = {xs.rd[0] + 65536, xs.rd[1] + 65536}; PIN(rdx2[0]); PIN(rdx2[1]);
  auto rdx = [&](auto C_, auto I_) {
    constexpr int C = CI(C_), s = CI(I_) >> 2, rt = CI(I_) & 3;
    xf[2 * C + s][rt] = ldsr<bf16x8>((C % 3 == 2 ? rdx2[s] : xs.rd[s] + (C % 3) * 32768) + rt * 2048);
  };
  unsigned rb[4];
#pragma unroll
  for (int t = 0; t < 4; ++t) rb[t] = (4 * g + q) * 128 + 32 * (t ^ ((2 * g + (q >> 1)) & 3)) + 8 * pp;
  float e0 = 0.f, e1 = 0.f, e3 = 0.f, h0 = 0.f, h1 = 0.f;
  unsigned e2 = 0;
  bf16x8 f[16];
  s16x4 flo;
  unsigned rb2[3];
  asm volatile("" ::"s"(c.E));
  wait_bar<VM<0, -1>>();
  STAMP(1);
  static_for<4>([&](auto I_) { rdx(IC(0), I_); });
  static_for<PD>([&](auto I_) { f[CI(I_)] = cat(trd(rb[CI(I_)]), trd(rb[CI(I_)] + 2048)); });
  static_for<4>([&](auto I_) { rdx(IC(0), IC(4 + CI(I_))); });
  SBAR();
  static_for<27>([&](auto N_) {
    static_for<16>([&](auto I_) {
      constexpr int N = CI(N_), I = CI(I_), F = N * 16 + I, FP = F % 144, ps = FP / 72, FQ = FP % 72, ks = FQ / 4, t = FQ % 4;
      constexpr int RG = (F + PD) / 16, pos = (F + PD) % 16, RR = pos == 0 ? 2 : 0;
      constexpr unsigned ro = (slot1(RG) % 4) * S1 + (pos >> 2) * 4096;
      const bf16x8 a = f[I];
      static_for<4>([&](auto R_) {
        constexpr int R = CI(R_);
        if constexpr (ps == 0) mf<(ks < 16), ks == 0>(ag[t][R], a, xf[ks][R]); else mf<(ks < 16), ks == 0>(au[t][R], a, xf[ks][R]);
        SBAR();
        if constexpr (R == 0 && F < 64 && F % 8 == 3) wait_vm<VM<0, F>>();
        if constexpr (pos == 0) {
          if constexpr (R < 2 && RG < 27 && slot1(RG) / 4 != slot1(RG - 1) / 4) {
#pragma unroll
            for (int i = 2 * R; i < 2 * R + 2; ++i) { rb[i] += (unsigned)(slot1(RG) / 4 - slot1(RG - 1) / 4) * 4 * S1; PIN(rb[i]); }
          }
          if constexpr (R == 0 && RG == 27) {
            const unsigned l4 = lane_id();
#pragma unroll
            for (int i = 0; i < 3; ++i) { rb2[i] = P2<8>::slot(0) + P2<8>::rbase(c, l4, i); PIN(rb2[i]); }
          }
          if constexpr (R == 2) {
            wait_bar<VM<0, F>>();
            STAMP(1 + RG);
            if constexpr (RG == 27) tieA(p2.bf[0][0], p2.bf[0][1], p2.bf[0][2], p2.bf[0][3]);
          }
        }
        if constexpr (R == RR) { if constexpr (RG < 27) flo = trd(rb[pos & 3] + ro); else flo = trd(rb2[pos]); }
        if constexpr (R == RR + 1) { if constexpr (RG < 27) f[pos] = cat(flo, trd(rb[pos & 3] + ro + 2048)); else f[pos] = cat(flo, trd(rb2[pos] + 4608)); }
        if constexpr (F < 64 && (F % 8 == 3 || F % 8 == 4)) rdx(IC(F / 8 + 1), IC(4 * (F % 8 - 3) + R));
        SBAR();
        if constexpr (pos == 0 && R == 3) {
          if constexpr (RG == 13 || RG == 22) { if (threadIdx.x == 0) flag_store(c, f1 + 8 * c.m, RG / 9); }
          if constexpr (RG == 20) p2.init(c);
          if constexpr (RG == 24) { PIN(junk); p2.template check<0>(c); }
        }
        if constexpr (ps == 1 && sig_idx(F) >= 0) {
          constexpr int e = sig_idx(F), d = e - 2;
          if constexpr (R == 0 && d >= 0) e2 = w_add1(e1);
          if constexpr (R == 1) { e3 = e0; if constexpr (e < 64) e0 = w_mul(ag[e / 16][(e / 4) % 4][e % 4]); }
          if constexpr (R == 2 && e >= 1 && e <= 64) e1 = w_exp(e3);
          if constexpr (R == 3 && d >= 0) { if constexpr (d % 2 == 0) sp[d / 16][(d / 4) % 4][(d % 4) / 2] = w_rcplo(e2); else w_rcphi(sp[d / 16][(d / 4) % 4][(d % 4) / 2], e2); }
        }
        if constexpr (F >= 144 && FP >= 4 && FP < 36) {
          constexpr int J = (FP - 4) / 4, i = FP % 4, e = J >> 2, rt = J & 3, tt = 2 * e + (i >> 1), k = 2 * (i & 1);
          if constexpr (R == 0) h0 = w_mixlo(au[tt][rt][k], sp[tt][rt][i & 1]);
          if constexpr (R == 1) h1 = w_mixhi(au[tt][rt][k + 1], sp[tt][rt][i & 1]);
          if constexpr (R == 2) hv[i] = w_cvt(h0, h1);
        }
        if constexpr (R >= 2) issue(IC(F), R_);
        SBAR();
      });
    });
  });
  asm volatile("s_nop 7" ::: "memory");
  static_for<8>([&](auto J_) {
    constexpr int e = CI(J_) >> 2, rt = CI(J_) & 3;
    u32x4 v;
#pragma unroll
    for (int i = 0; i < 4; ++i) { const int tt = 2 * e + (i >> 1), k = 2 * (i & 1); v[i] = w_cvt(w_mixlo(au[tt][rt][k], sp[tt][rt][i & 1]), w_mixhi(au[tt][rt][k + 1], sp[tt][rt][i & 1])); }
    __builtin_amdgcn_raw_buffer_store_b128(v, c.rws, vl16 + rt * 1024, sH + (24 + e) * 4096, 16);
    asm volatile("" ::: "memory");
  });
  SBAR();
  phase2<8>(c, p2, f);
}

DI u32x2 pk4(const f32x4& v) { return u32x2{cvtpk(v[0], v[1]), cvtpk(v[2], v[3])}; }
DI void sw16(float& a, float& b) { auto r = __builtin_amdgcn_permlane16_swap(__float_as_uint(a), __float_as_uint(b), false, false); a = __uint_as_float(r[0]); b = __uint_as_float(r[1]); }
DI void sw32(float& a, float& b) { auto r = __builtin_amdgcn_permlane32_swap(__float_as_uint(a), __float_as_uint(b), false, false); a = __uint_as_float(r[0]); b = __uint_as_float(r[1]); }
template <bool MX> DI float xred(float (&v)[4]) {
  sw16(v[0], v[1]); sw16(v[2], v[3]);
  float a = MX ? max2(v[0], v[1]) : v[0] + v[1], b = MX ? max2(v[2], v[3]) : v[2] + v[3];
  sw32(a, b);
  return MX ? max2(a, b) : a + b;
}
DI void xbc(float t, float (&v)[4]) {
  float a = t, b = t;
  sw32(a, b);
  v[0] = a; v[1] = a; v[2] = b; v[3] = b;
  sw16(v[0], v[1]); sw16(v[2], v[3]);
}

DI void role_att(Ctx& c) {
  const unsigned mp = c.m & 1;
  u32x2 vp[4][5], qp[4][5], hp[2];
  f32x4 gate;
  {
    const unsigned l = lane_id(), g = l >> 4, q = (l & 15) >> 2, pp = l & 3;
    const unsigned lc = (l & 15) ^ (g << 1), hb = 144 * (2 * c.a + mp);
    const unsigned vA = g * 3456 + hb + (lc < 10 ? 1152 + 16 * (lc < 9 ? lc : 8) : 2304 + 16 * (lc - 10));
    const unsigned vB = g * 3456 + hb + (lc < 4 ? 2304 + 16 * (lc < 3 ? 6 + lc : 8) : 16 * (lc - 4));
    const u64 maskB = __builtin_amdgcn_ballot_w64(lc < 13);
    const unsigned lw = c.w * 1152;
    unsigned sWk[9];
#pragma unroll
    for (int i = 0; i < 9; ++i) sWk[i] = (8 * c.w + 64 * c.kg[i]) * 3456;
    const int hr = 256 * (int)c.T + ((c.w & 1) ? -16 : -15) + 2 * (int)(l >> 3);
    const unsigned vHx = (unsigned)(hr < 0 ? 0 : hr) * 1152 + 16 * ((l & 7) ^ (4 * (c.w & 1) + g));
    const unsigned lHx = A_HX + c.w * 1024;
    XS xs; xs.init(c, A_X);
    unsigned rh[2] = {A_HX + (l & 15) * 128 + 16 * (g ^ ((l & 15) >> 1)), 0}; rh[1] = rh[0] ^ 64; PIN(rh[0]); PIN(rh[1]);
    bf16x8 xf[18][4];
    unsigned junk = 0;
    auto issue = [&](auto F_, auto R_) {
      constexpr Op o = att1_op(CI(F_), CI(R_));
      if constexpr (o.kind == O_W) {
        constexpr unsigned LO = (o.a % 4) * S2 + o.b * 4608, SO = (32 * (o.b >> 1) + 4 * (o.b & 1)) * 3456;
        if constexpr (o.a < 9) dma<LO, SO>(lw, vA, c.rqkv, sWk[o.a % 9]); else dma_m<LO, SO>(lw, vB, c.rqkv, sWk[o.a % 9], maskB);
      } else if constexpr (o.kind == O_X) {
        if constexpr (o.a >= 2 && o.b == 0) asm volatile("s_waitcnt lgkmcnt(4)" ::: "memory");
        xs.template dmax<2, o.a, o.b>(c);
      } else if constexpr (o.kind == O_TW) { touch_w2<o.a>(c, junk);
      } else if constexpr (o.kind == O_HX) {
        if constexpr (o.a < 4) dma<o.a * 4096, 0>(lHx, vHx, c.rx, (c.w < 2 ? c.kg[2 * o.a] : c.kg[2 * o.a + 1]) * 128);
        else dma<16384, 0>(lHx, c.w < 2 ? vHx : l * 16, c.w < 2 ? c.rx : c.rgw, c.w < 2 ? c.kg[8] * 128 : (c.w & 1) * 1024);
      }
    };
    auto rdx = [&](auto C_, auto I_) {
      constexpr int C = CI(C_), s = CI(I_) >> 2, rt = CI(I_) & 3;
      xf[2 * C + s][rt] = ldsr<bf16x8>(xs.rd[s] + (C % 2) * 32768 + rt * 2048);
    };
    static_for<21>([&](auto I_) { issue(IC(CI(I_) - 23), IC(2)); });
    unsigned rb[4], rx1, rx2;
#pragma unroll
    for (int t = 0; t < 4; ++t) rb[t] = g * 1152 + q * 256 + 32 * (t ^ q) + 8 * pp;
    rx1 = g * 1152 + q * 256 + 32 * (c.w ^ q) + 8 * pp;
    rx2 = g * 1152 + q * 256 + 128 + 32 * ((c.w < 2 ? c.w + 1 : 3) ^ q) + 8 * pp;
    const unsigned gsrc = A_GW + 2 * (32 * (l >> 5) + 8 * ((l >> 2) & 3) + 4 * ((l >> 4) & 1) + (l & 3));
    const unsigned gdst = (l >> 2) * 1152 + (l & 3) * 256 + 16 * (13 ^ ((l & 3) << 1));
    u16 gwv = 0;
    f32x4 acc[8][4], ah[3];
    bf16x8 f[16], hx, ha, hc;
    s16x4 flo, hlo;
    asm volatile("" ::"s"(c.E));
    wait_bar<VM<1, -1>>();
    STAMP(1);
    static_for<8>([&](auto I_) { rdx(IC(0), I_); });
    static_for<PD>([&](auto I_) { f[CI(I_)] = cat(trd(rb[CI(I_)]), trd(rb[CI(I_)] + 4608)); });
    auto pass = [&](auto PS_) {
      constexpr int PS = CI(PS_), NT = PS == 0 ? 8 : 7, FC = 2 * NT;
      static_for<9>([&](auto N_) {
        static_for<FC>([&](auto I_) {
          constexpr int N = CI(N_), I = CI(I_), F = 144 * PS + N * FC + I, ks = 2 * N + I / NT, t = I % NT;
          constexpr int RG = 9 * PS + N + (I + PD) / FC, pos = (I + PD) % FC;
          constexpr int NTN = RG < 9 ? 8 : 7, tn = pos % NTN, RR = pos == 0 ? 2 : 0;
          constexpr unsigned ro = (RG % 2) * S2 + (pos / NTN) * 9216 + 128 * (tn >> 2);
          constexpr unsigned rc = ((9 * PS + N) % 2) * S2 + (I / NT) * 9216;
          constexpr bool SW = PS == 0 ? t >= 5 : t < 2;
          const bf16x8 a = f[I];
          static_for<5>([&](auto R_) {
            constexpr int R = CI(R_);
            if constexpr (R < 4) { if constexpr (SW) mfs<(ks < 16), ks == 0>(acc[t][R], a, xf[ks][R]); else mf<(ks < 16), ks == 0>(acc[t][R], a, xf[ks][R]); }
            else if constexpr (PS == 0) {
              if constexpr (t == 4) mf<false, ks == 0>(ah[1], a, hx);
              if constexpr (t == 5) mf<false, ks == 0>(ah[0], ha, hx);
              if constexpr (t == 7) mfs<false, ks == 0>(ah[2], hc, hx);
            } else if constexpr (t == 5) mfs<false, ks == 0>(ah[0], ha, hx);
            SBAR();
            if constexpr (R == 0 && PS == 0 && I == 6 && N < 8) wait_vm<VM<1, F>>();
            if constexpr (pos == 0 && RG < 18) {
              if constexpr (R < 2 && RG % 2 == 0) {
                constexpr unsigned d = RG % 4 == 0 ? 0u - 2 * S2 : 2 * S2;
#pragma unroll
                for (int i = 2 * R; i < 2 * R + 2; ++i) { rb[i] += d; PIN(rb[i]); }
                if (R == 0) { rx1 += d; PIN(rx1); } else if (RG < 9) { rx2 += d; PIN(rx2); }
              }
              if constexpr (R == 0 && RG == 9) { rx1 ^= 32 * (c.w & 2); PIN(rx1); }
              if constexpr (R == 2) {
                wait_bar<VM<1, F>>();
                STAMP(1 + RG);
                if constexpr (RG == 15) PIN(junk);
              }
            }
            if constexpr (R == RR && RG < 18) flo = trd(rb[tn & 3] + ro);
            if constexpr (R == RR + 1 && RG < 18) f[pos] = cat(flo, trd(rb[tn & 3] + ro + 4608));
            if constexpr (PS == 0 && N < 8 && (I == 6 || I == 7) && R < 4) rdx(IC(N + 1), IC(4 * (I - 6) + R));
            if constexpr (t == 0 && R == 2) hx = ldsr<bf16x8>(rh[ks & 1] + (ks >> 1) * 2048);
            if constexpr (t == 1 && R == 2) hlo = trd(rx1 + rc);
            if constexpr (t == 2 && R == 2) ha = cat(hlo, trd(rx1 + rc + 4608));
            if constexpr (PS == 0 && t == 3 && R == 2) hlo = trd(rx2 + rc);
            if constexpr (PS == 0 && t == 4 && R == 2) hc = cat(hlo, trd(rx2 + rc + 4608));
            if constexpr (RG + 3 >= 9 && RG + 3 <= 17 && R == 3) {
              if constexpr (pos == 6) gwv = ldsr<u16>(gsrc + c.kg[RG + 3 - 9] * 128);
              if constexpr (pos == 9) ldsw<u16>(gdst + ((RG + 3) % 4) * S2, gwv);
            }
            SBAR();
            if constexpr (R == 2 || R == 3) issue(IC(F), R_);
            SBAR();
          });
        });
      });
    };
    pass(IC(0));
    asm volatile("s_nop 7" ::: "memory");
    {
      const unsigned l3 = lane_id(), wa = A_KX + (4 * c.w + 2) * 2560 + l3 * 8, e0 = A_KX + l3 * 8, e1 = A_KX + 2560 + (l3 ^ 8) * 8;
#pragma unroll
      for (int rt = 0; rt < 4; ++rt) {
#pragma unroll
        for (int t = 0; t < 5; ++t) ldsw<u32x2>(wa + rt * 2560 + t * 512, pk4(acc[t][rt]));
#pragma unroll
        for (int t = 0; t < 3; ++t) { vp[rt][t] = pk4(acc[5 + t][rt]); PIN(vp[rt][t]); }
      }
      const u32x2 h0 = pk4(ah[0]), h1 = pk4(ah[1]);
      ldsw<u32x2>(e0 + c.w * 512, h0); ldsw<u32x2>(e1 + c.w * 512, h0);
      ldsw<u32x2>(e0 + 4 * 512, h1); ldsw<u32x2>(e1 + 4 * 512, h1);
      hp[0] = pk4(ah[2]); PIN(hp[0]);
    }
    SBAR();
    pass(IC(1));
    asm volatile("s_nop 7\n\ts_waitcnt lgkmcnt(0)\n\ts_barrier" ::: "memory");
    STAMP(19);
#pragma unroll
    for (int rt = 0; rt < 4; ++rt) {
#pragma unroll
      for (int t = 0; t < 2; ++t) vp[rt][3 + t] = pk4(acc[t][rt]);
#pragma unroll
      for (int t = 0; t < 5; ++t) qp[rt][t] = pk4(acc[2 + t][rt]);
      gate[rt] = sigm(acc[6][rt][0]);
    }
    hp[1] = pk4(ah[0]);
  }
  P2<12> p2;
  p2.init(c);
  p2.poll<0>(c);
  auto pre = [&](auto A_, auto N_) { static_for<CI(N_)>([&](auto I_) { constexpr Op o = att2_op(CI(A_) + CI(I_) - 50, 2); p2.template issue<o.kind, o.a, o.b>(c); }); };
  pre(IC(0), IC(6));
  const unsigned l = lane_id(), g = (l >> 4) & 3, j = l & 15;
  {
    if (c.w < 3) {
      const unsigned wa = A_VX + (c.w + 1) * 5120 + l * 8;
#pragma unroll
      for (int pa = 0; pa < 2; ++pa)
#pragma unroll
        for (int t = 0; t < 5; ++t) ldsw<u32x2>(wa + (pa * 5 + t) * 512, vp[2 + pa][t]);
    }
    const unsigned e0 = A_VX + l * 8, ev = A_VX + 2560 + (l ^ 32) * 8;
    const unsigned tv1 = c.w < 2 ? c.w : 2, tv2 = 3 + (c.w & 1);
    ldsw<u32x2>(e0 + tv1 * 512, hp[0]); ldsw<u32x2>(ev + tv1 * 512, hp[0]);
    ldsw<u32x2>(e0 + tv2 * 512, hp[1]); ldsw<u32x2>(ev + tv2 * 512, hp[1]);
    if (mp == 0 && g == 2) __builtin_amdgcn_raw_buffer_store_b128(__builtin_bit_cast(u32x4, gate), c.rws, j * 16, WS_G + (c.a * 32 + 4 * c.T + c.w) * 256, 16);
#pragma unroll
    for (int rt = 0; rt < 4; ++rt) gate[rt] = __shfl(gate[rt], 32 + j);
  }
  asm volatile("s_waitcnt vmcnt(6) lgkmcnt(0)\n\ts_barrier" ::: "memory");
  STAMP(20);
  p2.check<0>(c);
  pre(IC(6), IC(4));
  {
    u32x10 kt[6];
    u32x6 vt[5][2];
    const unsigned ra = A_VX + c.w * 5120 + l * 8, rk = A_KX + 4 * c.w * 2560 + l * 8;
#pragma unroll
    for (int i = 0; i < 6; ++i)
#pragma unroll
      for (int t = 0; t < 5; ++t) { const u32x2 k2 = ldsr<u32x2>(rk + i * 2560 + t * 512); kt[i][2 * t] = k2[0]; kt[i][2 * t + 1] = k2[1]; }
#pragma unroll
    for (int pa = 0; pa < 2; ++pa)
#pragma unroll
      for (int t = 0; t < 5; ++t) {
        const u32x2 v2 = ldsr<u32x2>(ra + (pa * 5 + t) * 512);
        vt[t][pa] = u32x6{v2[0], v2[1], vp[pa][t][0], vp[pa][t][1], vp[2 + pa][t][0], vp[2 + pa][t][1]};
      }
    SBAR();
#pragma unroll
    for (int i = 0; i < 6; ++i) PIN(kt[i]);
#pragma unroll
    for (int i = 0; i < 10; ++i) PIN(vt[i >> 1][i & 1]);
    f32x4 bprev, bown, bprev0;
    const bool first = c.T == 0 && c.w == 0;
#pragma unroll
    for (int r = 0; r < 4; ++r) {
      const int key = 4 * (int)g + r;
      bprev[r] = key >= (int)j + 9 ? 0.f : -1e30f;
      bown[r] = (key <= (int)j && key >= (int)j - 7) ? 0.f : -1e30f;
      bprev0[r] = first ? -1e30f : bprev[r];
    }
    const unsigned mpv[2] = {(bprev[0] == 0.f ? 0xffffu : 0u) | (bprev[1] == 0.f ? 0xffff0000u : 0u), (bprev[2] == 0.f ? 0xffffu : 0u) | (bprev[3] == 0.f ? 0xffff0000u : 0u)};
    constexpr float SC2 = 0.23570226039551584f * 1.4426950408889634f;
    const unsigned sH = WS_H + (((4 * c.T + c.w) * 4 + c.a) * 42 + 36 + 3 * mp) * 4096;
    u32x4 qf[4][4];
#pragma unroll
    for (int rt = 0; rt < 4; ++rt)
#pragma unroll
      for (int h = 0; h < 4; ++h) {
#pragma unroll
        for (int i = 0; i < 4; ++i) { const int col = 16 * (h + (i >> 1)) + 4 * (int)g + 2 * (i & 1); qf[rt][h][i] = (col >= 18 * h && col < 18 * h + 18) ? qp[rt][h + (i >> 1)][i & 1] : 0u; }
        PIN(qf[rt][h]);
      }
    PIN(bprev); PIN(bown); PIN(bprev0);
    auto scores = [&](auto RT_, f32x4 (&sp)[4], f32x4 (&so)[4]) {
      constexpr int rt = CI(RT_);
      static_for<4>([&](auto H_) {
        constexpr int h = CI(H_);
        sp[h] = mfc(__builtin_shufflevector(kt[rt], kt[rt], 2 * h, 2 * h + 1, 2 * h + 2, 2 * h + 3), qf[rt][h], rt < 2 ? bprev0 : bprev);
        so[h] = mfc(__builtin_shufflevector(kt[rt + 2], kt[rt + 2], 2 * h, 2 * h + 1, 2 * h + 2, 2 * h + 3), qf[rt][h], bown);
      });
    };
    f32x4 sp[2][4], so[2][4];
    asm volatile("s_nop 1" ::: "memory");
    scores(IC(0), sp[0], so[0]);
    static_for<4>([&](auto RT_) {
      constexpr int rt = CI(RT_), pa = rt & 1, sn = rt >> 1;
      if constexpr (rt < 3) scores(IC(rt + 1), sp[(rt + 1) & 1], so[(rt + 1) & 1]);
      SBAR();
      float s[4][4], red[4], bc[4];
#pragma unroll
      for (int h = 0; h < 4; ++h) {
#pragma unroll
        for (int r = 0; r < 4; ++r) s[h][r] = max2(sp[pa][h][r], so[pa][h][r]);
        red[h] = max2(max3(s[h][0], s[h][1], s[h][2]), s[h][3]);
      }
      xbc(-SC2 * xred<true>(red), bc);
      if constexpr (rt == 1) pre(IC(19), IC(4));
      if constexpr (rt == 2) pre(IC(32), IC(4));
#pragma unroll
      for (int h = 0; h < 4; ++h) {
#pragma unroll
        for (int r = 0; r < 4; ++r) s[h][r] = __builtin_amdgcn_exp2f(__builtin_fmaf(s[h][r], SC2, bc[h]));
        red[h] = (s[h][0] + s[h][1]) + (s[h][2] + s[h][3]);
      }
      xbc(gate[rt] * __builtin_amdgcn_rcpf(xred<false>(red)), bc);
      if constexpr (rt == 0) pre(IC(10), IC(6));
      if constexpr (rt == 1) pre(IC(23), IC(6));
      u32x4 P[4];
#pragma unroll
      for (int h = 0; h < 4; ++h) {
        const unsigned p0 = cvtpk(s[h][0] * bc[h], s[h][1] * bc[h]), p1 = cvtpk(s[h][2] * bc[h], s[h][3] * bc[h]);
        P[h] = u32x4{p0 & mpv[0], p1 & mpv[1], p0 & ~mpv[0], p1 & ~mpv[1]};
        PIN(P[h]);
      }
      f32x4 hi[4], lo[4];
      asm volatile("s_nop 1" ::: "memory");
      static_for<4>([&](auto T_) {
        constexpr int t = CI(T_);
        hi[t] = mfz(__builtin_shufflevector(vt[t][pa], vt[t][pa], 2 * sn, 2 * sn + 1, 2 * sn + 2, 2 * sn + 3), P[t]);
        lo[t] = mfz(__builtin_shufflevector(vt[t + 1][pa], vt[t + 1][pa], 2 * sn, 2 * sn + 1, 2 * sn + 2, 2 * sn + 3), P[t]);
      });
      asm volatile("s_nop 7" ::: "memory");
      SBAR();
      u32x2 o[5];
#pragma unroll
      for (int t = 0; t < 5; ++t)
#pragma unroll
        for (int i = 0; i < 2; ++i) {
          const unsigned vh = t < 4 ? cvtpk(hi[t < 4 ? t : 0][2 * i], hi[t < 4 ? t : 0][2 * i + 1]) : 0u;
          o[t][i] = (4 * (int)g + 2 * i < 2 * t) ? cvtpk(lo[t > 0 ? t - 1 : 0][2 * i], lo[t > 0 ? t - 1 : 0][2 * i + 1]) : vh;
        }
      __builtin_amdgcn_raw_buffer_store_b128(u32x4{o[0][0], o[0][1], o[1][0], o[1][1]}, c.rws, l * 16 + rt * 1024, sH, 16);
      __builtin_amdgcn_raw_buffer_store_b128(u32x4{o[2][0], o[2][1], o[3][0], o[3][1]}, c.rws, l * 16 + rt * 1024, sH + 4096, 16);
      __builtin_amdgcn_raw_buffer_store_b128(u32x4{o[4][0], o[4][1], 0u, 0u}, c.rws, l * 16 + rt * 1024, sH + 8192, 16);
      SBAR();
    });
  }
  STAMP(21);
  bf16x8 f[16];
  phase2<12>(c, p2, f);
}

__global__ __launch_bounds__(256, 1) void fused_kernel(const u16* x, const u16* wqkv, const u16* wo, const u16* wg, const u16* wu, const u16* wd,
                                                        const u16* gw, char* ws, u16* out) {
  __shared__ __attribute__((aligned(16))) char smem[LDS_BYTES];
  asm volatile("" ::"v"((unsigned)(size_t)smem));
  Ctx c;
  c.rx = __builtin_amdgcn_make_buffer_rsrc((void*)x, (short)0, SEQ * DM * 2, 0x00020000);
  c.rqkv = __builtin_amdgcn_make_buffer_rsrc((void*)wqkv, (short)0, DM * 3 * DM * 2, 0x00020000);
  c.rwo = __builtin_amdgcn_make_buffer_rsrc((void*)wo, (short)0, DM * DM * 2, 0x00020000);
  c.rwg = __builtin_amdgcn_make_buffer_rsrc((void*)wg, (short)0, DM * FF * 2, 0x00020000);
  c.rwu = __builtin_amdgcn_make_buffer_rsrc((void*)wu, (short)0, DM * FF * 2, 0x00020000);
  c.rwd = __builtin_amdgcn_make_buffer_rsrc((void*)wd, (short)0, DM * FF * 2, 0x00020000);
  c.rgw = __builtin_amdgcn_make_buffer_rsrc((void*)gw, (short)0, DM * 2, 0x00020000);
  c.rws = __builtin_amdgcn_make_buffer_rsrc((void*)ws, (short)0, WS_BYTES, 0x00020000);
  c.rout = __builtin_amdgcn_make_buffer_rsrc((void*)out, (short)0, SEQ * DM * 2, 0x00020000);
  const unsigned bid = blockIdx.x, xcd = bid & 7, idx = bid >> 3;
  c.w = __builtin_amdgcn_readfirstlane(threadIdx.x >> 6);
  c.a = xcd & 3; c.T = 4 * (xcd >> 2) + (idx >> 3); c.m = (idx & 3) == 3 ? 6 + ((idx >> 2) & 1) : 3 * ((idx >> 2) & 1) + (idx & 3);
  {
    const unsigned rot = c.m + 2 * (c.T & 3);
#pragma unroll
    for (int i = 0; i < 9; ++i) { const unsigned k = i + rot; c.kg[i] = k >= 18 ? k - 18 : k >= 9 ? k - 9 : k; }
  }
  c.E = *(const __attribute__((address_space(4))) u64*)(ws + WS_E + c.T * 128);
  STAMP(0);
  if (c.m < 6) role_mlp(c); else role_att(c);
}

extern "C" void kernel_launch(void* const* d_in, const int* in_sizes, int n_in, void* d_out, int out_size, void* d_ws, size_t ws_size,
                              hipStream_t stream) {
  fused_kernel<<<256, 256, 0, stream>>>((const u16*)d_in[0], (const u16*)d_in[1], (const u16*)d_in[2], (const u16*)d_in[4], (const u16*)d_in[5],
                                        (const u16*)d_in[6], (const u16*)d_in[7], (char*)d_ws, (u16*)d_out);
}
```

```cpp
#include <hip/hip_runtime.h>
#include <utility>

typedef unsigned short u16;
typedef unsigned long long u64;
typedef short s16x4 __attribute__((ext_vector_type(4)));
typedef __bf16 bf16x8 __attribute__((ext_vector_type(8)));
typedef __bf16 bf16x2 __attribute__((ext_vector_type(2)));
typedef float f32x2 __attribute__((ext_vector_type(2)));
typedef float f32x4 __attribute__((ext_vector_type(4)));
typedef unsigned u32x2 __attribute__((ext_vector_type(2)));
typedef unsigned u32x4 __attribute__((ext_vector_type(4)));
typedef unsigned u32x6 __attribute__((ext_vector_type(6)));
typedef unsigned u32x10 __attribute__((ext_vector_type(10)));
typedef __amdgpu_buffer_rsrc_t rsrc_t;
#define LAS __attribute__((address_space(3)))
#define DI __device__ __forceinline__
#define SBAR() __builtin_amdgcn_sched_barrier(0)
#define IC(x) std::integral_constant<int, (x)>{}
#define CI(x) decltype(x)::value

constexpr int SEQ = 2048, DM = 576, FF = 4608;
constexpr unsigned WS_H = 0;
constexpr unsigned WS_PART = 24u << 20;
constexpr unsigned WS_G = 40u << 20;
constexpr unsigned WS_F1 = 41u << 20;
constexpr unsigned WS_F2 = WS_F1 + 8192;
constexpr unsigned WS_E = WS_F2 + 65536;
constexpr unsigned WS_BYTES = 42u << 20;
constexpr unsigned LDS_BYTES = 163840;
constexpr unsigned S1 = 16384;
constexpr unsigned S2 = 18432;
constexpr unsigned S3 = S2 + 9216;
constexpr unsigned M_X = 65536;
constexpr unsigned A_X = 4 * S2;
constexpr unsigned A_HX = A_X + 65536;
constexpr unsigned A_GW = A_HX + 18432;
constexpr unsigned A_KX = 83968;
constexpr unsigned A_VX = A_KX + 18 * 2560;
static_assert(A_GW + 2048 <= LDS_BYTES && A_VX <= A_HX && A_VX + 20480 <= LDS_BYTES && 3 * S3 <= A_KX, "lds");

template <int... Is> using iseq = std::integer_sequence<int, Is...>;
template <class Fn, int... Is> DI void static_for_(Fn&& f, iseq<Is...>) { (f(std::integral_constant<int, Is>{}), ...); }
template <int N, class Fn> DI void static_for(Fn&& f) { static_for_(f, std::make_integer_sequence<int, N>{}); }

template <int N> DI void wait_bar() { asm volatile("s_waitcnt vmcnt(%0) lgkmcnt(0)\n\ts_barrier" ::"i"(N) : "memory"); }
template <int N> DI void wait_vm() { asm volatile("s_waitcnt vmcnt(%0)" ::"i"(N) : "memory"); }
template <class T> DI void PIN(T& x) { asm volatile("" : "+v"(x)); }
DI void tieA(bf16x8& a, bf16x8& b, bf16x8& c, bf16x8& d) { asm volatile("" : "+a"(a), "+a"(b), "+a"(c), "+a"(d)); }
template <unsigned LO, unsigned SO> DI unsigned dma(unsigned lb, unsigned voff, rsrc_t srd, unsigned sb) {
  unsigned t;
  asm volatile("s_add_u32 m0, %1, %5\n\ts_add_u32 %0, %4, %6\n\tbuffer_load_dwordx4 %2, %3, %0 offen lds"
               : "=&s"(t) : "s"(lb), "v"(voff), "s"(srd), "s"(sb), "i"(LO), "i"(SO) : "memory", "scc");
  return t;
}
template <unsigned IMM> DI void dma_n(unsigned voff, rsrc_t srd, unsigned so) {
  asm volatile("buffer_load_dwordx4 %0, %1, %2 offen offset:%3 lds" ::"v"(voff), "s"(srd), "s"(so), "i"(IMM) : "memory");
}
template <unsigned LO> DI void dma_l(unsigned lb, unsigned voff, rsrc_t srd, unsigned so) {
  asm volatile("s_add_u32 m0, %0, %4\n\ts_nop 0\n\tbuffer_load_dwordx4 %1, %2, %3 offen lds" ::"s"(lb), "v"(voff), "s"(srd), "s"(so), "i"(LO) : "memory", "scc");
}
template <unsigned LO, unsigned SO> DI void dma_m(unsigned lb, unsigned voff, rsrc_t srd, unsigned sb, u64 mask) {
  unsigned t;
  asm volatile("s_add_u32 m0, %1, %5\n\ts_add_u32 %0, %4, %6\n\ts_mov_b64 exec, %7\n\tbuffer_load_dwordx4 %2, %3, %0 offen lds\n\ts_mov_b64 exec, -1"
               : "=&s"(t) : "s"(lb), "v"(voff), "s"(srd), "s"(sb), "i"(LO), "i"(SO), "s"(mask) : "memory", "scc");
}
template <unsigned IMM> DI void ldh(bf16x8& d, unsigned voff, rsrc_t srd, unsigned so) {
  asm volatile("buffer_load_dwordx4 %0, %1, %2, %3 offen offset:%4 sc1" : "=a"(d) : "v"(voff), "s"(srd), "s"(so), "i"(IMM) : "memory");
}
DI void ldg(f32x4& d, unsigned voff, rsrc_t srd, unsigned so) { asm volatile("buffer_load_dwordx4 %0, %1, %2, %3 offen sc1" : "=v"(d) : "v"(voff), "s"(srd), "s"(so) : "memory"); }
DI void touch(unsigned& d, unsigned voff, rsrc_t srd, unsigned so) {
  asm volatile("buffer_load_dword %0, %1, %2, %3 offen sc1" : "+v"(d) : "v"(voff), "s"(srd), "s"(so) : "memory");
}
template <bool AG, bool ZERO> DI void mf(f32x4& c, const bf16x8& a, const bf16x8& b) {
  if constexpr (ZERO) {
    if constexpr (AG) asm volatile("v_mfma_f32_16x16x32_bf16 %0, %1, %2, 0" : "=&v"(c) : "v"(a), "a"(b));
    else asm volatile("v_mfma_f32_16x16x32_bf16 %0, %1, %2, 0" : "=&v"(c) : "v"(a), "v"(b));
  } else {
    if constexpr (AG) asm volatile("v_mfma_f32_16x16x32_bf16 %0, %1, %2, %0" : "+v"(c) : "v"(a), "a"(b));
    else asm volatile("v_mfma_f32_16x16x32_bf16 %0, %1, %2, %0" : "+v"(c) : "v"(a), "v"(b));
  }
}
template <bool AG, bool ZERO> DI void mfs(f32x4& c, const bf16x8& a, const bf16x8& b) {
  if constexpr (ZERO) {
    if constexpr (AG) asm volatile("v_mfma_f32_16x16x32_bf16 %0, %2, %1, 0" : "=&v"(c) : "v"(a), "a"(b));
    else asm volatile("v_mfma_f32_16x16x32_bf16 %0, %2, %1, 0" : "=&v"(c) : "v"(a), "v"(b));
  } else {
    if constexpr (AG) asm volatile("v_mfma_f32_16x16x32_bf16 %0, %2, %1, %0" : "+v"(c) : "v"(a), "a"(b));
    else asm volatile("v_mfma_f32_16x16x32_bf16 %0, %2, %1, %0" : "+v"(c) : "v"(a), "v"(b));
  }
}
DI f32x4 mfc(const u32x4& a, const u32x4& b, const f32x4& c) { f32x4 d; asm volatile("v_mfma_f32_16x16x32_bf16 %0, %1, %2, %3" : "=&v"(d) : "v"(a), "v"(b), "v"(c)); return d; }
DI f32x4 mfz(const u32x4& a, const u32x4& b) { f32x4 d; asm volatile("v_mfma_f32_16x16x32_bf16 %0, %1, %2, 0" : "=&v"(d) : "v"(a), "v"(b)); return d; }
DI float max2(float a, float b) { float r; asm("v_max_f32 %0, %1, %2" : "=v"(r) : "v"(a), "v"(b)); return r; }
DI float max3(float a, float b, float c) { float r; asm("v_max3_f32 %0, %1, %2, %3" : "=v"(r) : "v"(a), "v"(b), "v"(c)); return r; }
DI s16x4 trd(unsigned a) { return __builtin_amdgcn_ds_read_tr16_b64_v4i16((LAS s16x4*)a); }
DI bf16x8 cat(s16x4 lo, s16x4 hi) { return __builtin_bit_cast(bf16x8, __builtin_shufflevector(lo, hi, 0, 1, 2, 3, 4, 5, 6, 7)); }
DI unsigned cvtpk(float lo, float hi) { f32x2 v = {lo, hi}; return __builtin_bit_cast(unsigned, __builtin_convertvector(v, bf16x2)); }
DI float w_mul(float a) { float r; asm volatile("v_mul_f32 %0, 0xbfb8aa3b, %1" : "=v"(r) : "v"(a)); return r; }
DI float w_exp(float a) { float r; asm volatile("v_exp_f32 %0, %1" : "=v"(r) : "v"(a)); return r; }
DI unsigned w_add1(float a) { unsigned r; asm volatile("v_fma_mixlo_f16 %0, %1, 1.0, 1.0" : "=v"(r) : "v"(a)); return r; }
DI unsigned w_rcplo(unsigned a) { unsigned r; asm volatile("v_rcp_f16 %0, %1" : "=v"(r) : "v"(a)); return r; }
DI void w_rcphi(unsigned& d, unsigned a) { asm volatile("v_rcp_f16_sdwa %0, %1 dst_sel:WORD_1 dst_unused:UNUSED_PRESERVE src0_sel:WORD_0" : "+v"(d) : "v"(a)); }
DI float w_mixlo(float u, unsigned s) { float r; asm volatile("v_fma_mix_f32 %0, %1, %2, 0 op_sel_hi:[0,1,0]" : "=v"(r) : "v"(u), "v"(s)); return r; }
DI float w_mixhi(float u, unsigned s) { float r; asm volatile("v_fma_mix_f32 %0, %1, %2, 0 op_sel:[0,1,0] op_sel_hi:[0,1,0]" : "=v"(r) : "v"(u), "v"(s)); return r; }
DI unsigned w_cvt(float lo, float hi) { unsigned r; asm volatile("v_cvt_pk_bf16_f32 %0, %1, %2" : "=v"(r) : "v"(lo), "v"(hi)); return r; }
DI float w_scale(float a, float s) { float r; asm volatile("v_mul_f32 %0, %1, %2" : "=v"(r) : "v"(a), "v"(s)); return r; }
DI float sigm(float x) { return __builtin_amdgcn_rcpf(1.0f + __builtin_amdgcn_exp2f(-1.4426950408889634f * x)); }
DI float bflo(unsigned v) { return __uint_as_float(v << 16); }
DI float bfhi(unsigned v) { return __uint_as_float(v & 0xffff0000u); }
template <class T> DI T ldsr(unsigned a) { return *(const LAS T*)a; }
template <class T> DI void ldsw(unsigned a, T v) { *(LAS T*)a = v; }
DI unsigned lane_id() { unsigned l; asm volatile("v_mbcnt_lo_u32_b32 %0, -1, 0\n\tv_mbcnt_hi_u32_b32 %0, -1, %0" : "=v"(l)); return l; }

#define STAMP(i)

struct Ctx {
  rsrc_t rx, rqkv, rwo, rwg, rwu, rwd, rgw, rws, rout;
  unsigned w, a, T, m;
  unsigned kg[9];
  u64 E;
  unsigned junk;
};

enum { O_NONE, O_W, O_X, O_HX, O_FIN, O_POLL, O_D2, O_B2, O_GATE, O_ST, O_TW, O_TH };
constexpr int NEVER = 1 << 20;
struct Op { int kind, a, b, need; };
constexpr int PD = 3;
constexpr int MLP_AD = 5, ATT_AD = 3;
constexpr Op NOP_ = {O_NONE, 0, 0, 0};
constexpr Op p2_op(int N, int R, int NT, int AD, int FB) {
  const int ND = NT == 8 ? 4 : 6, RG = (N + PD) / NT, pos = (N + PD) % NT, k = 2 * (pos - (RG == 0 ? PD : 1)) + (R - 2), U = RG + AD;
  if (RG > 20 || k < 0) return NOP_;
  if (U <= 20 && k < ND) return {O_D2, U, k, FB + U * NT - PD};
  if (U <= 20 && k < ND + 4) return {O_B2, U, k - ND, FB + U * NT - PD};
  if (U == 18 && k == ND + 4) return {O_GATE, 0, 0, FB + 17 * NT - PD};
  if ((U + 2 == 6 || U + 2 == 12 || U + 2 == 18) && k == ND + 5) return {O_POLL, (U + 2) / 6, 0, FB + (RG + 2) * NT - PD};
  return NOP_;
}
constexpr int FIN0 = 36, FLG0 = 16, FLG1 = 24;
constexpr int bar1(int n) { return n == 0 ? -1 : 16 * n - PD; }
constexpr Op opW(int n, int i) { return {O_W, n, i, bar1(n)}; }
constexpr Op opX(int c, int i) { return {O_X, c, i, c == 0 ? -1 : 8 * c - 5}; }
constexpr Op mlp_op(int F, int R) {
  if (F < 0) {
    const int i = F + 22;
    if (R != 2 || i < 0 || i >= 20) return NOP_;
    return i < 4 ? opW(0, i) : opX((i - 4) / 8, (i - 4) % 8);
  }
  if (F >= 440) return p2_op(F - 440, R, 8, MLP_AD, 440);
  if (F >= 432) return R == 2 ? Op{O_ST, 0, 0, 440 + 2 * 8 - PD} : NOP_;
  const int RG = (F + PD) / 16, pos = (F + PD) % 16, r = R - 2;
  if (pos == 0) return NOP_;
  if (F < 61) {
    constexpr int tab[13][2] = {{0, 2}, {4, -1}, {6, 3}, {10, -2}, {14, 4}, {18, 5}, {22, -3}, {24, 6}, {30, 7}, {34, -4}, {38, 8}, {42, -5}, {46, -6}};
    for (int i = 0; i < 13; ++i) {
      const int d = F - tab[i][0];
      if (d >= 0 && d < (tab[i][1] > 0 ? 4 : 2)) return tab[i][1] > 0 ? opX(tab[i][1], 2 * d + r) : opW(-tab[i][1], 2 * d + r);
    }
    return NOP_;
  }
  if (R == 2) {
    if (F >= 144 && F % 144 >= FIN0 && F % 144 < FIN0 + 32 && F % 4 == 3) return {O_FIN, F / 144 - 1, (F % 144 - FIN0) / 4, bar1(F < 288 ? FLG0 : FLG1)};
    return NOP_;
  }
  if (RG >= 5 && RG <= 8) return pos % 4 == 1 ? opW(RG + 4, pos / 4) : NOP_;
  constexpr int first[27] = {0, 0, 0, 0, 7, 0, 0, 0, 0, 13, 16, 19, 20, 21, 0, 0, 0, 0, 22, 25, 0, 0, 0, 0, 0, 0, 0};
  constexpr int cnt[27] = {0, 0, 0, 0, 2, 0, 0, 0, 0, 3, 3, 1, 1, 1, 0, 0, 0, 0, 3, 2, 0, 0, 0, 0, 0, 0, 0};
  if (RG < 27 && pos >= 3 && pos - 3 < 4 * cnt[RG]) return opW(first[RG] + (pos - 3) / 4, (pos - 3) % 4);
  if (RG == 14 && (pos == 2 || pos == 9)) return {O_TW, pos == 9, 0, bar1(24)};
  if (RG == 23 && pos == 1) return {O_POLL, 0, 0, bar1(24)};
  if (RG >= 20 && RG <= 22 && pos >= 2 && pos < 6) return {O_D2, RG - 20, pos - 2, RG == 20 ? bar1(27) : 440 + (RG - 20) * 8 - PD};
  if (RG >= 24 && RG <= 26) {
    const int k = (RG - 24) * 15 + pos - 1;
    if (k < 12) { const int U = k / 4; return {O_B2, U, k % 4, U == 0 ? bar1(27) : 440 + U * 8 - PD}; }
    if (k >= 16 && k < 24) { const int i = k - 16; return i < 4 ? Op{O_D2, 3, i, 440 + 24 - PD} : Op{O_B2, 3, i - 4, 440 + 24 - PD}; }
    if (k >= 31 && k < 39) { const int i = k - 31; return i < 4 ? Op{O_D2, 4, i, 440 + 32 - PD} : Op{O_B2, 4, i - 4, 440 + 32 - PD}; }
    if (k == 40) return {O_POLL, 1, 0, 440 + 8 - PD};
  }
  return NOP_;
}
constexpr int barA(int n) { return n == 0 ? -1 : n <= 9 ? 16 * n - PD : 144 + 14 * (n - 9) - PD; }
constexpr Op att1_op(int F, int R) {
  if (F < 0) {
    const int i = F + 23;
    if (R != 2 || i < 0 || i >= 21) return NOP_;
    if (i < 4) return {O_W, 0, i, -1};
    if (i < 12) return {O_X, 0, i - 4, -1};
    if (i == 12) return {O_HX, 0, 0, -1};
    return {O_X, 1, i - 13, 6};
  }
  const int FP = F + PD, RG = FP < 144 ? FP / 16 : 9 + (FP - 144) / 14, pos = FP < 144 ? FP % 16 : (FP - 144) % 14, r = R - 2;
  if (pos == 0) return NOP_;
  if (F < 2) return {O_W, 1, 2 * F + r, barA(1)};
  if (F < 6) return {O_X, 2, 2 * (F - 2) + r, 16 * 2 - 10};
  if (F < 8) return {O_W, 2, 2 * (F - 6) + r, barA(2)};
  if (F == 8) return {O_HX, 1 + r, 0, barA(2 + 2 * r)};
  if (F < 144 && F % 16 >= 9 && F % 16 < 13 && F / 16 + 3 <= 8) { const int c = F / 16 + 3; return {O_X, c, 2 * (F % 16 - 9) + r, 16 * c - 10}; }
  if (RG == 1 && pos <= 2) return {O_W, 3, 2 * (pos - 1) + r, barA(3)};
  if (RG == 1 && pos == 3) return {O_HX, 3 + r, 0, barA(5)};
  if (R == 2) return NOP_;
  if (RG == 1 && pos >= 4 && pos < 8) return {O_W, 4, pos - 4, barA(4)};
  if (RG >= 2 && RG + 3 <= 17 && pos <= 4) return {O_W, RG + 3, pos - 1, barA(RG + 3)};
  return NOP_;
}
constexpr Op att2_op(int F, int R) {
  if (F < 0) {
    const int i = F + 50;
    if (R != 2 || i < 0 || i >= 42) return NOP_;
    constexpr int n1 = 12 - PD, n2 = 24 - PD;
    if (i < 6) return {O_D2, 0, i, -1};
    if (i < 10) return {O_B2, 0, i - 6, -1};
    if (i < 16) return {O_D2, 1, i - 10, n1};
    if (i < 19) return {O_ST, 0, 0, n2};
    if (i < 23) return {O_B2, 1, i - 19, n1};
    if (i < 29) return {O_D2, 2, i - 23, n2};
    if (i < 32) return {O_ST, 0, 0, n2};
    if (i < 36) return {O_B2, 2, i - 32, n2};
    return {O_ST, 0, 0, n2};
  }
  return p2_op(F, R, 12, ATT_AD, 0);
}
template <int WHICH> constexpr Op op_at(int F, int R) { return WHICH == 0 ? mlp_op(F, R) : WHICH == 1 ? att1_op(F, R) : att2_op(F, R); }
template <int WHICH> constexpr int vm_at(int tau) {
  int s = 0;
  for (int F = tau - 1; F >= -64; --F)
    for (int R = 3; R >= 2; --R) {
      const Op o = op_at<WHICH>(F, R);
      if (o.kind == O_NONE) continue;
      if (o.need <= tau || s >= 63) return s;
      ++s;
    }
  return s;
}
template <int WHICH, int TAU> constexpr int VM = vm_at<WHICH>(TAU);

DI void flag_store(const Ctx& c, unsigned off, unsigned val) {
  const u64 v = c.E + val;
  __builtin_amdgcn_raw_buffer_store_b64(u32x2{(unsigned)v, (unsigned)(v >> 32)}, c.rws, 0, off, 16);
}
DI void poll_issue(const Ctx& c, u32x2& f, unsigned voff, unsigned off) {
  asm volatile("buffer_load_dwordx2 %0, %1, %2, %3 offen sc1" : "=v"(f) : "v"(voff), "s"(c.rws), "s"(off) : "memory");
}
DI bool flag_ok(const Ctx& c, u32x2 f, unsigned need) {
  const u64 d = (((u64)f[1] << 32) | f[0]) - c.E - need;
  return __builtin_amdgcn_ballot_w64(d >= 8) == 0;
}
DI void poll_check(const Ctx& c, u32x2& f, unsigned voff, unsigned off, unsigned need) {
  asm volatile("" : "+v"(f));
  if (__builtin_expect(!flag_ok(c, f, need), 0)) {
    for (unsigned spin = 0; spin < (1u << 16); ++spin) {
      __builtin_amdgcn_s_sleep(1);
      asm volatile("buffer_load_dwordx2 %0, %1, %2, %3 offen sc1\n\ts_waitcnt vmcnt(0)" : "=&v"(f) : "v"(voff), "s"(c.rws), "s"(off) : "memory");
      if (flag_ok(c, f, need)) break;
    }
  }
}

template <int NT> struct P2 {
  static constexpr int AD = NT == 8 ? MLP_AD : ATT_AD, NS = NT == 8 ? 8 : 4, FB = NT == 8 ? 440 : 0, ROT = NT == 8 ? 5 : 0;
  static constexpr unsigned SS = NT == 8 ? S2 : S3;
  static constexpr unsigned slot(int u) { return ((u + ROT) % NS) * SS; }
  static constexpr int WN = NT == 8 ? 3 : 2;
  static constexpr unsigned win(int u) { return (((u + ROT) % NS) / WN * WN) * SS; }
  unsigned v128[2], v64, vh[2], vf;
  unsigned lw, lw64, sd, so, sh[2];
  unsigned f1, cur, curh;
  bf16x8 bf[21][4];
  u32x2 pf;
  f32x4 omg;
  DI void init(const Ctx& c) {
    const unsigned l = lane_id(), colb = NT == 8 ? 256 * (c.m >> 1) : 768, kh = c.w >> 1;
    const unsigned lo = 16 * ((l & 15) ^ ((l >> 3) & 2) ^ (4 * c.a));
    v128[0] = (l >> 4) * 4608 + lo; v128[1] = (l >> 4) * 4608 + (lo ^ 64);
    v64 = (l >> 3) * 2304 + 16 * ((l & 7) ^ (((l >> 1) & 4) | ((l >> 3) & 2)) ^ (2 * c.a)) + 256;
    vh[0] = l * 16 + kh * 2048; vh[1] = l * 16 + (1 - kh) * 2048;
    vf = 8 * (l & 7);
    lw = c.w * 4608; lw64 = c.w * 2304 + S2;
    sd = (1152 * c.a + 16 * c.w) * 1152 + colb;
    so = (144 * c.a + 72 * kh + 16 * (c.w & 1)) * 1152 + colb;
    sh[0] = WS_H + (((4 * c.T + 2 * (c.m & 1) + (c.w & 1)) * 4 + c.a) * 42 + kh) * 4096; sh[1] = sh[0] + kh * 8192;
    f1 = WS_F1 + (c.T * 4 + c.a) * 128;
  }
  template <int S> DI unsigned pvo() const { return S < 3 ? (vf < 48 ? vf : 0) : 48 + (vf & 8); }
  template <int S> DI void poll(const Ctx& c) { poll_issue(c, pf, pvo<S>(), f1); }
  template <int S> DI void check(const Ctx& c) { poll_check(c, pf, pvo<S>(), f1, S < 3 ? S + 1 : 3); }
  template <int KIND, int U, int I> DI void issue(const Ctx& c) {
    if constexpr (KIND == O_D2) {
      constexpr unsigned row = U < 18 ? 192 * (U % 6) + 64 * (U / 6) : 32 * (U - 18);
      const rsrc_t rs = U < 18 ? c.rwd : c.rwo;
      if constexpr (I == 0) cur = dma<slot(U), row * 1152>(lw, v128[0], rs, U < 18 ? sd : so);
      else if constexpr (I < 4) dma_n<I * 1152>(v128[I >> 1], rs, cur);
      else if constexpr (I == 4) dma_l<slot(U)>(lw64, v64, rs, cur);
      else dma_n<1152>(v64, rs, cur);
    }
    if constexpr (KIND == O_B2) {
      if constexpr (I == 0) curh = sh[U >= 18] + (U < 18 ? 2 * U : 18 + U) * 4096;
      ldh<(I & 1) * 1024>(bf[U][I], vh[I >> 1], c.rws, curh);
    }
    if constexpr (KIND == O_POLL) poll<U>(c);
    if constexpr (KIND == O_GATE) ldg(omg, vh[0] & 255, c.rws, WS_G + (c.a * 32 + 4 * c.T + 2 * (c.m & 1) + (c.w & 1)) * 256);
  }
  static DI unsigned rbase(const Ctx& c, unsigned l, int t) {
    const unsigned g = l >> 4, q = (l >> 2) & 3;
    return (c.w >> 1) * 9216 + q * 1152 + g * 256 + 32 * (t ^ ((q & 2) | (g & 1))) + 8 * (l & 3);
  }
};

template <int NT> DI void phase2(Ctx& c, P2<NT>& p, bf16x8 (&f)[16]) {
  using P = P2<NT>;
  constexpr int AD = P::AD, WH = NT == 8 ? 0 : 2;
  const unsigned l = lane_id(), g = l >> 4, q = (l >> 2) & 3, kh = c.w >> 1;
  unsigned rb[4], rb64[4];
#pragma unroll
  for (int t = 0; t < 4; ++t) {
    rb[t] = P::win(0) + P::rbase(c, l, t); PIN(rb[t]);
    if (NT != 8) { rb64[t] = P::win(0) + S2 + kh * 4608 + (q & 1) * 1152 + (2 * g + (q >> 1)) * 128 + 32 * (t ^ ((q & 2) | (g & 1))) + 8 * (l & 3); PIN(rb64[t]); }
  }
  f32x4 acc[NT][4];
  s16x4 flo;
  auto rdlo = [&](auto U_, auto T_) { constexpr int t = CI(T_); constexpr unsigned o = P::slot(CI(U_)) - P::win(CI(U_)); return t < 8 ? trd(rb[t & 3] + o + 128 * (t >> 2)) : trd(rb64[t & 3] + o); };
  auto rdhi = [&](auto U_, auto T_) { constexpr int t = CI(T_); constexpr unsigned o = P::slot(CI(U_)) - P::win(CI(U_)); return t < 8 ? trd(rb[t & 3] + o + 128 * (t >> 2) + 4608) : trd(rb64[t & 3] + o + 2304); };
  if constexpr (NT != 8) {
    wait_bar<VM<2, -1>>();
    STAMP(24);
    tieA(p.bf[0][0], p.bf[0][1], p.bf[0][2], p.bf[0][3]);
    static_for<PD>([&](auto T_) { f[CI(T_)] = cat(rdlo(IC(0), T_), rdhi(IC(0), T_)); });
  }
  static_for<21>([&](auto U_) {
    static_for<NT>([&](auto T_) {
      constexpr int U = CI(U_), t = CI(T_), N = U * NT + t, UN = (N + PD) / NT, tn = (N + PD) % NT;
      constexpr int RR = tn == 0 ? 2 : 0;
      const bf16x8 a = f[t];
      static_for<4>([&](auto R_) {
        constexpr int R = CI(R_);
        mf<true, U == 0>(acc[t][R], a, p.bf[U][R]);
        SBAR();
        if constexpr (tn == 0 && UN <= 20) {
          if constexpr (R < 2 && P::win(UN) != P::win(UN - 1)) {
#pragma unroll
            for (int i = 2 * R; i < 2 * R + 2; ++i) { rb[i] += P::win(UN) - P::win(UN - 1); PIN(rb[i]); if (NT != 8) { rb64[i] += P::win(UN) - P::win(UN - 1); PIN(rb64[i]); } }
          }
          if constexpr (R == 2) {
            wait_bar<VM<WH, P::FB + N>>();
            STAMP(UN + (NT == 8 ? 28 : 24));
            tieA(p.bf[UN][0], p.bf[UN][1], p.bf[UN][2], p.bf[UN][3]);
          }
        }
        if constexpr (R == RR && UN <= 20) flo = rdlo(IC(UN), IC(tn));
        if constexpr (R == RR + 1 && UN <= 20) f[tn] = cat(flo, rdhi(IC(UN), IC(tn)));
        SBAR();
        if constexpr (tn == 0 && R == 3 && UN <= 20) {
          if constexpr (UN == 2) { if (threadIdx.x == 0) flag_store(c, p.f1 + 8 * c.m, 3); }
          if constexpr ((UN + AD) % 6 == 0 && UN + AD <= 18) p.template check<(UN + AD) / 6>(c);
          if constexpr (UN == 17) { PIN(p.omg); p.omg = 1.0f - (kh ? f32x4{p.omg[2], p.omg[3], p.omg[0], p.omg[1]} : p.omg); PIN(p.omg); }
        }
        if constexpr (R >= 2) { constexpr Op o = op_at<WH>(P::FB + N, R); p.template issue<o.kind, o.a, o.b>(c); }
        constexpr int ts = N - 2 - 17 * NT;
        if constexpr (ts >= 0 && ts < NT) { acc[ts][R] *= p.omg[R]; PIN(acc[ts][R]); }
        SBAR();
      });
    });
  });
  asm volatile("s_nop 7\n\ts_waitcnt lgkmcnt(0)\n\ts_barrier" ::: "memory");
  STAMP(NT == 8 ? 49 : 45);
  const unsigned l2 = lane_id();
  f32x4 val[NT][2];
  {
    const unsigned wa = c.w * (NT * 2048) + l2 * 16, ra = (c.w ^ 2) * (NT * 2048) + l2 * 16;
#pragma unroll
    for (int t = 0; t < NT; ++t) { ldsw<f32x4>(wa + t * 2048, acc[t][2]); ldsw<f32x4>(wa + t * 2048 + 1024, acc[t][3]); val[t][0] = acc[t][0]; val[t][1] = acc[t][1]; }
    asm volatile("s_waitcnt lgkmcnt(0)\n\ts_barrier" ::: "memory");
#pragma unroll
    for (int t = 0; t < NT; ++t) { val[t][0] += ldsr<f32x4>(ra + t * 2048); val[t][1] += ldsr<f32x4>(ra + t * 2048 + 1024); }
  }
  const unsigned pb = WS_PART + ((c.T * 8 + c.m) * 64 + c.w) * 3072;
#pragma unroll
  for (int t = 0; t < NT; ++t) {
    const int dq = t < 8 ? t >> 1 : t - 8, idx = t < 8 ? t & 1 : 2;
    if (dq != 0) {
      const u32x4 v = {cvtpk(val[t][0][0], val[t][0][1]), cvtpk(val[t][0][2], val[t][0][3]), cvtpk(val[t][1][0], val[t][1][1]), cvtpk(val[t][1][2], val[t][1][3])};
      __builtin_amdgcn_raw_buffer_store_b128(v, c.rws, l2 * 16, pb + ((dq ^ c.a) * 4 + c.a) * 12288 + idx * 1024, 16);
    }
  }
  asm volatile("s_waitcnt vmcnt(0)" ::: "memory");
  const unsigned f2 = WS_F2 + ((c.T * 8 + c.m) * 4 + c.w) * 128;
  if (l2 == 0) flag_store(c, f2 + 8 * c.a, 1);
  STAMP(NT == 8 ? 50 : 46);
  u32x2 pf = {0u, 0u};
  asm volatile("buffer_load_dwordx2 %0, %1, %2, %3 offen sc1\n\ts_waitcnt vmcnt(0)" : "=&v"(pf) : "v"(8 * (l2 & 3)), "s"(c.rws), "s"(f2) : "memory");
  poll_check(c, pf, 8 * (l2 & 3), f2, 1);
  STAMP(NT == 8 ? 51 : 47);
  constexpr int NI = NT / 4;
  u32x4 in[3][NI];
#pragma unroll
  for (int s = 1; s < 4; ++s)
#pragma unroll
    for (int i = 0; i < NI; ++i) in[s - 1][i] = __builtin_amdgcn_raw_buffer_load_b128(c.rws, l2 * 16, pb + (c.a * 4 + (s ^ c.a)) * 12288 + i * 1024, 16);
  const unsigned row0 = 256 * c.T + 128 * (c.m & 1) + 64 * (c.w & 1) + 32 * kh;
  const unsigned vo = (l2 & 15) * 2304 + 8 * (l2 >> 4), colb = NT == 8 ? 256 * (c.m >> 1) : 768;
#pragma unroll
  for (int i = 0; i < NI; ++i) {
    f32x4 s0 = val[i < 2 ? i : 8][0], s1 = val[i < 2 ? i : 8][1];
#pragma unroll
    for (int s = 0; s < 3; ++s) {
      s0 += f32x4{bflo(in[s][i][0]), bfhi(in[s][i][0]), bflo(in[s][i][1]), bfhi(in[s][i][1])};
      s1 += f32x4{bflo(in[s][i][2]), bfhi(in[s][i][2]), bflo(in[s][i][3]), bfhi(in[s][i][3])};
    }
    const unsigned cb = colb + (i < 2 ? 64 * c.a + 32 * i : 256 + 32 * c.a);
    __builtin_amdgcn_raw_buffer_store_b64(u32x2{cvtpk(s0[0], s0[1]), cvtpk(s0[2], s0[3])}, c.rout, vo, row0 * 1152 + cb, 0);
    __builtin_amdgcn_raw_buffer_store_b64(u32x2{cvtpk(s1[0], s1[1]), cvtpk(s1[2], s1[3])}, c.rout, vo, (row0 + 1) * 1152 + cb, 0);
  }
  if (NT == 8 && c.a == 0 && c.m == 0 && c.w == 0 && l2 == 0) flag_store(c, WS_E + c.T * 128, 16);
  STAMP(NT == 8 ? 52 : 48);
}

struct XS {
  unsigned v[2], lw, sx, rd[2], cur;
  DI void init(const Ctx& c, unsigned base) {
    const unsigned l = lane_id(), j = l & 15, g = l >> 4;
    v[0] = (l >> 3) * 2304 + 16 * ((l & 7) ^ g); v[1] = (l >> 3) * 2304 + 16 * ((l & 7) ^ (4 + g));
    lw = base + c.w * 8192; sx = (256 * c.T + 64 * c.w) * 1152;
    rd[0] = lw + j * 128 + 16 * (g ^ (j >> 1)); rd[1] = rd[0] ^ 64;
    PIN(rd[0]); PIN(rd[1]);
  }
  template <int NSL, int C, int I> DI void dmax(const Ctx& c) {
    if constexpr (I == 0) cur = sx + c.kg[C] * 128;
    dma<(C % NSL) * 32768 + I * 1024, (32 * (I >> 2) + 16 * (I & 1) + ((I >> 1) & 1)) * 1152>(lw, v[I & 1], c.rx, cur);
  }
};
template <int I> DI void touch_w2(const Ctx& c, unsigned& junk) {
  const unsigned l = lane_id(), idx = (c.T & 3) * 6 + c.m;
  if constexpr (I == 0) touch(junk, l * 128, c.rwd, (1152 * c.a + 48 * idx) * 1152 + c.w * 8192);
  else touch(junk, l * 128, c.w < 3 ? c.rwd : c.rwo, c.w < 3 ? (1152 * c.a + 48 * idx) * 1152 + 32768 + c.w * 7168 : (144 * c.a + 7 * idx) * 1152);
}

constexpr int slot1(int n) { return n < 8 ? n % 4 : (n - 4) % 9; }
constexpr int sig_idx(int F) {
  if ((F + PD) % 16 == 0) return -1;
  int e = 0;
  for (int G = F - F % 72; G < F; ++G) if ((G + PD) % 16 != 0) ++e;
  return e <= 65 ? e : -1;
}
DI void role_mlp(Ctx& c) {
  const unsigned l = lane_id(), g = l >> 4, q = (l & 15) >> 2, pp = l & 3, r8 = l >> 3;
  const unsigned vW = (8 * (r8 >> 2) + (r8 & 3)) * 9216 + 16 * ((l & 7) ^ (((r8 >> 1) & 3) << 1));
  const unsigned lw = c.w * 1024, sW = (16 * (c.w & 1) + 4 * (c.w >> 1)) * 9216 + (1152 * c.a + 192 * c.m) * 2;
  unsigned sWk[9];
#pragma unroll
  for (int i = 0; i < 9; ++i) sWk[i] = sW + c.kg[i] * (64 * 9216);
  const unsigned sH = WS_H + (((4 * c.T + c.w) * 4 + c.a) * 42 + 2 * c.m) * 4096;
  const unsigned f1 = WS_F1 + (c.T * 4 + c.a) * 128;
  XS xs; xs.init(c, M_X);
  bf16x8 xf[18][4];
  f32x4 ag[4][4], au[4][4];
  unsigned sp[4][4][2];
  P2<8> p2;
  unsigned junk = c.junk;
  u32x4 hv;
  unsigned vl16 = l * 16;
  auto issue = [&](auto F_, auto R_) {
    constexpr Op o = mlp_op(CI(F_), CI(R_));
    if constexpr (o.kind == O_W) {
      constexpr int pn = o.a / 9, ks = (o.a % 9) * 4 + o.b, k = ks % 18;
      dma<slot1(o.a) * S1 + o.b * 4096, (k & 1) * 32 * 9216 + pn * 128>(lw, vW, ks < 18 ? c.rwg : c.rwu, sWk[k >> 1]);
    } else if constexpr (o.kind == O_X) {
      if constexpr (o.a >= 3 && o.b == 0) asm volatile("s_waitcnt lgkmcnt(4)" ::: "memory");
      xs.template dmax<3, o.a, o.b>(c);
    } else if constexpr (o.kind == O_FIN) {
      __builtin_amdgcn_raw_buffer_store_b128(hv, c.rws, vl16 + (o.b & 3) * 1024, sH + (12 * o.a + (o.b >> 2)) * 4096, 16);
      asm volatile("" ::: "memory");
    } else if constexpr (o.kind == O_TW) touch_w2<o.a>(c, junk);
    else if constexpr (o.kind == O_TH) touch(junk, vl16 * 8, c.rws, sH + 12 * o.a * 4096);
    else p2.template issue<o.kind, o.a, o.b>(c);
  };
  static_for<20>([&](auto I_) { issue(IC(CI(I_) - 22), IC(2)); });
  unsigned rdx2[2] = {xs.rd[0] + 65536, xs.rd[1] + 65536}; PIN(rdx2[0]); PIN(rdx2[1]);
  auto rdx = [&](auto C_, auto I_) {
    constexpr int C = CI(C_), s = CI(I_) >> 2, rt = CI(I_) & 3;
    xf[2 * C + s][rt] = ldsr<bf16x8>((C % 3 == 2 ? rdx2[s] : xs.rd[s] + (C % 3) * 32768) + rt * 2048);
  };
  unsigned rb[4];
#pragma unroll
  for (int t = 0; t < 4; ++t) rb[t] = (4 * g + q) * 128 + 32 * (t ^ ((2 * g + (q >> 1)) & 3)) + 8 * pp;
  float e0 = 0.f, e1 = 0.f, e3 = 0.f, h0 = 0.f, h1 = 0.f;
  unsigned e2 = 0;
  bf16x8 f[16];
  s16x4 flo;
  unsigned rb2[3];
  asm volatile("" ::"s"(c.E));
  wait_bar<VM<0, -1>>();
  STAMP(1);
  static_for<4>([&](auto I_) { rdx(IC(0), I_); });
  static_for<PD>([&](auto I_) { f[CI(I_)] = cat(trd(rb[CI(I_)]), trd(rb[CI(I_)] + 2048)); });
  static_for<4>([&](auto I_) { rdx(IC(0), IC(4 + CI(I_))); });
  SBAR();
  static_for<27>([&](auto N_) {
    static_for<16>([&](auto I_) {
      constexpr int N = CI(N_), I = CI(I_), F = N * 16 + I, FP = F % 144, ps = FP / 72, FQ = FP % 72, ks = FQ / 4, t = FQ % 4;
      constexpr int RG = (F + PD) / 16, pos = (F + PD) % 16, RR = pos == 0 ? 2 : 0;
      constexpr unsigned ro = (slot1(RG) % 4) * S1 + (pos >> 2) * 4096;
      const bf16x8 a = f[I];
      static_for<4>([&](auto R_) {
        constexpr int R = CI(R_);
        if constexpr (ps == 0) mf<(ks < 16), ks == 0>(ag[t][R], a, xf[ks][R]); else mf<(ks < 16), ks == 0>(au[t][R], a, xf[ks][R]);
        SBAR();
        if constexpr (R == 0 && F < 64 && F % 8 == 3) wait_vm<VM<0, F>>();
        if constexpr (pos == 0) {
          if constexpr (R < 2 && RG < 27 && slot1(RG) / 4 != slot1(RG - 1) / 4) {
#pragma unroll
            for (int i = 2 * R; i < 2 * R + 2; ++i) { rb[i] += (unsigned)(slot1(RG) / 4 - slot1(RG - 1) / 4) * 4 * S1; PIN(rb[i]); }
          }
          if constexpr (R == 0 && RG == 27) {
            const unsigned l4 = lane_id();
#pragma unroll
            for (int i = 0; i < 3; ++i) { rb2[i] = P2<8>::slot(0) + P2<8>::rbase(c, l4, i); PIN(rb2[i]); }
          }
          if constexpr (R == 2) {
            wait_bar<VM<0, F>>();
            STAMP(1 + RG);
            if constexpr (RG == 27) tieA(p2.bf[0][0], p2.bf[0][1], p2.bf[0][2], p2.bf[0][3]);
          }
        }
        if constexpr (R == RR) { if constexpr (RG < 27) flo = trd(rb[pos & 3] + ro); else flo = trd(rb2[pos]); }
        if constexpr (R == RR + 1) { if constexpr (RG < 27) f[pos] = cat(flo, trd(rb[pos & 3] + ro + 2048)); else f[pos] = cat(flo, trd(rb2[pos] + 4608)); }
        if constexpr (F < 64 && (F % 8 == 3 || F % 8 == 4)) rdx(IC(F / 8 + 1), IC(4 * (F % 8 - 3) + R));
        SBAR();
        if constexpr (pos == 0 && R == 3) {
          if constexpr (RG == FLG0 || RG == FLG1) { if (threadIdx.x == 0) flag_store(c, f1 + 8 * c.m, RG == FLG0 ? 1 : 2); }
          if constexpr (RG == 20) p2.init(c);
          if constexpr (RG == 24) { PIN(junk); p2.template check<0>(c); }
        }
        if constexpr (ps == 1 && sig_idx(F) >= 0) {
          constexpr int e = sig_idx(F), d = e - 2;
          if constexpr (R == 0 && d >= 0) e2 = w_add1(e1);
          if constexpr (R == 1) { e3 = e0; if constexpr (e < 64) e0 = w_mul(ag[e / 16][(e / 4) % 4][e % 4]); }
          if constexpr (R == 2 && e >= 1 && e <= 64) e1 = w_exp(e3);
          if constexpr (R == 3 && d >= 0) { if constexpr (d % 2 == 0) sp[d / 16][(d / 4) % 4][(d % 4) / 2] = w_rcplo(e2); else w_rcphi(sp[d / 16][(d / 4) % 4][(d % 4) / 2], e2); }
        }
        if constexpr (F >= 144 && FP >= FIN0 && FP < FIN0 + 32) {
          constexpr int J = (FP - FIN0) / 4, i = FP % 4, e = J >> 2, rt = J & 3, tt = 2 * e + (i >> 1), k = 2 * (i & 1);
          if constexpr (R == 0) h0 = w_mixlo(au[tt][rt][k], sp[tt][rt][i & 1]);
          if constexpr (R == 1) h1 = w_mixhi(au[tt][rt][k + 1], sp[tt][rt][i & 1]);
          if constexpr (R == 2) hv[i] = w_cvt(h0, h1);
        }
        if constexpr (R >= 2) issue(IC(F), R_);
        SBAR();
      });
    });
  });
  asm volatile("s_nop 7" ::: "memory");
  static_for<8>([&](auto J_) {
    constexpr int e = CI(J_) >> 2, rt = CI(J_) & 3;
    u32x4 v;
#pragma unroll
    for (int i = 0; i < 4; ++i) { const int tt = 2 * e + (i >> 1), k = 2 * (i & 1); v[i] = w_cvt(w_mixlo(au[tt][rt][k], sp[tt][rt][i & 1]), w_mixhi(au[tt][rt][k + 1], sp[tt][rt][i & 1])); }
    __builtin_amdgcn_raw_buffer_store_b128(v, c.rws, vl16 + rt * 1024, sH + (24 + e) * 4096, 16);
    asm volatile("" ::: "memory");
  });
  SBAR();
  phase2<8>(c, p2, f);
}

DI u32x2 pk4(const f32x4& v) { return u32x2{cvtpk(v[0], v[1]), cvtpk(v[2], v[3])}; }
DI void sw16(float& a, float& b) { auto r = __builtin_amdgcn_permlane16_swap(__float_as_uint(a), __float_as_uint(b), false, false); a = __uint_as_float(r[0]); b = __uint_as_float(r[1]); }
DI void sw32(float& a, float& b) { auto r = __builtin_amdgcn_permlane32_swap(__float_as_uint(a), __float_as_uint(b), false, false); a = __uint_as_float(r[0]); b = __uint_as_float(r[1]); }
template <bool MX> DI float xred(float (&v)[4]) {
  sw16(v[0], v[1]); sw16(v[2], v[3]);
  float a = MX ? max2(v[0], v[1]) : v[0] + v[1], b = MX ? max2(v[2], v[3]) : v[2] + v[3];
  sw32(a, b);
  return MX ? max2(a, b) : a + b;
}
DI void xbc(float t, float (&v)[4]) {
  float a = t, b = t;
  sw32(a, b);
  v[0] = a; v[1] = a; v[2] = b; v[3] = b;
  sw16(v[0], v[1]); sw16(v[2], v[3]);
}

DI void role_att(Ctx& c) {
  const unsigned mp = c.m & 1;
  u32x2 vp[4][5], qp[4][5], hp[2];
  f32x4 gate;
  {
    const unsigned l = lane_id(), g = l >> 4, q = (l & 15) >> 2, pp = l & 3;
    const unsigned lc = (l & 15) ^ (g << 1), hb = 144 * (2 * c.a + mp);
    const unsigned vA = g * 3456 + hb + (lc < 10 ? 1152 + 16 * (lc < 9 ? lc : 8) : 2304 + 16 * (lc - 10));
    const unsigned vB = g * 3456 + hb + (lc < 4 ? 2304 + 16 * (lc < 3 ? 6 + lc : 8) : 16 * (lc - 4));
    const u64 maskB = __builtin_amdgcn_ballot_w64(lc < 13);
    const unsigned lw = c.w * 1152;
    unsigned sWk[9];
#pragma unroll
    for (int i = 0; i < 9; ++i) sWk[i] = (8 * c.w + 64 * c.kg[i]) * 3456;
    const int hr = 256 * (int)c.T + ((c.w & 1) ? -16 : -15) + 2 * (int)(l >> 3);
    const unsigned vHx = (unsigned)(hr < 0 ? 0 : hr) * 1152 + 16 * ((l & 7) ^ (4 * (c.w & 1) + g));
    const unsigned lHx = A_HX + c.w * 1024;
    XS xs; xs.init(c, A_X);
    unsigned rh[2] = {A_HX + (l & 15) * 128 + 16 * (g ^ ((l & 15) >> 1)), 0}; rh[1] = rh[0] ^ 64; PIN(rh[0]); PIN(rh[1]);
    bf16x8 xf[18][4];
    unsigned junk = c.junk;
    auto issue = [&](auto F_, auto R_) {
      constexpr Op o = att1_op(CI(F_), CI(R_));
      if constexpr (o.kind == O_W) {
        constexpr unsigned LO = (o.a % 4) * S2 + o.b * 4608, SO = (32 * (o.b >> 1) + 4 * (o.b & 1)) * 3456;
        if constexpr (o.a < 9) dma<LO, SO>(lw, vA, c.rqkv, sWk[o.a % 9]); else dma_m<LO, SO>(lw, vB, c.rqkv, sWk[o.a % 9], maskB);
      } else if constexpr (o.kind == O_X) {
        if constexpr (o.a >= 2 && o.b == 0) asm volatile("s_waitcnt lgkmcnt(4)" ::: "memory");
        xs.template dmax<2, o.a, o.b>(c);
      } else if constexpr (o.kind == O_HX) {
        if constexpr (o.a < 4) dma<o.a * 4096, 0>(lHx, vHx, c.rx, (c.w < 2 ? c.kg[2 * o.a] : c.kg[2 * o.a + 1]) * 128);
        else dma<16384, 0>(lHx, c.w < 2 ? vHx : l * 16, c.w < 2 ? c.rx : c.rgw, c.w < 2 ? c.kg[8] * 128 : (c.w & 1) * 1024);
      }
    };
    auto rdx = [&](auto C_, auto I_) {
      constexpr int C = CI(C_), s = CI(I_) >> 2, rt = CI(I_) & 3;
      xf[2 * C + s][rt] = ldsr<bf16x8>(xs.rd[s] + (C % 2) * 32768 + rt * 2048);
    };
    static_for<21>([&](auto I_) { issue(IC(CI(I_) - 23), IC(2)); });
    unsigned rb[4], rx1, rx2;
#pragma unroll
    for (int t = 0; t < 4; ++t) rb[t] = g * 1152 + q * 256 + 32 * (t ^ q) + 8 * pp;
    rx1 = g * 1152 + q * 256 + 32 * (c.w ^ q) + 8 * pp;
    rx2 = g * 1152 + q * 256 + 128 + 32 * ((c.w < 2 ? c.w + 1 : 3) ^ q) + 8 * pp;
    const unsigned gsrc = A_GW + 2 * (32 * (l >> 5) + 8 * ((l >> 2) & 3) + 4 * ((l >> 4) & 1) + (l & 3));
    const unsigned gdst = (l >> 2) * 1152 + (l & 3) * 256 + 16 * (13 ^ ((l & 3) << 1));
    u16 gwv = 0;
    f32x4 acc[8][4], ah[3];
    bf16x8 f[16], hx, ha, hc;
    s16x4 flo, hlo;
    asm volatile("" ::"s"(c.E));
    wait_bar<VM<1, -1>>();
    STAMP(1);
    static_for<8>([&](auto I_) { rdx(IC(0), I_); });
    static_for<PD>([&](auto I_) { f[CI(I_)] = cat(trd(rb[CI(I_)]), trd(rb[CI(I_)] + 4608)); });
    auto pass = [&](auto PS_) {
      constexpr int PS = CI(PS_), NT = PS == 0 ? 8 : 7, FC = 2 * NT;
      static_for<9>([&](auto N_) {
        static_for<FC>([&](auto I_) {
          constexpr int N = CI(N_), I = CI(I_), F = 144 * PS + N * FC + I, ks = 2 * N + I / NT, t = I % NT;
          constexpr int RG = 9 * PS + N + (I + PD) / FC, pos = (I + PD) % FC;
          constexpr int NTN = RG < 9 ? 8 : 7, tn = pos % NTN, RR = pos == 0 ? 2 : 0;
          constexpr unsigned ro = (RG % 2) * S2 + (pos / NTN) * 9216 + 128 * (tn >> 2);
          constexpr unsigned rc = ((9 * PS + N) % 2) * S2 + (I / NT) * 9216;
          constexpr bool SW = PS == 0 ? t >= 5 : t < 2;
          const bf16x8 a = f[I];
          static_for<5>([&](auto R_) {
            constexpr int R = CI(R_);
            if constexpr (R < 4) { if constexpr (SW) mfs<(ks < 16), ks == 0>(acc[t][R], a, xf[ks][R]); else mf<(ks < 16), ks == 0>(acc[t][R], a, xf[ks][R]); }
            else if constexpr (PS == 0) {
              if constexpr (t == 4) mf<false, ks == 0>(ah[1], a, hx);
              if constexpr (t == 5) mf<false, ks == 0>(ah[0], ha, hx);
              if constexpr (t == 7) mfs<false, ks == 0>(ah[2], hc, hx);
            } else if constexpr (t == 5) mfs<false, ks == 0>(ah[0], ha, hx);
            SBAR();
            if constexpr (R == 0 && PS == 0 && I == 6 && N < 8) wait_vm<VM<1, F>>();
            if constexpr (pos == 0 && RG < 18) {
              if constexpr (R < 2 && RG % 2 == 0) {
                constexpr unsigned d = RG % 4 == 0 ? 0u - 2 * S2 : 2 * S2;
#pragma unroll
                for (int i = 2 * R; i < 2 * R + 2; ++i) { rb[i] += d; PIN(rb[i]); }
                if (R == 0) { rx1 += d; PIN(rx1); } else if (RG < 9) { rx2 += d; PIN(rx2); }
              }
              if constexpr (R == 0 && RG == 9) { rx1 ^= 32 * (c.w & 2); PIN(rx1); }
              if constexpr (R == 2) {
                wait_bar<VM<1, F>>();
                STAMP(1 + RG);
                if constexpr (RG == 1) PIN(junk);
              }
            }
            if constexpr (R == RR && RG < 18) flo = trd(rb[tn & 3] + ro);
            if constexpr (R == RR + 1 && RG < 18) f[pos] = cat(flo, trd(rb[tn & 3] + ro + 4608));
            if constexpr (PS == 0 && N < 8 && (I == 6 || I == 7) && R < 4) rdx(IC(N + 1), IC(4 * (I - 6) + R));
            if constexpr (t == 0 && R == 2) hx = ldsr<bf16x8>(rh[ks & 1] + (ks >> 1) * 2048);
            if constexpr (t == 1 && R == 2) hlo = trd(rx1 + rc);
            if constexpr (t == 2 && R == 2) ha = cat(hlo, trd(rx1 + rc + 4608));
            if constexpr (PS == 0 && t == 3 && R == 2) hlo = trd(rx2 + rc);
            if constexpr (PS == 0 && t == 4 && R == 2) hc = cat(hlo, trd(rx2 + rc + 4608));
            if constexpr (RG + 3 >= 9 && RG + 3 <= 17 && R == 3) {
              if constexpr (pos == 6) gwv = ldsr<u16>(gsrc + c.kg[RG + 3 - 9] * 128);
              if constexpr (pos == 9) ldsw<u16>(gdst + ((RG + 3) % 4) * S2, gwv);
            }
            SBAR();
            if constexpr (R == 2 || R == 3) issue(IC(F), R_);
            SBAR();
          });
        });
      });
    };
    pass(IC(0));
    asm volatile("s_nop 7" ::: "memory");
    {
      const unsigned l3 = lane_id(), wa = A_KX + (4 * c.w + 2) * 2560 + l3 * 8, e0 = A_KX + l3 * 8, e1 = A_KX + 2560 + (l3 ^ 8) * 8;
#pragma unroll
      for (int rt = 0; rt < 4; ++rt) {
#pragma unroll
        for (int t = 0; t < 5; ++t) ldsw<u32x2>(wa + rt * 2560 + t * 512, pk4(acc[t][rt]));
#pragma unroll
        for (int t = 0; t < 3; ++t) { vp[rt][t] = pk4(acc[5 + t][rt]); PIN(vp[rt][t]); }
      }
      const u32x2 h0 = pk4(ah[0]), h1 = pk4(ah[1]);
      ldsw<u32x2>(e0 + c.w * 512, h0); ldsw<u32x2>(e1 + c.w * 512, h0);
      ldsw<u32x2>(e0 + 4 * 512, h1); ldsw<u32x2>(e1 + 4 * 512, h1);
      hp[0] = pk4(ah[2]); PIN(hp[0]);
    }
    SBAR();
    pass(IC(1));
    asm volatile("s_nop 7\n\ts_waitcnt lgkmcnt(0)\n\ts_barrier" ::: "memory");
    STAMP(19);
#pragma unroll
    for (int rt = 0; rt < 4; ++rt) {
#pragma unroll
      for (int t = 0; t < 2; ++t) vp[rt][3 + t] = pk4(acc[t][rt]);
#pragma unroll
      for (int t = 0; t < 5; ++t) qp[rt][t] = pk4(acc[2 + t][rt]);
      gate[rt] = sigm(acc[6][rt][0]);
    }
    hp[1] = pk4(ah[0]);
  }
  P2<12> p2;
  p2.init(c);
  p2.poll<0>(c);
  auto pre = [&](auto A_, auto N_) { static_for<CI(N_)>([&](auto I_) { constexpr Op o = att2_op(CI(A_) + CI(I_) - 50, 2); p2.template issue<o.kind, o.a, o.b>(c); }); };
  pre(IC(0), IC(6));
  const unsigned l = lane_id(), g = (l >> 4) & 3, j = l & 15;
  {
    if (c.w < 3) {
      const unsigned wa = A_VX + (c.w + 1) * 5120 + l * 8;
#pragma unroll
      for (int pa = 0; pa < 2; ++pa)
#pragma unroll
        for (int t = 0; t < 5; ++t) ldsw<u32x2>(wa + (pa * 5 + t) * 512, vp[2 + pa][t]);
    }
    const unsigned e0 = A_VX + l * 8, ev = A_VX + 2560 + (l ^ 32) * 8;
    const unsigned tv1 = c.w < 2 ? c.w : 2, tv2 = 3 + (c.w & 1);
    ldsw<u32x2>(e0 + tv1 * 512, hp[0]); ldsw<u32x2>(ev + tv1 * 512, hp[0]);
    ldsw<u32x2>(e0 + tv2 * 512, hp[1]); ldsw<u32x2>(ev + tv2 * 512, hp[1]);
    if (mp == 0 && g == 2) __builtin_amdgcn_raw_buffer_store_b128(__builtin_bit_cast(u32x4, gate), c.rws, j * 16, WS_G + (c.a * 32 + 4 * c.T + c.w) * 256, 16);
#pragma unroll
    for (int rt = 0; rt < 4; ++rt) gate[rt] = __shfl(gate[rt], 32 + j);
  }
  asm volatile("s_waitcnt vmcnt(6) lgkmcnt(0)\n\ts_barrier" ::: "memory");
  STAMP(20);
  p2.check<0>(c);
  pre(IC(6), IC(4));
  {
    u32x10 kt[6];
    u32x6 vt[5][2];
    const unsigned ra = A_VX + c.w * 5120 + l * 8, rk = A_KX + 4 * c.w * 2560 + l * 8;
#pragma unroll
    for (int i = 0; i < 6; ++i)
#pragma unroll
      for (int t = 0; t < 5; ++t) { const u32x2 k2 = ldsr<u32x2>(rk + i * 2560 + t * 512); kt[i][2 * t] = k2[0]; kt[i][2 * t + 1] = k2[1]; }
#pragma unroll
    for (int pa = 0; pa < 2; ++pa)
#pragma unroll
      for (int t = 0; t < 5; ++t) {
        const u32x2 v2 = ldsr<u32x2>(ra + (pa * 5 + t) * 512);
        vt[t][pa] = u32x6{v2[0], v2[1], vp[pa][t][0], vp[pa][t][1], vp[2 + pa][t][0], vp[2 + pa][t][1]};
      }
    SBAR();
#pragma unroll
    for (int i = 0; i < 6; ++i) PIN(kt[i]);
#pragma unroll
    for (int i = 0; i < 10; ++i) PIN(vt[i >> 1][i & 1]);
    f32x4 bprev, bown, bprev0;
    const bool first = c.T == 0 && c.w == 0;
#pragma unroll
    for (int r = 0; r < 4; ++r) {
      const int key = 4 * (int)g + r;
      bprev[r] = key >= (int)j + 9 ? 0.f : -1e30f;
      bown[r] = (key <= (int)j && key >= (int)j - 7) ? 0.f : -1e30f;
      bprev0[r] = first ? -1e30f : bprev[r];
    }
    const unsigned mpv[2] = {(bprev[0] == 0.f ? 0xffffu : 0u) | (bprev[1] == 0.f ? 0xffff0000u : 0u), (bprev[2] == 0.f ? 0xffffu : 0u) | (bprev[3] == 0.f ? 0xffff0000u : 0u)};
    constexpr float SC2 = 0.23570226039551584f * 1.4426950408889634f;
    const unsigned sH = WS_H + (((4 * c.T + c.w) * 4 + c.a) * 42 + 36 + 3 * mp) * 4096;
    u32x4 qf[4][4];
#pragma unroll
    for (int rt = 0; rt < 4; ++rt)
#pragma unroll
      for (int h = 0; h < 4; ++h) {
#pragma unroll
        for (int i = 0; i < 4; ++i) { const int col = 16 * (h + (i >> 1)) + 4 * (int)g + 2 * (i & 1); qf[rt][h][i] = (col >= 18 * h && col < 18 * h + 18) ? qp[rt][h + (i >> 1)][i & 1] : 0u; }
        PIN(qf[rt][h]);
      }
    PIN(bprev); PIN(bown); PIN(bprev0);
    auto scores = [&](auto RT_, f32x4 (&sp)[4], f32x4 (&so)[4]) {
      constexpr int rt = CI(RT_);
      static_for<4>([&](auto H_) {
        constexpr int h = CI(H_);
        sp[h] = mfc(__builtin_shufflevector(kt[rt], kt[rt], 2 * h, 2 * h + 1, 2 * h + 2, 2 * h + 3), qf[rt][h], rt < 2 ? bprev0 : bprev);
        so[h] = mfc(__builtin_shufflevector(kt[rt + 2], kt[rt + 2], 2 * h, 2 * h + 1, 2 * h + 2, 2 * h + 3), qf[rt][h], bown);
      });
    };
    f32x4 sp[2][4], so[2][4];
    asm volatile("s_nop 1" ::: "memory");
    scores(IC(0), sp[0], so[0]);
    static_for<4>([&](auto RT_) {
      constexpr int rt = CI(RT_), pa = rt & 1, sn = rt >> 1;
      if constexpr (rt < 3) scores(IC(rt + 1), sp[(rt + 1) & 1], so[(rt + 1) & 1]);
      SBAR();
      float s[4][4], red[4], bc[4];
#pragma unroll
      for (int h = 0; h < 4; ++h) {
#pragma unroll
        for (int r = 0; r < 4; ++r) s[h][r] = max2(sp[pa][h][r], so[pa][h][r]);
        red[h] = max2(max3(s[h][0], s[h][1], s[h][2]), s[h][3]);
      }
      xbc(-SC2 * xred<true>(red), bc);
      if constexpr (rt == 1) pre(IC(19), IC(4));
      if constexpr (rt == 2) pre(IC(32), IC(4));
#pragma unroll
      for (int h = 0; h < 4; ++h) {
#pragma unroll
        for (int r = 0; r < 4; ++r) s[h][r] = __builtin_amdgcn_exp2f(__builtin_fmaf(s[h][r], SC2, bc[h]));
        red[h] = (s[h][0] + s[h][1]) + (s[h][2] + s[h][3]);
      }
      xbc(gate[rt] * __builtin_amdgcn_rcpf(xred<false>(red)), bc);
      if constexpr (rt == 0) pre(IC(10), IC(6));
      if constexpr (rt == 1) pre(IC(23), IC(6));
      u32x4 P[4];
#pragma unroll
      for (int h = 0; h < 4; ++h) {
        const unsigned p0 = cvtpk(s[h][0] * bc[h], s[h][1] * bc[h]), p1 = cvtpk(s[h][2] * bc[h], s[h][3] * bc[h]);
        P[h] = u32x4{p0 & mpv[0], p1 & mpv[1], p0 & ~mpv[0], p1 & ~mpv[1]};
        PIN(P[h]);
      }
      f32x4 hi[4], lo[4];
      asm volatile("s_nop 1" ::: "memory");
      static_for<4>([&](auto T_) {
        constexpr int t = CI(T_);
        hi[t] = mfz(__builtin_shufflevector(vt[t][pa], vt[t][pa], 2 * sn, 2 * sn + 1, 2 * sn + 2, 2 * sn + 3), P[t]);
        lo[t] = mfz(__builtin_shufflevector(vt[t + 1][pa], vt[t + 1][pa], 2 * sn, 2 * sn + 1, 2 * sn + 2, 2 * sn + 3), P[t]);
      });
      asm volatile("s_nop 7" ::: "memory");
      SBAR();
      u32x2 o[5];
#pragma unroll
      for (int t = 0; t < 5; ++t)
#pragma unroll
        for (int i = 0; i < 2; ++i) {
          const unsigned vh = t < 4 ? cvtpk(hi[t < 4 ? t : 0][2 * i], hi[t < 4 ? t : 0][2 * i + 1]) : 0u;
          o[t][i] = (4 * (int)g + 2 * i < 2 * t) ? cvtpk(lo[t > 0 ? t - 1 : 0][2 * i], lo[t > 0 ? t - 1 : 0][2 * i + 1]) : vh;
        }
      __builtin_amdgcn_raw_buffer_store_b128(u32x4{o[0][0], o[0][1], o[1][0], o[1][1]}, c.rws, l * 16 + rt * 1024, sH, 16);
      __builtin_amdgcn_raw_buffer_store_b128(u32x4{o[2][0], o[2][1], o[3][0], o[3][1]}, c.rws, l * 16 + rt * 1024, sH + 4096, 16);
      __builtin_amdgcn_raw_buffer_store_b128(u32x4{o[4][0], o[4][1], 0u, 0u}, c.rws, l * 16 + rt * 1024, sH + 8192, 16);
      SBAR();
    });
  }
  STAMP(21);
  bf16x8 f[16];
  phase2<12>(c, p2, f);
}

__global__ __launch_bounds__(256, 1) void fused_kernel(const u16* x, const u16* wqkv, const u16* wo, const u16* wg, const u16* wu, const u16* wd,
                                                        const u16* gw, char* ws, u16* out) {
  __shared__ __attribute__((aligned(16))) char smem[LDS_BYTES];
  asm volatile("" ::"v"((unsigned)(size_t)smem));
  Ctx c;
  c.rx = __builtin_amdgcn_make_buffer_rsrc((void*)x, (short)0, SEQ * DM * 2, 0x00020000);
  c.rqkv = __builtin_amdgcn_make_buffer_rsrc((void*)wqkv, (short)0, DM * 3 * DM * 2, 0x00020000);
  c.rwo = __builtin_amdgcn_make_buffer_rsrc((void*)wo, (short)0, DM * DM * 2, 0x00020000);
  c.rwg = __builtin_amdgcn_make_buffer_rsrc((void*)wg, (short)0, DM * FF * 2, 0x00020000);
  c.rwu = __builtin_amdgcn_make_buffer_rsrc((void*)wu, (short)0, DM * FF * 2, 0x00020000);
  c.rwd = __builtin_amdgcn_make_buffer_rsrc((void*)wd, (short)0, DM * FF * 2, 0x00020000);
  c.rgw = __builtin_amdgcn_make_buffer_rsrc((void*)gw, (short)0, DM * 2, 0x00020000);
  c.rws = __builtin_amdgcn_make_buffer_rsrc((void*)ws, (short)0, WS_BYTES, 0x00020000);
  c.rout = __builtin_amdgcn_make_buffer_rsrc((void*)out, (short)0, SEQ * DM * 2, 0x00020000);
  const unsigned bid = blockIdx.x, xcd = bid & 7, idx = bid >> 3;
  c.w = __builtin_amdgcn_readfirstlane(threadIdx.x >> 6);
  c.a = xcd & 3; c.T = 4 * (xcd >> 2) + (idx >> 3); c.m = (idx & 3) == 3 ? 6 + ((idx >> 2) & 1) : 3 * ((idx >> 2) & 1) + (idx & 3);
  {
    const unsigned rot = c.m + 2 * (c.T & 3);
#pragma unroll
    for (int i = 0; i < 9; ++i) { const unsigned k = i + rot; c.kg[i] = k >= 18 ? k - 18 : k >= 9 ? k - 9 : k; }
  }
  c.E = *(const __attribute__((address_space(4))) u64*)(ws + WS_E + c.T * 128);
  STAMP(0);
  {
    u64 pc; unsigned len;
    asm volatile("s_getpc_b64 %0\n.Lgm_code0:\n\ts_mov_b32 %1, .Lgm_code1-.Lgm_code0" : "=s"(pc), "=s"(len));
    const rsrc_t rc = __builtin_amdgcn_make_buffer_rsrc((void*)pc, (short)0, len, 0x00020000);
    c.junk = 0;
    const unsigned l = lane_id();
    if (c.w == 0) touch(c.junk, l * 128, rc, (idx & 15) * 8192);
    if (c.w == 1) {
      const unsigned o = l < 4 ? WS_H + (((4 * c.T + l) * 4 + c.a) * 42 + 2 * c.m) * 4096 : l < 8 ? WS_PART + ((c.T * 8 + c.m) * 64 + (l - 4) * 16) * 3072 : l < 9 ? WS_G : WS_F1;
      touch(c.junk, o, c.rws, 0);
    }
    if (c.w == 2) touch(c.junk, (l & 3) * (64 * 1152), c.rout, 256 * c.T * 1152);
  }
  if (c.m < 6) role_mlp(c); else role_att(c);
  asm volatile(".Lgm_code1:" ::: "memory");
}

extern "C" void kernel_launch(void* const* d_in, const int* in_sizes, int n_in, void* d_out, int out_size, void* d_ws, size_t ws_size,
                              hipStream_t stream) {
  fused_kernel<<<256, 256, 0, stream>>>((const u16*)d_in[0], (const u16*)d_in[1], (const u16*)d_in[2], (const u16*)d_in[4], (const u16*)d_in[5],
                                        (const u16*)d_in[6], (const u16*)d_in[7], (char*)d_ws, (u16*)d_out);
}
```

```cpp
#include <hip/hip_runtime.h>
#include <utility>

typedef unsigned short u16;
typedef unsigned long long u64;
typedef short s16x4 __attribute__((ext_vector_type(4)));
typedef __bf16 bf16x8 __attribute__((ext_vector_type(8)));
typedef __bf16 bf16x2 __attribute__((ext_vector_type(2)));
typedef float f32x2 __attribute__((ext_vector_type(2)));
typedef float f32x4 __attribute__((ext_vector_type(4)));
typedef unsigned u32x2 __attribute__((ext_vector_type(2)));
typedef unsigned u32x4 __attribute__((ext_vector_type(4)));
typedef unsigned u32x6 __attribute__((ext_vector_type(6)));
typedef unsigned u32x10 __attribute__((ext_vector_type(10)));
typedef __amdgpu_buffer_rsrc_t rsrc_t;
#define LAS __attribute__((address_space(3)))
#define DI __device__ __forceinline__
#define SBAR() __builtin_amdgcn_sched_barrier(0)
#define IC(x) std::integral_constant<int, (x)>{}
#define CI(x) decltype(x)::value

constexpr int SEQ = 2048, DM = 576, FF = 4608;
constexpr unsigned WS_H = 0;
constexpr unsigned WS_PART = 24u << 20;
constexpr unsigned WS_G = 40u << 20;
constexpr unsigned WS_F1 = 41u << 20;
constexpr unsigned WS_F2 = WS_F1 + 8192;
constexpr unsigned WS_E = WS_F2 + 65536;
constexpr unsigned WS_BYTES = 42u << 20;
constexpr unsigned LDS_BYTES = 163840;
constexpr unsigned S1 = 16384;
constexpr unsigned S2 = 18432;
constexpr unsigned S3 = S2 + 9216;
constexpr unsigned M_X = 65536;
constexpr unsigned A_X = 4 * S2;
constexpr unsigned A_HX = A_X + 65536;
constexpr unsigned A_GW = A_HX + 18432;
constexpr unsigned A_KX = 83968;
constexpr unsigned A_VX = A_KX + 18 * 2560;
static_assert(A_GW + 2048 <= LDS_BYTES && A_VX <= A_HX && A_VX + 20480 <= LDS_BYTES && 3 * S3 <= A_KX, "lds");

template <int... Is> using iseq = std::integer_sequence<int, Is...>;
template <class Fn, int... Is> DI void static_for_(Fn&& f, iseq<Is...>) { (f(std::integral_constant<int, Is>{}), ...); }
template <int N, class Fn> DI void static_for(Fn&& f) { static_for_(f, std::make_integer_sequence<int, N>{}); }

template <int N> DI void wait_bar() { asm volatile("s_waitcnt vmcnt(%0) lgkmcnt(0)\n\ts_barrier" ::"i"(N) : "memory"); }
template <int N> DI void wait_vm() { asm volatile("s_waitcnt vmcnt(%0)" ::"i"(N) : "memory"); }
template <class T> DI void PIN(T& x) { asm volatile("" : "+v"(x)); }
DI void tieA(bf16x8& a, bf16x8& b, bf16x8& c, bf16x8& d) { asm volatile("" : "+a"(a), "+a"(b), "+a"(c), "+a"(d)); }
template <unsigned LO, unsigned SO> DI unsigned dprep(unsigned lb, unsigned sb) {
  unsigned t;
  asm volatile("s_add_u32 m0, %1, %3\n\ts_add_u32 %0, %2, %4" : "=s"(t) : "s"(lb), "s"(sb), "i"(LO), "i"(SO) : "scc");
  return t;
}
template <unsigned LO> DI void dprep_l(unsigned lb) { asm volatile("s_add_u32 m0, %0, %1\n\ts_nop 0" ::"s"(lb), "i"(LO) : "scc"); }
template <unsigned IMM> DI void dfire(unsigned voff, rsrc_t srd, unsigned so) {
  asm volatile("buffer_load_dwordx4 %0, %1, %2 offen offset:%3 lds" ::"v"(voff), "s"(srd), "s"(so), "i"(IMM) : "memory");
}
DI void dfire_m(unsigned voff, rsrc_t srd, unsigned so, u64 mask) {
  asm volatile("s_mov_b64 exec, %3\n\tbuffer_load_dwordx4 %0, %1, %2 offen lds\n\ts_mov_b64 exec, -1" ::"v"(voff), "s"(srd), "s"(so), "s"(mask) : "memory");
}
template <unsigned IMM> DI void ldh(bf16x8& d, unsigned voff, rsrc_t srd, unsigned so) {
  asm volatile("buffer_load_dwordx4 %0, %1, %2, %3 offen offset:%4 sc1" : "=a"(d) : "v"(voff), "s"(srd), "s"(so), "i"(IMM) : "memory");
}
DI void ldg(f32x4& d, unsigned voff, rsrc_t srd, unsigned so) { asm volatile("buffer_load_dwordx4 %0, %1, %2, %3 offen sc1" : "=v"(d) : "v"(voff), "s"(srd), "s"(so) : "memory"); }
template <unsigned LA> DI void touch(unsigned voff, rsrc_t srd, unsigned so) {
  asm volatile("s_mov_b32 m0, %3\n\ts_nop 0\n\tbuffer_load_dword %0, %1, %2 offen sc1 lds" ::"v"(voff), "s"(srd), "s"(so), "i"(LA) : "memory");
}
DI void st16(const u32x4& v, rsrc_t srd, unsigned voff, unsigned so) {
  __builtin_amdgcn_raw_buffer_store_b128(v, srd, voff, so, 16);
  asm volatile("s_nop 1" ::"v"(v) : "memory");
}
DI void keep(const u32x4& v) { asm volatile("" ::"v"(v)); }
template <bool AG, bool ZERO> DI void mf(f32x4& c, const bf16x8& a, const bf16x8& b) {
  if constexpr (ZERO) {
    if constexpr (AG) asm volatile("v_mfma_f32_16x16x32_bf16 %0, %1, %2, 0" : "=&v"(c) : "v"(a), "a"(b));
    else asm volatile("v_mfma_f32_16x16x32_bf16 %0, %1, %2, 0" : "=&v"(c) : "v"(a), "v"(b));
  } else {
    if constexpr (AG) asm volatile("v_mfma_f32_16x16x32_bf16 %0, %1, %2, %0" : "+v"(c) : "v"(a), "a"(b));
    else asm volatile("v_mfma_f32_16x16x32_bf16 %0, %1, %2, %0" : "+v"(c) : "v"(a), "v"(b));
  }
}
template <bool AG, bool ZERO> DI void mfs(f32x4& c, const bf16x8& a, const bf16x8& b) {
  if constexpr (ZERO) {
    if constexpr (AG) asm volatile("v_mfma_f32_16x16x32_bf16 %0, %2, %1, 0" : "=&v"(c) : "v"(a), "a"(b));
    else asm volatile("v_mfma_f32_16x16x32_bf16 %0, %2, %1, 0" : "=&v"(c) : "v"(a), "v"(b));
  } else {
    if constexpr (AG) asm volatile("v_mfma_f32_16x16x32_bf16 %0, %2, %1, %0" : "+v"(c) : "v"(a), "a"(b));
    else asm volatile("v_mfma_f32_16x16x32_bf16 %0, %2, %1, %0" : "+v"(c) : "v"(a), "v"(b));
  }
}
DI f32x4 mfc(const u32x4& a, const u32x4& b, const f32x4& c) { f32x4 d; asm volatile("v_mfma_f32_16x16x32_bf16 %0, %1, %2, %3" : "=&v"(d) : "v"(a), "v"(b), "v"(c)); return d; }
DI f32x4 mfz(const u32x4& a, const u32x4& b) { f32x4 d; asm volatile("v_mfma_f32_16x16x32_bf16 %0, %1, %2, 0" : "=&v"(d) : "v"(a), "v"(b)); return d; }
DI float max2(float a, float b) { float r; asm("v_max_f32 %0, %1, %2" : "=v"(r) : "v"(a), "v"(b)); return r; }
DI float max3(float a, float b, float c) { float r; asm("v_max3_f32 %0, %1, %2, %3" : "=v"(r) : "v"(a), "v"(b), "v"(c)); return r; }
DI s16x4 trd(unsigned a) { return __builtin_amdgcn_ds_read_tr16_b64_v4i16((LAS s16x4*)a); }
DI bf16x8 cat(s16x4 lo, s16x4 hi) { return __builtin_bit_cast(bf16x8, __builtin_shufflevector(lo, hi, 0, 1, 2, 3, 4, 5, 6, 7)); }
DI unsigned cvtpk(float lo, float hi) { f32x2 v = {lo, hi}; return __builtin_bit_cast(unsigned, __builtin_convertvector(v, bf16x2)); }
DI float w_mul(float a) { float r; asm volatile("v_mul_f32 %0, 0xbfb8aa3b, %1" : "=v"(r) : "v"(a)); return r; }
DI float w_exp(float a) { float r; asm volatile("v_exp_f32 %0, %1" : "=v"(r) : "v"(a)); return r; }
DI unsigned w_add1(float a) { unsigned r; asm volatile("v_fma_mixlo_f16 %0, %1, 1.0, 1.0" : "=v"(r) : "v"(a)); return r; }
DI unsigned w_rcplo(unsigned a) { unsigned r; asm volatile("v_rcp_f16 %0, %1" : "=v"(r) : "v"(a)); return r; }
DI void w_rcphi(unsigned& d, unsigned a) { asm volatile("v_rcp_f16_sdwa %0, %1 dst_sel:WORD_1 dst_unused:UNUSED_PRESERVE src0_sel:WORD_0" : "+v"(d) : "v"(a)); }
DI float w_mixlo(float u, unsigned s) { float r; asm volatile("v_fma_mix_f32 %0, %1, %2, 0 op_sel_hi:[0,1,0]" : "=v"(r) : "v"(u), "v"(s)); return r; }
DI float w_mixhi(float u, unsigned s) { float r; asm volatile("v_fma_mix_f32 %0, %1, %2, 0 op_sel:[0,1,0] op_sel_hi:[0,1,0]" : "=v"(r) : "v"(u), "v"(s)); return r; }
DI unsigned w_cvt(float lo, float hi) { unsigned r; asm volatile("v_cvt_pk_bf16_f32 %0, %1, %2" : "=v"(r) : "v"(lo), "v"(hi)); return r; }
DI float w_scale(float a, float s) { float r; asm volatile("v_mul_f32 %0, %1, %2" : "=v"(r) : "v"(a), "v"(s)); return r; }
DI float sigm(float x) { return __builtin_amdgcn_rcpf(1.0f + __builtin_amdgcn_exp2f(-1.4426950408889634f * x)); }
DI float bflo(unsigned v) { return __uint_as_float(v << 16); }
DI float bfhi(unsigned v) { return __uint_as_float(v & 0xffff0000u); }
template <class T> DI T ldsr(unsigned a) { return *(const LAS T*)a; }
template <class T> DI void ldsw(unsigned a, T v) { *(LAS T*)a = v; }
DI unsigned lane_id() { unsigned l; asm volatile("v_mbcnt_lo_u32_b32 %0, -1, 0\n\tv_mbcnt_hi_u32_b32 %0, -1, %0" : "=v"(l)); return l; }

#define STAMP(i)

struct Ctx {
  rsrc_t rx, rqkv, rwo, rwg, rwu, rwd, rgw, rws, rout;
  unsigned w, a, T, m;
  unsigned kg[9];
  u64 E, ws;
};

enum { O_NONE, O_W, O_X, O_HX, O_FIN, O_POLL, O_D2, O_B2, O_GATE, O_ST, O_TW, O_TL };
constexpr int NEVER = 1 << 20;
struct Op { int kind, a, b, need; };
constexpr int PD = 3;
#define MLPAD 3
constexpr int MLP_AD = MLPAD, ATT_AD = 3;
constexpr Op NOP_ = {O_NONE, 0, 0, 0};
constexpr Op p2_op(int N, int R, int NT, int AD, int FB) {
  const int ND = NT == 8 ? 4 : 6, RG = (N + PD) / NT, pos = (N + PD) % NT, U = RG + AD;
  if (RG > 20 || pos == 0) return NOP_;
  int k = -1;
  if (RG == 0) k = 2 * (pos - PD) + (R - 2);
  else if (NT == 8) k = R == 2 ? (pos == 1 ? 0 : pos) : pos == 1 ? 1 : pos == 7 ? 8 : -1;
  else k = R == 2 ? pos - 1 : -1;
  if (k < 0) return NOP_;
  if (U <= 20 && k < ND) return {O_D2, U, k, FB + U * NT - PD};
  if (U <= 20 && k < ND + 4) return {O_B2, U, k - ND, FB + U * NT - PD};
  if (U == 18 && k == ND + 4) return {O_GATE, 0, 0, FB + 17 * NT - PD};
  if ((U + 2 == 6 || U + 2 == 12 || U + 2 == 18) && k == ND + 4) return {O_POLL, (U + 2) / 6, 0, FB + (RG + 2) * NT - PD};
  return NOP_;
}
constexpr int FIN0 = 36, FLG0 = 16, FLG1 = 24;
constexpr int bar1(int n) { return n == 0 ? -1 : 16 * n - PD; }
constexpr Op opW(int n, int i) { return {O_W, n, i, bar1(n)}; }
constexpr Op opX(int c, int i) { return {O_X, c, i, c == 0 ? -1 : 8 * c - 5}; }
constexpr Op mlp_op(int F, int R) {
  if (F < 0) {
    const int i = F + 23;
    if (R != 2 || i < 0 || i >= 21) return NOP_;
    return i < 8 ? opX(0, i) : i < 12 ? opW(0, i - 8) : i < 20 ? opX(1, i - 12) : Op{O_TL, 0, 0, bar1(1)};
  }
  if (F >= 440) return p2_op(F - 440, R, 8, MLP_AD, 440);
  if (F >= 432) return R == 2 ? Op{O_ST, 0, 0, 440 + 2 * 8 - PD} : NOP_;
  const int RG = (F + PD) / 16, pos = (F + PD) % 16, r = R - 2;
  if (pos == 0) return NOP_;
  if (F < 61) {
    constexpr int tab[13][2] = {{0, 2}, {4, -1}, {6, 3}, {10, -2}, {14, 4}, {18, 5}, {22, -3}, {24, 6}, {30, 7}, {34, -4}, {38, 8}, {42, -5}, {46, -6}};
    for (int i = 0; i < 13; ++i) {
      const int d = F - tab[i][0];
      if (d >= 0 && d < (tab[i][1] > 0 ? 4 : 2)) return tab[i][1] > 0 ? opX(tab[i][1], 2 * d + r) : opW(-tab[i][1], 2 * d + r);
    }
    return NOP_;
  }
  if (R == 3) return NOP_;
  if (F >= 144 && F % 144 > FIN0 && F % 144 <= FIN0 + 32 && F % 4 == 0) return {O_FIN, F / 144 - 1, (F % 144 - FIN0) / 4 - 1, bar1(F < 288 ? FLG0 : FLG1)};
  if (RG == 4) return pos <= 8 ? opW(7 + (pos - 1) / 4, (pos - 1) % 4) : NOP_;
  if (RG >= 5 && RG <= 8) return pos % 4 == 1 ? opW(RG + 4, pos / 4) : NOP_;
  if (RG == 9 || RG == 10 || RG == 18 || RG == 19) {
    const int n = (RG < 18 ? 13 + 3 * (RG - 9) : 22 + 3 * (RG - 18)) + (pos - 3) / 4;
    return pos >= 3 && pos < 15 && n <= 26 ? opW(n, (pos - 3) % 4) : NOP_;
  }
  const int k4 = pos == 1 ? 0 : pos == 2 ? 1 : pos == 4 ? 2 : pos == 5 ? 3 : -1;
  if (RG >= 11 && RG <= 13 && k4 >= 0) return opW(RG + 8, k4);
  if (RG >= 20 && RG <= 22 && k4 >= 0) return {O_D2, RG - 20, k4, RG == 20 ? bar1(27) : 440 + (RG - 20) * 8 - PD};
  if (RG == 14 && (pos == 2 || pos == 9)) return {O_TW, pos == 9, 0, NEVER};
  if (RG == 23 && pos == 1) return {O_POLL, 0, 0, bar1(25)};
  if (RG >= 25 && RG <= 26) {
    const int k = (RG - 25) * 15 + pos - 1;
    if (k < 24 && k % 2 == 0) { const int U = k / 8; return {O_B2, U, (k / 2) % 4, U == 0 ? bar1(27) : 440 + U * 8 - PD}; }
    if (MLP_AD > 3 && k < 16 && k % 2 == 1) { const int i = k / 2; return i < 4 ? Op{O_D2, 3, i, 440 + 24 - PD} : NOP_; }
    if (MLP_AD > 3 && k >= 24 && k < 28) return {O_B2, 3, k - 24, 440 + 24 - PD};
  }
  return NOP_;
}
static_assert(MLP_AD == 3 || MLP_AD == 4, "pre-issue table");
constexpr int barA(int n) { return n == 0 ? -1 : n <= 9 ? 16 * n - PD : 144 + 14 * (n - 9) - PD; }
constexpr Op att1_op(int F, int R) {
  if (F < 0) {
    const int i = F + 24;
    if (R != 2 || i < 0 || i >= 22) return NOP_;
    if (i < 8) return {O_X, 0, i, -1};
    if (i < 12) return {O_W, 0, i - 8, -1};
    if (i == 12) return {O_HX, 0, 0, -1};
    return i < 21 ? Op{O_X, 1, i - 13, 6} : Op{O_TL, 0, 0, NEVER};
  }
  const int FP = F + PD, RG = FP < 144 ? FP / 16 : 9 + (FP - 144) / 14, pos = FP < 144 ? FP % 16 : (FP - 144) % 14, r = R - 2;
  if (pos == 0) return NOP_;
  if (F < 2) return {O_W, 1, 2 * F + r, barA(1)};
  if (F < 6) return {O_X, 2, 2 * (F - 2) + r, 16 * 2 - 10};
  if (F < 8) return {O_W, 2, 2 * (F - 6) + r, barA(2)};
  if (F == 8) return {O_HX, 1 + r, 0, barA(2 + 2 * r)};
  if (F < 13) return {O_X, 3, 2 * (F - 9) + r, 16 * 3 - 10};
  if (RG == 1 && pos <= 2) return {O_W, 3, 2 * (pos - 1) + r, barA(3)};
  if (RG == 1 && pos == 3) return {O_HX, 3 + r, 0, barA(5)};
  if (R == 3) return NOP_;
  if (RG == 1 && pos >= 4 && pos < 8) return {O_W, 4, pos - 4, barA(4)};
  if (F < 144 && F >= 25) {
    const int G = F - 25, c = G / 16 + 4, o = G % 16, i = o < 4 ? o : o >= 5 && o < 9 ? o - 1 : -1;
    if (c <= 8 && i >= 0) return {O_X, c, i, 16 * c - 10};
  }
  if (RG >= 2 && RG + 3 <= 17) { const int p0 = RG <= 7 ? 5 : 1; if (pos >= p0 && pos < p0 + 4) return {O_W, RG + 3, pos - p0, barA(RG + 3)}; }
  return NOP_;
}
constexpr Op att2_op(int F, int R) {
  if (F < 0) {
    const int i = F + 50;
    if (R != 2 || i < 0 || i >= 42) return NOP_;
    constexpr int n1 = 12 - PD, n2 = 24 - PD;
    if (i < 6) return {O_D2, 0, i, -1};
    if (i < 10) return {O_B2, 0, i - 6, -1};
    if (i < 16) return {O_D2, 1, i - 10, n1};
    if (i < 19) return {O_ST, 0, 0, n2};
    if (i < 23) return {O_B2, 1, i - 19, n1};
    if (i < 29) return {O_D2, 2, i - 23, n2};
    if (i < 32) return {O_ST, 0, 0, n2};
    if (i < 36) return {O_B2, 2, i - 32, n2};
    return {O_ST, 0, 0, n2};
  }
  return p2_op(F, R, 12, ATT_AD, 0);
}
template <int WHICH> constexpr Op op_at(int F, int R) { return WHICH == 0 ? mlp_op(F, R) : WHICH == 1 ? att1_op(F, R) : att2_op(F, R); }
template <int WHICH> constexpr int vm_at(int tau) {
  int s = 0;
  for (int F = tau - 1; F >= -64; --F)
    for (int R = 3; R >= 2; --R) {
      const Op o = op_at<WHICH>(F, R);
      if (o.kind == O_NONE) continue;
      if (o.need <= tau || s >= 63) return s;
      ++s;
    }
  return s;
}
template <int WHICH, int TAU> constexpr int VM = vm_at<WHICH>(TAU);

DI void load_epoch(Ctx& c) {
  asm volatile("s_load_dwordx2 %0, %1, %2" : "=s"(c.E) : "s"(c.ws), "s"(WS_E + c.T * 128) : "memory");
}
DI void flag_store(const Ctx& c, unsigned off, unsigned val) {
  const u64 v = c.E + val;
  __builtin_amdgcn_raw_buffer_store_b64(u32x2{(unsigned)v, (unsigned)(v >> 32)}, c.rws, 0, off, 16);
}
DI void poll_issue(const Ctx& c, u32x2& f, unsigned voff, unsigned off) {
  asm volatile("buffer_load_dwordx2 %0, %1, %2, %3 offen sc1" : "=v"(f) : "v"(voff), "s"(c.rws), "s"(off) : "memory");
}
DI bool flag_ok(const Ctx& c, u32x2 f, unsigned need) {
  const u64 d = (((u64)f[1] << 32) | f[0]) - c.E - need;
  return __builtin_amdgcn_ballot_w64(d >= 8) == 0;
}
DI void poll_check(const Ctx& c, u32x2& f, unsigned voff, unsigned off, unsigned need) {
  asm volatile("" : "+v"(f));
  if (__builtin_expect(!flag_ok(c, f, need), 0)) {
    for (unsigned spin = 0; spin < (1u << 16); ++spin) {
      __builtin_amdgcn_s_sleep(1);
      asm volatile("buffer_load_dwordx2 %0, %1, %2, %3 offen sc1\n\ts_waitcnt vmcnt(0)" : "=&v"(f) : "v"(voff), "s"(c.rws), "s"(off) : "memory");
      if (flag_ok(c, f, need)) break;
    }
  }
}

template <int NT> struct P2 {
  static constexpr int AD = NT == 8 ? MLP_AD : ATT_AD, NS = NT == 8 ? 8 : 4, FB = NT == 8 ? 440 : 0, ROT = NT == 8 ? 5 : 0;
  static constexpr unsigned SS = NT == 8 ? S2 : S3;
  static constexpr unsigned slot(int u) { return ((u + ROT) % NS) * SS; }
  static constexpr int WN = NT == 8 ? 3 : 2;
  static constexpr unsigned win(int u) { return (((u + ROT) % NS) / WN * WN) * SS; }
  unsigned v128[2], v64, vh[2], vf;
  unsigned lw, lw64, sd, so, sh[2];
  unsigned f1, cur, curh;
  bf16x8 bf[21][4];
  u32x2 pf;
  f32x4 omg;
  DI void init(const Ctx& c) {
    const unsigned l = lane_id(), colb = NT == 8 ? 256 * (c.m >> 1) : 768, kh = c.w >> 1;
    const unsigned lo = 16 * ((l & 15) ^ ((l >> 3) & 2) ^ (4 * c.a));
    v128[0] = (l >> 4) * 4608 + lo; v128[1] = (l >> 4) * 4608 + (lo ^ 64);
    v64 = (l >> 3) * 2304 + 16 * ((l & 7) ^ (((l >> 1) & 4) | ((l >> 3) & 2)) ^ (2 * c.a)) + 256;
    vh[0] = l * 16 + kh * 2048; vh[1] = l * 16 + (1 - kh) * 2048;
    vf = 8 * (l & 7);
    lw = c.w * 4608; lw64 = c.w * 2304 + S2;
    sd = (1152 * c.a + 16 * c.w) * 1152 + colb;
    so = (144 * c.a + 72 * kh + 16 * (c.w & 1)) * 1152 + colb;
    sh[0] = WS_H + (((4 * c.T + 2 * (c.m & 1) + (c.w & 1)) * 4 + c.a) * 42 + kh) * 4096; sh[1] = sh[0] + kh * 8192;
    f1 = WS_F1 + (c.T * 4 + c.a) * 128;
  }
  template <int S> DI unsigned pvo() const { return S < 3 ? (vf < 48 ? vf : 0) : 48 + (vf & 8); }
  template <int S> DI void poll(const Ctx& c) { poll_issue(c, pf, pvo<S>(), f1); }
  template <int S> DI void check(const Ctx& c) { poll_check(c, pf, pvo<S>(), f1, S < 3 ? S + 1 : 3); }
  template <int KIND, int U, int I> DI void prep(const Ctx& c) {
    if constexpr (KIND == O_D2) {
      constexpr unsigned row = U < 18 ? 192 * (U % 6) + 64 * (U / 6) : 32 * (U - 18);
      if constexpr (I == 0) cur = dprep<slot(U), row * 1152>(lw, U < 18 ? sd : so);
      if constexpr (I == 4) dprep_l<slot(U)>(lw64);
    }
    if constexpr (KIND == O_B2 && I == 0) { curh = sh[U >= 18] + (U < 18 ? 2 * U : 18 + U) * 4096; asm volatile("" : "+s"(curh)); }
  }
  template <int KIND, int U, int I> DI void fire(const Ctx& c) {
    if constexpr (KIND == O_D2) dfire<(I & 3) * 1152>(I < 4 ? v128[I >> 1] : v64, U < 18 ? c.rwd : c.rwo, cur);
    if constexpr (KIND == O_B2) ldh<(I & 1) * 1024>(bf[U][I], vh[I >> 1], c.rws, curh);
    if constexpr (KIND == O_POLL) poll<U>(c);
    if constexpr (KIND == O_GATE) ldg(omg, vh[0] & 255, c.rws, WS_G + (c.a * 32 + 4 * c.T + 2 * (c.m & 1) + (c.w & 1)) * 256);
  }
  static DI unsigned rbase(const Ctx& c, unsigned l, int t) {
    const unsigned g = l >> 4, q = (l >> 2) & 3;
    return (c.w >> 1) * 9216 + q * 1152 + g * 256 + 32 * (t ^ ((q & 2) | (g & 1))) + 8 * (l & 3);
  }
};

template <int NT> DI void phase2(Ctx& c, P2<NT>& p, bf16x8 (&f)[16]) {
  using P = P2<NT>;
  constexpr int AD = P::AD, WH = NT == 8 ? 0 : 2;
  const unsigned l = lane_id(), g = l >> 4, q = (l >> 2) & 3, kh = c.w >> 1;
  unsigned rb[4], rb64[4];
#pragma unroll
  for (int t = 0; t < 4; ++t) {
    rb[t] = P::win(0) + P::rbase(c, l, t); PIN(rb[t]);
    if (NT != 8) { rb64[t] = P::win(0) + S2 + kh * 4608 + (q & 1) * 1152 + (2 * g + (q >> 1)) * 128 + 32 * (t ^ ((q & 2) | (g & 1))) + 8 * (l & 3); PIN(rb64[t]); }
  }
  f32x4 acc[NT][4];
  s16x4 flo;
  auto rdlo = [&](auto U_, auto T_) { constexpr int t = CI(T_); constexpr unsigned o = P::slot(CI(U_)) - P::win(CI(U_)); return t < 8 ? trd(rb[t & 3] + o + 128 * (t >> 2)) : trd(rb64[t & 3] + o); };
  auto rdhi = [&](auto U_, auto T_) { constexpr int t = CI(T_); constexpr unsigned o = P::slot(CI(U_)) - P::win(CI(U_)); return t < 8 ? trd(rb[t & 3] + o + 128 * (t >> 2) + 4608) : trd(rb64[t & 3] + o + 2304); };
  if constexpr (NT != 8) {
    wait_bar<VM<2, -1>>();
    STAMP(24);
    tieA(p.bf[0][0], p.bf[0][1], p.bf[0][2], p.bf[0][3]);
    static_for<PD>([&](auto T_) { f[CI(T_)] = cat(rdlo(IC(0), T_), rdhi(IC(0), T_)); });
  }
  static_for<21>([&](auto U_) {
    static_for<NT>([&](auto T_) {
      constexpr int U = CI(U_), t = CI(T_), N = U * NT + t, UN = (N + PD) / NT, tn = (N + PD) % NT;
      constexpr int RR = tn == 0 ? 2 : 0;
      const bf16x8 a = f[t];
      static_for<4>([&](auto R_) {
        constexpr int R = CI(R_);
        mf<true, U == 0>(acc[t][R], a, p.bf[U][R]);
        SBAR();
        if constexpr (tn == 0 && UN <= 20) {
          if constexpr (R < 2 && P::win(UN) != P::win(UN - 1)) {
#pragma unroll
            for (int i = 2 * R; i < 2 * R + 2; ++i) { rb[i] += P::win(UN) - P::win(UN - 1); PIN(rb[i]); if (NT != 8) { rb64[i] += P::win(UN) - P::win(UN - 1); PIN(rb64[i]); } }
          }
          if constexpr (R == 2) {
            wait_bar<VM<WH, P::FB + N>>();
            STAMP(UN + (NT == 8 ? 28 : 24));
            tieA(p.bf[UN][0], p.bf[UN][1], p.bf[UN][2], p.bf[UN][3]);
          }
        }
        if constexpr (R == RR && UN <= 20) flo = rdlo(IC(UN), IC(tn));
        if constexpr (R == RR + 1 && UN <= 20) f[tn] = cat(flo, rdhi(IC(UN), IC(tn)));
        SBAR();
        if constexpr (tn == 0 && R == 3 && UN <= 20) {
          if constexpr (UN == 2) { if (threadIdx.x == 0) flag_store(c, p.f1 + 8 * c.m, 3); }
          if constexpr ((UN + AD) % 6 == 0 && UN + AD <= 18) p.template check<(UN + AD) / 6>(c);
          if constexpr (UN == 17) { PIN(p.omg); p.omg = 1.0f - (kh ? f32x4{p.omg[2], p.omg[3], p.omg[0], p.omg[1]} : p.omg); PIN(p.omg); }
        }
        if constexpr (R >= 2) {
          constexpr Op o = op_at<WH>(P::FB + N, R), n = op_at<WH>(P::FB + N + 1, 2);
          if constexpr (R == 3 || N == 0) p.template prep<o.kind, o.a, o.b>(c);
          p.template fire<o.kind, o.a, o.b>(c);
          if constexpr (R == 3) p.template prep<n.kind, n.a, n.b>(c);
        }
        constexpr int ts = N - 2 - 17 * NT;
        if constexpr (ts >= 0 && ts < NT) { acc[ts][R] *= p.omg[R]; PIN(acc[ts][R]); }
        SBAR();
      });
    });
  });
  asm volatile("s_nop 7\n\ts_waitcnt lgkmcnt(0)\n\ts_barrier" ::: "memory");
  STAMP(NT == 8 ? 49 : 45);
  const unsigned l2 = lane_id();
  f32x4 val[NT][2];
  {
    const unsigned wa = c.w * (NT * 2048) + l2 * 16, ra = (c.w ^ 2) * (NT * 2048) + l2 * 16;
#pragma unroll
    for (int t = 0; t < NT; ++t) { ldsw<f32x4>(wa + t * 2048, acc[t][2]); ldsw<f32x4>(wa + t * 2048 + 1024, acc[t][3]); val[t][0] = acc[t][0]; val[t][1] = acc[t][1]; }
    asm volatile("s_waitcnt lgkmcnt(0)\n\ts_barrier" ::: "memory");
#pragma unroll
    for (int t = 0; t < NT; ++t) { val[t][0] += ldsr<f32x4>(ra + t * 2048); val[t][1] += ldsr<f32x4>(ra + t * 2048 + 1024); }
  }
  const unsigned pb = WS_PART + ((c.T * 8 + c.m) * 64 + c.w) * 3072;
#pragma unroll
  for (int t = 0; t < NT; ++t) {
    const int dq = t < 8 ? t >> 1 : t - 8, idx = t < 8 ? t & 1 : 2;
    if (dq != 0) {
      const u32x4 v = {cvtpk(val[t][0][0], val[t][0][1]), cvtpk(val[t][0][2], val[t][0][3]), cvtpk(val[t][1][0], val[t][1][1]), cvtpk(val[t][1][2], val[t][1][3])};
      st16(v, c.rws, l2 * 16, pb + ((dq ^ c.a) * 4 + c.a) * 12288 + idx * 1024);
    }
  }
  asm volatile("s_waitcnt vmcnt(0)" ::: "memory");
  const unsigned f2 = WS_F2 + ((c.T * 8 + c.m) * 4 + c.w) * 128;
  if (l2 == 0) flag_store(c, f2 + 8 * c.a, 1);
  STAMP(NT == 8 ? 50 : 46);
  u32x2 pf = {0u, 0u};
  asm volatile("buffer_load_dwordx2 %0, %1, %2, %3 offen sc1\n\ts_waitcnt vmcnt(0)" : "=&v"(pf) : "v"(8 * (l2 & 3)), "s"(c.rws), "s"(f2) : "memory");
  poll_check(c, pf, 8 * (l2 & 3), f2, 1);
  STAMP(NT == 8 ? 51 : 47);
  constexpr int NI = NT / 4;
  u32x4 in[3][NI];
#pragma unroll
  for (int s = 1; s < 4; ++s)
#pragma unroll
    for (int i = 0; i < NI; ++i) in[s - 1][i] = __builtin_amdgcn_raw_buffer_load_b128(c.rws, l2 * 16, pb + (c.a * 4 + (s ^ c.a)) * 12288 + i * 1024, 16);
  const unsigned row0 = 256 * c.T + 128 * (c.m & 1) + 64 * (c.w & 1) + 32 * kh;
  const unsigned vo = (l2 & 15) * 2304 + 8 * (l2 >> 4), colb = NT == 8 ? 256 * (c.m >> 1) : 768;
#pragma unroll
  for (int i = 0; i < NI; ++i) {
    f32x4 s0 = val[i < 2 ? i : 8][0], s1 = val[i < 2 ? i : 8][1];
#pragma unroll
    for (int s = 0; s < 3; ++s) {
      s0 += f32x4{bflo(in[s][i][0]), bfhi(in[s][i][0]), bflo(in[s][i][1]), bfhi(in[s][i][1])};
      s1 += f32x4{bflo(in[s][i][2]), bfhi(in[s][i][2]), bflo(in[s][i][3]), bfhi(in[s][i][3])};
    }
    const unsigned cb = colb + (i < 2 ? 64 * c.a + 32 * i : 256 + 32 * c.a);
    __builtin_amdgcn_raw_buffer_store_b64(u32x2{cvtpk(s0[0], s0[1]), cvtpk(s0[2], s0[3])}, c.rout, vo, row0 * 1152 + cb, 0);
    __builtin_amdgcn_raw_buffer_store_b64(u32x2{cvtpk(s1[0], s1[1]), cvtpk(s1[2], s1[3])}, c.rout, vo, (row0 + 1) * 1152 + cb, 0);
  }
  if (NT == 8 && c.a == 0 && c.m == 0 && c.w == 0 && l2 == 0) flag_store(c, WS_E + c.T * 128, 16);
  STAMP(NT == 8 ? 52 : 48);
}

struct XS {
  unsigned v[2], lw, sx, rd[2], cur, sg;
  DI void init(const Ctx& c, unsigned base) {
    const unsigned l = lane_id(), j = l & 15, g = l >> 4;
    v[0] = (l >> 3) * 2304 + 16 * ((l & 7) ^ g); v[1] = (l >> 3) * 2304 + 16 * ((l & 7) ^ (4 + g));
    lw = base + c.w * 8192; sx = (256 * c.T + 64 * c.w) * 1152;
    rd[0] = lw + j * 128 + 16 * (g ^ (j >> 1)); rd[1] = rd[0] ^ 64;
    PIN(rd[0]); PIN(rd[1]);
  }
  template <int NSL, int C, int I> DI void prep(const Ctx& c) {
    if constexpr (I == 0) sg = sx + c.kg[C] * 128;
    cur = dprep<(C % NSL) * 32768 + I * 1024, (32 * (I >> 2) + 16 * (I & 1) + ((I >> 1) & 1)) * 1152>(lw, sg);
  }
  template <int I> DI void fire(const Ctx& c) { dfire<0>(v[I & 1], c.rx, cur); }
};
template <unsigned LA> DI void touch_pages(const Ctx& c) {
  const unsigned l = lane_id();
  const unsigned o = l < 4 ? WS_H + (((4 * c.T + l) * 4 + c.a) * 42 + 2 * c.m) * 4096 : l < 8 ? WS_PART + ((c.T * 8 + c.m) * 64 + (l - 4) * 16) * 3072 : l < 9 ? WS_G : WS_F1;
  touch<LA>(c.w < 2 ? o : (256 * c.T + 64 * (l & 3)) * 1152, c.w < 2 ? c.rws : c.rout, 0);
}
template <int I> DI void touch_w2(const Ctx& c) {
  const unsigned l = lane_id(), idx = (c.T & 3) * 6 + c.m;
  if constexpr (I == 0) touch<9 * S1>(l * 128, c.rwd, (1152 * c.a + 48 * idx) * 1152 + c.w * 8192);
  else touch<9 * S1>(l * 128, c.w < 3 ? c.rwd : c.rwo, c.w < 3 ? (1152 * c.a + 48 * idx) * 1152 + 32768 + c.w * 7168 : (144 * c.a + 7 * idx) * 1152);
}

constexpr int slot1(int n) { return n < 8 ? n % 4 : (n - 4) % 9; }
constexpr int sig_idx(int F) {
  if ((F + PD) % 16 == 0) return -1;
  int e = 0;
  for (int G = F - F % 72; G < F; ++G) if ((G + PD) % 16 != 0) ++e;
  return e <= 65 ? e : -1;
}
DI void role_mlp(Ctx& c, XS& xs) {
  const unsigned l = lane_id(), g = l >> 4, q = (l & 15) >> 2, pp = l & 3, r8 = l >> 3;
  const unsigned vW = (8 * (r8 >> 2) + (r8 & 3)) * 9216 + 16 * ((l & 7) ^ (((r8 >> 1) & 3) << 1));
  const unsigned lw = c.w * 1024, sW = (16 * (c.w & 1) + 4 * (c.w >> 1)) * 9216 + (1152 * c.a + 192 * c.m) * 2;
  unsigned sWk[9];
#pragma unroll
  for (int i = 0; i < 9; ++i) sWk[i] = sW + c.kg[i] * (64 * 9216);
  const unsigned sH = WS_H + (((4 * c.T + c.w) * 4 + c.a) * 42 + 2 * c.m) * 4096;
  const unsigned f1 = WS_F1 + (c.T * 4 + c.a) * 128;
  bf16x8 xf[18][4];
  f32x4 ag[4][4], au[4][4];
  unsigned sp[4][4][2];
  P2<8> p2;
  u32x4 hv, hk;
  unsigned vl16 = l * 16;
  unsigned cur = 0;
  auto prep = [&](auto F_, auto R_) {
    constexpr Op o = mlp_op(CI(F_), CI(R_));
    if constexpr (o.kind == O_W) {
      constexpr int pn = o.a / 9, k = ((o.a % 9) * 4 + o.b) % 18;
      cur = dprep<slot1(o.a) * S1 + o.b * 4096, (k & 1) * 32 * 9216 + pn * 128>(lw, sWk[k >> 1]);
    } else if constexpr (o.kind == O_X) xs.template prep<3, o.a, o.b>(c);
    else p2.template prep<o.kind, o.a, o.b>(c);
  };
  auto fire = [&](auto F_, auto R_) {
    constexpr Op o = mlp_op(CI(F_), CI(R_));
    if constexpr (o.kind == O_W) dfire<0>(vW, (o.a % 9) * 4 + o.b < 18 ? c.rwg : c.rwu, cur);
    else if constexpr (o.kind == O_X) {
      if constexpr (o.a >= 3 && o.b == 0) asm volatile("s_waitcnt lgkmcnt(4)" ::: "memory");
      xs.template fire<o.b>(c);
    } else if constexpr (o.kind == O_FIN) {
      __builtin_amdgcn_raw_buffer_store_b128(hv, c.rws, vl16 + (o.b & 3) * 1024, sH + (12 * o.a + (o.b >> 2)) * 4096, 16);
      asm volatile("" ::: "memory");
      hk = hv;
    } else if constexpr (o.kind == O_TW) touch_w2<o.a>(c);
    else if constexpr (o.kind == O_TL) touch_pages<3 * S1>(c);
    else p2.template fire<o.kind, o.a, o.b>(c);
  };
  static_for<13>([&](auto I_) { prep(IC(CI(I_) - 15), IC(2)); fire(IC(CI(I_) - 15), IC(2)); });
  load_epoch(c);
  unsigned rdx2[2] = {xs.rd[0] + 65536, xs.rd[1] + 65536}; PIN(rdx2[0]); PIN(rdx2[1]);
  auto rdx = [&](auto C_, auto I_) {
    constexpr int C = CI(C_), s = CI(I_) >> 2, rt = CI(I_) & 3;
    xf[2 * C + s][rt] = ldsr<bf16x8>((C % 3 == 2 ? rdx2[s] : xs.rd[s] + (C % 3) * 32768) + rt * 2048);
  };
  unsigned rb[4];
#pragma unroll
  for (int t = 0; t < 4; ++t) rb[t] = (4 * g + q) * 128 + 32 * (t ^ ((2 * g + (q >> 1)) & 3)) + 8 * pp;
  float e0 = 0.f, e1 = 0.f, e3 = 0.f, h0 = 0.f, h1 = 0.f;
  unsigned e2 = 0;
  bf16x8 f[16];
  s16x4 flo;
  unsigned rb2[3];
  wait_bar<VM<0, -1>>();
  asm volatile("" : "+s"(c.E));
  STAMP(1);
  static_for<4>([&](auto I_) { rdx(IC(0), I_); });
  static_for<PD>([&](auto I_) { f[CI(I_)] = cat(trd(rb[CI(I_)]), trd(rb[CI(I_)] + 2048)); });
  static_for<4>([&](auto I_) { rdx(IC(0), IC(4 + CI(I_))); });
  SBAR();
  static_for<27>([&](auto N_) {
    static_for<16>([&](auto I_) {
      constexpr int N = CI(N_), I = CI(I_), F = N * 16 + I, FP = F % 144, ps = FP / 72, FQ = FP % 72, ks = FQ / 4, t = FQ % 4;
      constexpr int RG = (F + PD) / 16, pos = (F + PD) % 16, RR = pos == 0 ? 2 : 0;
      constexpr unsigned ro = (slot1(RG) % 4) * S1 + (pos >> 2) * 4096;
      const bf16x8 a = f[I];
      static_for<4>([&](auto R_) {
        constexpr int R = CI(R_);
        if constexpr (ps == 0) mf<(ks < 16), ks == 0>(ag[t][R], a, xf[ks][R]); else mf<(ks < 16), ks == 0>(au[t][R], a, xf[ks][R]);
        SBAR();
        if constexpr (R == 0 && F < 64 && F % 8 == 3) wait_vm<VM<0, F>>();
        if constexpr (pos == 0) {
          if constexpr (R < 2 && RG < 27 && slot1(RG) / 4 != slot1(RG - 1) / 4) {
#pragma unroll
            for (int i = 2 * R; i < 2 * R + 2; ++i) { rb[i] += (unsigned)(slot1(RG) / 4 - slot1(RG - 1) / 4) * 4 * S1; PIN(rb[i]); }
          }
          if constexpr (R == 0 && RG == 27) {
            const unsigned l4 = lane_id();
#pragma unroll
            for (int i = 0; i < 3; ++i) { rb2[i] = P2<8>::slot(0) + P2<8>::rbase(c, l4, i); PIN(rb2[i]); }
          }
          if constexpr (R == 2) {
            wait_bar<VM<0, F>>();
            STAMP(1 + RG);
            if constexpr (RG == 27) tieA(p2.bf[0][0], p2.bf[0][1], p2.bf[0][2], p2.bf[0][3]);
          }
        }
        if constexpr (R == RR) { if constexpr (RG < 27) flo = trd(rb[pos & 3] + ro); else flo = trd(rb2[pos]); }
        if constexpr (R == RR + 1) { if constexpr (RG < 27) f[pos] = cat(flo, trd(rb[pos & 3] + ro + 2048)); else f[pos] = cat(flo, trd(rb2[pos] + 4608)); }
        if constexpr (F < 64 && (F % 8 == 3 || F % 8 == 4)) rdx(IC(F / 8 + 1), IC(4 * (F % 8 - 3) + R));
        SBAR();
        if constexpr (pos == 0 && R == 3) {
          if constexpr (RG == FLG0 || RG == FLG1) { if (threadIdx.x == 0) flag_store(c, f1 + 8 * c.m, RG == FLG0 ? 1 : 2); }
          if constexpr (RG == 19) p2.init(c);
          if constexpr (RG == 25) p2.template check<0>(c);
        }
        if constexpr (R >= 2) { if constexpr (R == 3 || F == 0) prep(IC(F), R_); fire(IC(F), R_); }
        if constexpr (ps == 1 && sig_idx(F) >= 0) {
          constexpr int e = sig_idx(F), d = e - 2;
          if constexpr (R == 0 && d >= 0) e2 = w_add1(e1);
          if constexpr (R == 1) { e3 = e0; if constexpr (e < 64) e0 = w_mul(ag[e / 16][(e / 4) % 4][e % 4]); }
          if constexpr (R == 2 && e >= 1 && e <= 64) e1 = w_exp(e3);
          if constexpr (R == 3 && d >= 0) { if constexpr (d % 2 == 0) sp[d / 16][(d / 4) % 4][(d % 4) / 2] = w_rcplo(e2); else w_rcphi(sp[d / 16][(d / 4) % 4][(d % 4) / 2], e2); }
        }
        if constexpr (F >= 144 && FP >= FIN0 && FP < FIN0 + 32) {
          constexpr int J = (FP - FIN0) / 4, i = FP % 4, e = J >> 2, rt = J & 3, tt = 2 * e + (i >> 1), k = 2 * (i & 1);
          if constexpr (R == 0) h0 = w_mixlo(au[tt][rt][k], sp[tt][rt][i & 1]);
          if constexpr (R == 1) h1 = w_mixhi(au[tt][rt][k + 1], sp[tt][rt][i & 1]);
          if constexpr (R == 3) hv[i] = w_cvt(h0, h1);
        }
        if constexpr (R == 3 && mlp_op(F, 2).kind == O_FIN) keep(hk);
        if constexpr (R == 3 && F < 431) prep(IC(F + 1), IC(2));
        SBAR();
      });
    });
  });
  asm volatile("s_nop 7" ::: "memory");
  static_for<8>([&](auto J_) {
    constexpr int e = CI(J_) >> 2, rt = CI(J_) & 3;
    u32x4 v;
#pragma unroll
    for (int i = 0; i < 4; ++i) { const int tt = 2 * e + (i >> 1), k = 2 * (i & 1); v[i] = w_cvt(w_mixlo(au[tt][rt][k], sp[tt][rt][i & 1]), w_mixhi(au[tt][rt][k + 1], sp[tt][rt][i & 1])); }
    st16(v, c.rws, vl16 + rt * 1024, sH + (24 + e) * 4096);
  });
  SBAR();
  phase2<8>(c, p2, f);
}

DI u32x2 pk4(const f32x4& v) { return u32x2{cvtpk(v[0], v[1]), cvtpk(v[2], v[3])}; }
DI void sw16(float& a, float& b) { auto r = __builtin_amdgcn_permlane16_swap(__float_as_uint(a), __float_as_uint(b), false, false); a = __uint_as_float(r[0]); b = __uint_as_float(r[1]); }
DI void sw32(float& a, float& b) { auto r = __builtin_amdgcn_permlane32_swap(__float_as_uint(a), __float_as_uint(b), false, false); a = __uint_as_float(r[0]); b = __uint_as_float(r[1]); }
template <bool MX> DI float xred(float (&v)[4]) {
  sw16(v[0], v[1]); sw16(v[2], v[3]);
  float a = MX ? max2(v[0], v[1]) : v[0] + v[1], b = MX ? max2(v[2], v[3]) : v[2] + v[3];
  sw32(a, b);
  return MX ? max2(a, b) : a + b;
}
DI void xbc(float t, float (&v)[4]) {
  float a = t, b = t;
  sw32(a, b);
  v[0] = a; v[1] = a; v[2] = b; v[3] = b;
  sw16(v[0], v[1]); sw16(v[2], v[3]);
}

DI void role_att(Ctx& c, XS& xs) {
  const unsigned mp = c.m & 1;
  u32x2 vp[4][5], qp[4][5], hp[2];
  f32x4 gate;
  {
    const unsigned l = lane_id(), g = l >> 4, q = (l & 15) >> 2, pp = l & 3;
    const unsigned lc = (l & 15) ^ (g << 1), hb = 144 * (2 * c.a + mp);
    const unsigned vA = g * 3456 + hb + (lc < 10 ? 1152 + 16 * (lc < 9 ? lc : 8) : 2304 + 16 * (lc - 10));
    const unsigned vB = g * 3456 + hb + (lc < 4 ? 2304 + 16 * (lc < 3 ? 6 + lc : 8) : 16 * (lc - 4));
    const u64 maskB = __builtin_amdgcn_ballot_w64(lc < 13);
    const unsigned lw = c.w * 1152;
    unsigned sWk[9];
#pragma unroll
    for (int i = 0; i < 9; ++i) sWk[i] = (8 * c.w + 64 * c.kg[i]) * 3456;
    const int hr = 256 * (int)c.T + ((c.w & 1) ? -16 : -15) + 2 * (int)(l >> 3);
    const unsigned vHx = (unsigned)(hr < 0 ? 0 : hr) * 1152 + 16 * ((l & 7) ^ (4 * (c.w & 1) + g));
    const unsigned lHx = A_HX + c.w * 1024;
    unsigned rh[2] = {A_HX + (l & 15) * 128 + 16 * (g ^ ((l & 15) >> 1)), 0}; rh[1] = rh[0] ^ 64; PIN(rh[0]); PIN(rh[1]);
    bf16x8 xf[18][4];
      unsigned cur = 0;
    auto prep = [&](auto F_, auto R_) {
      constexpr Op o = att1_op(CI(F_), CI(R_));
      if constexpr (o.kind == O_W) cur = dprep<(o.a % 4) * S2 + o.b * 4608, (32 * (o.b >> 1) + 4 * (o.b & 1)) * 3456>(lw, sWk[o.a % 9]);
      else if constexpr (o.kind == O_X) xs.template prep<2, o.a, o.b>(c);
      else if constexpr (o.kind == O_HX) {
        if constexpr (o.a < 4) cur = dprep<o.a * 4096, 0>(lHx, (c.w < 2 ? c.kg[2 * o.a] : c.kg[2 * o.a + 1]) * 128);
        else cur = dprep<16384, 0>(lHx, c.w < 2 ? c.kg[8] * 128 : (c.w & 1) * 1024);
      }
    };
    auto fire = [&](auto F_, auto R_) {
      constexpr Op o = att1_op(CI(F_), CI(R_));
      if constexpr (o.kind == O_W) { if constexpr (o.a < 9) dfire<0>(vA, c.rqkv, cur); else dfire_m(vB, c.rqkv, cur, maskB); }
      else if constexpr (o.kind == O_X) {
        if constexpr (o.a >= 2 && o.b == 0) asm volatile("s_waitcnt lgkmcnt(4)" ::: "memory");
        xs.template fire<o.b>(c);
      } else if constexpr (o.kind == O_HX) {
        if constexpr (o.a < 4) dfire<0>(vHx, c.rx, cur); else dfire<0>(c.w < 2 ? vHx : l * 16, c.w < 2 ? c.rx : c.rgw, cur);
      } else if constexpr (o.kind == O_TL) touch_pages<A_GW + 2048>(c);
    };
    auto rdx = [&](auto C_, auto I_) {
      constexpr int C = CI(C_), s = CI(I_) >> 2, rt = CI(I_) & 3;
      xf[2 * C + s][rt] = ldsr<bf16x8>(xs.rd[s] + (C % 2) * 32768 + rt * 2048);
    };
    static_for<14>([&](auto I_) { prep(IC(CI(I_) - 16), IC(2)); fire(IC(CI(I_) - 16), IC(2)); });
    load_epoch(c);
    unsigned rb[4], rx1, rx2;
#pragma unroll
    for (int t = 0; t < 4; ++t) rb[t] = g * 1152 + q * 256 + 32 * (t ^ q) + 8 * pp;
    rx1 = g * 1152 + q * 256 + 32 * (c.w ^ q) + 8 * pp;
    rx2 = g * 1152 + q * 256 + 128 + 32 * ((c.w < 2 ? c.w + 1 : 3) ^ q) + 8 * pp;
    const unsigned gsrc = A_GW + 2 * (32 * (l >> 5) + 8 * ((l >> 2) & 3) + 4 * ((l >> 4) & 1) + (l & 3));
    const unsigned gdst = (l >> 2) * 1152 + (l & 3) * 256 + 16 * (13 ^ ((l & 3) << 1));
    u16 gwv = 0;
    f32x4 acc[8][4], ah[3];
    bf16x8 f[16], hx, ha, hc;
    s16x4 flo, hlo;
    wait_bar<VM<1, -1>>();
    asm volatile("" : "+s"(c.E));
    STAMP(1);
    static_for<8>([&](auto I_) { rdx(IC(0), I_); });
    static_for<PD>([&](auto I_) { f[CI(I_)] = cat(trd(rb[CI(I_)]), trd(rb[CI(I_)] + 4608)); });
    auto pass = [&](auto PS_) {
      constexpr int PS = CI(PS_), NT = PS == 0 ? 8 : 7, FC = 2 * NT;
      static_for<9>([&](auto N_) {
        static_for<FC>([&](auto I_) {
          constexpr int N = CI(N_), I = CI(I_), F = 144 * PS + N * FC + I, ks = 2 * N + I / NT, t = I % NT;
          constexpr int RG = 9 * PS + N + (I + PD) / FC, pos = (I + PD) % FC;
          constexpr int NTN = RG < 9 ? 8 : 7, tn = pos % NTN, RR = pos == 0 ? 2 : 0;
          constexpr unsigned ro = (RG % 2) * S2 + (pos / NTN) * 9216 + 128 * (tn >> 2);
          constexpr unsigned rc = ((9 * PS + N) % 2) * S2 + (I / NT) * 9216;
          constexpr bool SW = PS == 0 ? t >= 5 : t < 2;
          const bf16x8 a = f[I];
          static_for<5>([&](auto R_) {
            constexpr int R = CI(R_);
            if constexpr (R < 4) { if constexpr (SW) mfs<(ks < 16), ks == 0>(acc[t][R], a, xf[ks][R]); else mf<(ks < 16), ks == 0>(acc[t][R], a, xf[ks][R]); }
            else if constexpr (PS == 0) {
              if constexpr (t == 4) mf<false, ks == 0>(ah[1], a, hx);
              if constexpr (t == 5) mf<false, ks == 0>(ah[0], ha, hx);
              if constexpr (t == 7) mfs<false, ks == 0>(ah[2], hc, hx);
            } else if constexpr (t == 5) mfs<false, ks == 0>(ah[0], ha, hx);
            SBAR();
            if constexpr (R == 0 && PS == 0 && I == 6 && N < 8) wait_vm<VM<1, F>>();
            if constexpr (pos == 0 && RG < 18) {
              if constexpr (R < 2 && RG % 2 == 0) {
                constexpr unsigned d = RG % 4 == 0 ? 0u - 2 * S2 : 2 * S2;
#pragma unroll
                for (int i = 2 * R; i < 2 * R + 2; ++i) { rb[i] += d; PIN(rb[i]); }
                if (R == 0) { rx1 += d; PIN(rx1); } else if (RG < 9) { rx2 += d; PIN(rx2); }
              }
              if constexpr (R == 0 && RG == 9) { rx1 ^= 32 * (c.w & 2); PIN(rx1); }
              if constexpr (R == 2) {
                wait_bar<VM<1, F>>();
                STAMP(1 + RG);
              }
            }
            if constexpr (R == RR && RG < 18) flo = trd(rb[tn & 3] + ro);
            if constexpr (R == RR + 1 && RG < 18) f[pos] = cat(flo, trd(rb[tn & 3] + ro + 4608));
            if constexpr (PS == 0 && N < 8 && (I == 6 || I == 7) && R < 4) rdx(IC(N + 1), IC(4 * (I - 6) + R));
            if constexpr (t == 0 && R == 3) hx = ldsr<bf16x8>(rh[ks & 1] + (ks >> 1) * 2048);
            if constexpr (t == 1 && R == 3) hlo = trd(rx1 + rc);
            if constexpr (t == 2 && R == 3) ha = cat(hlo, trd(rx1 + rc + 4608));
            if constexpr (PS == 0 && t == 3 && R == 3) hlo = trd(rx2 + rc);
            if constexpr (PS == 0 && t == 4 && R == 3) hc = cat(hlo, trd(rx2 + rc + 4608));
            if constexpr (RG + 3 >= 9 && RG + 3 <= 17 && R == 2) {
              if constexpr (pos == 9) gwv = ldsr<u16>(gsrc + c.kg[RG + 3 - 9] * 128);
              if constexpr (pos == 12) ldsw<u16>(gdst + ((RG + 3) % 4) * S2, gwv);
            }
            SBAR();
            if constexpr (R == 2 || R == 3) { if constexpr (R == 3 || F == 0) prep(IC(F), R_); fire(IC(F), R_); }
            if constexpr (R == 4) prep(IC(F + 1), IC(2));
            SBAR();
          });
        });
      });
    };
    pass(IC(0));
    asm volatile("s_nop 7" ::: "memory");
    {
      const unsigned l3 = lane_id(), wa = A_KX + (4 * c.w + 2) * 2560 + l3 * 8, e0 = A_KX + l3 * 8, e1 = A_KX + 2560 + (l3 ^ 8) * 8;
#pragma unroll
      for (int rt = 0; rt < 4; ++rt) {
#pragma unroll
        for (int t = 0; t < 5; ++t) ldsw<u32x2>(wa + rt * 2560 + t * 512, pk4(acc[t][rt]));
#pragma unroll
        for (int t = 0; t < 3; ++t) { vp[rt][t] = pk4(acc[5 + t][rt]); PIN(vp[rt][t]); }
      }
      const u32x2 h0 = pk4(ah[0]), h1 = pk4(ah[1]);
      ldsw<u32x2>(e0 + c.w * 512, h0); ldsw<u32x2>(e1 + c.w * 512, h0);
      ldsw<u32x2>(e0 + 4 * 512, h1); ldsw<u32x2>(e1 + 4 * 512, h1);
      hp[0] = pk4(ah[2]); PIN(hp[0]);
    }
    SBAR();
    pass(IC(1));
    asm volatile("s_nop 7\n\ts_waitcnt lgkmcnt(0)\n\ts_barrier" ::: "memory");
    STAMP(19);
#pragma unroll
    for (int rt = 0; rt < 4; ++rt) {
#pragma unroll
      for (int t = 0; t < 2; ++t) vp[rt][3 + t] = pk4(acc[t][rt]);
#pragma unroll
      for (int t = 0; t < 5; ++t) qp[rt][t] = pk4(acc[2 + t][rt]);
      gate[rt] = sigm(acc[6][rt][0]);
    }
    hp[1] = pk4(ah[0]);
  }
  P2<12> p2;
  p2.init(c);
  p2.poll<0>(c);
  auto pre = [&](auto A_, auto N_) { static_for<CI(N_)>([&](auto I_) { constexpr Op o = att2_op(CI(A_) + CI(I_) - 50, 2); p2.template prep<o.kind, o.a, o.b>(c); p2.template fire<o.kind, o.a, o.b>(c); }); };
  pre(IC(0), IC(6));
  const unsigned l = lane_id(), g = (l >> 4) & 3, j = l & 15;
  {
    if (c.w < 3) {
      const unsigned wa = A_VX + (c.w + 1) * 5120 + l * 8;
#pragma unroll
      for (int pa = 0; pa < 2; ++pa)
#pragma unroll
        for (int t = 0; t < 5; ++t) ldsw<u32x2>(wa + (pa * 5 + t) * 512, vp[2 + pa][t]);
    }
    const unsigned e0 = A_VX + l * 8, ev = A_VX + 2560 + (l ^ 32) * 8;
    const unsigned tv1 = c.w < 2 ? c.w : 2, tv2 = 3 + (c.w & 1);
    ldsw<u32x2>(e0 + tv1 * 512, hp[0]); ldsw<u32x2>(ev + tv1 * 512, hp[0]);
    ldsw<u32x2>(e0 + tv2 * 512, hp[1]); ldsw<u32x2>(ev + tv2 * 512, hp[1]);
    if (mp == 0 && g == 2) st16(__builtin_bit_cast(u32x4, gate), c.rws, j * 16, WS_G + (c.a * 32 + 4 * c.T + c.w) * 256);
#pragma unroll
    for (int rt = 0; rt < 4; ++rt) gate[rt] = __shfl(gate[rt], 32 + j);
  }
  asm volatile("s_waitcnt vmcnt(6) lgkmcnt(0)\n\ts_barrier" ::: "memory");
  STAMP(20);
  p2.check<0>(c);
  pre(IC(6), IC(4));
  {
    u32x10 kt[6];
    u32x6 vt[5][2];
    const unsigned ra = A_VX + c.w * 5120 + l * 8, rk = A_KX + 4 * c.w * 2560 + l * 8;
#pragma unroll
    for (int i = 0; i < 6; ++i)
#pragma unroll
      for (int t = 0; t < 5; ++t) { const u32x2 k2 = ldsr<u32x2>(rk + i * 2560 + t * 512); kt[i][2 * t] = k2[0]; kt[i][2 * t + 1] = k2[1]; }
#pragma unroll
    for (int pa = 0; pa < 2; ++pa)
#pragma unroll
      for (int t = 0; t < 5; ++t) {
        const u32x2 v2 = ldsr<u32x2>(ra + (pa * 5 + t) * 512);
        vt[t][pa] = u32x6{v2[0], v2[1], vp[pa][t][0], vp[pa][t][1], vp[2 + pa][t][0], vp[2 + pa][t][1]};
      }
    SBAR();
#pragma unroll
    for (int i = 0; i < 6; ++i) PIN(kt[i]);
#pragma unroll
    for (int i = 0; i < 10; ++i) PIN(vt[i >> 1][i & 1]);
    f32x4 bprev, bown, bprev0;
    const bool first = c.T == 0 && c.w == 0;
#pragma unroll
    for (int r = 0; r < 4; ++r) {
      const int key = 4 * (int)g + r;
      bprev[r] = key >= (int)j + 9 ? 0.f : -1e30f;
      bown[r] = (key <= (int)j && key >= (int)j - 7) ? 0.f : -1e30f;
      bprev0[r] = first ? -1e30f : bprev[r];
    }
    const unsigned mpv[2] = {(bprev[0] == 0.f ? 0xffffu : 0u) | (bprev[1] == 0.f ? 0xffff0000u : 0u), (bprev[2] == 0.f ? 0xffffu : 0u) | (bprev[3] == 0.f ? 0xffff0000u : 0u)};
    constexpr float SC2 = 0.23570226039551584f * 1.4426950408889634f;
    const unsigned sH = WS_H + (((4 * c.T + c.w) * 4 + c.a) * 42 + 36 + 3 * mp) * 4096;
    u32x4 qf[4][4];
#pragma unroll
    for (int rt = 0; rt < 4; ++rt)
#pragma unroll
      for (int h = 0; h < 4; ++h) {
#pragma unroll
        for (int i = 0; i < 4; ++i) { const int col = 16 * (h + (i >> 1)) + 4 * (int)g + 2 * (i & 1); qf[rt][h][i] = (col >= 18 * h && col < 18 * h + 18) ? qp[rt][h + (i >> 1)][i & 1] : 0u; }
        PIN(qf[rt][h]);
      }
    PIN(bprev); PIN(bown); PIN(bprev0);
    auto scores = [&](auto RT_, f32x4 (&sp)[4], f32x4 (&so)[4]) {
      constexpr int rt = CI(RT_);
      static_for<4>([&](auto H_) {
        constexpr int h = CI(H_);
        sp[h] = mfc(__builtin_shufflevector(kt[rt], kt[rt], 2 * h, 2 * h + 1, 2 * h + 2, 2 * h + 3), qf[rt][h], rt < 2 ? bprev0 : bprev);
        so[h] = mfc(__builtin_shufflevector(kt[rt + 2], kt[rt + 2], 2 * h, 2 * h + 1, 2 * h + 2, 2 * h + 3), qf[rt][h], bown);
      });
    };
    f32x4 sp[2][4], so[2][4];
    asm volatile("s_nop 1" ::: "memory");
    scores(IC(0), sp[0], so[0]);
    static_for<4>([&](auto RT_) {
      constexpr int rt = CI(RT_), pa = rt & 1, sn = rt >> 1;
      if constexpr (rt < 3) scores(IC(rt + 1), sp[(rt + 1) & 1], so[(rt + 1) & 1]);
      SBAR();
      float s[4][4], red[4], bc[4];
#pragma unroll
      for (int h = 0; h < 4; ++h) {
#pragma unroll
        for (int r = 0; r < 4; ++r) s[h][r] = max2(sp[pa][h][r], so[pa][h][r]);
        red[h] = max2(max3(s[h][0], s[h][1], s[h][2]), s[h][3]);
      }
      xbc(-SC2 * xred<true>(red), bc);
      if constexpr (rt == 1) pre(IC(19), IC(4));
      if constexpr (rt == 2) pre(IC(32), IC(4));
#pragma unroll
      for (int h = 0; h < 4; ++h) {
#pragma unroll
        for (int r = 0; r < 4; ++r) s[h][r] = __builtin_amdgcn_exp2f(__builtin_fmaf(s[h][r], SC2, bc[h]));
        red[h] = (s[h][0] + s[h][1]) + (s[h][2] + s[h][3]);
      }
      xbc(gate[rt] * __builtin_amdgcn_rcpf(xred<false>(red)), bc);
      if constexpr (rt == 0) pre(IC(10), IC(6));
      if constexpr (rt == 1) pre(IC(23), IC(6));
      u32x4 P[4];
#pragma unroll
      for (int h = 0; h < 4; ++h) {
        const unsigned p0 = cvtpk(s[h][0] * bc[h], s[h][1] * bc[h]), p1 = cvtpk(s[h][2] * bc[h], s[h][3] * bc[h]);
        P[h] = u32x4{p0 & mpv[0], p1 & mpv[1], p0 & ~mpv[0], p1 & ~mpv[1]};
        PIN(P[h]);
      }
      f32x4 hi[4], lo[4];
      asm volatile("s_nop 1" ::: "memory");
      static_for<4>([&](auto T_) {
        constexpr int t = CI(T_);
        hi[t] = mfz(__builtin_shufflevector(vt[t][pa], vt[t][pa], 2 * sn, 2 * sn + 1, 2 * sn + 2, 2 * sn + 3), P[t]);
        lo[t] = mfz(__builtin_shufflevector(vt[t + 1][pa], vt[t + 1][pa], 2 * sn, 2 * sn + 1, 2 * sn + 2, 2 * sn + 3), P[t]);
      });
      asm volatile("s_nop 7" ::: "memory");
      SBAR();
      u32x2 o[5];
#pragma unroll
      for (int t = 0; t < 5; ++t)
#pragma unroll
        for (int i = 0; i < 2; ++i) {
          const unsigned vh = t < 4 ? cvtpk(hi[t < 4 ? t : 0][2 * i], hi[t < 4 ? t : 0][2 * i + 1]) : 0u;
          o[t][i] = (4 * (int)g + 2 * i < 2 * t) ? cvtpk(lo[t > 0 ? t - 1 : 0][2 * i], lo[t > 0 ? t - 1 : 0][2 * i + 1]) : vh;
        }
      st16(u32x4{o[0][0], o[0][1], o[1][0], o[1][1]}, c.rws, l * 16 + rt * 1024, sH);
      st16(u32x4{o[2][0], o[2][1], o[3][0], o[3][1]}, c.rws, l * 16 + rt * 1024, sH + 4096);
      st16(u32x4{o[4][0], o[4][1], 0u, 0u}, c.rws, l * 16 + rt * 1024, sH + 8192);
      SBAR();
    });
  }
  STAMP(21);
  bf16x8 f[16];
  phase2<12>(c, p2, f);
}

__global__ __launch_bounds__(256, 1) void fused_kernel(const u16* x, const u16* wqkv, const u16* wo, const u16* wg, const u16* wu, const u16* wd,
                                                        const u16* gw, char* ws, u16* out) {
  __shared__ __attribute__((aligned(16))) char smem[LDS_BYTES];
  asm volatile("" ::"v"((unsigned)(size_t)smem));
  Ctx c;
  const unsigned bid = blockIdx.x, xcd = bid & 7, idx = bid >> 3;
  c.w = __builtin_amdgcn_readfirstlane(threadIdx.x >> 6);
  {
    u64 pc; unsigned len;
    asm volatile("s_getpc_b64 %0\n.Lgm_code0:\n\ts_mov_b32 %1, .Lgm_code1-.Lgm_code0" : "=s"(pc), "=s"(len));
    const rsrc_t rc = __builtin_amdgcn_make_buffer_rsrc((void*)pc, (short)0, len, 0x00020000);
    if (idx < 16) touch<3 * S1>((threadIdx.x & 15) * 128, rc, idx * 8192 + c.w * 2048);
  }
  STAMP(0);
  asm volatile("" ::"s"(x), "s"(out));
  c.rx = __builtin_amdgcn_make_buffer_rsrc((void*)x, (short)0, SEQ * DM * 2, 0x00020000);
  c.rqkv = __builtin_amdgcn_make_buffer_rsrc((void*)wqkv, (short)0, DM * 3 * DM * 2, 0x00020000);
  c.rwo = __builtin_amdgcn_make_buffer_rsrc((void*)wo, (short)0, DM * DM * 2, 0x00020000);
  c.rwg = __builtin_amdgcn_make_buffer_rsrc((void*)wg, (short)0, DM * FF * 2, 0x00020000);
  c.rwu = __builtin_amdgcn_make_buffer_rsrc((void*)wu, (short)0, DM * FF * 2, 0x00020000);
  c.rwd = __builtin_amdgcn_make_buffer_rsrc((void*)wd, (short)0, DM * FF * 2, 0x00020000);
  c.rgw = __builtin_amdgcn_make_buffer_rsrc((void*)gw, (short)0, DM * 2, 0x00020000);
  c.rws = __builtin_amdgcn_make_buffer_rsrc((void*)ws, (short)0, WS_BYTES, 0x00020000);
  c.rout = __builtin_amdgcn_make_buffer_rsrc((void*)out, (short)0, SEQ * DM * 2, 0x00020000);
  c.ws = (u64)ws;
  c.a = xcd & 3; c.T = 4 * (xcd >> 2) + (idx >> 3);
  c.m = (idx & 3) == 3 ? 6 + ((idx >> 2) & 1) : 3 * ((idx >> 2) & 1) + (idx & 3);
  const unsigned rot = c.m + 2 * (c.T & 3);
  c.kg[0] = rot >= 9 ? rot - 9 : rot;
  XS xs; xs.init(c, c.m < 6 ? M_X : A_X);
  static_for<8>([&](auto I_) { xs.template prep<1, 0, CI(I_)>(c); xs.template fire<CI(I_)>(c); });
  SBAR();
#pragma unroll
  for (int i = 1; i < 9; ++i) c.kg[i] = c.kg[i - 1] == 8 ? 0 : c.kg[i - 1] + 1;
  if (c.m < 6) role_mlp(c, xs); else role_att(c, xs);
  asm volatile(".Lgm_code1:" ::: "memory");
}

extern "C" void kernel_launch(void* const* d_in, const int* in_sizes, int n_in, void* d_out, int out_size, void* d_ws, size_t ws_size,
                              hipStream_t stream) {
  fused_kernel<<<256, 256, 0, stream>>>((const u16*)d_in[0], (const u16*)d_in[1], (const u16*)d_in[2], (const u16*)d_in[4], (const u16*)d_in[5],
                                        (const u16*)d_in[6], (const u16*)d_in[7], (char*)d_ws, (u16*)d_out);
}
```

```cpp
#include <hip/hip_runtime.h>
#include <utility>

typedef unsigned short u16;
typedef unsigned long long u64;
typedef short s16x4 __attribute__((ext_vector_type(4)));
typedef __bf16 bf16x8 __attribute__((ext_vector_type(8)));
typedef __bf16 bf16x2 __attribute__((ext_vector_type(2)));
typedef float f32x2 __attribute__((ext_vector_type(2)));
typedef float f32x4 __attribute__((ext_vector_type(4)));
typedef unsigned u32x2 __attribute__((ext_vector_type(2)));
typedef unsigned u32x4 __attribute__((ext_vector_type(4)));
typedef unsigned u32x6 __attribute__((ext_vector_type(6)));
typedef unsigned u32x10 __attribute__((ext_vector_type(10)));
typedef __amdgpu_buffer_rsrc_t rsrc_t;
#define LAS __attribute__((address_space(3)))
#define DI __device__ __forceinline__
#define SBAR() __builtin_amdgcn_sched_barrier(0)
#define IC(x) std::integral_constant<int, (x)>{}
#define CI(x) decltype(x)::value

constexpr int SEQ = 2048, DM = 576, FF = 4608;
constexpr unsigned WS_H = 0;
constexpr unsigned WS_PART = 24u << 20;
constexpr unsigned WS_G = 40u << 20;
constexpr unsigned WS_F1 = 41u << 20;
constexpr unsigned WS_F2 = WS_F1 + 8192;
constexpr unsigned WS_E = WS_F2 + 65536;
constexpr unsigned WS_BYTES = 42u << 20;
constexpr unsigned LDS_BYTES = 163840;
constexpr unsigned S1 = 16384;
constexpr unsigned S2 = 18432;
constexpr unsigned S3 = S2 + 9216;
constexpr unsigned M_X = 65536;
constexpr unsigned A_X = 4 * S2;
constexpr unsigned A_HX = A_X + 65536;
constexpr unsigned A_GW = A_HX + 18432;
constexpr unsigned A_KX = 83968;
constexpr unsigned A_VX = A_KX + 18 * 2560;
static_assert(A_GW + 2048 <= LDS_BYTES && A_VX <= A_HX && A_VX + 20480 <= LDS_BYTES && 3 * S3 <= A_KX, "lds");

#if defined(__HIP_DEVICE_COMPILE__)
template <int... Is> using iseq = std::integer_sequence<int, Is...>;
template <class Fn, int... Is> DI void static_for_(Fn&& f, iseq<Is...>) { (f(std::integral_constant<int, Is>{}), ...); }
template <int N, class Fn> DI void static_for(Fn&& f) { static_for_(f, std::make_integer_sequence<int, N>{}); }

template <int N> DI void wait_bar() { asm volatile("s_waitcnt vmcnt(%0) lgkmcnt(0)\n\ts_barrier" ::"i"(N) : "memory"); }
template <int N> DI void wait_vm() { asm volatile("s_waitcnt vmcnt(%0)" ::"i"(N) : "memory"); }
template <class T> DI void PIN(T& x) { asm volatile("" : "+v"(x)); }
DI void tieA(bf16x8& a, bf16x8& b, bf16x8& c, bf16x8& d) { asm volatile("" : "+a"(a), "+a"(b), "+a"(c), "+a"(d)); }
template <unsigned LO, unsigned SO> DI unsigned dprep(unsigned lb, unsigned sb) {
  unsigned t;
  asm volatile("s_add_u32 m0, %1, %3\n\ts_add_u32 %0, %2, %4" : "=s"(t) : "s"(lb), "s"(sb), "i"(LO), "i"(SO) : "scc");
  return t;
}
template <unsigned LO> DI void dprep_l(unsigned lb) { asm volatile("s_add_u32 m0, %0, %1\n\ts_nop 0" ::"s"(lb), "i"(LO) : "scc"); }
template <unsigned IMM> DI void dfire(unsigned voff, rsrc_t srd, unsigned so) {
  asm volatile("buffer_load_dwordx4 %0, %1, %2 offen offset:%3 lds" ::"v"(voff), "s"(srd), "s"(so), "i"(IMM) : "memory");
}
DI void dfire_m(unsigned voff, rsrc_t srd, unsigned so, u64 mask) {
  asm volatile("s_mov_b64 exec, %3\n\tbuffer_load_dwordx4 %0, %1, %2 offen lds\n\ts_mov_b64 exec, -1" ::"v"(voff), "s"(srd), "s"(so), "s"(mask) : "memory");
}
template <unsigned IMM> DI void ldh(bf16x8& d, unsigned voff, rsrc_t srd, unsigned so) {
  asm volatile("buffer_load_dwordx4 %0, %1, %2, %3 offen offset:%4 sc1" : "=a"(d) : "v"(voff), "s"(srd), "s"(so), "i"(IMM) : "memory");
}
DI void ldg(f32x4& d, unsigned voff, rsrc_t srd, unsigned so) { asm volatile("buffer_load_dwordx4 %0, %1, %2, %3 offen sc1" : "=v"(d) : "v"(voff), "s"(srd), "s"(so) : "memory"); }
template <unsigned LA> DI void touch(unsigned voff, rsrc_t srd, unsigned so) {
  asm volatile("s_mov_b32 m0, %3\n\ts_nop 0\n\tbuffer_load_dword %0, %1, %2 offen sc1 lds" ::"v"(voff), "s"(srd), "s"(so), "i"(LA) : "memory");
}
DI void st16(const u32x4& v, rsrc_t srd, unsigned voff, unsigned so) {
  __builtin_amdgcn_raw_buffer_store_b128(v, srd, voff, so, 16);
  asm volatile("s_nop 1" ::"v"(v) : "memory");
}
DI void keep(const u32x4& v) { asm volatile("" ::"v"(v)); }
template <bool AG, bool ZERO> DI void mf(f32x4& c, const bf16x8& a, const bf16x8& b) {
  if constexpr (ZERO) {
    if constexpr (AG) asm volatile("v_mfma_f32_16x16x32_bf16 %0, %1, %2, 0" : "=&v"(c) : "v"(a), "a"(b));
    else asm volatile("v_mfma_f32_16x16x32_bf16 %0, %1, %2, 0" : "=&v"(c) : "v"(a), "v"(b));
  } else {
    if constexpr (AG) asm volatile("v_mfma_f32_16x16x32_bf16 %0, %1, %2, %0" : "+v"(c) : "v"(a), "a"(b));
    else asm volatile("v_mfma_f32_16x16x32_bf16 %0, %1, %2, %0" : "+v"(c) : "v"(a), "v"(b));
  }
}
template <bool AG, bool ZERO> DI void mfs(f32x4& c, const bf16x8& a, const bf16x8& b) {
  if constexpr (ZERO) {
    if constexpr (AG) asm volatile("v_mfma_f32_16x16x32_bf16 %0, %2, %1, 0" : "=&v"(c) : "v"(a), "a"(b));
    else asm volatile("v_mfma_f32_16x16x32_bf16 %0, %2, %1, 0" : "=&v"(c) : "v"(a), "v"(b));
  } else {
    if constexpr (AG) asm volatile("v_mfma_f32_16x16x32_bf16 %0, %2, %1, %0" : "+v"(c) : "v"(a), "a"(b));
    else asm volatile("v_mfma_f32_16x16x32_bf16 %0, %2, %1, %0" : "+v"(c) : "v"(a), "v"(b));
  }
}
DI f32x4 mfc(const u32x4& a, const u32x4& b, const f32x4& c) { f32x4 d; asm volatile("v_mfma_f32_16x16x32_bf16 %0, %1, %2, %3" : "=&v"(d) : "v"(a), "v"(b), "v"(c)); return d; }
DI f32x4 mfz(const u32x4& a, const u32x4& b) { f32x4 d; asm volatile("v_mfma_f32_16x16x32_bf16 %0, %1, %2, 0" : "=&v"(d) : "v"(a), "v"(b)); return d; }
DI float max2(float a, float b) { float r; asm("v_max_f32 %0, %1, %2" : "=v"(r) : "v"(a), "v"(b)); return r; }
DI float max3(float a, float b, float c) { float r; asm("v_max3_f32 %0, %1, %2, %3" : "=v"(r) : "v"(a), "v"(b), "v"(c)); return r; }
DI s16x4 trd(unsigned a) { return __builtin_amdgcn_ds_read_tr16_b64_v4i16((LAS s16x4*)a); }
DI bf16x8 cat(s16x4 lo, s16x4 hi) { return __builtin_bit_cast(bf16x8, __builtin_shufflevector(lo, hi, 0, 1, 2, 3, 4, 5, 6, 7)); }
DI unsigned cvtpk(float lo, float hi) { f32x2 v = {lo, hi}; return __builtin_bit_cast(unsigned, __builtin_convertvector(v, bf16x2)); }
DI float w_mul(float a) { float r; asm volatile("v_mul_f32 %0, 0xbfb8aa3b, %1" : "=v"(r) : "v"(a)); return r; }
DI float w_exp(float a) { float r; asm volatile("v_exp_f32 %0, %1" : "=v"(r) : "v"(a)); return r; }
DI unsigned w_add1(float a) { unsigned r; asm volatile("v_fma_mixlo_f16 %0, %1, 1.0, 1.0" : "=v"(r) : "v"(a)); return r; }
DI unsigned w_rcplo(unsigned a) { unsigned r; asm volatile("v_rcp_f16 %0, %1" : "=v"(r) : "v"(a)); return r; }
DI void w_rcphi(unsigned& d, unsigned a) { asm volatile("v_rcp_f16_sdwa %0, %1 dst_sel:WORD_1 dst_unused:UNUSED_PRESERVE src0_sel:WORD_0" : "+v"(d) : "v"(a)); }
DI float w_mixlo(float u, unsigned s) { float r; asm volatile("v_fma_mix_f32 %0, %1, %2, 0 op_sel_hi:[0,1,0]" : "=v"(r) : "v"(u), "v"(s)); return r; }
DI float w_mixhi(float u, unsigned s) { float r; asm volatile("v_fma_mix_f32 %0, %1, %2, 0 op_sel:[0,1,0] op_sel_hi:[0,1,0]" : "=v"(r) : "v"(u), "v"(s)); return r; }
DI unsigned w_cvt(float lo, float hi) { unsigned r; asm volatile("v_cvt_pk_bf16_f32 %0, %1, %2" : "=v"(r) : "v"(lo), "v"(hi)); return r; }
DI float w_scale(float a, float s) { float r; asm volatile("v_mul_f32 %0, %1, %2" : "=v"(r) : "v"(a), "v"(s)); return r; }
DI float sigm(float x) { return __builtin_amdgcn_rcpf(1.0f + __builtin_amdgcn_exp2f(-1.4426950408889634f * x)); }
DI float bflo(unsigned v) { return __uint_as_float(v << 16); }
DI float bfhi(unsigned v) { return __uint_as_float(v & 0xffff0000u); }
template <class T> DI T ldsr(unsigned a) { return *(const LAS T*)a; }
template <class T> DI void ldsw(unsigned a, T v) { *(LAS T*)a = v; }
DI unsigned lane_id() { unsigned l; asm volatile("v_mbcnt_lo_u32_b32 %0, -1, 0\n\tv_mbcnt_hi_u32_b32 %0, -1, %0" : "=v"(l)); return l; }

#define STAMP(i)

struct Ctx {
  rsrc_t rx, rqkv, rwo, rwg, rwu, rwd, rgw, rws, rout;
  unsigned w, a, T, m;
  unsigned kg[9];
  u64 E, ws;
};

enum { O_NONE, O_W, O_X, O_HX, O_FIN, O_POLL, O_D2, O_B2, O_GATE, O_ST, O_TW, O_TL, O_PZ };
constexpr int NEVER = 1 << 20;
struct Op { int kind, a, b, need; };
constexpr int PD = 3;
#define MLPAD 3
constexpr int MLP_AD = MLPAD, ATT_AD = 3;
constexpr Op NOP_ = {O_NONE, 0, 0, 0};
constexpr Op p2_op(int N, int R, int NT, int AD, int FB) {
  const int ND = NT == 8 ? 4 : 6, RG = (N + PD) / NT, pos = (N + PD) % NT, U = RG + AD;
  if (RG > 20 || pos == 0) return NOP_;
  int k = -1;
  if (RG == 0) k = 2 * (pos - PD) + (R - 2);
  else if (NT == 8) k = R == 2 ? (pos == 1 ? 0 : pos) : pos == 1 ? 1 : pos == 7 ? 8 : -1;
  else k = R == 2 ? pos - 1 : -1;
  if (k < 0) return NOP_;
  if (U <= 20 && k < ND) return {O_D2, U, k, FB + U * NT - PD};
  if (U <= 20 && k < ND + 4) return {O_B2, U, k - ND, FB + U * NT - PD};
  if (U == 18 && k == ND + 4) return {O_GATE, 0, 0, FB + 17 * NT - PD};
  if ((U + 2 == 6 || U + 2 == 12 || U + 2 == 18) && k == ND + 4) return {O_POLL, (U + 2) / 6, 0, FB + (RG + 2) * NT - PD};
  if (RG == 18 && k == 0) return {O_POLL, 4, 0, FB + 20 * NT - PD};
  return NOP_;
}
constexpr int FIN0 = 36, FLG0 = 16, FLG1 = 24;
#define PZR 17
constexpr int FLG2 = 4;
constexpr int lastfin(int N) { return N == 1 ? 0 : N == 3 ? 1 : N == 7 ? 2 : N == 8 ? 3 : N == 10 ? 4 : N == 11 ? 5 : N == 15 ? 6 : N == 16 ? 7 : -1; }
constexpr int bar1(int n) { return n == 0 ? -1 : 16 * n - PD; }
constexpr Op opW(int n, int i) { return {O_W, n, i, bar1(n)}; }
constexpr Op opX(int c, int i) { return {O_X, c, i, c == 0 ? -1 : 8 * c - 5}; }
constexpr Op mlp_op(int F, int R) {
  if (F < 0) {
    const int i = F + 23;
    if (R != 2 || i < 0 || i >= 21) return NOP_;
    return i < 8 ? opX(0, i) : i < 12 ? opW(0, i - 8) : i < 20 ? opX(1, i - 12) : Op{O_TL, 0, 0, bar1(1)};
  }
  if (F >= 440) {
    const int N = F - 440;
    if (N < 5) return R == 2 ? Op{N < 4 ? O_D2 : O_B2, MLP_AD, N & 3, 440 + MLP_AD * 8 - PD} : N == 0 || N == 2 || N == 4 ? Op{O_B2, MLP_AD, N / 2 + 1, 440 + MLP_AD * 8 - PD} : Op{O_FIN, 2, N / 2, 440 + FLG2 * 8 - PD};
    if (R == 3 && N < 20 && lastfin(N) >= 0) return {O_FIN, 2, lastfin(N), 440 + FLG2 * 8 - PD};
    return p2_op(N, R, 8, MLP_AD, 440);
  }
  if (F >= 432) return NOP_;
  const int RG = (F + PD) / 16, pos = (F + PD) % 16, r = R - 2;
  if (pos == 0) return NOP_;
  if (F < 61) {
    constexpr int tab[13][2] = {{0, 2}, {4, -1}, {6, 3}, {10, -2}, {14, 4}, {18, 5}, {22, -3}, {24, 6}, {30, 7}, {34, -4}, {38, 8}, {42, -5}, {46, -6}};
    for (int i = 0; i < 13; ++i) {
      const int d = F - tab[i][0];
      if (d >= 0 && d < (tab[i][1] > 0 ? 4 : 2)) return tab[i][1] > 0 ? opX(tab[i][1], 2 * d + r) : opW(-tab[i][1], 2 * d + r);
    }
    return NOP_;
  }
  if (R == 3) return NOP_;
  if (F >= 144 && F % 144 > FIN0 && F % 144 <= FIN0 + 32 && F % 4 == 0) return {O_FIN, F / 144 - 1, (F % 144 - FIN0) / 4 - 1, bar1(F < 288 ? FLG0 : FLG1)};
  if (RG == 4) return pos <= 8 ? opW(7 + (pos - 1) / 4, (pos - 1) % 4) : NOP_;
  if (RG >= 5 && RG <= 8) return pos % 4 == 1 ? opW(RG + 4, pos / 4) : NOP_;
  if (RG == 9 || RG == 10 || RG == 18 || RG == 19) {
    const int n = (RG < 18 ? 13 + 3 * (RG - 9) : 22 + 3 * (RG - 18)) + (pos - 3) / 4;
    return pos >= 3 && pos < 15 && n <= 26 ? opW(n, (pos - 3) % 4) : NOP_;
  }
  const int k4 = pos == 1 ? 0 : pos == 2 ? 1 : pos == 4 ? 2 : pos == 5 ? 3 : -1;
  if (RG >= 11 && RG <= 13 && k4 >= 0) return opW(RG + 8, k4);
  if (RG >= 20 && RG <= 22 && k4 >= 0) return {O_D2, RG - 20, k4, RG == 20 ? bar1(27) : 440 + (RG - 20) * 8 - PD};
  if (RG == 14 && (pos == 2 || pos == 9)) return {O_TW, pos == 9, 0, NEVER};
  if (RG == PZR && pos % 2 == 1 && pos < 12) return {O_PZ, pos / 2, 0, bar1(PZR + 4)};
  if (RG == 23 && pos == 1) return {O_POLL, 0, 0, bar1(25)};
  if (RG >= 25 && RG <= 26) {
    const int k = (RG - 25) * 15 + pos - 1;
    if (k < 24 && k % 2 == 0) { const int U = k / 8; return {O_B2, U, (k / 2) % 4, U == 0 ? bar1(27) : 440 + U * 8 - PD}; }
    if (MLP_AD > 3 && k < 16 && k % 2 == 1) { const int i = k / 2; return i < 4 ? Op{O_D2, 3, i, 440 + 24 - PD} : NOP_; }
    if (MLP_AD > 3 && k >= 24 && k < 28) return {O_B2, 3, k - 24, 440 + 24 - PD};
  }
  return NOP_;
}
static_assert(MLP_AD == 3 || MLP_AD == 4, "pre-issue table");
constexpr int barA(int n) { return n == 0 ? -1 : n <= 9 ? 16 * n - PD : 144 + 14 * (n - 9) - PD; }
constexpr Op att1_op(int F, int R) {
  if (F < 0) {
    const int i = F + 24;
    if (R != 2 || i < 0 || i >= 22) return NOP_;
    if (i < 8) return {O_X, 0, i, -1};
    if (i < 12) return {O_W, 0, i - 8, -1};
    if (i == 12) return {O_HX, 0, 0, -1};
    return i < 21 ? Op{O_X, 1, i - 13, 6} : Op{O_TL, 0, 0, NEVER};
  }
  const int FP = F + PD, RG = FP < 144 ? FP / 16 : 9 + (FP - 144) / 14, pos = FP < 144 ? FP % 16 : (FP - 144) % 14, r = R - 2;
  if (pos == 0) return NOP_;
  if (F < 2) return {O_W, 1, 2 * F + r, barA(1)};
  if (F < 6) return {O_X, 2, 2 * (F - 2) + r, 16 * 2 - 10};
  if (F < 8) return {O_W, 2, 2 * (F - 6) + r, barA(2)};
  if (F == 8) return {O_HX, 1 + r, 0, barA(2 + 2 * r)};
  if (F < 13) return {O_X, 3, 2 * (F - 9) + r, 16 * 3 - 10};
  if (RG == 1 && pos <= 2) return {O_W, 3, 2 * (pos - 1) + r, barA(3)};
  if (RG == 1 && pos == 3) return {O_HX, 3 + r, 0, barA(5)};
  if (R == 3) return NOP_;
  if (RG == 1 && pos >= 4 && pos < 8) return {O_W, 4, pos - 4, barA(4)};
  if (F < 144 && F >= 25) {
    const int G = F - 25, c = G / 16 + 4, o = G % 16, i = o < 4 ? o : o >= 5 && o < 9 ? o - 1 : -1;
    if (c <= 8 && i >= 0) return {O_X, c, i, 16 * c - 10};
  }
  if (RG == 15 && pos <= 9) return {O_PZ, pos - 1, 0, barA(17)};
  if (RG >= 2 && RG + 3 <= 17) { const int p0 = RG <= 7 ? 5 : 1; if (pos >= p0 && pos < p0 + 4) return {O_W, RG + 3, pos - p0, barA(RG + 3)}; }
  return NOP_;
}
constexpr Op att2_op(int F, int R) {
  if (F < 0) {
    const int i = F + 50;
    if (R != 2 || i < 0 || i >= 42) return NOP_;
    constexpr int n1 = 12 - PD, n2 = 24 - PD, ns = FLG2 * 12 - PD;
    if (i < 6) return {O_D2, 0, i, -1};
    if (i < 10) return {O_B2, 0, i - 6, -1};
    if (i < 16) return {O_D2, 1, i - 10, n1};
    if (i < 19) return {O_ST, 0, 0, ns};
    if (i < 23) return {O_B2, 1, i - 19, n1};
    if (i < 29) return {O_D2, 2, i - 23, n2};
    if (i < 32) return {O_ST, 0, 0, ns};
    if (i < 36) return {O_B2, 2, i - 32, n2};
    return {O_ST, 0, 0, ns};
  }
  return p2_op(F, R, 12, ATT_AD, 0);
}
template <int WHICH> constexpr Op op_at(int F, int R) { return WHICH == 0 ? mlp_op(F, R) : WHICH == 1 ? att1_op(F, R) : att2_op(F, R); }
template <int WHICH> constexpr int vm_at(int tau) {
  int s = 0;
  for (int F = tau - 1; F >= -64; --F)
    for (int R = 3; R >= 2; --R) {
      const Op o = op_at<WHICH>(F, R);
      if (o.kind == O_NONE) continue;
      if (o.need <= tau || s >= 63) return s;
      ++s;
    }
  return s;
}
template <int WHICH, int TAU> constexpr int VM = vm_at<WHICH>(TAU);

DI void load_epoch(Ctx& c) {
  asm volatile("s_load_dwordx2 %0, %1, %2" : "=s"(c.E) : "s"(c.ws), "s"(WS_E + c.T * 128) : "memory");
}
DI void flag_store(const Ctx& c, unsigned off, unsigned val) {
  const u64 v = c.E + val;
  __builtin_amdgcn_raw_buffer_store_b64(u32x2{(unsigned)v, (unsigned)(v >> 32)}, c.rws, 0, off, 16);
}
DI void poll_issue(const Ctx& c, u32x2& f, unsigned voff, unsigned off) {
  asm volatile("buffer_load_dwordx2 %0, %1, %2, %3 offen sc1" : "=v"(f) : "v"(voff), "s"(c.rws), "s"(off) : "memory");
}
DI bool flag_ok(const Ctx& c, u32x2 f, unsigned need) {
  const u64 d = (((u64)f[1] << 32) | f[0]) - c.E - need;
  return __builtin_amdgcn_ballot_w64(d >= 8) == 0;
}
DI void poll_check(const Ctx& c, u32x2& f, unsigned voff, unsigned off, unsigned need) {
  asm volatile("" : "+v"(f));
  if (__builtin_expect(!flag_ok(c, f, need), 0)) {
    for (unsigned spin = 0; spin < (1u << 16); ++spin) {
      __builtin_amdgcn_s_sleep(1);
      asm volatile("buffer_load_dwordx2 %0, %1, %2, %3 offen sc1\n\ts_waitcnt vmcnt(0)" : "=&v"(f) : "v"(voff), "s"(c.rws), "s"(off) : "memory");
      if (flag_ok(c, f, need)) break;
    }
  }
}

constexpr unsigned POISON = 0xffffffffu;
DI unsigned inbox(const Ctx& c) { return WS_PART + ((c.T * 8 + c.m) * 64 + c.w) * 3072; }
DI unsigned flag2(const Ctx& c) { return WS_F2 + ((c.T * 8 + c.m) * 4 + c.w) * 128; }
template <int NI, int J> DI void poison(const Ctx& c, unsigned vl16) {
  st16(u32x4{POISON, POISON, POISON, POISON}, c.rws, vl16 + (J % NI) * 1024, inbox(c) + (c.a * 4 + ((J / NI + 1) ^ c.a)) * 12288);
}
template <int NT> struct P2 {
  static constexpr int AD = NT == 8 ? MLP_AD : ATT_AD, NS = NT == 8 ? 8 : 4, FB = NT == 8 ? 440 : 0, ROT = NT == 8 ? 5 : 0;
  static constexpr unsigned SS = NT == 8 ? S2 : S3;
  static constexpr unsigned slot(int u) { return ((u + ROT) % NS) * SS; }
  static constexpr int WN = NT == 8 ? 3 : 2;
  static constexpr unsigned win(int u) { return (((u + ROT) % NS) / WN * WN) * SS; }
  unsigned v128[2], v64, vh[2], vf;
  unsigned lw, lw64, sd, so, sh[2];
  unsigned f1, cur, curh;
  bf16x8 bf[21][4];
  u32x2 pf;
  f32x4 omg;
  DI void init(const Ctx& c) {
    const unsigned l = lane_id(), colb = NT == 8 ? 256 * (c.m >> 1) : 768, kh = c.w >> 1;
    const unsigned lo = 16 * ((l & 15) ^ ((l >> 3) & 2) ^ (4 * c.a));
    v128[0] = (l >> 4) * 4608 + lo; v128[1] = (l >> 4) * 4608 + (lo ^ 64);
    v64 = (l >> 3) * 2304 + 16 * ((l & 7) ^ (((l >> 1) & 4) | ((l >> 3) & 2)) ^ (2 * c.a)) + 256;
    vh[0] = l * 16 + kh * 2048; vh[1] = l * 16 + (1 - kh) * 2048;
    vf = 8 * (l & 7);
    lw = c.w * 4608; lw64 = c.w * 2304 + S2;
    sd = (1152 * c.a + 16 * c.w) * 1152 + colb;
    so = (144 * c.a + 72 * kh + 16 * (c.w & 1)) * 1152 + colb;
    sh[0] = WS_H + (((4 * c.T + 2 * (c.m & 1) + (c.w & 1)) * 4 + c.a) * 42 + kh) * 4096; sh[1] = sh[0] + kh * 8192;
    f1 = WS_F1 + (c.T * 4 + c.a) * 128;
  }
  template <int S> DI unsigned pvo() const { return S < 3 ? (vf < 48 ? vf : 0) : S == 3 ? 48 + (vf & 8) : vf & 24; }
  template <int S> DI void poll(const Ctx& c) { poll_issue(c, pf, pvo<S>(), S < 4 ? f1 : flag2(c)); }
  template <int S> DI void check(const Ctx& c) { poll_check(c, pf, pvo<S>(), S < 4 ? f1 : flag2(c), S < 3 ? S + 1 : S == 3 ? 3 : 1); }
  template <int KIND, int U, int I> DI void prep(const Ctx& c) {
    if constexpr (KIND == O_D2) {
      constexpr unsigned row = U < 18 ? 192 * (U % 6) + 64 * (U / 6) : 32 * (U - 18);
      if constexpr (I == 0) cur = dprep<slot(U), row * 1152>(lw, U < 18 ? sd : so);
      if constexpr (I == 4) dprep_l<slot(U)>(lw64);
    }
    if constexpr (KIND == O_B2 && (I == 0 || (NT == 8 && U == AD && I == 1))) { curh = sh[U >= 18] + (U < 18 ? 2 * U : 18 + U) * 4096; asm volatile("" : "+s"(curh)); }
  }
  template <int KIND, int U, int I> DI void fire(const Ctx& c) {
    if constexpr (KIND == O_D2) dfire<(I & 3) * 1152>(I < 4 ? v128[I >> 1] : v64, U < 18 ? c.rwd : c.rwo, cur);
    if constexpr (KIND == O_B2) ldh<(I & 1) * 1024>(bf[U][I], vh[I >> 1], c.rws, curh);
    if constexpr (KIND == O_POLL) poll<U>(c);
    if constexpr (KIND == O_GATE) ldg(omg, vh[0] & 255, c.rws, WS_G + (c.a * 32 + 4 * c.T + 2 * (c.m & 1) + (c.w & 1)) * 256);
  }
  static DI unsigned rbase(const Ctx& c, unsigned l, int t) {
    const unsigned g = l >> 4, q = (l >> 2) & 3;
    return (c.w >> 1) * 9216 + q * 1152 + g * 256 + 32 * (t ^ ((q & 2) | (g & 1))) + 8 * (l & 3);
  }
};

template <int NT, class Extra> DI void phase2(Ctx& c, P2<NT>& p, bf16x8 (&f)[16], Extra&& extra) {
  using P = P2<NT>;
  constexpr int AD = P::AD, WH = NT == 8 ? 0 : 2;
  const unsigned l = lane_id(), g = l >> 4, q = (l >> 2) & 3, kh = c.w >> 1;
  unsigned rb[4], rb64[4];
#pragma unroll
  for (int t = 0; t < 4; ++t) {
    rb[t] = P::win(0) + P::rbase(c, l, t); PIN(rb[t]);
    if (NT != 8) { rb64[t] = P::win(0) + S2 + kh * 4608 + (q & 1) * 1152 + (2 * g + (q >> 1)) * 128 + 32 * (t ^ ((q & 2) | (g & 1))) + 8 * (l & 3); PIN(rb64[t]); }
  }
  f32x4 acc[NT][4];
  s16x4 flo;
  constexpr unsigned XB = NT == 8 ? 40960 : 28672;
  unsigned wa = 0;
  auto rdlo = [&](auto U_, auto T_) { constexpr int t = CI(T_); constexpr unsigned o = P::slot(CI(U_)) - P::win(CI(U_)); return t < 8 ? trd(rb[t & 3] + o + 128 * (t >> 2)) : trd(rb64[t & 3] + o); };
  auto rdhi = [&](auto U_, auto T_) { constexpr int t = CI(T_); constexpr unsigned o = P::slot(CI(U_)) - P::win(CI(U_)); return t < 8 ? trd(rb[t & 3] + o + 128 * (t >> 2) + 4608) : trd(rb64[t & 3] + o + 2304); };
  if constexpr (NT != 8) {
    wait_bar<VM<2, -1>>();
    STAMP(24);
    tieA(p.bf[0][0], p.bf[0][1], p.bf[0][2], p.bf[0][3]);
    static_for<PD>([&](auto T_) { f[CI(T_)] = cat(rdlo(IC(0), T_), rdhi(IC(0), T_)); });
  }
  static_for<21>([&](auto U_) {
    static_for<NT>([&](auto T_) {
      constexpr int U = CI(U_), t = CI(T_), N = U * NT + t, UN = (N + PD) / NT, tn = (N + PD) % NT;
      constexpr int RR = tn == 0 ? 2 : 0;
      const bf16x8 a = f[t];
      static_for<4>([&](auto R_) {
        constexpr int R = CI(R_);
        mf<true, U == 0>(acc[t][R], a, p.bf[U][R]);
        SBAR();
        if constexpr (tn == 0 && UN <= 20) {
          if constexpr (R < 2 && P::win(UN) != P::win(UN - 1)) {
#pragma unroll
            for (int i = 2 * R; i < 2 * R + 2; ++i) { rb[i] += P::win(UN) - P::win(UN - 1); PIN(rb[i]); if (NT != 8) { rb64[i] += P::win(UN) - P::win(UN - 1); PIN(rb64[i]); } }
          }
          if constexpr (R == 2) {
            wait_bar<VM<WH, P::FB + N>>();
            STAMP(UN + (NT == 8 ? 28 : 24));
            tieA(p.bf[UN][0], p.bf[UN][1], p.bf[UN][2], p.bf[UN][3]);
          }
        }
        if constexpr (R == RR && UN <= 20) flo = rdlo(IC(UN), IC(tn));
        if constexpr (R == RR + 1 && UN <= 20) f[tn] = cat(flo, rdhi(IC(UN), IC(tn)));
        SBAR();
        if constexpr (tn == 0 && R == 3 && UN <= 20) {
          if constexpr (UN == FLG2) { if (threadIdx.x == 0) flag_store(c, p.f1 + 8 * c.m, 3); }
          if constexpr (UN == 20) { p.template check<4>(c); wa = XB + c.w * (NT * 2048) + lane_id() * 16; PIN(wa); }
          if constexpr ((UN + AD) % 6 == 0 && UN + AD <= 18) p.template check<(UN + AD) / 6>(c);
          if constexpr (UN == 17) { PIN(p.omg); p.omg = 1.0f - (kh ? f32x4{p.omg[2], p.omg[3], p.omg[0], p.omg[1]} : p.omg); PIN(p.omg); }
        }
        extra(IC(N), R_);
        if constexpr (U == 20 && t >= 2 && R < 2) ldsw<f32x4>(wa + (t - 2) * 2048 + R * 1024, acc[t - 2][2 + R]);
        if constexpr (R >= 2) {
          constexpr Op o = op_at<WH>(P::FB + N, R), n = op_at<WH>(P::FB + N + 1, 2);
          if constexpr (R == 3 || N == 0) p.template prep<o.kind, o.a, o.b>(c);
          p.template fire<o.kind, o.a, o.b>(c);
          if constexpr (R == 3) p.template prep<n.kind, n.a, n.b>(c);
        }
        constexpr int ts = N - 2 - 17 * NT;
        if constexpr (ts >= 0 && ts < NT) { acc[ts][R] *= p.omg[R]; PIN(acc[ts][R]); }
        SBAR();
      });
    });
  });
  ldsw<f32x4>(wa + (NT - 2) * 2048, acc[NT - 2][2]); ldsw<f32x4>(wa + (NT - 2) * 2048 + 1024, acc[NT - 2][3]);
  asm volatile("s_nop 5" ::: "memory");
  ldsw<f32x4>(wa + (NT - 1) * 2048, acc[NT - 1][2]); ldsw<f32x4>(wa + (NT - 1) * 2048 + 1024, acc[NT - 1][3]);
  const unsigned l2 = lane_id(), ra = XB + (c.w ^ 2) * (NT * 2048) + l2 * 16, pb = inbox(c);
  asm volatile("s_waitcnt lgkmcnt(0)\n\ts_barrier" ::: "memory");
  STAMP(NT == 8 ? 49 : 45);
  f32x4 oth[NT][2];
#pragma unroll
  for (int k = 0; k < NT; ++k) { const int t = (k + 2) % NT; oth[t][0] = ldsr<f32x4>(ra + t * 2048); oth[t][1] = ldsr<f32x4>(ra + t * 2048 + 1024); }
#pragma unroll
  for (int t = 0; t < NT; ++t) {
    const int dq = t < 8 ? t >> 1 : t - 8, idx = t < 8 ? t & 1 : 2;
    if (dq != 0) {
      const f32x4 v0 = acc[t][0] + oth[t][0], v1 = acc[t][1] + oth[t][1];
      const u32x4 v = {cvtpk(v0[0], v0[1]), cvtpk(v0[2], v0[3]), cvtpk(v1[0], v1[1]), cvtpk(v1[2], v1[3])};
      st16(__builtin_elementwise_min(v, u32x4{POISON - 1, POISON - 1, POISON - 1, POISON - 1}), c.rws, l2 * 16 + idx * 1024, pb + ((dq ^ c.a) * 4 + c.a) * 12288);
    }
  }
  STAMP(NT == 8 ? 50 : 46);
  constexpr int NI = NT / 4;
  u32x4 in[3][NI];
  for (unsigned spin = 0; spin < (1u << 16); ++spin) {
    unsigned mx = 0;
#pragma unroll
    for (int s = 1; s < 4; ++s)
#pragma unroll
      for (int i = 0; i < NI; ++i) in[s - 1][i] = __builtin_amdgcn_raw_buffer_load_b128(c.rws, l2 * 16 + i * 1024, pb + (c.a * 4 + (s ^ c.a)) * 12288, 16);
    asm volatile("" ::: "memory");
#pragma unroll
    for (int s = 0; s < 3; ++s)
#pragma unroll
      for (int i = 0; i < NI; ++i) mx = max(max(mx, max(in[s][i][0], in[s][i][1])), max(in[s][i][2], in[s][i][3]));
    if (__builtin_amdgcn_ballot_w64(mx == POISON) == 0) break;
  }
  STAMP(NT == 8 ? 51 : 47);
  const unsigned row0 = 256 * c.T + 128 * (c.m & 1) + 64 * (c.w & 1) + 32 * kh;
  const unsigned vo = (l2 & 15) * 2304 + 8 * (l2 >> 4), colb = NT == 8 ? 256 * (c.m >> 1) : 768;
#pragma unroll
  for (int i = 0; i < NI; ++i) {
    const int t = i < 2 ? i : 8;
    f32x4 s0 = acc[t][0] + oth[t][0], s1 = acc[t][1] + oth[t][1];
#pragma unroll
    for (int s = 0; s < 3; ++s) {
      s0 += f32x4{bflo(in[s][i][0]), bfhi(in[s][i][0]), bflo(in[s][i][1]), bfhi(in[s][i][1])};
      s1 += f32x4{bflo(in[s][i][2]), bfhi(in[s][i][2]), bflo(in[s][i][3]), bfhi(in[s][i][3])};
    }
    const unsigned cb = colb + (i < 2 ? 64 * c.a + 32 * i : 256 + 32 * c.a);
    __builtin_amdgcn_raw_buffer_store_b64(u32x2{cvtpk(s0[0], s0[1]), cvtpk(s0[2], s0[3])}, c.rout, vo, row0 * 1152 + cb, 0);
    __builtin_amdgcn_raw_buffer_store_b64(u32x2{cvtpk(s1[0], s1[1]), cvtpk(s1[2], s1[3])}, c.rout, vo, (row0 + 1) * 1152 + cb, 0);
  }
  if (NT == 8 && c.a == 0 && c.m == 0 && c.w == 0 && l2 == 0) flag_store(c, WS_E + c.T * 128, 16);
  STAMP(NT == 8 ? 52 : 48);
}

struct XS {
  unsigned v[2], lw, sx, rd[2], cur, sg;
  DI void init(const Ctx& c, unsigned base) {
    const unsigned l = lane_id(), j = l & 15, g = l >> 4;
    v[0] = (l >> 3) * 2304 + 16 * ((l & 7) ^ g); v[1] = (l >> 3) * 2304 + 16 * ((l & 7) ^ (4 + g));
    lw = base + c.w * 8192; sx = (256 * c.T + 64 * c.w) * 1152;
    rd[0] = lw + j * 128 + 16 * (g ^ (j >> 1)); rd[1] = rd[0] ^ 64;
    PIN(rd[0]); PIN(rd[1]);
  }
  template <int NSL, int C, int I> DI void prep(const Ctx& c) {
    if constexpr (I == 0) sg = sx + c.kg[C] * 128;
    cur = dprep<(C % NSL) * 32768 + I * 1024, (32 * (I >> 2) + 16 * (I & 1) + ((I >> 1) & 1)) * 1152>(lw, sg);
  }
  template <int I> DI void fire(const Ctx& c) { dfire<0>(v[I & 1], c.rx, cur); }
};
template <unsigned LA> DI void touch_pages(const Ctx& c) {
  const unsigned l = lane_id();
  const unsigned o = l < 4 ? WS_H + (((4 * c.T + l) * 4 + c.a) * 42 + 2 * c.m) * 4096 : l < 8 ? WS_PART + ((c.T * 8 + c.m) * 64 + (l - 4) * 16) * 3072 : l < 9 ? WS_G : WS_F1;
  touch<LA>(c.w < 2 ? o : (256 * c.T + 64 * (l & 3)) * 1152, c.w < 2 ? c.rws : c.rout, 0);
}
template <int I> DI void touch_w2(const Ctx& c) {
  const unsigned l = lane_id(), idx = (c.T & 3) * 6 + c.m;
  if constexpr (I == 0) touch<9 * S1>(l * 128, c.rwd, (1152 * c.a + 48 * idx) * 1152 + c.w * 8192);
  else touch<9 * S1>(l * 128, c.w < 3 ? c.rwd : c.rwo, c.w < 3 ? (1152 * c.a + 48 * idx) * 1152 + 32768 + c.w * 7168 : (144 * c.a + 7 * idx) * 1152);
}

constexpr int slot1(int n) { return n < 8 ? n % 4 : (n - 4) % 9; }
constexpr int sig_idx(int F) {
  if ((F + PD) % 16 == 0) return -1;
  int e = 0;
  for (int G = F - F % 72; G < F; ++G) if ((G + PD) % 16 != 0) ++e;
  return e <= 65 ? e : -1;
}
DI void role_mlp(Ctx& c, XS& xs) {
  const unsigned l = lane_id(), g = l >> 4, q = (l & 15) >> 2, pp = l & 3, r8 = l >> 3;
  const unsigned vW = (8 * (r8 >> 2) + (r8 & 3)) * 9216 + 16 * ((l & 7) ^ (((r8 >> 1) & 3) << 1));
  const unsigned lw = c.w * 1024, sW = (16 * (c.w & 1) + 4 * (c.w >> 1)) * 9216 + (1152 * c.a + 192 * c.m) * 2;
  unsigned sWk[9];
#pragma unroll
  for (int i = 0; i < 9; ++i) sWk[i] = sW + c.kg[i] * (64 * 9216);
  const unsigned sH = WS_H + (((4 * c.T + c.w) * 4 + c.a) * 42 + 2 * c.m) * 4096;
  const unsigned f1 = WS_F1 + (c.T * 4 + c.a) * 128;
  bf16x8 xf[18][4];
  f32x4 ag[4][4], au[4][4];
  unsigned sp[4][4][2];
  P2<8> p2;
  u32x4 hv, hk;
  unsigned vl16 = l * 16;
  unsigned cur = 0;
  auto prep = [&](auto F_, auto R_) {
    constexpr Op o = mlp_op(CI(F_), CI(R_));
    if constexpr (o.kind == O_W) {
      constexpr int pn = o.a / 9, k = ((o.a % 9) * 4 + o.b) % 18;
      cur = dprep<slot1(o.a) * S1 + o.b * 4096, (k & 1) * 32 * 9216 + pn * 128>(lw, sWk[k >> 1]);
    } else if constexpr (o.kind == O_X) xs.template prep<3, o.a, o.b>(c);
    else p2.template prep<o.kind, o.a, o.b>(c);
  };
  auto fire = [&](auto F_, auto R_) {
    constexpr Op o = mlp_op(CI(F_), CI(R_));
    if constexpr (o.kind == O_W) dfire<0>(vW, (o.a % 9) * 4 + o.b < 18 ? c.rwg : c.rwu, cur);
    else if constexpr (o.kind == O_X) {
      if constexpr (o.a >= 3 && o.b == 0) asm volatile("s_waitcnt lgkmcnt(4)" ::: "memory");
      xs.template fire<o.b>(c);
    } else if constexpr (o.kind == O_FIN) {
      __builtin_amdgcn_raw_buffer_store_b128(hv, c.rws, vl16 + (o.b & 3) * 1024, sH + (12 * o.a + (o.b >> 2)) * 4096, 16);
      asm volatile("" ::: "memory");
      hk = hv;
    } else if constexpr (o.kind == O_PZ) poison<2, o.a>(c, vl16);
    else if constexpr (o.kind == O_TW) touch_w2<o.a>(c);
    else if constexpr (o.kind == O_TL) touch_pages<3 * S1>(c);
    else p2.template fire<o.kind, o.a, o.b>(c);
  };
  static_for<13>([&](auto I_) { prep(IC(CI(I_) - 15), IC(2)); fire(IC(CI(I_) - 15), IC(2)); });
  load_epoch(c);
  unsigned rdx2[2] = {xs.rd[0] + 65536, xs.rd[1] + 65536}; PIN(rdx2[0]); PIN(rdx2[1]);
  auto rdx = [&](auto C_, auto I_) {
    constexpr int C = CI(C_), s = CI(I_) >> 2, rt = CI(I_) & 3;
    xf[2 * C + s][rt] = ldsr<bf16x8>((C % 3 == 2 ? rdx2[s] : xs.rd[s] + (C % 3) * 32768) + rt * 2048);
  };
  unsigned rb[4];
#pragma unroll
  for (int t = 0; t < 4; ++t) rb[t] = (4 * g + q) * 128 + 32 * (t ^ ((2 * g + (q >> 1)) & 3)) + 8 * pp;
  float e0 = 0.f, e1 = 0.f, e3 = 0.f, h0 = 0.f, h1 = 0.f;
  unsigned e2 = 0;
  bf16x8 f[16];
  s16x4 flo;
  unsigned rb2[3];
  wait_bar<VM<0, -1>>();
  asm volatile("" : "+s"(c.E));
  STAMP(1);
  static_for<4>([&](auto I_) { rdx(IC(0), I_); });
  static_for<PD>([&](auto I_) { f[CI(I_)] = cat(trd(rb[CI(I_)]), trd(rb[CI(I_)] + 2048)); });
  static_for<4>([&](auto I_) { rdx(IC(0), IC(4 + CI(I_))); });
  SBAR();
  static_for<27>([&](auto N_) {
    static_for<16>([&](auto I_) {
      constexpr int N = CI(N_), I = CI(I_), F = N * 16 + I, FP = F % 144, ps = FP / 72, FQ = FP % 72, ks = FQ / 4, t = FQ % 4;
      constexpr int RG = (F + PD) / 16, pos = (F + PD) % 16, RR = pos == 0 ? 2 : 0;
      constexpr unsigned ro = (slot1(RG) % 4) * S1 + (pos >> 2) * 4096;
      const bf16x8 a = f[I];
      static_for<4>([&](auto R_) {
        constexpr int R = CI(R_);
        if constexpr (ps == 0) mf<(ks < 16), ks == 0>(ag[t][R], a, xf[ks][R]); else mf<(ks < 16), ks == 0>(au[t][R], a, xf[ks][R]);
        SBAR();
        if constexpr (R == 0 && F < 64 && F % 8 == 3) wait_vm<VM<0, F>>();
        if constexpr (pos == 0) {
          if constexpr (R < 2 && RG < 27 && slot1(RG) / 4 != slot1(RG - 1) / 4) {
#pragma unroll
            for (int i = 2 * R; i < 2 * R + 2; ++i) { rb[i] += (unsigned)(slot1(RG) / 4 - slot1(RG - 1) / 4) * 4 * S1; PIN(rb[i]); }
          }
          if constexpr (R == 0 && RG == 27) {
            const unsigned l4 = lane_id();
#pragma unroll
            for (int i = 0; i < 3; ++i) { rb2[i] = P2<8>::slot(0) + P2<8>::rbase(c, l4, i); PIN(rb2[i]); }
          }
          if constexpr (R == 2) {
            wait_bar<VM<0, F>>();
            STAMP(1 + RG);
            if constexpr (RG == 27) tieA(p2.bf[0][0], p2.bf[0][1], p2.bf[0][2], p2.bf[0][3]);
          }
        }
        if constexpr (R == RR) { if constexpr (RG < 27) flo = trd(rb[pos & 3] + ro); else flo = trd(rb2[pos]); }
        if constexpr (R == RR + 1) { if constexpr (RG < 27) f[pos] = cat(flo, trd(rb[pos & 3] + ro + 2048)); else f[pos] = cat(flo, trd(rb2[pos] + 4608)); }
        if constexpr (F < 64 && (F % 8 == 3 || F % 8 == 4)) rdx(IC(F / 8 + 1), IC(4 * (F % 8 - 3) + R));
        SBAR();
        if constexpr (pos == 0 && R == 3) {
          if constexpr (RG == FLG0 || RG == FLG1) { if (threadIdx.x == 0) flag_store(c, f1 + 8 * c.m, RG == FLG0 ? 1 : 2); }
          if constexpr (RG == PZR + 4) { if (vl16 == 0) flag_store(c, flag2(c) + 8 * c.a, 1); }
          if constexpr (RG == 19) p2.init(c);
          if constexpr (RG == 25) p2.template check<0>(c);
        }
        if constexpr (R >= 2) { if constexpr (R == 3 || F == 0) prep(IC(F), R_); fire(IC(F), R_); }
        if constexpr (ps == 1 && sig_idx(F) >= 0) {
          constexpr int e = sig_idx(F), d = e - 2;
          if constexpr (R == 0 && d >= 0) e2 = w_add1(e1);
          if constexpr (R == 1) { e3 = e0; if constexpr (e < 64) e0 = w_mul(ag[e / 16][(e / 4) % 4][e % 4]); }
          if constexpr (R == 2 && e >= 1 && e <= 64) e1 = w_exp(e3);
          if constexpr (R == 3 && d >= 0) { if constexpr (d % 2 == 0) sp[d / 16][(d / 4) % 4][(d % 4) / 2] = w_rcplo(e2); else w_rcphi(sp[d / 16][(d / 4) % 4][(d % 4) / 2], e2); }
        }
        if constexpr (F >= 144 && FP >= FIN0 && FP < FIN0 + 32) {
          constexpr int J = (FP - FIN0) / 4, i = FP % 4, e = J >> 2, rt = J & 3, tt = 2 * e + (i >> 1), k = 2 * (i & 1);
          if constexpr (R == 0) h0 = w_mixlo(au[tt][rt][k], sp[tt][rt][i & 1]);
          if constexpr (R == 1) h1 = w_mixhi(au[tt][rt][k + 1], sp[tt][rt][i & 1]);
          if constexpr (R == 3) hv[i] = w_cvt(h0, h1);
        }
        if constexpr (R == 3 && mlp_op(F, 2).kind == O_FIN) keep(hk);
        if constexpr (R == 3 && F < 431) prep(IC(F + 1), IC(2));
        SBAR();
      });
    });
  });
  u32x4 hq[8];
  phase2<8>(c, p2, f, [&](auto N_, auto R_) {
    constexpr int N = CI(N_), R = CI(R_);
    if constexpr (N < 16 && R < 3) {
      constexpr int J = N / 2, e = J >> 2, rt = J & 3, tt = 2 * e + (N & 1);
      if constexpr (R == 0) { h0 = w_mixlo(au[tt][rt][0], sp[tt][rt][0]); h1 = w_mixhi(au[tt][rt][1], sp[tt][rt][0]); }
      if constexpr (R == 1) { e0 = w_mixlo(au[tt][rt][2], sp[tt][rt][1]); e1 = w_mixhi(au[tt][rt][3], sp[tt][rt][1]); }
      if constexpr (R == 2) { hq[J][2 * (N & 1)] = w_cvt(h0, h1); hq[J][2 * (N & 1) + 1] = w_cvt(e0, e1); }
    }
    if constexpr (R == 3 && N < 20 && lastfin(N) >= 0) {
      constexpr int J = lastfin(N);
      __builtin_amdgcn_raw_buffer_store_b128(hq[J], c.rws, vl16 + (J & 3) * 1024, sH + (24 + (J >> 2)) * 4096, 16);
      asm volatile("" ::: "memory");
    }
    if constexpr (R == 1 && N >= 1 && N < 21 && lastfin(N - 1) >= 0) keep(hq[lastfin(N - 1)]);
  });
}

DI u32x2 pk4(const f32x4& v) { return u32x2{cvtpk(v[0], v[1]), cvtpk(v[2], v[3])}; }
DI void sw16(float& a, float& b) { auto r = __builtin_amdgcn_permlane16_swap(__float_as_uint(a), __float_as_uint(b), false, false); a = __uint_as_float(r[0]); b = __uint_as_float(r[1]); }
DI void sw32(float& a, float& b) { auto r = __builtin_amdgcn_permlane32_swap(__float_as_uint(a), __float_as_uint(b), false, false); a = __uint_as_float(r[0]); b = __uint_as_float(r[1]); }
template <bool MX> DI float xred(float (&v)[4]) {
  sw16(v[0], v[1]); sw16(v[2], v[3]);
  float a = MX ? max2(v[0], v[1]) : v[0] + v[1], b = MX ? max2(v[2], v[3]) : v[2] + v[3];
  sw32(a, b);
  return MX ? max2(a, b) : a + b;
}
DI void xbc(float t, float (&v)[4]) {
  float a = t, b = t;
  sw32(a, b);
  v[0] = a; v[1] = a; v[2] = b; v[3] = b;
  sw16(v[0], v[1]); sw16(v[2], v[3]);
}

DI void role_att(Ctx& c, XS& xs) {
  const unsigned mp = c.m & 1;
  u32x2 vp[4][5], qp[4][5], hp[2];
  f32x4 gate;
  {
    const unsigned l = lane_id(), g = l >> 4, q = (l & 15) >> 2, pp = l & 3;
    const unsigned lc = (l & 15) ^ (g << 1), hb = 144 * (2 * c.a + mp);
    const unsigned vA = g * 3456 + hb + (lc < 10 ? 1152 + 16 * (lc < 9 ? lc : 8) : 2304 + 16 * (lc - 10));
    const unsigned vB = g * 3456 + hb + (lc < 4 ? 2304 + 16 * (lc < 3 ? 6 + lc : 8) : 16 * (lc - 4));
    const u64 maskB = __builtin_amdgcn_ballot_w64(lc < 13);
    const unsigned lw = c.w * 1152;
    unsigned sWk[9];
#pragma unroll
    for (int i = 0; i < 9; ++i) sWk[i] = (8 * c.w + 64 * c.kg[i]) * 3456;
    const int hr = 256 * (int)c.T + ((c.w & 1) ? -16 : -15) + 2 * (int)(l >> 3);
    const unsigned vHx = (unsigned)(hr < 0 ? 0 : hr) * 1152 + 16 * ((l & 7) ^ (4 * (c.w & 1) + g));
    const unsigned lHx = A_HX + c.w * 1024;
    unsigned rh[2] = {A_HX + (l & 15) * 128 + 16 * (g ^ ((l & 15) >> 1)), 0}; rh[1] = rh[0] ^ 64; PIN(rh[0]); PIN(rh[1]);
    bf16x8 xf[18][4];
      unsigned cur = 0;
    auto prep = [&](auto F_, auto R_) {
      constexpr Op o = att1_op(CI(F_), CI(R_));
      if constexpr (o.kind == O_W) cur = dprep<(o.a % 4) * S2 + o.b * 4608, (32 * (o.b >> 1) + 4 * (o.b & 1)) * 3456>(lw, sWk[o.a % 9]);
      else if constexpr (o.kind == O_X) xs.template prep<2, o.a, o.b>(c);
      else if constexpr (o.kind == O_HX) {
        if constexpr (o.a < 4) cur = dprep<o.a * 4096, 0>(lHx, (c.w < 2 ? c.kg[2 * o.a] : c.kg[2 * o.a + 1]) * 128);
        else cur = dprep<16384, 0>(lHx, c.w < 2 ? c.kg[8] * 128 : (c.w & 1) * 1024);
      }
    };
    auto fire = [&](auto F_, auto R_) {
      constexpr Op o = att1_op(CI(F_), CI(R_));
      if constexpr (o.kind == O_W) { if constexpr (o.a < 9) dfire<0>(vA, c.rqkv, cur); else dfire_m(vB, c.rqkv, cur, maskB); }
      else if constexpr (o.kind == O_X) {
        if constexpr (o.a >= 2 && o.b == 0) asm volatile("s_waitcnt lgkmcnt(4)" ::: "memory");
        xs.template fire<o.b>(c);
      } else if constexpr (o.kind == O_HX) {
        if constexpr (o.a < 4) dfire<0>(vHx, c.rx, cur); else dfire<0>(c.w < 2 ? vHx : l * 16, c.w < 2 ? c.rx : c.rgw, cur);
      } else if constexpr (o.kind == O_TL) touch_pages<A_GW + 2048>(c);
      else if constexpr (o.kind == O_PZ) poison<3, o.a>(c, lane_id() * 16);
    };
    auto rdx = [&](auto C_, auto I_) {
      constexpr int C = CI(C_), s = CI(I_) >> 2, rt = CI(I_) & 3;
      xf[2 * C + s][rt] = ldsr<bf16x8>(xs.rd[s] + (C % 2) * 32768 + rt * 2048);
    };
    static_for<14>([&](auto I_) { prep(IC(CI(I_) - 16), IC(2)); fire(IC(CI(I_) - 16), IC(2)); });
    load_epoch(c);
    unsigned rb[4], rx1, rx2;
#pragma unroll
    for (int t = 0; t < 4; ++t) rb[t] = g * 1152 + q * 256 + 32 * (t ^ q) + 8 * pp;
    rx1 = g * 1152 + q * 256 + 32 * (c.w ^ q) + 8 * pp;
    rx2 = g * 1152 + q * 256 + 128 + 32 * ((c.w < 2 ? c.w + 1 : 3) ^ q) + 8 * pp;
    const unsigned gsrc = A_GW + 2 * (32 * (l >> 5) + 8 * ((l >> 2) & 3) + 4 * ((l >> 4) & 1) + (l & 3));
    const unsigned gdst = (l >> 2) * 1152 + (l & 3) * 256 + 16 * (13 ^ ((l & 3) << 1));
    u16 gwv = 0;
    f32x4 acc[8][4], ah[3];
    bf16x8 f[16], hx, ha, hc;
    s16x4 flo, hlo;
    wait_bar<VM<1, -1>>();
    asm volatile("" : "+s"(c.E));
    STAMP(1);
    static_for<8>([&](auto I_) { rdx(IC(0), I_); });
    static_for<PD>([&](auto I_) { f[CI(I_)] = cat(trd(rb[CI(I_)]), trd(rb[CI(I_)] + 4608)); });
    auto pass = [&](auto PS_) {
      constexpr int PS = CI(PS_), NT = PS == 0 ? 8 : 7, FC = 2 * NT;
      static_for<9>([&](auto N_) {
        static_for<FC>([&](auto I_) {
          constexpr int N = CI(N_), I = CI(I_), F = 144 * PS + N * FC + I, ks = 2 * N + I / NT, t = I % NT;
          constexpr int RG = 9 * PS + N + (I + PD) / FC, pos = (I + PD) % FC;
          constexpr int NTN = RG < 9 ? 8 : 7, tn = pos % NTN, RR = pos == 0 ? 2 : 0;
          constexpr unsigned ro = (RG % 2) * S2 + (pos / NTN) * 9216 + 128 * (tn >> 2);
          constexpr unsigned rc = ((9 * PS + N) % 2) * S2 + (I / NT) * 9216;
          constexpr bool SW = PS == 0 ? t >= 5 : t < 2;
          const bf16x8 a = f[I];
          static_for<5>([&](auto R_) {
            constexpr int R = CI(R_);
            if constexpr (R < 4) { if constexpr (SW) mfs<(ks < 16), ks == 0>(acc[t][R], a, xf[ks][R]); else mf<(ks < 16), ks == 0>(acc[t][R], a, xf[ks][R]); }
            else if constexpr (PS == 0) {
              if constexpr (t == 4) mf<false, ks == 0>(ah[1], a, hx);
              if constexpr (t == 5) mf<false, ks == 0>(ah[0], ha, hx);
              if constexpr (t == 7) mfs<false, ks == 0>(ah[2], hc, hx);
            } else if constexpr (t == 5) mfs<false, ks == 0>(ah[0], ha, hx);
            SBAR();
            if constexpr (R == 0 && PS == 0 && I == 6 && N < 8) wait_vm<VM<1, F>>();
            if constexpr (pos == 0 && RG < 18) {
              if constexpr (R < 2 && RG % 2 == 0) {
                constexpr unsigned d = RG % 4 == 0 ? 0u - 2 * S2 : 2 * S2;
#pragma unroll
                for (int i = 2 * R; i < 2 * R + 2; ++i) { rb[i] += d; PIN(rb[i]); }
                if (R == 0) { rx1 += d; PIN(rx1); } else if (RG < 9) { rx2 += d; PIN(rx2); }
              }
              if constexpr (R == 0 && RG == 9) { rx1 ^= 32 * (c.w & 2); PIN(rx1); }
              if constexpr (R == 2) {
                wait_bar<VM<1, F>>();
                STAMP(1 + RG);
              }
            }
            if constexpr (R == RR && RG < 18) flo = trd(rb[tn & 3] + ro);
            if constexpr (R == RR + 1 && RG < 18) f[pos] = cat(flo, trd(rb[tn & 3] + ro + 4608));
            if constexpr (PS == 0 && N < 8 && (I == 6 || I == 7) && R < 4) rdx(IC(N + 1), IC(4 * (I - 6) + R));
            if constexpr (t == 0 && R == 3) hx = ldsr<bf16x8>(rh[ks & 1] + (ks >> 1) * 2048);
            if constexpr (t == 1 && R == 3) hlo = trd(rx1 + rc);
            if constexpr (t == 2 && R == 3) ha = cat(hlo, trd(rx1 + rc + 4608));
            if constexpr (PS == 0 && t == 3 && R == 3) hlo = trd(rx2 + rc);
            if constexpr (PS == 0 && t == 4 && R == 3) hc = cat(hlo, trd(rx2 + rc + 4608));
            if constexpr (RG + 3 >= 9 && RG + 3 <= 17 && R == 2) {
              if constexpr (pos == 9) gwv = ldsr<u16>(gsrc + c.kg[RG + 3 - 9] * 128);
              if constexpr (pos == 12) ldsw<u16>(gdst + ((RG + 3) % 4) * S2, gwv);
            }
            SBAR();
            if constexpr (R == 2 || R == 3) { if constexpr (R == 3 || F == 0) prep(IC(F), R_); fire(IC(F), R_); }
            if constexpr (R == 4) prep(IC(F + 1), IC(2));
            SBAR();
          });
        });
      });
    };
    pass(IC(0));
    asm volatile("s_nop 7" ::: "memory");
    {
      const unsigned l3 = lane_id(), wa = A_KX + (4 * c.w + 2) * 2560 + l3 * 8, e0 = A_KX + l3 * 8, e1 = A_KX + 2560 + (l3 ^ 8) * 8;
#pragma unroll
      for (int rt = 0; rt < 4; ++rt) {
#pragma unroll
        for (int t = 0; t < 5; ++t) ldsw<u32x2>(wa + rt * 2560 + t * 512, pk4(acc[t][rt]));
#pragma unroll
        for (int t = 0; t < 3; ++t) { vp[rt][t] = pk4(acc[5 + t][rt]); PIN(vp[rt][t]); }
      }
      const u32x2 h0 = pk4(ah[0]), h1 = pk4(ah[1]);
      ldsw<u32x2>(e0 + c.w * 512, h0); ldsw<u32x2>(e1 + c.w * 512, h0);
      ldsw<u32x2>(e0 + 4 * 512, h1); ldsw<u32x2>(e1 + 4 * 512, h1);
      hp[0] = pk4(ah[2]); PIN(hp[0]);
    }
    SBAR();
    pass(IC(1));
    asm volatile("s_nop 7\n\ts_waitcnt lgkmcnt(0)\n\ts_barrier" ::: "memory");
    STAMP(19);
    if (lane_id() == 0) flag_store(c, flag2(c) + 8 * c.a, 1);
#pragma unroll
    for (int rt = 0; rt < 4; ++rt) {
#pragma unroll
      for (int t = 0; t < 2; ++t) vp[rt][3 + t] = pk4(acc[t][rt]);
#pragma unroll
      for (int t = 0; t < 5; ++t) qp[rt][t] = pk4(acc[2 + t][rt]);
      gate[rt] = sigm(acc[6][rt][0]);
    }
    hp[1] = pk4(ah[0]);
  }
  P2<12> p2;
  p2.init(c);
  p2.poll<0>(c);
  auto pre = [&](auto A_, auto N_) { static_for<CI(N_)>([&](auto I_) { constexpr Op o = att2_op(CI(A_) + CI(I_) - 50, 2); p2.template prep<o.kind, o.a, o.b>(c); p2.template fire<o.kind, o.a, o.b>(c); }); };
  pre(IC(0), IC(6));
  const unsigned l = lane_id(), g = (l >> 4) & 3, j = l & 15;
  {
    if (c.w < 3) {
      const unsigned wa = A_VX + (c.w + 1) * 5120 + l * 8;
#pragma unroll
      for (int pa = 0; pa < 2; ++pa)
#pragma unroll
        for (int t = 0; t < 5; ++t) ldsw<u32x2>(wa + (pa * 5 + t) * 512, vp[2 + pa][t]);
    }
    const unsigned e0 = A_VX + l * 8, ev = A_VX + 2560 + (l ^ 32) * 8;
    const unsigned tv1 = c.w < 2 ? c.w : 2, tv2 = 3 + (c.w & 1);
    ldsw<u32x2>(e0 + tv1 * 512, hp[0]); ldsw<u32x2>(ev + tv1 * 512, hp[0]);
    ldsw<u32x2>(e0 + tv2 * 512, hp[1]); ldsw<u32x2>(ev + tv2 * 512, hp[1]);
    if (mp == 0 && g == 2) st16(__builtin_bit_cast(u32x4, gate), c.rws, j * 16, WS_G + (c.a * 32 + 4 * c.T + c.w) * 256);
#pragma unroll
    for (int rt = 0; rt < 4; ++rt) gate[rt] = __shfl(gate[rt], 32 + j);
  }
  asm volatile("s_waitcnt vmcnt(6) lgkmcnt(0)\n\ts_barrier" ::: "memory");
  STAMP(20);
  p2.check<0>(c);
  pre(IC(6), IC(4));
  {
    u32x10 kt[6];
    u32x6 vt[5][2];
    const unsigned ra = A_VX + c.w * 5120 + l * 8, rk = A_KX + 4 * c.w * 2560 + l * 8;
#pragma unroll
    for (int i = 0; i < 6; ++i)
#pragma unroll
      for (int t = 0; t < 5; ++t) { const u32x2 k2 = ldsr<u32x2>(rk + i * 2560 + t * 512); kt[i][2 * t] = k2[0]; kt[i][2 * t + 1] = k2[1]; }
#pragma unroll
    for (int pa = 0; pa < 2; ++pa)
#pragma unroll
      for (int t = 0; t < 5; ++t) {
        const u32x2 v2 = ldsr<u32x2>(ra + (pa * 5 + t) * 512);
        vt[t][pa] = u32x6{v2[0], v2[1], vp[pa][t][0], vp[pa][t][1], vp[2 + pa][t][0], vp[2 + pa][t][1]};
      }
    SBAR();
#pragma unroll
    for (int i = 0; i < 6; ++i) PIN(kt[i]);
#pragma unroll
    for (int i = 0; i < 10; ++i) PIN(vt[i >> 1][i & 1]);
    f32x4 bprev, bown, bprev0;
    const bool first = c.T == 0 && c.w == 0;
#pragma unroll
    for (int r = 0; r < 4; ++r) {
      const int key = 4 * (int)g + r;
      bprev[r] = key >= (int)j + 9 ? 0.f : -1e30f;
      bown[r] = (key <= (int)j && key >= (int)j - 7) ? 0.f : -1e30f;
      bprev0[r] = first ? -1e30f : bprev[r];
    }
    const unsigned mpv[2] = {(bprev[0] == 0.f ? 0xffffu : 0u) | (bprev[1] == 0.f ? 0xffff0000u : 0u), (bprev[2] == 0.f ? 0xffffu : 0u) | (bprev[3] == 0.f ? 0xffff0000u : 0u)};
    constexpr float SC2 = 0.23570226039551584f * 1.4426950408889634f;
    const unsigned sH = WS_H + (((4 * c.T + c.w) * 4 + c.a) * 42 + 36 + 3 * mp) * 4096;
    PIN(bprev); PIN(bown); PIN(bprev0);
    u32x4 qf[4], P[4];
    f32x4 sp[4], so[4], hi[4], lo[4];
    auto mkq = [&](auto RT_) {
      constexpr int rt = CI(RT_);
#pragma unroll
      for (int h = 0; h < 4; ++h) {
#pragma unroll
        for (int i = 0; i < 4; ++i) { const int col = 16 * (h + (i >> 1)) + 4 * (int)g + 2 * (i & 1); qf[h][i] = (col >= 18 * h && col < 18 * h + 18) ? qp[rt][h + (i >> 1)][i & 1] : 0u; }
        PIN(qf[h]);
      }
    };
    auto qk = [&](auto RT_, auto H_, auto W_) {
      constexpr int rt = CI(RT_), h = CI(H_);
      if constexpr (CI(W_) == 0) sp[h] = mfc(__builtin_shufflevector(kt[rt], kt[rt], 2 * h, 2 * h + 1, 2 * h + 2, 2 * h + 3), qf[h], rt < 2 ? bprev0 : bprev);
      else so[h] = mfc(__builtin_shufflevector(kt[rt + 2], kt[rt + 2], 2 * h, 2 * h + 1, 2 * h + 2, 2 * h + 3), qf[h], bown);
    };
    auto pv = [&](auto RT_, auto T_, auto W_) {
      constexpr int rt = CI(RT_), t = CI(T_) + CI(W_), pa = rt & 1, sn = rt >> 1;
      const f32x4 r = mfz(__builtin_shufflevector(vt[t][pa], vt[t][pa], 2 * sn, 2 * sn + 1, 2 * sn + 2, 2 * sn + 3), P[CI(T_)]);
      if constexpr (CI(W_) == 0) hi[CI(T_)] = r; else lo[CI(T_)] = r;
    };
    auto outp = [&](auto RT_) {
      constexpr int rt = CI(RT_);
      u32x2 o[5];
      PIN(lo[0]);
#pragma unroll
      for (int t = 0; t < 5; ++t)
#pragma unroll
        for (int i = 0; i < 2; ++i) {
          const unsigned vh = t < 4 ? cvtpk(hi[t < 4 ? t : 0][2 * i], hi[t < 4 ? t : 0][2 * i + 1]) : 0u;
          o[t][i] = (4 * (int)g + 2 * i < 2 * t) ? cvtpk(lo[t > 0 ? t - 1 : 0][2 * i], lo[t > 0 ? t - 1 : 0][2 * i + 1]) : vh;
        }
      st16(u32x4{o[0][0], o[0][1], o[1][0], o[1][1]}, c.rws, l * 16 + rt * 1024, sH);
      st16(u32x4{o[2][0], o[2][1], o[3][0], o[3][1]}, c.rws, l * 16 + rt * 1024, sH + 4096);
      st16(u32x4{o[4][0], o[4][1], 0u, 0u}, c.rws, l * 16 + rt * 1024, sH + 8192);
    };
    mkq(IC(0));
    asm volatile("s_nop 1" ::: "memory");
    static_for<8>([&](auto I_) { qk(IC(0), IC(CI(I_) >> 1), IC(CI(I_) & 1)); });
    SBAR();
    mkq(IC(1));
    static_for<4>([&](auto RT_) {
      constexpr int rt = CI(RT_);
      float s[4][4], red[4], bc[4];
      SBAR();
      static_for<4>([&](auto H_) {
        constexpr int h = CI(H_);
        if constexpr (rt > 0) pv(IC(rt - 1), H_, IC(0));
        s[h][0] = max2(sp[h][0], so[h][0]); s[h][1] = max2(sp[h][1], so[h][1]); s[h][2] = max2(sp[h][2], so[h][2]);
        SBAR();
        if constexpr (rt > 0) pv(IC(rt - 1), H_, IC(1));
        s[h][3] = max2(sp[h][3], so[h][3]);
        red[h] = max2(max3(s[h][0], s[h][1], s[h][2]), s[h][3]);
        SBAR();
      });
      xbc(-SC2 * xred<true>(red), bc);
      if constexpr (rt == 0) pre(IC(10), IC(6));
      if constexpr (rt > 0) outp(IC(rt - 1));
      SBAR();
      static_for<4>([&](auto H_) {
        constexpr int h = CI(H_);
        if constexpr (rt < 3) qk(IC(rt + 1), H_, IC(0));
        s[h][0] = __builtin_amdgcn_exp2f(__builtin_fmaf(s[h][0], SC2, bc[h])); s[h][1] = __builtin_amdgcn_exp2f(__builtin_fmaf(s[h][1], SC2, bc[h]));
        SBAR();
        if constexpr (rt < 3) qk(IC(rt + 1), H_, IC(1));
        s[h][2] = __builtin_amdgcn_exp2f(__builtin_fmaf(s[h][2], SC2, bc[h])); s[h][3] = __builtin_amdgcn_exp2f(__builtin_fmaf(s[h][3], SC2, bc[h]));
        SBAR();
      });
#pragma unroll
      for (int h = 0; h < 4; ++h) red[h] = (s[h][0] + s[h][1]) + (s[h][2] + s[h][3]);
      xbc(gate[rt] * __builtin_amdgcn_rcpf(xred<false>(red)), bc);
      if constexpr (rt == 1) pre(IC(19), IC(10));
      if constexpr (rt == 2) pre(IC(32), IC(4));
      if constexpr (rt < 2) mkq(IC(rt + 2));
#pragma unroll
      for (int h = 0; h < 4; ++h) {
        const unsigned p0 = cvtpk(s[h][0] * bc[h], s[h][1] * bc[h]), p1 = cvtpk(s[h][2] * bc[h], s[h][3] * bc[h]);
        P[h] = u32x4{p0 & mpv[0], p1 & mpv[1], p0 & ~mpv[0], p1 & ~mpv[1]};
        PIN(P[h]);
      }
      asm volatile("s_nop 1" ::: "memory");
    });
    static_for<8>([&](auto I_) { pv(IC(3), IC(CI(I_) >> 1), IC(CI(I_) & 1)); });
    asm volatile("s_nop 7" ::: "memory");
    SBAR();
    outp(IC(3));
  }
  STAMP(21);
  bf16x8 f[16];
  phase2<12>(c, p2, f, [](auto, auto) {});
}

#endif
#define STAMP_OFF (64u << 20)
__global__ __launch_bounds__(256, 1) void fused_kernel(const u16* x, const u16* wqkv, const u16* wo, const u16* wg, const u16* wu, const u16* wd,
                                                        const u16* gw, char* ws, u16* out) {
#if defined(__HIP_DEVICE_COMPILE__)
  __shared__ __attribute__((aligned(16))) char smem[LDS_BYTES];
  asm volatile("" ::"v"((unsigned)(size_t)smem));
  Ctx c;
  const unsigned bid = blockIdx.x, xcd = bid & 7, idx = bid >> 3;
  c.w = __builtin_amdgcn_readfirstlane(threadIdx.x >> 6);
  {
    u64 pc; unsigned len;
    asm volatile("s_getpc_b64 %0\n.Lgm_code0:\n\ts_mov_b32 %1, .Lgm_code1-.Lgm_code0" : "=s"(pc), "=s"(len));
    const rsrc_t rc = __builtin_amdgcn_make_buffer_rsrc((void*)pc, (short)0, len, 0x00020000);
    if (idx < 16) touch<3 * S1>((threadIdx.x & 15) * 128, rc, idx * 8192 + c.w * 2048);
  }
  STAMP(0);
  asm volatile("" ::"s"(x), "s"(out));
  c.rx = __builtin_amdgcn_make_buffer_rsrc((void*)x, (short)0, SEQ * DM * 2, 0x00020000);
  c.rqkv = __builtin_amdgcn_make_buffer_rsrc((void*)wqkv, (short)0, DM * 3 * DM * 2, 0x00020000);
  c.rwo = __builtin_amdgcn_make_buffer_rsrc((void*)wo, (short)0, DM * DM * 2, 0x00020000);
  c.rwg = __builtin_amdgcn_make_buffer_rsrc((void*)wg, (short)0, DM * FF * 2, 0x00020000);
  c.rwu = __builtin_amdgcn_make_buffer_rsrc((void*)wu, (short)0, DM * FF * 2, 0x00020000);
  c.rwd = __builtin_amdgcn_make_buffer_rsrc((void*)wd, (short)0, DM * FF * 2, 0x00020000);
  c.rgw = __builtin_amdgcn_make_buffer_rsrc((void*)gw, (short)0, DM * 2, 0x00020000);
  c.rws = __builtin_amdgcn_make_buffer_rsrc((void*)ws, (short)0, WS_BYTES, 0x00020000);
  c.rout = __builtin_amdgcn_make_buffer_rsrc((void*)out, (short)0, SEQ * DM * 2, 0x00020000);
  c.ws = (u64)ws;
  c.a = xcd & 3; c.T = 4 * (xcd >> 2) + (idx >> 3);
  c.m = (idx & 3) == 3 ? 6 + ((idx >> 2) & 1) : 3 * ((idx >> 2) & 1) + (idx & 3);
  const unsigned rot = c.m + 2 * (c.T & 3);
  c.kg[0] = rot >= 9 ? rot - 9 : rot;
  XS xs; xs.init(c, c.m < 6 ? M_X : A_X);
  static_for<8>([&](auto I_) { xs.template prep<1, 0, CI(I_)>(c); xs.template fire<CI(I_)>(c); });
  SBAR();
#pragma unroll
  for (int i = 1; i < 9; ++i) c.kg[i] = c.kg[i - 1] == 8 ? 0 : c.kg[i - 1] + 1;
  if (c.m < 6) role_mlp(c, xs); else role_att(c, xs);
  asm volatile(".Lgm_code1:" ::: "memory");
#endif
}

extern "C" void kernel_launch(void* const* d_in, const int* in_sizes, int n_in, void* d_out, int out_size, void* d_ws, size_t ws_size,
                              hipStream_t stream) {
  fused_kernel<<<256, 256, 0, stream>>>((const u16*)d_in[0], (const u16*)d_in[1], (const u16*)d_in[2], (const u16*)d_in[4], (const u16*)d_in[5],
                                        (const u16*)d_in[6], (const u16*)d_in[7], (char*)d_ws, (u16*)d_out);
}
```

```cpp
#include <hip/hip_runtime.h>
#include <utility>

typedef unsigned short u16;
typedef unsigned long long u64;
typedef short s16x4 __attribute__((ext_vector_type(4)));
typedef __bf16 bf16x8 __attribute__((ext_vector_type(8)));
typedef __bf16 bf16x2 __attribute__((ext_vector_type(2)));
typedef float f32x2 __attribute__((ext_vector_type(2)));
typedef float f32x4 __attribute__((ext_vector_type(4)));
typedef unsigned u32x2 __attribute__((ext_vector_type(2)));
typedef unsigned u32x4 __attribute__((ext_vector_type(4)));
typedef unsigned u32x6 __attribute__((ext_vector_type(6)));
typedef unsigned u32x10 __attribute__((ext_vector_type(10)));
typedef __amdgpu_buffer_rsrc_t rsrc_t;
#define LAS __attribute__((address_space(3)))
#define DI __device__ __forceinline__
#define SBAR() __builtin_amdgcn_sched_barrier(0)
#define SNAKE 0
#define SN(t, R) ((SNAKE && ((t) & 1)) ? 3 - (R) : (R))
#define IC(x) std::integral_constant<int, (x)>{}
#define CI(x) decltype(x)::value

constexpr int SEQ = 2048, DM = 576, FF = 4608;
constexpr unsigned WS_H = 0;
constexpr unsigned WS_PART = 24u << 20;
constexpr unsigned WS_G = 40u << 20;
constexpr unsigned WS_F1 = 41u << 20;
constexpr unsigned WS_F2 = WS_F1 + 8192;
constexpr unsigned WS_E = WS_F2 + 65536;
constexpr unsigned WS_BYTES = 42u << 20;
constexpr unsigned LDS_BYTES = 163840;
constexpr unsigned S1 = 16384;
constexpr unsigned S2 = 18432;
constexpr unsigned S3 = S2 + 9216;
constexpr unsigned M_X = 65536;
constexpr unsigned A_X = 4 * S2;
constexpr unsigned A_HX = A_X + 65536;
constexpr unsigned A_GW = A_HX + 18432;
constexpr unsigned A_KX = 83968;
constexpr unsigned A_VX = A_KX + 18 * 2560;
static_assert(A_GW + 2048 <= LDS_BYTES && A_VX <= A_HX && A_VX + 20480 <= LDS_BYTES && 3 * S3 <= A_KX, "lds");

#if defined(__HIP_DEVICE_COMPILE__)
template <int... Is> using iseq = std::integer_sequence<int, Is...>;
template <class Fn, int... Is> DI void static_for_(Fn&& f, iseq<Is...>) { (f(std::integral_constant<int, Is>{}), ...); }
template <int N, class Fn> DI void static_for(Fn&& f) { static_for_(f, std::make_integer_sequence<int, N>{}); }

template <int N> DI void wait_bar() { asm volatile("s_waitcnt vmcnt(%0) lgkmcnt(0)\n\ts_barrier" ::"i"(N) : "memory"); }
template <int N> DI void wait_vm() { asm volatile("s_waitcnt vmcnt(%0)" ::"i"(N) : "memory"); }
template <class T> DI void PIN(T& x) { asm volatile("" : "+v"(x)); }
DI void tieA(bf16x8& a, bf16x8& b, bf16x8& c, bf16x8& d) { asm volatile("" : "+a"(a), "+a"(b), "+a"(c), "+a"(d)); }
template <unsigned LO, unsigned SO> DI unsigned dprep(unsigned lb, unsigned sb) {
  unsigned t;
  asm volatile("s_add_u32 m0, %1, %3\n\ts_add_u32 %0, %2, %4" : "=s"(t) : "s"(lb), "s"(sb), "i"(LO), "i"(SO) : "scc");
  return t;
}
template <unsigned LO> DI void dprep_l(unsigned lb) { asm volatile("s_add_u32 m0, %0, %1\n\ts_nop 0" ::"s"(lb), "i"(LO) : "scc"); }
template <unsigned IMM> DI void dfire(unsigned voff, rsrc_t srd, unsigned so) {
  asm volatile("buffer_load_dwordx4 %0, %1, %2 offen offset:%3 lds" ::"v"(voff), "s"(srd), "s"(so), "i"(IMM) : "memory");
}
DI void dfire_m(unsigned voff, rsrc_t srd, unsigned so, u64 mask) {
  asm volatile("s_mov_b64 exec, %3\n\tbuffer_load_dwordx4 %0, %1, %2 offen lds\n\ts_mov_b64 exec, -1" ::"v"(voff), "s"(srd), "s"(so), "s"(mask) : "memory");
}
template <unsigned IMM> DI void ldh(bf16x8& d, unsigned voff, rsrc_t srd, unsigned so) {
  asm volatile("buffer_load_dwordx4 %0, %1, %2, %3 offen offset:%4 sc1" : "=a"(d) : "v"(voff), "s"(srd), "s"(so), "i"(IMM) : "memory");
}
DI void ldg(f32x4& d, unsigned voff, rsrc_t srd, unsigned so) { asm volatile("buffer_load_dwordx4 %0, %1, %2, %3 offen sc1" : "=v"(d) : "v"(voff), "s"(srd), "s"(so) : "memory"); }
template <unsigned LA> DI void touch(unsigned voff, rsrc_t srd, unsigned so) {
  asm volatile("s_mov_b32 m0, %3\n\ts_nop 0\n\tbuffer_load_dword %0, %1, %2 offen sc1 lds" ::"v"(voff), "s"(srd), "s"(so), "i"(LA) : "memory");
}
DI void st16(const u32x4& v, rsrc_t srd, unsigned voff, unsigned so) {
  __builtin_amdgcn_raw_buffer_store_b128(v, srd, voff, so, 16);
  asm volatile("s_nop 1" ::"v"(v) : "memory");
}
DI void keep(const u32x4& v) { asm volatile("" ::"v"(v)); }
template <bool AG, bool ZERO> DI void mf(f32x4& c, const bf16x8& a, const bf16x8& b) {
  if constexpr (ZERO) {
    if constexpr (AG) asm volatile("v_mfma_f32_16x16x32_bf16 %0, %1, %2, 0" : "=&v"(c) : "v"(a), "a"(b));
    else asm volatile("v_mfma_f32_16x16x32_bf16 %0, %1, %2, 0" : "=&v"(c) : "v"(a), "v"(b));
  } else {
    if constexpr (AG) asm volatile("v_mfma_f32_16x16x32_bf16 %0, %1, %2, %0" : "+v"(c) : "v"(a), "a"(b));
    else asm volatile("v_mfma_f32_16x16x32_bf16 %0, %1, %2, %0" : "+v"(c) : "v"(a), "v"(b));
  }
}
template <bool AG, bool ZERO> DI void mfs(f32x4& c, const bf16x8& a, const bf16x8& b) {
  if constexpr (ZERO) {
    if constexpr (AG) asm volatile("v_mfma_f32_16x16x32_bf16 %0, %2, %1, 0" : "=&v"(c) : "v"(a), "a"(b));
    else asm volatile("v_mfma_f32_16x16x32_bf16 %0, %2, %1, 0" : "=&v"(c) : "v"(a), "v"(b));
  } else {
    if constexpr (AG) asm volatile("v_mfma_f32_16x16x32_bf16 %0, %2, %1, %0" : "+v"(c) : "v"(a), "a"(b));
    else asm volatile("v_mfma_f32_16x16x32_bf16 %0, %2, %1, %0" : "+v"(c) : "v"(a), "v"(b));
  }
}
DI f32x4 mfc(const u32x4& a, const u32x4& b, const f32x4& c) { f32x4 d; asm volatile("v_mfma_f32_16x16x32_bf16 %0, %1, %2, %3" : "=&v"(d) : "v"(a), "v"(b), "v"(c)); return d; }
DI f32x4 mfz(const u32x4& a, const u32x4& b) { f32x4 d; asm volatile("v_mfma_f32_16x16x32_bf16 %0, %1, %2, 0" : "=&v"(d) : "v"(a), "v"(b)); return d; }
DI float max2(float a, float b) { float r; asm("v_max_f32 %0, %1, %2" : "=v"(r) : "v"(a), "v"(b)); return r; }
DI float max3(float a, float b, float c) { float r; asm("v_max3_f32 %0, %1, %2, %3" : "=v"(r) : "v"(a), "v"(b), "v"(c)); return r; }
DI s16x4 trd(unsigned a) { return __builtin_amdgcn_ds_read_tr16_b64_v4i16((LAS s16x4*)a); }
DI bf16x8 cat(s16x4 lo, s16x4 hi) { return __builtin_bit_cast(bf16x8, __builtin_shufflevector(lo, hi, 0, 1, 2, 3, 4, 5, 6, 7)); }
DI unsigned cvtpk(float lo, float hi) { f32x2 v = {lo, hi}; return __builtin_bit_cast(unsigned, __builtin_convertvector(v, bf16x2)); }
DI float w_mul(float a) { float r; asm volatile("v_mul_f32 %0, 0xbfb8aa3b, %1" : "=v"(r) : "v"(a)); return r; }
DI float w_exp(float a) { float r; asm volatile("v_exp_f32 %0, %1" : "=v"(r) : "v"(a)); return r; }
DI unsigned w_add1(float a) { unsigned r; asm volatile("v_fma_mixlo_f16 %0, %1, 1.0, 1.0" : "=v"(r) : "v"(a)); return r; }
DI unsigned w_rcplo(unsigned a) { unsigned r; asm volatile("v_rcp_f16 %0, %1" : "=v"(r) : "v"(a)); return r; }
DI void w_rcphi(unsigned& d, unsigned a) { asm volatile("v_rcp_f16_sdwa %0, %1 dst_sel:WORD_1 dst_unused:UNUSED_PRESERVE src0_sel:WORD_0" : "+v"(d) : "v"(a)); }
DI unsigned w_pklo(float a, float k) { unsigned r; asm volatile("v_fma_mixlo_f16 %0, %1, %2, 0" : "=v"(r) : "v"(a), "s"(k)); return r; }
DI void w_pkhi(unsigned& d, float a, float k) { asm volatile("v_fma_mixhi_f16 %0, %1, %2, 0" : "+v"(d) : "v"(a), "s"(k)); }
template <int H> DI void w_exph(unsigned& d) {
  if constexpr (H == 0) asm volatile("v_exp_f16_sdwa %0, %0 dst_sel:WORD_0 dst_unused:UNUSED_PRESERVE src0_sel:WORD_0" : "+v"(d));
  else asm volatile("v_exp_f16_sdwa %0, %0 dst_sel:WORD_1 dst_unused:UNUSED_PRESERVE src0_sel:WORD_1" : "+v"(d));
}
template <int H> DI void w_rcph(unsigned& d) {
  if constexpr (H == 0) asm volatile("v_rcp_f16_sdwa %0, %0 dst_sel:WORD_0 dst_unused:UNUSED_PRESERVE src0_sel:WORD_0" : "+v"(d));
  else asm volatile("v_rcp_f16_sdwa %0, %0 dst_sel:WORD_1 dst_unused:UNUSED_PRESERVE src0_sel:WORD_1" : "+v"(d));
}
DI void w_add1h(unsigned& d) { asm volatile("v_pk_add_f16 %0, %0, 1.0 op_sel_hi:[1,0]" : "+v"(d)); }
DI float w_mixlo(float u, unsigned s) { float r; asm volatile("v_fma_mix_f32 %0, %1, %2, 0 op_sel_hi:[0,1,0]" : "=v"(r) : "v"(u), "v"(s)); return r; }
DI float w_mixhi(float u, unsigned s) { float r; asm volatile("v_fma_mix_f32 %0, %1, %2, 0 op_sel:[0,1,0] op_sel_hi:[0,1,0]" : "=v"(r) : "v"(u), "v"(s)); return r; }
DI unsigned w_cvt(float lo, float hi) { unsigned r; asm volatile("v_cvt_pk_bf16_f32 %0, %1, %2" : "=v"(r) : "v"(lo), "v"(hi)); return r; }
DI float w_scale(float a, float s) { float r; asm volatile("v_mul_f32 %0, %1, %2" : "=v"(r) : "v"(a), "v"(s)); return r; }
DI float bflo(unsigned v) { return __uint_as_float(v << 16); }
DI float bfhi(unsigned v) { return __uint_as_float(v & 0xffff0000u); }
template <class T> DI T ldsr(unsigned a) { return *(const LAS T*)a; }
template <class T> DI void ldsw(unsigned a, T v) { *(LAS T*)a = v; }
DI unsigned lane_id() { unsigned l; asm volatile("v_mbcnt_lo_u32_b32 %0, -1, 0\n\tv_mbcnt_hi_u32_b32 %0, -1, %0" : "=v"(l)); return l; }

#define STAMP(i)

struct Ctx {
  rsrc_t rx, rqkv, rwo, rwg, rwu, rwd, rgw, rws, rout;
  unsigned w, a, T, m;
  unsigned kg[9];
  u64 E, ws;
};

enum { O_NONE, O_W, O_X, O_HX, O_FIN, O_POLL, O_D2, O_B2, O_GATE, O_ST, O_TW, O_TL, O_PZ };
constexpr int NEVER = 1 << 20;
struct Op { int kind, a, b, need; };
constexpr int PD = 3;
#define MLPAD 3
constexpr int MLP_AD = MLPAD, ATT_AD = 3;
constexpr Op NOP_ = {O_NONE, 0, 0, 0};
constexpr int uk(int U) { return U < 12 ? U : U < 15 ? U + 6 : U - 3; }
constexpr int dep(int U) { return U == 6 ? 1 : U == 12 ? 3 : U == 15 ? 2 : -1; }
constexpr Op p2_op(int N, int R, int NT, int AD, int FB) {
  const int ND = NT == 8 ? 4 : 6, RG = (N + PD) / NT, pos = (N + PD) % NT, U = RG + AD;
  if (RG > 20 || pos == 0) return NOP_;
  int k = -1;
  if (RG == 0) k = 2 * (pos - PD) + (R - 2);
  else if (NT == 8) k = R == 2 ? (pos == 1 ? 0 : pos) : pos == 1 ? 1 : pos == 7 ? 8 : -1;
  else k = R == 2 ? pos - 1 : -1;
  if (k < 0) return NOP_;
  if (U <= 20 && k < ND) return {O_D2, U, k, FB + U * NT - PD};
  if (U <= 20 && k < ND + 4) return {O_B2, U, k - ND, FB + U * NT - PD};
  if (dep(U + 2) >= 0 && k == ND + 4) return {O_POLL, dep(U + 2), 0, FB + (RG + 2) * NT - PD};
  if (RG == 18 && k == 0) return {O_POLL, 4, 0, FB + 20 * NT - PD};
  if (RG == 18 && k == 2) return {O_GATE, 0, 0, FB + 20 * NT - PD};
  return NOP_;
}
constexpr int FIN0 = 36, ST0 = 74, FLG0 = 17, FLG1 = 26;
#define PZR 16
constexpr int FLGA = 3;
constexpr int FLG2 = 4;
constexpr int lastfin(int N) { return N == 1 ? 0 : N == 3 ? 1 : N == 7 ? 2 : N == 8 ? 3 : N == 10 ? 4 : N == 11 ? 5 : N == 15 ? 6 : N == 16 ? 7 : -1; }
constexpr int bar1(int n) { return n == 0 ? -1 : 16 * n - PD; }
constexpr Op opW(int n, int i) { return {O_W, n, i, bar1(n)}; }
constexpr int XR = 10;
constexpr Op opX(int c, int i) { return {O_X, c, i, c == 0 ? -1 : 16 * (c - 1) + XR}; }
constexpr Op mlp_op(int F, int R) {
  if (F < 0) {
    const int i = F + 15;
    if (R != 2 || i < 0 || i >= 13) return NOP_;
    return i < 8 ? opX(0, i) : i < 12 ? opW(0, i - 8) : Op{O_TL, 0, 0, bar1(1)};
  }
  if (F >= 440) {
    const int N = F - 440;
    if (N < 5) return R == 2 ? Op{N < 4 ? O_D2 : O_B2, MLP_AD, N & 3, 440 + MLP_AD * 8 - PD} : N == 0 || N == 2 || N == 4 ? Op{O_B2, MLP_AD, N / 2 + 1, 440 + MLP_AD * 8 - PD} : Op{O_FIN, 2, N / 2, 440 + FLG2 * 8 - PD};
    if (R == 3 && N < 20 && lastfin(N) >= 0) return {O_FIN, 2, lastfin(N), 440 + FLG2 * 8 - PD};
    return p2_op(N, R, 8, MLP_AD, 440);
  }
  if (F >= 432) return NOP_;
  const int RG = (F + PD) / 16, pos = (F + PD) % 16, r = R - 2;
  if (pos == 0) return NOP_;
  if (F < 4) return opX(1, 2 * F + r);
  if (F < 6) return opW(1, 2 * (F - 4) + r);
  if (R == 3) return NOP_;
  if (RG <= 5) {
    const int n = F - 6 - RG, c = 2 + n / 12, i = n % 12;
    const int cap = RG + 3;
    if (RG <= 3) { if (c <= cap) return i < 8 ? opX(c, i) : opW(c, i - 8); return NOP_; }
    if (RG == 4 && pos <= 8) return pos <= 4 ? opX(6, pos + 3) : opW(6, pos - 5);
    if (RG == 4) return opX(7, pos - 9);
    if (pos == 1) return opX(7, 7);
    if (pos <= 5) return opW(7, pos - 2);
    return pos <= 13 ? opX(8, pos - 6) : opW(8, pos - 14);
  }
  if (RG == 6 && pos <= 2) return opW(8, pos + 1);
  const int k8 = pos >= 3 && pos < 15 && (pos - 3) % 3 != 2 ? (pos - 3) - (pos - 3) / 3 : -1;
  if (RG >= 6 && RG <= 8 && k8 >= 0) return opW(9 + 2 * (RG - 6) + k8 / 4, k8 % 4);
  if (F >= 144 && F % 144 >= ST0 && F % 144 < ST0 + 32 && F % 4 == 2) return {O_FIN, F / 144 - 1, (F % 144 - ST0) / 4, bar1(F < 288 ? FLG0 : FLG1)};
  if (RG >= 9 && RG <= 12 && (pos == 1 || pos == 15)) return {O_TW, 2 * (RG - 9) + (pos == 15), 0, NEVER};
  if (RG >= 9 && RG <= 10 && k8 >= 0) return opW(15 + 2 * (RG - 9) + k8 / 4, k8 % 4);
  if (RG >= 11 && RG <= 12 && pos % 4 == 2) return opW(RG + 8, pos / 4);
  if (RG == 13 && pos <= 4) return opW(21, pos - 1);
  if (RG >= 18 && RG <= 19 && k8 >= 0) return opW(22 + 2 * (RG - 18) + k8 / 4, k8 % 4);
  if (RG == 20 && k8 >= 0) return k8 < 4 ? opW(26, k8) : Op{O_D2, 0, k8 - 4, bar1(27)};
  if (RG >= 21 && RG <= 22 && pos <= 4) return {O_D2, RG - 20, pos - 1, 440 + (RG - 20) * 8 - PD};
  if (RG == PZR && pos % 2 == 1 && pos < 12) return {O_PZ, pos / 2, 0, bar1(PZR + 6)};
  if (RG == 22 && pos == 7) return {O_POLL, 0, 0, bar1(25)};
  if (RG >= 25 && RG <= 26) {
    const int k = (RG - 25) * 15 + pos - 1;
    if (k < 24 && k % 2 == 0) { const int U = k / 8; return {O_B2, U, (k / 2) % 4, U == 0 ? bar1(27) : 440 + U * 8 - PD}; }
    if (MLP_AD > 3 && k < 16 && k % 2 == 1) { const int i = k / 2; return i < 4 ? Op{O_D2, 3, i, 440 + 24 - PD} : NOP_; }
    if (MLP_AD > 3 && k >= 24 && k < 28) return {O_B2, 3, k - 24, 440 + 24 - PD};
  }
  return NOP_;
}
static_assert(MLP_AD == 3 || MLP_AD == 4, "pre-issue table");
constexpr int barA(int n) { return n == 0 ? -1 : n <= 9 ? 16 * n - PD : 144 + 14 * (n - 9) - PD; }
constexpr Op att1_op(int F, int R) {
  if (F < 0) {
    const int i = F + 16;
    if (R != 2 || i < 0 || i >= 14) return NOP_;
    return i < 8 ? opX(0, i) : i < 12 ? Op{O_W, 0, i - 8, -1} : i == 12 ? Op{O_HX, 0, 0, -1} : Op{O_TL, 0, 0, NEVER};
  }
  const int FP = F + PD, RG = FP < 144 ? FP / 16 : 9 + (FP - 144) / 14, pos = FP < 144 ? FP % 16 : (FP - 144) % 14, r = R - 2;
  if (pos == 0) return NOP_;
  if (F < 4) return opX(1, 2 * F + r);
  if (F < 6) return {O_W, 1, 2 * (F - 4) + r, barA(1)};
  if (F < 10) return opX(2, 2 * (F - 6) + r);
  if (F < 12) return {O_W, 2, 2 * (F - 10) + r, barA(2)};
  if (F == 12) return {O_HX, 1 + r, 0, barA(2 + 2 * r)};
  if (RG == 1 && pos <= 4) return opX(3, 2 * (pos - 1) + r);
  if (RG == 1 && pos <= 6) return {O_W, 3, 2 * (pos - 5) + r, barA(3)};
  if (RG == 1 && pos == 7) return {O_HX, 3 + r, 0, barA(5)};
  if (R == 3) return NOP_;
  if (RG == 1 && pos <= 11) return {O_W, 4, pos - 8, barA(4)};
  if (RG >= 2 && RG <= 6 && pos <= 8) return opX(RG + 2, pos - 1);
  if (RG == 15 && pos <= 9) return {O_PZ, pos - 1, 0, barA(17)};
  if (RG >= 2 && RG + 3 <= 17) { const int p0 = RG <= 6 ? 9 : 1; if (pos >= p0 && pos < p0 + 4) return {O_W, RG + 3, pos - p0, barA(RG + 3)}; }
  return NOP_;
}
constexpr Op att2_op(int F, int R) {
  if (F < 0) {
    const int i = F + 50;
    if (R != 2 || i < 0 || i >= 42) return NOP_;
    constexpr int n1 = 12 - PD, n2 = 24 - PD, ns = FLGA * 12 - PD;
    if (i < 6) return {O_D2, 0, i, -1};
    if (i < 10) return {O_B2, 0, i - 6, -1};
    if (i < 16) return {O_D2, 1, i - 10, n1};
    if (i < 19) return {O_ST, 0, 0, ns};
    if (i < 23) return {O_B2, 1, i - 19, n1};
    if (i < 29) return {O_D2, 2, i - 23, n2};
    if (i < 32) return {O_ST, 0, 0, ns};
    if (i < 36) return {O_B2, 2, i - 32, n2};
    return {O_ST, 0, 0, ns};
  }
  return p2_op(F, R, 12, ATT_AD, 0);
}
template <int WHICH> constexpr Op op_at(int F, int R) { return WHICH == 0 ? mlp_op(F, R) : WHICH == 1 ? att1_op(F, R) : att2_op(F, R); }
template <int WHICH> constexpr int vm_at(int tau) {
  int s = 0;
  for (int F = tau - 1; F >= -64; --F)
    for (int R = 3; R >= 2; --R) {
      const Op o = op_at<WHICH>(F, R);
      if (o.kind == O_NONE) continue;
      if (o.need <= tau || s >= 63) return s;
      ++s;
    }
  return s;
}
template <int WHICH, int TAU> constexpr int VM = vm_at<WHICH>(TAU);

DI void load_epoch(Ctx& c) {
  asm volatile("s_load_dwordx2 %0, %1, %2" : "=s"(c.E) : "s"(c.ws), "s"(WS_E + c.T * 128) : "memory");
}
DI void flag_store(const Ctx& c, unsigned off, unsigned val) {
  const u64 v = c.E + val;
  __builtin_amdgcn_raw_buffer_store_b64(u32x2{(unsigned)v, (unsigned)(v >> 32)}, c.rws, 0, off, 16);
}
DI void poll_issue(const Ctx& c, u32x2& f, unsigned voff, unsigned off) {
  asm volatile("buffer_load_dwordx2 %0, %1, %2, %3 offen sc1" : "=v"(f) : "v"(voff), "s"(c.rws), "s"(off) : "memory");
}
DI bool flag_ok(const Ctx& c, u32x2 f, unsigned need) {
  const u64 d = (((u64)f[1] << 32) | f[0]) - c.E - need;
  return __builtin_amdgcn_ballot_w64(d >= 8) == 0;
}
DI void poll_check(const Ctx& c, u32x2& f, unsigned voff, unsigned off, unsigned need) {
  asm volatile("" : "+v"(f));
  if (__builtin_expect(!flag_ok(c, f, need), 0)) {
    for (unsigned spin = 0; spin < (1u << 16); ++spin) {
      __builtin_amdgcn_s_sleep(1);
      asm volatile("buffer_load_dwordx2 %0, %1, %2, %3 offen sc1\n\ts_waitcnt vmcnt(0)" : "=&v"(f) : "v"(voff), "s"(c.rws), "s"(off) : "memory");
      if (flag_ok(c, f, need)) break;
    }
  }
}

constexpr unsigned POISON = 0xffffffffu;
DI unsigned inbox(const Ctx& c) { return WS_PART + ((c.T * 8 + c.m) * 64 + c.w) * 3072; }
DI unsigned flag2(const Ctx& c) { return WS_F2 + ((c.T * 8 + c.m) * 4 + c.w) * 128; }
template <int NI, int J> DI void poison(const Ctx& c, unsigned vl16) {
  st16(u32x4{POISON, POISON, POISON, POISON}, c.rws, vl16 + (J % NI) * 1024, inbox(c) + (c.a * 4 + ((J / NI + 1) ^ c.a)) * 12288);
}
template <int NT> struct P2 {
  static constexpr int AD = NT == 8 ? MLP_AD : ATT_AD, NS = NT == 8 ? 8 : 4, FB = NT == 8 ? 440 : 0, ROT = NT == 8 ? 4 : 0;
  static constexpr unsigned SS = NT == 8 ? S2 : S3;
  static constexpr unsigned slot(int u) { return ((u + ROT) % NS) * SS; }
  static constexpr int WN = NT == 8 ? 3 : 2;
  static constexpr unsigned win(int u) { return (((u + ROT) % NS) / WN * WN) * SS; }
  unsigned v128[2], v64, vh[2], vf;
  unsigned lw, lw64, sd, so, sh[2];
  unsigned f1, cur, curh;
  bf16x8 bf[21][4];
  u32x2 pf;
  f32x4 omg;
  DI void init(const Ctx& c) {
    const unsigned l = lane_id(), colb = NT == 8 ? 256 * (c.m >> 1) : 768, kh = c.w >> 1;
    const unsigned lo = 16 * ((l & 15) ^ ((l >> 3) & 2) ^ (4 * c.a));
    v128[0] = (l >> 4) * 4608 + lo; v128[1] = (l >> 4) * 4608 + (lo ^ 64);
    v64 = (l >> 3) * 2304 + 16 * ((l & 7) ^ (((l >> 1) & 4) | ((l >> 3) & 2)) ^ (2 * c.a)) + 256;
    vh[0] = l * 16 + kh * 2048; vh[1] = l * 16 + (1 - kh) * 2048;
    vf = 8 * (l & 7);
    lw = c.w * 4608; lw64 = c.w * 2304 + S2;
    sd = (1152 * c.a + 16 * c.w) * 1152 + colb;
    so = (144 * c.a + 72 * kh + 16 * (c.w & 1)) * 1152 + colb;
    sh[0] = WS_H + (((4 * c.T + 2 * (c.m & 1) + (c.w & 1)) * 4 + c.a) * 42 + kh) * 4096; sh[1] = sh[0] + kh * 8192;
    f1 = WS_F1 + (c.T * 4 + c.a) * 128;
  }
  template <int S> DI unsigned pvo() const { return S < 3 ? (vf < 48 ? vf : 0) : S == 3 ? 48 + (vf & 8) : vf & 24; }
  template <int S> DI void poll(const Ctx& c) { poll_issue(c, pf, pvo<S>(), S < 4 ? f1 : flag2(c)); }
  template <int S> DI void check(const Ctx& c) { poll_check(c, pf, pvo<S>(), S < 4 ? f1 : flag2(c), S < 3 ? S + 1 : S == 3 ? 3 : 1); }
  template <int KIND, int U, int I> DI void prep(const Ctx& c) {
    if constexpr (KIND == O_D2) {
      constexpr int K = uk(U);
      constexpr unsigned row = K < 18 ? 64 * K : 32 * (K - 18);
      if constexpr (I == 0) cur = dprep<slot(U), row * 1152>(lw, K < 18 ? sd : so);
      if constexpr (I == 4) dprep_l<slot(U)>(lw64);
    }
    if constexpr (KIND == O_B2 && (I == 0 || (NT == 8 && U == AD && I == 1))) { curh = sh[uk(U) >= 18] + (uk(U) < 18 ? 2 * uk(U) : 18 + uk(U)) * 4096; asm volatile("" : "+s"(curh)); }
  }
  template <int KIND, int U, int I> DI void fire(const Ctx& c) {
    if constexpr (KIND == O_D2) dfire<(I & 3) * 1152>(I < 4 ? v128[I >> 1] : v64, uk(U) < 18 ? c.rwd : c.rwo, cur);
    if constexpr (KIND == O_B2) ldh<(I & 1) * 1024>(bf[U][I], vh[I >> 1], c.rws, curh);
    if constexpr (KIND == O_POLL) poll<U>(c);
    if constexpr (KIND == O_GATE) ldg(omg, vh[0] & 255, c.rws, WS_G + (c.a * 32 + 4 * c.T + 2 * (c.m & 1) + (c.w & 1)) * 256);
  }
  static DI unsigned rbase(const Ctx& c, unsigned l, int t) {
    const unsigned g = l >> 4, q = (l >> 2) & 3;
    return (c.w >> 1) * 9216 + q * 1152 + g * 256 + 32 * (t ^ ((q & 2) | (g & 1))) + 8 * (l & 3);
  }
};

template <int NT, class Extra> DI void phase2(Ctx& c, P2<NT>& p, bf16x8 (&f)[16], Extra&& extra) {
  using P = P2<NT>;
  constexpr int AD = P::AD, WH = NT == 8 ? 0 : 2;
  const unsigned l = lane_id(), g = l >> 4, q = (l >> 2) & 3, kh = c.w >> 1;
  unsigned rb[4], rb64[4];
#pragma unroll
  for (int t = 0; t < 4; ++t) {
    rb[t] = P::win(0) + P::rbase(c, l, t); PIN(rb[t]);
    if (NT != 8) { rb64[t] = P::win(0) + S2 + kh * 4608 + (q & 1) * 1152 + (2 * g + (q >> 1)) * 128 + 32 * (t ^ ((q & 2) | (g & 1))) + 8 * (l & 3); PIN(rb64[t]); }
  }
  f32x4 acc[NT][4];
  s16x4 flo;
  constexpr unsigned XB = NT == 8 ? 40960 : 28672;
  unsigned wa = 0;
  auto rdlo = [&](auto U_, auto T_) { constexpr int t = CI(T_); constexpr unsigned o = P::slot(CI(U_)) - P::win(CI(U_)); return t < 8 ? trd(rb[t & 3] + o + 128 * (t >> 2)) : trd(rb64[t & 3] + o); };
  auto rdhi = [&](auto U_, auto T_) { constexpr int t = CI(T_); constexpr unsigned o = P::slot(CI(U_)) - P::win(CI(U_)); return t < 8 ? trd(rb[t & 3] + o + 128 * (t >> 2) + 4608) : trd(rb64[t & 3] + o + 2304); };
  if constexpr (NT != 8) {
    wait_bar<VM<2, -1>>();
    STAMP(24);
    tieA(p.bf[0][0], p.bf[0][1], p.bf[0][2], p.bf[0][3]);
    static_for<PD>([&](auto T_) { f[CI(T_)] = cat(rdlo(IC(0), T_), rdhi(IC(0), T_)); });
  }
  static_for<21>([&](auto U_) {
    static_for<NT>([&](auto T_) {
      constexpr int U = CI(U_), t = CI(T_), N = U * NT + t, UN = (N + PD) / NT, tn = (N + PD) % NT;
      constexpr int RR = tn == 0 ? 2 : 0;
      const bf16x8 a = f[t];
      static_for<4>([&](auto R_) {
        constexpr int R = CI(R_);
        mf<true, U == 0>(acc[t][SN(t, R)], a, p.bf[U][SN(t, R)]);
        SBAR();
        if constexpr (tn == 0 && UN <= 20) {
          if constexpr (R < 2 && P::win(UN) != P::win(UN - 1)) {
#pragma unroll
            for (int i = 2 * R; i < 2 * R + 2; ++i) { rb[i] += P::win(UN) - P::win(UN - 1); PIN(rb[i]); if (NT != 8) { rb64[i] += P::win(UN) - P::win(UN - 1); PIN(rb64[i]); } }
          }
          if constexpr (R == 2) {
            wait_bar<VM<WH, P::FB + N>>();
            STAMP(UN + (NT == 8 ? 28 : 24));
            tieA(p.bf[UN][0], p.bf[UN][1], p.bf[UN][2], p.bf[UN][3]);
          }
        }
        if constexpr (R == RR && UN <= 20) flo = rdlo(IC(UN), IC(tn));
        if constexpr (R == RR + 1 && UN <= 20) f[tn] = cat(flo, rdhi(IC(UN), IC(tn)));
        SBAR();
        if constexpr (tn == 0 && R == 3 && UN <= 20) {
          if constexpr (UN == (NT == 8 ? FLG2 : FLGA)) { if (threadIdx.x == 0) flag_store(c, p.f1 + 8 * c.m, 3); }
          if constexpr (UN == 20) { p.template check<4>(c); wa = XB + c.w * (NT * 2048) + lane_id() * 16; PIN(wa); }
          if constexpr (dep(UN + AD) >= 0) p.template check<dep(UN + AD)>(c);
          if constexpr (UN == 20) PIN(p.omg);
        }
        extra(IC(N), R_);
        if constexpr (U == 20 && t >= 2 && R < 2) ldsw<f32x4>(wa + (t - 2) * 2048 + R * 1024, acc[t - 2][2 + R]);
        if constexpr (R >= 2) {
          constexpr Op o = op_at<WH>(P::FB + N, R), n = op_at<WH>(P::FB + N + 1, 2);
          if constexpr (R == 3 || N == 0) p.template prep<o.kind, o.a, o.b>(c);
          p.template fire<o.kind, o.a, o.b>(c);
          if constexpr (R == 3) p.template prep<n.kind, n.a, n.b>(c);
        }
        SBAR();
      });
    });
  });
  ldsw<f32x4>(wa + (NT - 2) * 2048, acc[NT - 2][2]); ldsw<f32x4>(wa + (NT - 2) * 2048 + 1024, acc[NT - 2][3]);
  asm volatile("s_nop 5" ::: "memory");
  ldsw<f32x4>(wa + (NT - 1) * 2048, acc[NT - 1][2]); ldsw<f32x4>(wa + (NT - 1) * 2048 + 1024, acc[NT - 1][3]);
  const unsigned l2 = lane_id(), ra = XB + (c.w ^ 2) * (NT * 2048) + l2 * 16, pb = inbox(c);
  asm volatile("s_waitcnt lgkmcnt(0)\n\ts_barrier" ::: "memory");
  STAMP(NT == 8 ? 49 : 45);
  f32x4 oth[NT][2];
#pragma unroll
  for (int k = 0; k < NT; ++k) { const int t = (k + 2) % NT; oth[t][0] = ldsr<f32x4>(ra + t * 2048); oth[t][1] = ldsr<f32x4>(ra + t * 2048 + 1024); }
#pragma unroll
  for (int t = 0; t < NT; ++t) {
    const int dq = t < 8 ? t >> 1 : t - 8, idx = t < 8 ? t & 1 : 2;
    if (dq != 0) {
      const f32x4 v0 = acc[t][0] + oth[t][0], v1 = acc[t][1] + oth[t][1];
      const u32x4 v = {cvtpk(v0[0], v0[1]), cvtpk(v0[2], v0[3]), cvtpk(v1[0], v1[1]), cvtpk(v1[2], v1[3])};
      st16(__builtin_elementwise_min(v, u32x4{POISON - 1, POISON - 1, POISON - 1, POISON - 1}), c.rws, l2 * 16 + idx * 1024, pb + ((dq ^ c.a) * 4 + c.a) * 12288);
    }
  }
  STAMP(NT == 8 ? 50 : 46);
  constexpr int NI = NT / 4;
  u32x4 in[3][NI];
  for (unsigned spin = 0; spin < (1u << 16); ++spin) {
    unsigned mx = 0;
#pragma unroll
    for (int s = 1; s < 4; ++s)
#pragma unroll
      for (int i = 0; i < NI; ++i) in[s - 1][i] = __builtin_amdgcn_raw_buffer_load_b128(c.rws, l2 * 16 + i * 1024, pb + (c.a * 4 + (s ^ c.a)) * 12288, 16);
    asm volatile("" ::: "memory");
#pragma unroll
    for (int s = 0; s < 3; ++s)
#pragma unroll
      for (int i = 0; i < NI; ++i) mx = max(max(mx, max(in[s][i][0], in[s][i][1])), max(in[s][i][2], in[s][i][3]));
    if (__builtin_amdgcn_ballot_w64(mx == POISON) == 0) break;
  }
  STAMP(NT == 8 ? 51 : 47);
  const unsigned row0 = 256 * c.T + 128 * (c.m & 1) + 64 * (c.w & 1) + 32 * kh;
  const unsigned vo = (l2 & 15) * 2304 + 8 * (l2 >> 4), colb = NT == 8 ? 256 * (c.m >> 1) : 768;
#pragma unroll
  for (int i = 0; i < NI; ++i) {
    const int t = i < 2 ? i : 8;
    f32x4 s0 = acc[t][0] + oth[t][0], s1 = acc[t][1] + oth[t][1];
#pragma unroll
    for (int s = 0; s < 3; ++s) {
      s0 += f32x4{bflo(in[s][i][0]), bfhi(in[s][i][0]), bflo(in[s][i][1]), bfhi(in[s][i][1])};
      s1 += f32x4{bflo(in[s][i][2]), bfhi(in[s][i][2]), bflo(in[s][i][3]), bfhi(in[s][i][3])};
    }
    s0 *= kh ? p.omg[2] : p.omg[0]; s1 *= kh ? p.omg[3] : p.omg[1];
    const unsigned cb = colb + (i < 2 ? 64 * c.a + 32 * i : 256 + 32 * c.a);
    __builtin_amdgcn_raw_buffer_store_b64(u32x2{cvtpk(s0[0], s0[1]), cvtpk(s0[2], s0[3])}, c.rout, vo, row0 * 1152 + cb, 0);
    __builtin_amdgcn_raw_buffer_store_b64(u32x2{cvtpk(s1[0], s1[1]), cvtpk(s1[2], s1[3])}, c.rout, vo, (row0 + 1) * 1152 + cb, 0);
  }
  if (NT == 8 && c.a == 0 && c.m == 0 && c.w == 0 && l2 == 0) flag_store(c, WS_E + c.T * 128, 16);
  if constexpr (NT == 8) asm volatile(".Lgm_code1:" ::: "memory"); else asm volatile(".Lgm_code2:" ::: "memory");
  STAMP(NT == 8 ? 52 : 48);
}

struct XS {
  unsigned v[2], lw, sx, rd[2], cur, sg;
  DI void init(const Ctx& c, unsigned base) {
    const unsigned l = lane_id(), j = l & 15, g = l >> 4;
    v[0] = (l >> 3) * 2304 + 16 * ((l & 7) ^ g); v[1] = (l >> 3) * 2304 + 16 * ((l & 7) ^ (4 + g));
    lw = base + c.w * 8192; sx = (256 * c.T + 64 * c.w) * 1152;
    rd[0] = lw + j * 128 + 16 * (g ^ (j >> 1)); rd[1] = rd[0] ^ 64;
    PIN(rd[0]); PIN(rd[1]);
  }
  template <int NSL, int C, int I> DI void prep(const Ctx& c) {
    if constexpr (I == 0) sg = sx + c.kg[C] * 128;
    cur = dprep<(C % NSL) * 32768 + I * 1024, (32 * (I >> 2) + 16 * (I & 1) + ((I >> 1) & 1)) * 1152>(lw, sg);
  }
  template <int I> DI void fire(const Ctx& c) { dfire<0>(v[I & 1], c.rx, cur); }
};
template <unsigned LA> DI void touch_pages(const Ctx& c) {
  const unsigned l = lane_id();
  const unsigned o = l < 4 ? WS_H + (((4 * c.T + l) * 4 + c.a) * 42 + 2 * c.m) * 4096 : l < 8 ? WS_PART + ((c.T * 8 + c.m) * 64 + (l - 4) * 16) * 3072 : l < 9 ? WS_G : WS_F1;
  touch<LA>(c.w < 2 ? o : (256 * c.T + 64 * (l & 3)) * 1152, c.w < 2 ? c.rws : c.rout, 0);
}
template <int I> DI void touch_w2(const Ctx& c, unsigned v128) {
  const unsigned idx = (c.T & 3) * 6 + c.m;
  const unsigned so = I < 7 ? (1152 * c.a + 48 * idx) * 1152 + c.w * 2048 + I * 8192 : (144 * c.a + 7 * idx) * 1152 + c.w * 2048;
  asm volatile("s_mov_b32 m0, %3\n\ts_mov_b64 exec, 0xffff\n\tbuffer_load_dword %0, %1, %2 offen sc1 lds\n\ts_mov_b64 exec, -1"
               ::"v"(v128), "s"(I < 7 ? c.rwd : c.rwo), "s"(so), "i"(9 * S1) : "memory");
}

constexpr int slot1(int n) { return n < 9 ? n % 4 : n % 9 < 5 ? 4 + n % 9 : n % 9 % 4; }
constexpr int sig_idx(int F) {
  if ((F + PD) % 16 == 0) return -1;
  int e = 0;
  for (int G = F - F % 72; G < F; ++G) if ((G + PD) % 16 != 0) ++e;
  return e;
}
constexpr int w_ks(int n, int I) { return n < 9 ? 2 * n + (I >> 1) : ((n % 9) * 4 + I) % 18; }
constexpr int w_up(int n, int I) { return n < 9 ? I & 1 : (n % 9) * 4 + I >= 18; }
DI void role_mlp(Ctx& c, XS& xs) {
  const unsigned l = lane_id(), g = l >> 4, q = (l & 15) >> 2, pp = l & 3, r8 = l >> 3;
  const unsigned vW = (8 * (r8 >> 2) + (r8 & 3)) * 9216 + 16 * ((l & 7) ^ (((r8 >> 1) & 3) << 1));
  const unsigned lw = c.w * 1024, sW = (16 * (c.w & 1) + 4 * (c.w >> 1)) * 9216 + (1152 * c.a + 64 * c.m) * 2;
  unsigned sWk[9];
#pragma unroll
  for (int i = 0; i < 9; ++i) sWk[i] = sW + c.kg[i] * (64 * 9216);
  const unsigned sH = WS_H + (((4 * c.T + c.w) * 4 + c.a) * 42 + 2 * c.m) * 4096;
  const unsigned f1 = WS_F1 + (c.T * 4 + c.a) * 128;
  bf16x8 xf[18][4];
  f32x4 ag0[4][4], ag[4][4], au[4][4];
  const float nl2e = -1.4426950408889634f;
  unsigned sp[4][4][2];
  P2<8> p2;
  u32x4 hq[8];
  unsigned vl16 = l * 16;
  unsigned cur = 0;
  auto prep = [&](auto F_, auto R_) {
    constexpr Op o = mlp_op(CI(F_), CI(R_));
    if constexpr (o.kind == O_W) {
      constexpr int pn = o.a / 9, k = w_ks(o.a, o.b);
      cur = dprep<slot1(o.a) * S1 + o.b * 4096, (k & 1) * 32 * 9216 + pn * 768>(lw, sWk[k >> 1]);
    } else if constexpr (o.kind == O_X) xs.template prep<3, o.a, o.b>(c);
    else p2.template prep<o.kind, o.a, o.b>(c);
  };
  auto fire = [&](auto F_, auto R_) {
    constexpr Op o = mlp_op(CI(F_), CI(R_));
    if constexpr (o.kind == O_W) dfire<0>(vW, w_up(o.a, o.b) ? c.rwu : c.rwg, cur);
    else if constexpr (o.kind == O_X) {
      if constexpr (o.a >= 3 && o.b == 0) asm volatile("s_waitcnt lgkmcnt(4)" ::: "memory");
      xs.template fire<o.b>(c);
    } else if constexpr (o.kind == O_FIN) {
      __builtin_amdgcn_raw_buffer_store_b128(hq[o.b], c.rws, vl16 + (o.b & 3) * 1024, sH + (12 * o.a + (o.b >> 2)) * 4096, 16);
      asm volatile("" ::: "memory");
    } else if constexpr (o.kind == O_PZ) poison<2, o.a>(c, vl16);
    else if constexpr (o.kind == O_TW) touch_w2<o.a>(c, vl16 * 8);
    else if constexpr (o.kind == O_TL) touch_pages<3 * S1>(c);
    else p2.template fire<o.kind, o.a, o.b>(c);
  };
  static_for<5>([&](auto I_) { prep(IC(CI(I_) - 7), IC(2)); fire(IC(CI(I_) - 7), IC(2)); });
  STAMP(56);
  load_epoch(c);
  unsigned rdx2[2] = {xs.rd[0] + 65536, xs.rd[1] + 65536}; PIN(rdx2[0]); PIN(rdx2[1]);
  auto rdx = [&](auto C_, auto I_) {
    constexpr int C = CI(C_), s = CI(I_) >> 2, rt = CI(I_) & 3;
    xf[2 * C + s][rt] = ldsr<bf16x8>((C % 3 == 2 ? rdx2[s] : xs.rd[s] + (C % 3) * 32768) + rt * 2048);
  };
  unsigned rb[4];
#pragma unroll
  for (int t = 0; t < 4; ++t) rb[t] = (4 * g + q) * 128 + 32 * (t ^ ((2 * g + (q >> 1)) & 3)) + 8 * pp;
  float e0 = 0.f, e1 = 0.f, e3 = 0.f, h0 = 0.f, h1 = 0.f;
  unsigned e2 = 0;
  bf16x8 f[16];
  s16x4 flo;
  unsigned rb2[3];
  wait_bar<VM<0, -1>>();
  asm volatile("" : "+s"(c.E));
  STAMP(1);
  static_for<4>([&](auto I_) { rdx(IC(0), I_); });
  static_for<PD>([&](auto I_) { f[CI(I_)] = cat(trd(rb[CI(I_)]), trd(rb[CI(I_)] + 2048)); });
  static_for<4>([&](auto I_) { rdx(IC(0), IC(4 + CI(I_))); });
  SBAR();
  static_for<27>([&](auto N_) {
    static_for<16>([&](auto I_) {
      constexpr int N = CI(N_), I = CI(I_), F = N * 16 + I, FP = F % 144, ps = w_up(N, I >> 2), ks = w_ks(N, I >> 2), t = I & 3;
      constexpr int RG = (F + PD) / 16, pos = (F + PD) % 16, RR = pos == 0 ? 2 : 0;
      constexpr unsigned ro = (slot1(RG) % 4) * S1 + (pos >> 2) * 4096;
      const bf16x8 a = f[I];
      static_for<4>([&](auto R_) {
        constexpr int R = CI(R_);
        if constexpr (ps == 0) mf<(ks < 16), ks == 0>(F < 144 ? ag0[t][SN(t, R)] : ag[t][SN(t, R)], a, xf[ks][SN(t, R)]); else mf<(ks < 16), ks == 0>(au[t][SN(t, R)], a, xf[ks][SN(t, R)]);
        SBAR();
        if constexpr (R == 0 && N < 8 && I == XR) { if constexpr (N == 0) STAMP(57); wait_vm<VM<0, F>>(); if constexpr (N == 0) STAMP(58); }
        if constexpr (pos == 0) {
          if constexpr (R < 2 && RG < 27 && slot1(RG) / 4 != slot1(RG - 1) / 4) {
#pragma unroll
            for (int i = 2 * R; i < 2 * R + 2; ++i) { rb[i] += (unsigned)(slot1(RG) / 4 - slot1(RG - 1) / 4) * 4 * S1; PIN(rb[i]); }
          }
          if constexpr (R == 0 && RG == 27) {
            const unsigned l4 = lane_id();
#pragma unroll
            for (int i = 0; i < 3; ++i) { rb2[i] = P2<8>::slot(0) + P2<8>::rbase(c, l4, i); PIN(rb2[i]); }
          }
          if constexpr (R == 2) {
            if constexpr (RG == 1) STAMP(59);
            wait_bar<VM<0, F>>();
            STAMP(1 + RG);
            if constexpr (RG == 27) tieA(p2.bf[0][0], p2.bf[0][1], p2.bf[0][2], p2.bf[0][3]);
          }
        }
        if constexpr (R == RR) { if constexpr (RG < 27) flo = trd(rb[pos & 3] + ro); else flo = trd(rb2[pos]); }
        if constexpr (R == RR + 1) { if constexpr (RG < 27) f[pos] = cat(flo, trd(rb[pos & 3] + ro + 2048)); else f[pos] = cat(flo, trd(rb2[pos] + 4608)); }
        if constexpr (N < 8 && (I == XR || I == XR + 1)) rdx(IC(N + 1), IC(4 * (I - XR) + R));
        SBAR();
        if constexpr (pos == 0 && R == 3) {
          if constexpr (RG == FLG0 || RG == FLG1) { if (threadIdx.x == 0) flag_store(c, f1 + 8 * c.m, RG == FLG0 ? 1 : 2); }
          if constexpr (RG == PZR + 6) { if (vl16 == 0) flag_store(c, flag2(c) + 8 * c.a, 1); }
          if constexpr (RG == 19) p2.init(c);
          if constexpr (RG == 25) p2.template check<0>(c);
        }
        if constexpr (R >= 2) { if constexpr (R == 3 || F == 0) prep(IC(F), R_); fire(IC(F), R_); }
        if constexpr (F >= 144 && ps == 1 && sig_idx(F) >= 0 && sig_idx(F) <= 65) {
          constexpr int e = sig_idx(F), d = e - 2;
          if constexpr (R == 0 && d >= 0) e2 = w_add1(e1);
          if constexpr (R == 1) { e3 = e0; if constexpr (e < 64) e0 = w_mul(ag[e / 16][(e / 4) % 4][e % 4]); }
          if constexpr (R == 2 && e >= 1 && e <= 64) e1 = w_exp(e3);
          if constexpr (R == 3 && d >= 0) { if constexpr (d % 2 == 0) sp[d / 16][(d / 4) % 4][(d % 4) / 2] = w_rcplo(e2); else w_rcphi(sp[d / 16][(d / 4) % 4][(d % 4) / 2], e2); }
        }
        if constexpr (F >= 138 && F < 146) {
          constexpr int tt = (F - 138) / 2, rt = 2 * (F & 1) + (R >> 1), k = R & 1;
          sp[tt][rt][k] = w_pklo(ag0[tt][rt][2 * k], nl2e); w_pkhi(sp[tt][rt][k], ag0[tt][rt][2 * k + 1], nl2e);
        }
        if constexpr (F >= 144 && F < 216 && sig_idx(F) >= 0) {
          constexpr int e = sig_idx(F), d = e - 2, pa = (e - 2) / 2, pf = (e - 4) / 2;
          if constexpr (R == 0 && e % 2 == 0 && e >= 2 && pa < 32) w_add1h(sp[pa / 8][(pa / 2) % 4][pa % 2]);
          if constexpr (R == 0 && e % 2 == 1 && e >= 5) h1 = w_mixhi(au[pf / 8][(pf / 2) % 4][2 * (pf % 2) + 1], sp[pf / 8][(pf / 2) % 4][pf % 2]);
          if constexpr (R == 1 && e % 2 == 0 && e >= 4) h0 = w_mixlo(au[pf / 8][(pf / 2) % 4][2 * (pf % 2)], sp[pf / 8][(pf / 2) % 4][pf % 2]);
          if constexpr (R == 1 && e % 2 == 1 && e >= 5) hq[(pf / 16) * 4 + (pf / 2) % 4][2 * ((pf / 8) & 1) + pf % 2] = w_cvt(h0, h1);
          if constexpr (R == 2 && e < 64) w_exph<e & 1>(sp[e / 16][(e / 4) % 4][(e % 4) / 2]);
          if constexpr (R == 3 && d >= 0 && d < 64) w_rcph<d & 1>(sp[d / 16][(d / 4) % 4][(d % 4) / 2]);
        }
        if constexpr (F >= 288 && FP >= FIN0 && FP < FIN0 + 32) {
          constexpr int J = (FP - FIN0) / 4, i = FP % 4, e = J >> 2, rt = J & 3, tt = 2 * e + (i >> 1), k = 2 * (i & 1);
          if constexpr (R == 0) h0 = w_mixlo(au[tt][rt][k], sp[tt][rt][i & 1]);
          if constexpr (R == 1) h1 = w_mixhi(au[tt][rt][k + 1], sp[tt][rt][i & 1]);
          if constexpr (R == 3) hq[J][i] = w_cvt(h0, h1);
        }
        if constexpr (R == 3 && mlp_op(F, 2).kind == O_FIN) keep(hq[mlp_op(F, 2).b]);
        if constexpr (R == 3 && F < 431) prep(IC(F + 1), IC(2));
        SBAR();
      });
    });
  });
  phase2<8>(c, p2, f, [&](auto N_, auto R_) {
    constexpr int N = CI(N_), R = CI(R_);
    if constexpr (N < 16 && R < 3) {
      constexpr int J = N / 2, e = J >> 2, rt = J & 3, tt = 2 * e + (N & 1);
      if constexpr (R == 0) { h0 = w_mixlo(au[tt][rt][0], sp[tt][rt][0]); h1 = w_mixhi(au[tt][rt][1], sp[tt][rt][0]); }
      if constexpr (R == 1) { e0 = w_mixlo(au[tt][rt][2], sp[tt][rt][1]); e1 = w_mixhi(au[tt][rt][3], sp[tt][rt][1]); }
      if constexpr (R == 2) { hq[J][2 * (N & 1)] = w_cvt(h0, h1); hq[J][2 * (N & 1) + 1] = w_cvt(e0, e1); }
    }
    if constexpr (R == 3 && N < 20 && lastfin(N) >= 0) {
      constexpr int J = lastfin(N);
      __builtin_amdgcn_raw_buffer_store_b128(hq[J], c.rws, vl16 + (J & 3) * 1024, sH + (24 + (J >> 2)) * 4096, 16);
      asm volatile("" ::: "memory");
    }
    if constexpr (R == 1 && N >= 1 && N < 21 && lastfin(N - 1) >= 0) keep(hq[lastfin(N - 1)]);
  });
}

DI u32x2 pk4(const f32x4& v) { return u32x2{cvtpk(v[0], v[1]), cvtpk(v[2], v[3])}; }
DI void sw16(float& a, float& b) { auto r = __builtin_amdgcn_permlane16_swap(__float_as_uint(a), __float_as_uint(b), false, false); a = __uint_as_float(r[0]); b = __uint_as_float(r[1]); }
DI void sw32(float& a, float& b) { auto r = __builtin_amdgcn_permlane32_swap(__float_as_uint(a), __float_as_uint(b), false, false); a = __uint_as_float(r[0]); b = __uint_as_float(r[1]); }
template <bool MX> DI float xred(float (&v)[4]) {
  sw16(v[0], v[1]); sw16(v[2], v[3]);
  float a = MX ? max2(v[0], v[1]) : v[0] + v[1], b = MX ? max2(v[2], v[3]) : v[2] + v[3];
  sw32(a, b);
  return MX ? max2(a, b) : a + b;
}
DI void xbc(float t, float (&v)[4]) {
  float a = t, b = t;
  sw32(a, b);
  v[0] = a; v[1] = a; v[2] = b; v[3] = b;
  sw16(v[0], v[1]); sw16(v[2], v[3]);
}

DI void role_att(Ctx& c, XS& xs) {
  const unsigned mp = c.m & 1;
  u32x2 vp[4][5], qp[4][5], hp[2];
  f32x4 gate;
  {
    const unsigned l = lane_id(), g = l >> 4, q = (l & 15) >> 2, pp = l & 3;
    const unsigned lc = (l & 15) ^ (g << 1), hb = 144 * (2 * c.a + mp);
    const unsigned vA = g * 3456 + hb + (lc < 10 ? 1152 + 16 * (lc < 9 ? lc : 8) : 2304 + 16 * (lc - 10));
    const unsigned vB = g * 3456 + hb + (lc < 4 ? 2304 + 16 * (lc < 3 ? 6 + lc : 8) : 16 * (lc - 4));
    const u64 maskB = __builtin_amdgcn_ballot_w64(lc < 13);
    const unsigned lw = c.w * 1152;
    unsigned sWk[9];
#pragma unroll
    for (int i = 0; i < 9; ++i) sWk[i] = (8 * c.w + 64 * c.kg[i]) * 3456;
    const int hr = 256 * (int)c.T + ((c.w & 1) ? -16 : -15) + 2 * (int)(l >> 3);
    const unsigned vHx = (unsigned)(hr < 0 ? 0 : hr) * 1152 + 16 * ((l & 7) ^ (4 * (c.w & 1) + g));
    const unsigned lHx = A_HX + c.w * 1024;
    unsigned rh[2] = {A_HX + (l & 15) * 128 + 16 * (g ^ ((l & 15) >> 1)), 0}; rh[1] = rh[0] ^ 64; PIN(rh[0]); PIN(rh[1]);
    bf16x8 xf[18][4];
      unsigned cur = 0;
    auto prep = [&](auto F_, auto R_) {
      constexpr Op o = att1_op(CI(F_), CI(R_));
      if constexpr (o.kind == O_W) cur = dprep<(o.a % 4) * S2 + o.b * 4608, (32 * (o.b >> 1) + 4 * (o.b & 1)) * 3456>(lw, sWk[o.a % 9]);
      else if constexpr (o.kind == O_X) xs.template prep<2, o.a, o.b>(c);
      else if constexpr (o.kind == O_HX) {
        if constexpr (o.a < 4) cur = dprep<o.a * 4096, 0>(lHx, (c.w < 2 ? c.kg[2 * o.a] : c.kg[2 * o.a + 1]) * 128);
        else cur = dprep<16384, 0>(lHx, c.w < 2 ? c.kg[8] * 128 : (c.w & 1) * 1024);
      }
    };
    auto fire = [&](auto F_, auto R_) {
      constexpr Op o = att1_op(CI(F_), CI(R_));
      if constexpr (o.kind == O_W) { if constexpr (o.a < 9) dfire<0>(vA, c.rqkv, cur); else dfire_m(vB, c.rqkv, cur, maskB); }
      else if constexpr (o.kind == O_X) {
        if constexpr (o.a >= 2 && o.b == 0) asm volatile("s_waitcnt lgkmcnt(4)" ::: "memory");
        xs.template fire<o.b>(c);
      } else if constexpr (o.kind == O_HX) {
        if constexpr (o.a < 4) dfire<0>(vHx, c.rx, cur); else dfire<0>(c.w < 2 ? vHx : l * 16, c.w < 2 ? c.rx : c.rgw, cur);
      } else if constexpr (o.kind == O_TL) touch_pages<A_GW + 2048>(c);
      else if constexpr (o.kind == O_PZ) poison<3, o.a>(c, lane_id() * 16);
    };
    auto rdx = [&](auto C_, auto I_) {
      constexpr int C = CI(C_), s = CI(I_) >> 2, rt = CI(I_) & 3;
      xf[2 * C + s][rt] = ldsr<bf16x8>(xs.rd[s] + (C % 2) * 32768 + rt * 2048);
    };
    static_for<6>([&](auto I_) { prep(IC(CI(I_) - 8), IC(2)); fire(IC(CI(I_) - 8), IC(2)); });
    STAMP(56);
  load_epoch(c);
    unsigned rb[4], rx1, rx2;
#pragma unroll
    for (int t = 0; t < 4; ++t) rb[t] = g * 1152 + q * 256 + 32 * (t ^ q) + 8 * pp;
    rx1 = g * 1152 + q * 256 + 32 * (c.w ^ q) + 8 * pp;
    rx2 = g * 1152 + q * 256 + 128 + 32 * ((c.w < 2 ? c.w + 1 : 3) ^ q) + 8 * pp;
    const unsigned gsrc = A_GW + 2 * (32 * (l >> 5) + 8 * ((l >> 2) & 3) + 4 * ((l >> 4) & 1) + (l & 3));
    const unsigned gdst = (l >> 2) * 1152 + (l & 3) * 256 + 16 * (13 ^ ((l & 3) << 1));
    u16 gwv = 0;
    f32x4 acc[8][4], ah[3];
    bf16x8 f[16], hx, ha, hc;
    s16x4 flo, hlo;
    wait_bar<VM<1, -1>>();
    asm volatile("" : "+s"(c.E));
    STAMP(1);
    static_for<8>([&](auto I_) { rdx(IC(0), I_); });
    static_for<PD>([&](auto I_) { f[CI(I_)] = cat(trd(rb[CI(I_)]), trd(rb[CI(I_)] + 4608)); });
    auto pass = [&](auto PS_) {
      constexpr int PS = CI(PS_), NT = PS == 0 ? 8 : 7, FC = 2 * NT;
      static_for<9>([&](auto N_) {
        static_for<FC>([&](auto I_) {
          constexpr int N = CI(N_), I = CI(I_), F = 144 * PS + N * FC + I, ks = 2 * N + I / NT, t = I % NT;
          constexpr int RG = 9 * PS + N + (I + PD) / FC, pos = (I + PD) % FC;
          constexpr int NTN = RG < 9 ? 8 : 7, tn = pos % NTN, RR = pos == 0 ? 2 : 0;
          constexpr unsigned ro = (RG % 2) * S2 + (pos / NTN) * 9216 + 128 * (tn >> 2);
          constexpr unsigned rc = ((9 * PS + N) % 2) * S2 + (I / NT) * 9216;
          constexpr bool SW = PS == 0 ? t >= 5 : t < 2;
          const bf16x8 a = f[I];
          static_for<5>([&](auto R_) {
            constexpr int R = CI(R_);
            if constexpr (R < 4) { if constexpr (SW) mfs<(ks < 16), ks == 0>(acc[t][SN(t, R)], a, xf[ks][SN(t, R)]); else mf<(ks < 16), ks == 0>(acc[t][SN(t, R)], a, xf[ks][SN(t, R)]); }
            else if constexpr (PS == 0) {
              if constexpr (t == 4) mf<false, ks == 0>(ah[1], a, hx);
              if constexpr (t == 5) mf<false, ks == 0>(ah[0], ha, hx);
              if constexpr (t == 7) mfs<false, ks == 0>(ah[2], hc, hx);
            } else if constexpr (t == 5) mfs<false, ks == 0>(ah[0], ha, hx);
            SBAR();
            if constexpr (R == 0 && PS == 0 && I == XR && N < 8) wait_vm<VM<1, F>>();
            if constexpr (pos == 0 && RG < 18) {
              if constexpr (R < 2 && RG % 2 == 0) {
                constexpr unsigned d = RG % 4 == 0 ? 0u - 2 * S2 : 2 * S2;
#pragma unroll
                for (int i = 2 * R; i < 2 * R + 2; ++i) { rb[i] += d; PIN(rb[i]); }
                if (R == 0) { rx1 += d; PIN(rx1); } else if (RG < 9) { rx2 += d; PIN(rx2); }
              }
              if constexpr (R == 0 && RG == 9) { rx1 ^= 32 * (c.w & 2); PIN(rx1); }
              if constexpr (R == 2) {
                wait_bar<VM<1, F>>();
                STAMP(1 + RG);
              }
            }
            if constexpr (R == RR && RG < 18) flo = trd(rb[tn & 3] + ro);
            if constexpr (R == RR + 1 && RG < 18) f[pos] = cat(flo, trd(rb[tn & 3] + ro + 4608));
            if constexpr (PS == 0 && N < 8 && (I == XR || I == XR + 1) && R < 4) rdx(IC(N + 1), IC(4 * (I - XR) + R));
            if constexpr (t == 0 && R == 3) hx = ldsr<bf16x8>(rh[ks & 1] + (ks >> 1) * 2048);
            if constexpr (t == 1 && R == 3) hlo = trd(rx1 + rc);
            if constexpr (t == 2 && R == 3) ha = cat(hlo, trd(rx1 + rc + 4608));
            if constexpr (PS == 0 && t == 3 && R == 3) hlo = trd(rx2 + rc);
            if constexpr (PS == 0 && t == 4 && R == 3) hc = cat(hlo, trd(rx2 + rc + 4608));
            if constexpr (RG + 3 >= 9 && RG + 3 <= 17 && R == 2) {
              if constexpr (pos == 6) gwv = ldsr<u16>(gsrc + c.kg[RG + 3 - 9] * 128);
              if constexpr (pos == 13) ldsw<u16>(gdst + ((RG + 3) % 4) * S2, gwv);
            }
            SBAR();
            if constexpr (R == 2 || R == 3) { if constexpr (R == 3 || F == 0) prep(IC(F), R_); fire(IC(F), R_); }
            if constexpr (R == 4) prep(IC(F + 1), IC(2));
            SBAR();
          });
        });
      });
    };
    pass(IC(0));
    asm volatile("s_nop 7" ::: "memory");
    {
      const unsigned l3 = lane_id(), wa = A_KX + (4 * c.w + 2) * 2560 + l3 * 8, e0 = A_KX + l3 * 8, e1 = A_KX + 2560 + (l3 ^ 8) * 8;
#pragma unroll
      for (int rt = 0; rt < 4; ++rt) {
#pragma unroll
        for (int t = 0; t < 5; ++t) ldsw<u32x2>(wa + rt * 2560 + t * 512, pk4(acc[t][rt]));
#pragma unroll
        for (int t = 0; t < 3; ++t) { vp[rt][t] = pk4(acc[5 + t][rt]); PIN(vp[rt][t]); }
      }
      const u32x2 h0 = pk4(ah[0]), h1 = pk4(ah[1]);
      ldsw<u32x2>(e0 + c.w * 512, h0); ldsw<u32x2>(e1 + c.w * 512, h0);
      ldsw<u32x2>(e0 + 4 * 512, h1); ldsw<u32x2>(e1 + 4 * 512, h1);
      hp[0] = pk4(ah[2]); PIN(hp[0]);
    }
    SBAR();
    pass(IC(1));
    asm volatile("s_nop 7\n\ts_waitcnt lgkmcnt(0)\n\ts_barrier" ::: "memory");
    STAMP(19);
    if (lane_id() == 0) flag_store(c, flag2(c) + 8 * c.a, 1);
#pragma unroll
    for (int rt = 0; rt < 4; ++rt) {
#pragma unroll
      for (int t = 0; t < 2; ++t) vp[rt][3 + t] = pk4(acc[t][rt]);
#pragma unroll
      for (int t = 0; t < 5; ++t) qp[rt][t] = pk4(acc[2 + t][rt]);
      gate[rt] = __builtin_amdgcn_exp2f(fminf(1.4426950408889634f * acc[6][rt][0], 80.0f));
    }
    hp[1] = pk4(ah[0]);
  }
  P2<12> p2;
  p2.init(c);
  p2.poll<0>(c);
  auto pre = [&](auto A_, auto N_) { static_for<CI(N_)>([&](auto I_) { constexpr Op o = att2_op(CI(A_) + CI(I_) - 50, 2); p2.template prep<o.kind, o.a, o.b>(c); p2.template fire<o.kind, o.a, o.b>(c); }); };
  pre(IC(0), IC(6));
  const unsigned l = lane_id(), g = (l >> 4) & 3, j = l & 15;
  {
    if (c.w < 3) {
      const unsigned wa = A_VX + (c.w + 1) * 5120 + l * 8;
#pragma unroll
      for (int pa = 0; pa < 2; ++pa)
#pragma unroll
        for (int t = 0; t < 5; ++t) ldsw<u32x2>(wa + (pa * 5 + t) * 512, vp[2 + pa][t]);
    }
    const unsigned e0 = A_VX + l * 8, ev = A_VX + 2560 + (l ^ 32) * 8;
    const unsigned tv1 = c.w < 2 ? c.w : 2, tv2 = 3 + (c.w & 1);
    ldsw<u32x2>(e0 + tv1 * 512, hp[0]); ldsw<u32x2>(ev + tv1 * 512, hp[0]);
    ldsw<u32x2>(e0 + tv2 * 512, hp[1]); ldsw<u32x2>(ev + tv2 * 512, hp[1]);
    const f32x4 omg = {__builtin_amdgcn_rcpf(1.0f + gate[0]), __builtin_amdgcn_rcpf(1.0f + gate[1]), __builtin_amdgcn_rcpf(1.0f + gate[2]), __builtin_amdgcn_rcpf(1.0f + gate[3])};
    if (mp == 0 && g == 2) st16(__builtin_bit_cast(u32x4, omg), c.rws, j * 16, WS_G + (c.a * 32 + 4 * c.T + c.w) * 256);
#pragma unroll
    for (int rt = 0; rt < 4; ++rt) gate[rt] = __shfl(gate[rt], 32 + j);
  }
  asm volatile("s_waitcnt vmcnt(6) lgkmcnt(0)\n\ts_barrier" ::: "memory");
  STAMP(20);
  p2.check<0>(c);
  pre(IC(6), IC(4));
  {
    u32x10 kt[6];
    u32x6 vt[5][2];
    const unsigned ra = A_VX + c.w * 5120 + l * 8, rk = A_KX + 4 * c.w * 2560 + l * 8;
#pragma unroll
    for (int i = 0; i < 6; ++i)
#pragma unroll
      for (int t = 0; t < 5; ++t) { const u32x2 k2 = ldsr<u32x2>(rk + i * 2560 + t * 512); kt[i][2 * t] = k2[0]; kt[i][2 * t + 1] = k2[1]; }
#pragma unroll
    for (int pa = 0; pa < 2; ++pa)
#pragma unroll
      for (int t = 0; t < 5; ++t) {
        const u32x2 v2 = ldsr<u32x2>(ra + (pa * 5 + t) * 512);
        vt[t][pa] = u32x6{v2[0], v2[1], vp[pa][t][0], vp[pa][t][1], vp[2 + pa][t][0], vp[2 + pa][t][1]};
      }
    SBAR();
#pragma unroll
    for (int i = 0; i < 6; ++i) PIN(kt[i]);
#pragma unroll
    for (int i = 0; i < 10; ++i) PIN(vt[i >> 1][i & 1]);
    f32x4 bprev, bown, bprev0;
    const bool first = c.T == 0 && c.w == 0;
#pragma unroll
    for (int r = 0; r < 4; ++r) {
      const int key = 4 * (int)g + r;
      bprev[r] = key >= (int)j + 9 ? 0.f : -1e30f;
      bown[r] = (key <= (int)j && key >= (int)j - 7) ? 0.f : -1e30f;
      bprev0[r] = first ? -1e30f : bprev[r];
    }
    const unsigned mpv[2] = {(bprev[0] == 0.f ? 0xffffu : 0u) | (bprev[1] == 0.f ? 0xffff0000u : 0u), (bprev[2] == 0.f ? 0xffffu : 0u) | (bprev[3] == 0.f ? 0xffff0000u : 0u)};
    constexpr float SC2 = 0.23570226039551584f * 1.4426950408889634f;
    const unsigned sH = WS_H + (((4 * c.T + c.w) * 4 + c.a) * 42 + 36 + 3 * mp) * 4096;
    PIN(bprev); PIN(bown); PIN(bprev0);
    u32x4 qf[4], P[4];
    f32x4 sp[4], so[4], hi[4], lo[4];
    auto mkq = [&](auto RT_) {
      constexpr int rt = CI(RT_);
#pragma unroll
      for (int h = 0; h < 4; ++h) {
#pragma unroll
        for (int i = 0; i < 4; ++i) { const int col = 16 * (h + (i >> 1)) + 4 * (int)g + 2 * (i & 1); qf[h][i] = (col >= 18 * h && col < 18 * h + 18) ? qp[rt][h + (i >> 1)][i & 1] : 0u; }
        PIN(qf[h]);
      }
    };
    auto qk = [&](auto RT_, auto H_, auto W_) {
      constexpr int rt = CI(RT_), h = CI(H_);
      if constexpr (CI(W_) == 0) sp[h] = mfc(__builtin_shufflevector(kt[rt], kt[rt], 2 * h, 2 * h + 1, 2 * h + 2, 2 * h + 3), qf[h], rt < 2 ? bprev0 : bprev);
      else so[h] = mfc(__builtin_shufflevector(kt[rt + 2], kt[rt + 2], 2 * h, 2 * h + 1, 2 * h + 2, 2 * h + 3), qf[h], bown);
    };
    auto pv = [&](auto RT_, auto T_, auto W_) {
      constexpr int rt = CI(RT_), t = CI(T_) + CI(W_), pa = rt & 1, sn = rt >> 1;
      const f32x4 r = mfz(__builtin_shufflevector(vt[t][pa], vt[t][pa], 2 * sn, 2 * sn + 1, 2 * sn + 2, 2 * sn + 3), P[CI(T_)]);
      if constexpr (CI(W_) == 0) hi[CI(T_)] = r; else lo[CI(T_)] = r;
    };
    auto outp = [&](auto RT_) {
      constexpr int rt = CI(RT_);
      u32x2 o[5];
      PIN(lo[0]);
#pragma unroll
      for (int t = 0; t < 5; ++t)
#pragma unroll
        for (int i = 0; i < 2; ++i) {
          const unsigned vh = t < 4 ? cvtpk(hi[t < 4 ? t : 0][2 * i], hi[t < 4 ? t : 0][2 * i + 1]) : 0u;
          o[t][i] = (4 * (int)g + 2 * i < 2 * t) ? cvtpk(lo[t > 0 ? t - 1 : 0][2 * i], lo[t > 0 ? t - 1 : 0][2 * i + 1]) : vh;
        }
      st16(u32x4{o[0][0], o[0][1], o[1][0], o[1][1]}, c.rws, l * 16 + rt * 1024, sH);
      st16(u32x4{o[2][0], o[2][1], o[3][0], o[3][1]}, c.rws, l * 16 + rt * 1024, sH + 4096);
      st16(u32x4{o[4][0], o[4][1], 0u, 0u}, c.rws, l * 16 + rt * 1024, sH + 8192);
    };
    mkq(IC(0));
    asm volatile("s_nop 1" ::: "memory");
    static_for<8>([&](auto I_) { qk(IC(0), IC(CI(I_) >> 1), IC(CI(I_) & 1)); });
    SBAR();
    mkq(IC(1));
    static_for<4>([&](auto RT_) {
      constexpr int rt = CI(RT_);
      float s[4][4], red[4], bc[4];
      SBAR();
      static_for<4>([&](auto H_) {
        constexpr int h = CI(H_);
        if constexpr (rt > 0) pv(IC(rt - 1), H_, IC(0));
        s[h][0] = max2(sp[h][0], so[h][0]); s[h][1] = max2(sp[h][1], so[h][1]); s[h][2] = max2(sp[h][2], so[h][2]);
        SBAR();
        if constexpr (rt > 0) pv(IC(rt - 1), H_, IC(1));
        s[h][3] = max2(sp[h][3], so[h][3]);
        red[h] = max2(max3(s[h][0], s[h][1], s[h][2]), s[h][3]);
        SBAR();
      });
      xbc(-SC2 * xred<true>(red), bc);
      if constexpr (rt == 0) pre(IC(10), IC(6));
      if constexpr (rt > 0) outp(IC(rt - 1));
      SBAR();
      static_for<4>([&](auto H_) {
        constexpr int h = CI(H_);
        if constexpr (rt < 3) qk(IC(rt + 1), H_, IC(0));
        s[h][0] = __builtin_amdgcn_exp2f(__builtin_fmaf(s[h][0], SC2, bc[h])); s[h][1] = __builtin_amdgcn_exp2f(__builtin_fmaf(s[h][1], SC2, bc[h]));
        SBAR();
        if constexpr (rt < 3) qk(IC(rt + 1), H_, IC(1));
        s[h][2] = __builtin_amdgcn_exp2f(__builtin_fmaf(s[h][2], SC2, bc[h])); s[h][3] = __builtin_amdgcn_exp2f(__builtin_fmaf(s[h][3], SC2, bc[h]));
        SBAR();
      });
#pragma unroll
      for (int h = 0; h < 4; ++h) red[h] = (s[h][0] + s[h][1]) + (s[h][2] + s[h][3]);
      xbc(gate[rt] * __builtin_amdgcn_rcpf(xred<false>(red)), bc);
      if constexpr (rt == 1) pre(IC(19), IC(10));
      if constexpr (rt == 2) pre(IC(32), IC(4));
      if constexpr (rt < 2) mkq(IC(rt + 2));
#pragma unroll
      for (int h = 0; h < 4; ++h) {
        const unsigned p0 = cvtpk(s[h][0] * bc[h], s[h][1] * bc[h]), p1 = cvtpk(s[h][2] * bc[h], s[h][3] * bc[h]);
        P[h] = u32x4{p0 & mpv[0], p1 & mpv[1], p0 & ~mpv[0], p1 & ~mpv[1]};
        PIN(P[h]);
      }
      asm volatile("s_nop 1" ::: "memory");
    });
    static_for<8>([&](auto I_) { pv(IC(3), IC(CI(I_) >> 1), IC(CI(I_) & 1)); });
    asm volatile("s_nop 7" ::: "memory");
    SBAR();
    outp(IC(3));
  }
  STAMP(21);
  bf16x8 f[16];
  phase2<12>(c, p2, f, [](auto, auto) {});
}

#endif
#define STAMP_OFF (64u << 20)
__global__ __launch_bounds__(256, 1) void fused_kernel(const u16* x, const u16* wqkv, const u16* wo, const u16* wg, const u16* wu, const u16* wd,
                                                        const u16* gw, char* ws, u16* out) {
#if defined(__HIP_DEVICE_COMPILE__)
  __shared__ __attribute__((aligned(16))) char smem[LDS_BYTES];
  asm volatile("" ::"v"((unsigned)(size_t)smem));
  Ctx c;
  STAMP(0);
  const unsigned bid = blockIdx.x, xcd = bid & 7, idx = bid >> 3;
  c.w = __builtin_amdgcn_readfirstlane(threadIdx.x >> 6);
  {
    u64 pc; unsigned len;
    asm volatile("s_getpc_b64 %0\n.Lgm_code0:\n\ts_mov_b32 %1, .Lgm_code1-.Lgm_code0\n\ts_max_u32 %1, %1, .Lgm_code2-.Lgm_code0" : "=s"(pc), "=s"(len) :: "scc");
    const rsrc_t rc = __builtin_amdgcn_make_buffer_rsrc((void*)pc, (short)0, len, 0x00020000);
    if (idx < 16) touch<3 * S1>((threadIdx.x & 15) * 128, rc, idx * 8192 + c.w * 2048);
  }
  asm volatile("" ::"s"(x), "s"(out));
  STAMP(54);
  c.rx = __builtin_amdgcn_make_buffer_rsrc((void*)x, (short)0, SEQ * DM * 2, 0x00020000);
  c.rqkv = __builtin_amdgcn_make_buffer_rsrc((void*)wqkv, (short)0, DM * 3 * DM * 2, 0x00020000);
  c.rwo = __builtin_amdgcn_make_buffer_rsrc((void*)wo, (short)0, DM * DM * 2, 0x00020000);
  c.rwg = __builtin_amdgcn_make_buffer_rsrc((void*)wg, (short)0, DM * FF * 2, 0x00020000);
  c.rwu = __builtin_amdgcn_make_buffer_rsrc((void*)wu, (short)0, DM * FF * 2, 0x00020000);
  c.rwd = __builtin_amdgcn_make_buffer_rsrc((void*)wd, (short)0, DM * FF * 2, 0x00020000);
  c.rgw = __builtin_amdgcn_make_buffer_rsrc((void*)gw, (short)0, DM * 2, 0x00020000);
  c.rws = __builtin_amdgcn_make_buffer_rsrc((void*)ws, (short)0, WS_BYTES, 0x00020000);
  c.rout = __builtin_amdgcn_make_buffer_rsrc((void*)out, (short)0, SEQ * DM * 2, 0x00020000);
  c.ws = (u64)ws;
  c.a = xcd & 3; c.T = 4 * (xcd >> 2) + (idx >> 3);
  c.m = (idx & 3) == 3 ? 6 + ((idx >> 2) & 1) : 3 * ((idx >> 2) & 1) + (idx & 3);
#define ROTM 1
  const unsigned rot = ROTM == 0 ? 0 : ROTM == 1 ? c.m + 2 * (c.T & 3) : ROTM == 2 ? 3 * ((c.m + c.T) % 3) : 4 * ((c.m + c.T) & 1);
  c.kg[0] = ROTM >= 4 ? 0 : rot >= 9 ? rot - 9 : rot;
  XS xs; xs.init(c, c.m < 6 ? M_X : A_X);
  static_for<8>([&](auto I_) { xs.template prep<1, 0, CI(I_)>(c); xs.template fire<CI(I_)>(c); });
  STAMP(55);
  SBAR();
#pragma unroll
  for (int i = 1; i < 9; ++i) {
    if (ROTM == 4) c.kg[i] = 1 + ((i + c.m + 2 * c.T) & 7);
    else if (ROTM == 5) c.kg[i] = i == 1 ? 1 : i == 2 ? 2 + (c.m + 2 * (c.T & 3)) % 7 : c.kg[i - 1] == 8 ? 2 : c.kg[i - 1] + 1;
    else c.kg[i] = c.kg[i - 1] == 8 ? 0 : c.kg[i - 1] + 1;
  }
  if (__builtin_expect(c.m < 6, 1)) role_mlp(c, xs); else role_att(c, xs);
#endif
}

extern "C" void kernel_launch(void* const* d_in, const int* in_sizes, int n_in, void* d_out, int out_size, void* d_ws, size_t ws_size,
                              hipStream_t stream) {
  fused_kernel<<<256, 256, 0, stream>>>((const u16*)d_in[0], (const u16*)d_in[1], (const u16*)d_in[2], (const u16*)d_in[4], (const u16*)d_in[5],
                                        (const u16*)d_in[6], (const u16*)d_in[7], (char*)d_ws, (u16*)d_out);
}
```

```cpp
#define PROBE 1
#include <hip/hip_runtime.h>
#include <utility>

typedef unsigned short u16;
typedef unsigned long long u64;
typedef short s16x4 __attribute__((ext_vector_type(4)));
typedef __bf16 bf16x8 __attribute__((ext_vector_type(8)));
typedef __bf16 bf16x2 __attribute__((ext_vector_type(2)));
typedef float f32x2 __attribute__((ext_vector_type(2)));
typedef float f32x4 __attribute__((ext_vector_type(4)));
typedef unsigned u32x2 __attribute__((ext_vector_type(2)));
typedef unsigned u32x4 __attribute__((ext_vector_type(4)));
typedef unsigned u32x6 __attribute__((ext_vector_type(6)));
typedef unsigned u32x10 __attribute__((ext_vector_type(10)));
typedef __amdgpu_buffer_rsrc_t rsrc_t;
#define LAS __attribute__((address_space(3)))
#define DI __device__ __forceinline__
#define SBAR() __builtin_amdgcn_sched_barrier(0)
#define SNAKE 0
#define SN(t, R) ((SNAKE && ((t) & 1)) ? 3 - (R) : (R))
#define ABL 0
#define DPOL ""
#define IC(x) std::integral_constant<int, (x)>{}
#define CI(x) decltype(x)::value

constexpr int SEQ = 2048, DM = 576, FF = 4608;
constexpr unsigned WS_H = 0;
constexpr unsigned WS_PART = 24u << 20;
constexpr unsigned WS_G = 40u << 20;
constexpr unsigned WS_F1 = 41u << 20;
constexpr unsigned WS_F2 = WS_F1 + 8192;
constexpr unsigned WS_E = WS_F2 + 65536;
constexpr unsigned WS_BYTES = 42u << 20;
constexpr unsigned LDS_BYTES = 163840;
constexpr unsigned S1 = 16384;
constexpr unsigned S2 = 18432;
constexpr unsigned S3 = S2 + 9216;
constexpr unsigned M_X = 65536;
constexpr unsigned A_X = 4 * S2;
constexpr unsigned A_HX = A_X + 65536;
constexpr unsigned A_GW = A_HX + 18432;
constexpr unsigned A_KX = 83968;
constexpr unsigned A_VX = A_KX + 18 * 2560;
static_assert(A_GW + 2048 <= LDS_BYTES && A_VX <= A_HX && A_VX + 20480 <= LDS_BYTES && 3 * S3 <= A_KX, "lds");

#if defined(__HIP_DEVICE_COMPILE__)
template <int... Is> using iseq = std::integer_sequence<int, Is...>;
template <class Fn, int... Is> DI void static_for_(Fn&& f, iseq<Is...>) { (f(std::integral_constant<int, Is>{}), ...); }
template <int N, class Fn> DI void static_for(Fn&& f) { static_for_(f, std::make_integer_sequence<int, N>{}); }

template <int N> DI void wait_bar() { asm volatile("s_waitcnt vmcnt(%0) lgkmcnt(0)\n\ts_barrier" ::"i"(N) : "memory"); }
template <int N> DI void wait_vm() { asm volatile("s_waitcnt vmcnt(%0)" ::"i"(N) : "memory"); }
template <class T> DI void PIN(T& x) { asm volatile("" : "+v"(x)); }
DI void tieA(bf16x8& a, bf16x8& b, bf16x8& c, bf16x8& d) { asm volatile("" : "+a"(a), "+a"(b), "+a"(c), "+a"(d)); }
template <unsigned LO, unsigned SO> DI unsigned dprep(unsigned lb, unsigned sb) {
  unsigned t;
  asm volatile("s_add_u32 m0, %1, %3\n\ts_add_u32 %0, %2, %4" : "=s"(t) : "s"(lb), "s"(sb), "i"(LO), "i"(SO) : "scc");
  return t;
}
template <unsigned LO> DI void dprep_l(unsigned lb) { asm volatile("s_add_u32 m0, %0, %1\n\ts_nop 0" ::"s"(lb), "i"(LO) : "scc"); }
template <unsigned IMM> DI void dfire(unsigned voff, rsrc_t srd, unsigned so) {
  asm volatile("buffer_load_dwordx4 %0, %1, %2 offen offset:%3" DPOL " lds" ::"v"(voff), "s"(srd), "s"(so), "i"(IMM) : "memory");
}
DI void dfire_m(unsigned voff, rsrc_t srd, unsigned so, u64 mask) {
  asm volatile("s_mov_b64 exec, %3\n\tbuffer_load_dwordx4 %0, %1, %2 offen" DPOL " lds\n\ts_mov_b64 exec, -1" ::"v"(voff), "s"(srd), "s"(so), "s"(mask) : "memory");
}
template <unsigned IMM> DI void ldh(bf16x8& d, unsigned voff, rsrc_t srd, unsigned so) {
  if constexpr (ABL & 4) asm volatile("" : "=a"(d)); else
  asm volatile("buffer_load_dwordx4 %0, %1, %2, %3 offen offset:%4 sc1" : "=a"(d) : "v"(voff), "s"(srd), "s"(so), "i"(IMM) : "memory");
}
DI void ldg(f32x4& d, unsigned voff, rsrc_t srd, unsigned so) { asm volatile("buffer_load_dwordx4 %0, %1, %2, %3 offen sc1" : "=v"(d) : "v"(voff), "s"(srd), "s"(so) : "memory"); }
template <unsigned LA> DI void touch(unsigned voff, rsrc_t srd, unsigned so) {
  asm volatile("s_mov_b32 m0, %3\n\ts_nop 0\n\tbuffer_load_dword %0, %1, %2 offen sc1 lds" ::"v"(voff), "s"(srd), "s"(so), "i"(LA) : "memory");
}
DI void st16(const u32x4& v, rsrc_t srd, unsigned voff, unsigned so) {
  __builtin_amdgcn_raw_buffer_store_b128(v, srd, voff, so, 16);
  asm volatile("s_nop 1" ::"v"(v) : "memory");
}
DI void keep(const u32x4& v) { asm volatile("" ::"v"(v)); }
template <bool AG, bool ZERO> DI void mf(f32x4& c, const bf16x8& a, const bf16x8& b) {
  if constexpr (ZERO) {
    if constexpr (AG) asm volatile("v_mfma_f32_16x16x32_bf16 %0, %1, %2, 0" : "=&v"(c) : "v"(a), "a"(b));
    else asm volatile("v_mfma_f32_16x16x32_bf16 %0, %1, %2, 0" : "=&v"(c) : "v"(a), "v"(b));
  } else {
    if constexpr (AG) asm volatile("v_mfma_f32_16x16x32_bf16 %0, %1, %2, %0" : "+v"(c) : "v"(a), "a"(b));
    else asm volatile("v_mfma_f32_16x16x32_bf16 %0, %1, %2, %0" : "+v"(c) : "v"(a), "v"(b));
  }
}
template <bool AG, bool ZERO> DI void mfs(f32x4& c, const bf16x8& a, const bf16x8& b) {
  if constexpr (ZERO) {
    if constexpr (AG) asm volatile("v_mfma_f32_16x16x32_bf16 %0, %2, %1, 0" : "=&v"(c) : "v"(a), "a"(b));
    else asm volatile("v_mfma_f32_16x16x32_bf16 %0, %2, %1, 0" : "=&v"(c) : "v"(a), "v"(b));
  } else {
    if constexpr (AG) asm volatile("v_mfma_f32_16x16x32_bf16 %0, %2, %1, %0" : "+v"(c) : "v"(a), "a"(b));
    else asm volatile("v_mfma_f32_16x16x32_bf16 %0, %2, %1, %0" : "+v"(c) : "v"(a), "v"(b));
  }
}
DI f32x4 mfc(const u32x4& a, const u32x4& b, const f32x4& c) { f32x4 d; asm volatile("v_mfma_f32_16x16x32_bf16 %0, %1, %2, %3" : "=&v"(d) : "a"(a), "v"(b), "v"(c)); return d; }
template <class T> DI void PINA(T& x) { asm volatile("" : "+a"(x)); }
DI f32x4 mfz(const u32x4& a, const u32x4& b) { f32x4 d; asm volatile("v_mfma_f32_16x16x32_bf16 %0, %1, %2, 0" : "=&v"(d) : "v"(a), "v"(b)); return d; }
DI float max2(float a, float b) { float r; asm("v_max_f32 %0, %1, %2" : "=v"(r) : "v"(a), "v"(b)); return r; }
DI float max3(float a, float b, float c) { float r; asm("v_max3_f32 %0, %1, %2, %3" : "=v"(r) : "v"(a), "v"(b), "v"(c)); return r; }
DI s16x4 trd(unsigned a) { return __builtin_amdgcn_ds_read_tr16_b64_v4i16((LAS s16x4*)a); }
DI bf16x8 cat(s16x4 lo, s16x4 hi) { return __builtin_bit_cast(bf16x8, __builtin_shufflevector(lo, hi, 0, 1, 2, 3, 4, 5, 6, 7)); }
DI unsigned cvtpk(float lo, float hi) { f32x2 v = {lo, hi}; return __builtin_bit_cast(unsigned, __builtin_convertvector(v, bf16x2)); }
DI float w_mul(float a) { if constexpr (ABL & 32) { asm volatile("" :: "v"(a)); return a; } float r; asm volatile("v_mul_f32 %0, 0xbfb8aa3b, %1" : "=v"(r) : "v"(a)); return r; }
DI float w_exp(float a) { if constexpr (ABL & 32) return a; float r; asm volatile("v_exp_f32 %0, %1" : "=v"(r) : "v"(a)); return r; }
DI unsigned w_add1(float a) { if constexpr (ABL & 32) return 0; unsigned r; asm volatile("v_fma_mixlo_f16 %0, %1, 1.0, 1.0" : "=v"(r) : "v"(a)); return r; }
DI unsigned w_rcplo(unsigned a) { if constexpr (ABL & 32) return 0x38003800u; unsigned r; asm volatile("v_rcp_f16 %0, %1" : "=v"(r) : "v"(a)); return r; }
DI void w_rcphi(unsigned& d, unsigned a) { if constexpr (ABL & 32) return; asm volatile("v_rcp_f16_sdwa %0, %1 dst_sel:WORD_1 dst_unused:UNUSED_PRESERVE src0_sel:WORD_0" : "+v"(d) : "v"(a)); }
DI unsigned w_pklo(float a, float k) { unsigned r; asm volatile("v_fma_mixlo_f16 %0, %1, %2, 0" : "=v"(r) : "v"(a), "s"(k)); return r; }
DI void w_pkhi(unsigned& d, float a, float k) { asm volatile("v_fma_mixhi_f16 %0, %1, %2, 0" : "+v"(d) : "v"(a), "s"(k)); }
template <int H> DI void w_exph(unsigned& d) { if constexpr (ABL & 32) return;
  if constexpr (H == 0) asm volatile("v_exp_f16_sdwa %0, %0 dst_sel:WORD_0 dst_unused:UNUSED_PRESERVE src0_sel:WORD_0" : "+v"(d));
  else asm volatile("v_exp_f16_sdwa %0, %0 dst_sel:WORD_1 dst_unused:UNUSED_PRESERVE src0_sel:WORD_1" : "+v"(d));
}
template <int H> DI void w_rcph(unsigned& d) { if constexpr (ABL & 32) return;
  if constexpr (H == 0) asm volatile("v_rcp_f16_sdwa %0, %0 dst_sel:WORD_0 dst_unused:UNUSED_PRESERVE src0_sel:WORD_0" : "+v"(d));
  else asm volatile("v_rcp_f16_sdwa %0, %0 dst_sel:WORD_1 dst_unused:UNUSED_PRESERVE src0_sel:WORD_1" : "+v"(d));
}
DI void w_add1h(unsigned& d) { asm volatile("v_pk_add_f16 %0, %0, 1.0 op_sel_hi:[1,0]" : "+v"(d)); }
DI float w_mixlo(float u, unsigned s) { float r; asm volatile("v_fma_mix_f32 %0, %1, %2, 0 op_sel_hi:[0,1,0]" : "=v"(r) : "v"(u), "v"(s)); return r; }
DI float w_mixhi(float u, unsigned s) { float r; asm volatile("v_fma_mix_f32 %0, %1, %2, 0 op_sel:[0,1,0] op_sel_hi:[0,1,0]" : "=v"(r) : "v"(u), "v"(s)); return r; }
DI unsigned w_cvt(float lo, float hi) { unsigned r; asm volatile("v_cvt_pk_bf16_f32 %0, %1, %2" : "=v"(r) : "v"(lo), "v"(hi)); return r; }
DI float w_scale(float a, float s) { float r; asm volatile("v_mul_f32 %0, %1, %2" : "=v"(r) : "v"(a), "v"(s)); return r; }
DI float bflo(unsigned v) { return __uint_as_float(v << 16); }
DI float bfhi(unsigned v) { return __uint_as_float(v & 0xffff0000u); }
template <class T> DI T ldsr(unsigned a) { return *(const LAS T*)a; }
template <class T> DI void ldsw(unsigned a, T v) { *(LAS T*)a = v; }
DI unsigned lane_id() { unsigned l; asm volatile("v_mbcnt_lo_u32_b32 %0, -1, 0\n\tv_mbcnt_hi_u32_b32 %0, -1, %0" : "=v"(l)); return l; }

template <int N> DI void STUB() { asm volatile(".Lgm_s%c0:\n\ts_cbranch_execz .Lgm_ret" ::"i"(N)); }
template <int N> DI void visit(u64 pc0) {
  asm volatile("s_add_u32 s98, %0, .Lgm_s%c2-.Lgm_code0\n\ts_addc_u32 s99, %1, 0\n\ts_mov_b64 exec, 0\n\ts_swappc_b64 s[100:101], s[98:99]\n\ts_mov_b64 exec, -1"
               ::"s"((unsigned)pc0), "s"((unsigned)(pc0 >> 32)), "i"(N) : "s98", "s99", "s100", "s101", "scc", "memory");
}
template <int B, int CNT> DI void visit_all(u64 pc0, unsigned w) {
  static_for<4>([&](auto W_) { if (w == CI(W_)) static_for<(CNT + 3 - CI(W_)) / 4>([&](auto I_) { visit<B + CI(W_) + 4 * CI(I_)>(pc0); }); });
}
DI u64 rt_() { u64 t; asm volatile("s_memrealtime %0\n\ts_waitcnt lgkmcnt(0)" : "=s"(t) :: "memory"); return t; }
#define STAMP(i) do { if constexpr ((i) == 0) c.t0 = rt_(); if constexpr ((i) == PROBE) c.t1 = rt_(); } while (0)

struct Ctx {
  rsrc_t rx, rqkv, rwo, rwg, rwu, rwd, rgw, rws, rout;
  unsigned w, a, T, m;
  unsigned kg[9];
  u64 E, ws, pc0, t0, t1;
};

enum { O_NONE, O_W, O_X, O_HX, O_FIN, O_POLL, O_D2, O_B2, O_GATE, O_ST, O_TW, O_TL, O_PZ };
constexpr int NEVER = 1 << 20;
struct Op { int kind, a, b, need; };
constexpr int PD = 3;
#define MLPAD 3
constexpr int MLP_AD = MLPAD, ATT_AD = 3;
constexpr Op NOP_ = {O_NONE, 0, 0, 0};
constexpr int uk(int U, int NT) { const int o = NT == 8 ? 12 : 6; return U < o ? U : U < o + 3 ? U - o + 18 : U - 3; }
constexpr int dep(int U, int NT) { return U == 15 ? 2 : NT == 8 ? (U == 6 ? 1 : U == 12 ? 3 : -1) : (U == 6 ? 3 : U == 9 ? 1 : -1); }
constexpr int FLG2 = 4, FLGA = 1;
constexpr int RS = 19;
constexpr Op p2_op(int N, int R, int NT, int AD, int FB) {
  const int ND = NT == 8 ? 4 : 6, RG = (N + PD) / NT, pos = (N + PD) % NT, U = RG + AD;
  if (RG > 20 || pos == 0) return NOP_;
  int k = -1;
  if (RG == 0) k = 2 * (pos - PD) + (R - 2);
  else if (NT == 8 && RG == FLG2 - 1) k = 2 * (pos - 1) + (R - 2);
  else if (NT == 8) k = R == 2 ? (pos == 1 ? 0 : pos) : pos == 1 ? 1 : pos == 7 ? 8 : -1;
  else k = R == 2 ? pos - 1 : -1;
  if (NT != 8 && R == 3 && pos == 1 && dep(U + 1, NT) >= 0) return {O_POLL, dep(U + 1, NT), 0, FB + (RG + 1) * NT - PD};
  if (k < 0) return NOP_;
  if (U <= 20 && k < ND) return {O_D2, U, k, FB + U * NT - PD};
  if (U <= 20 && k < ND + 4) return {O_B2, U, k - ND, FB + U * NT - PD};
  if (NT == 8 && dep(U + 2, NT) >= 0 && k == ND + 4) return {O_POLL, dep(U + 2, NT), 0, FB + (RG + 2) * NT - PD};
  if (RG == 14 && k == ND + 4) return {O_POLL, 4, 0, FB + 17 * NT - PD};
  if (RG == 15 && k == ND + 4) return {O_GATE, 0, 0, FB + 18 * NT - PD};
  return NOP_;
}
constexpr int FIN0 = 36, ST0 = 72, FLG0 = 18, FLG1 = 25;
#define PZR 16
constexpr int lastfin(int N) { return N == 1 ? 0 : N == 3 ? 1 : N == 7 ? 2 : N == 8 ? 3 : N == 10 ? 4 : N == 11 ? 5 : N == 15 ? 6 : N == 16 ? 7 : -1; }
constexpr int bar1(int n) { return n == 0 ? -1 : 16 * n - PD; }
constexpr Op opW(int n, int i) { return {O_W, n, i, bar1(n)}; }
constexpr int XR = 10;
constexpr Op opX(int c, int i) { return {O_X, c, i, c == 0 ? -1 : 16 * (c - 1) + XR}; }
constexpr Op mlp_op(int F, int R) {
  if (F < 0) {
    const int i = F + 15;
    if (R != 2 || i < 0 || i >= 13) return NOP_;
    return i < 8 ? opX(0, i) : i < 12 ? opW(0, i - 8) : Op{O_TL, 0, 0, bar1(1)};
  }
  if (F >= 440) {
    const int N = F - 440;
    if (N < 5) return R == 2 ? Op{N < 4 ? O_D2 : O_B2, MLP_AD, N & 3, 440 + MLP_AD * 8 - PD} : N == 0 || N == 2 || N == 4 ? Op{O_B2, MLP_AD, N / 2 + 1, 440 + MLP_AD * 8 - PD} : Op{O_FIN, 2, N / 2, 440 + FLG2 * 8 - PD};
    if (R == 3 && N < 20 && lastfin(N) >= 0) return {O_FIN, 2, lastfin(N), 440 + FLG2 * 8 - PD};
    return p2_op(N, R, 8, MLP_AD, 440);
  }
  if (F >= 432) return NOP_;
  const int RG = (F + PD) / 16, pos = (F + PD) % 16, r = R - 2;
  if (pos == 0) return NOP_;
  if (F < 4) return opX(1, 2 * F + r);
  if (F < 6) return opW(1, 2 * (F - 4) + r);
  if (R == 3) return NOP_;
  if (RG <= 5) {
    const int n = F - 6 - RG, c = 2 + n / 12, i = n % 12;
    const int cap = RG + 3;
    if (RG <= 3) { if (c <= cap) return i < 8 ? opX(c, i) : opW(c, i - 8); return NOP_; }
    if (RG == 4 && pos <= 8) return pos <= 4 ? opX(6, pos + 3) : opW(6, pos - 5);
    if (RG == 4) return opX(7, pos - 9);
    if (pos == 1) return opX(7, 7);
    if (pos <= 5) return opW(7, pos - 2);
    return pos <= 13 ? opX(8, pos - 6) : opW(8, pos - 14);
  }
  if (RG == 6 && pos <= 2) return opW(8, pos + 1);
  const int k8 = pos >= 3 && pos < 15 && (pos - 3) % 3 != 2 ? (pos - 3) - (pos - 3) / 3 : -1;
  if (RG >= 6 && RG <= 8 && k8 >= 0) return opW(9 + 2 * (RG - 6) + k8 / 4, k8 % 4);
  if (F >= 144 + ST0 && F < 144 + ST0 + 32 && F % 4 == 2) return {O_FIN, 0, (F - 144 - ST0) / 4, bar1(FLG0)};
  if (F >= 288 + ST0 && F < 288 + ST0 + 16 && F % 2 == 0) return {O_FIN, 1, (F - 288 - ST0) / 2, bar1(FLG1)};
  if (RG >= 9 && RG <= 12 && (pos == 1 || pos == 15)) return {O_TW, 2 * (RG - 9) + (pos == 15), 0, NEVER};
  if (RG >= 9 && RG <= 10 && k8 >= 0) return opW(15 + 2 * (RG - 9) + k8 / 4, k8 % 4);
  if (RG >= 11 && RG <= 12 && pos % 4 == 2) return opW(RG + 8, pos / 4);
  if (RG == 13 && pos <= 4) return opW(21, pos - 1);
  if (RG >= 18 && RG <= 19 && k8 >= 0) return opW(22 + 2 * (RG - 18) + k8 / 4, k8 % 4);
  if (RG == 20 && k8 >= 0) return k8 < 4 ? opW(26, k8) : Op{O_D2, 0, k8 - 4, bar1(27)};
  if (RG >= 21 && RG <= 22 && pos <= 4) return {O_D2, RG - 20, pos - 1, 440 + (RG - 20) * 8 - PD};
  if (RG == PZR && pos % 2 == 1 && pos < 12) return {O_PZ, pos / 2, 0, bar1(FLG0)};
  if (RG == 22 && pos == 7) return {O_POLL, 0, 0, bar1(25)};
  if (RG >= 25 && RG <= 26) {
    const int k = (RG - 25) * 15 + pos - 1;
    if (k < 24 && k % 2 == 0) { const int U = k / 8; return {O_B2, U, (k / 2) % 4, U == 0 ? bar1(27) : 440 + U * 8 - PD}; }
    if (MLP_AD > 3 && k < 16 && k % 2 == 1) { const int i = k / 2; return i < 4 ? Op{O_D2, 3, i, 440 + 24 - PD} : NOP_; }
    if (MLP_AD > 3 && k >= 24 && k < 28) return {O_B2, 3, k - 24, 440 + 24 - PD};
  }
  return NOP_;
}
static_assert(MLP_AD == 3 || MLP_AD == 4, "pre-issue table");
constexpr int barA(int n) { return n == 0 ? -1 : n <= 9 ? 16 * n - PD : 144 + 14 * (n - 9) - PD; }
constexpr int rgA(int F) { return F + PD < 144 ? (F + PD) / 16 : 9 + (F + PD - 144) / 14; }
constexpr int posA(int F) { return F + PD < 144 ? (F + PD) % 16 : (F + PD - 144) % 14; }
struct AttSeq {
  Op op[270] = {};
  constexpr AttSeq() {
    constexpr int seq[27][2] = {{O_X, 2}, {O_W, 2}, {O_HX, 1}, {O_X, 3}, {O_W, 3}, {O_HX, 2}, {O_W, 4}, {O_X, 4}, {O_HX, 3}, {O_HX, 4}, {O_W, 5}, {O_X, 5}, {O_W, 6}, {O_X, 6},
                                {O_W, 7}, {O_X, 7}, {O_W, 8}, {O_X, 8}, {O_W, 9}, {O_W, 10}, {O_W, 11}, {O_W, 12}, {O_W, 13}, {O_W, 14}, {O_W, 15}, {O_W, 16}, {O_W, 17}};
    int F = 6;
    for (int e = 0; e < 27; ++e) {
      const int k = seq[e][0], a = seq[e][1], n = k == O_X ? 8 : k == O_W ? 4 : 1, rg = k == O_X ? a - 2 : k == O_W ? a - 3 : 0;
      for (int i = 0; i < n; ++i) {
        while (posA(F) == 0 || rgA(F) < rg || (F >= 138 && F < 148)) ++F;
        op[F++] = k == O_X ? opX(a, i) : k == O_W ? Op{O_W, a, i, barA(a)} : Op{O_HX, a, 0, barA(a < 3 ? 2 * a : 5)};
      }
    }
    int z = 0;
    for (int G = 150; G < 270; ++G) {
      const int RG = rgA(G), pos = posA(G);
      if (RG >= 10 && RG <= 14 && (pos == 7 || pos == 10) && z < 9) op[G] = {O_PZ, z++, 0, barA(16)};
      if (RG == 16 && pos >= 2 && pos < 8) op[G] = {O_D2, 0, pos - 2, NEVER};
    }
  }
};
constexpr AttSeq ATT_SEQ{};
constexpr Op att1_op(int F, int R) {
  if (F < 0) {
    const int i = F + 16;
    if (R != 2 || i < 0 || i >= 14) return NOP_;
    return i < 8 ? opX(0, i) : i < 12 ? Op{O_W, 0, i - 8, -1} : i == 12 ? Op{O_HX, 0, 0, -1} : Op{O_TL, 0, 0, NEVER};
  }
  if (F < 4) return opX(1, 2 * F + R - 2);
  if (F < 6) return {O_W, 1, 2 * (F - 4) + R - 2, barA(1)};
  return R == 2 && F < 270 ? ATT_SEQ.op[F] : NOP_;
}
constexpr Op att2_op(int F, int R) {
  if (F < 0) {
    const int i = F + 50;
    if (R != 2 || i < 0 || i >= 43) return NOP_;
    constexpr int n1 = 12 - PD, n2 = 24 - PD, ns = FLGA * 12 - PD;
    if (i < 6) return {O_D2, 0, i, -1};
    if (i == 6) return {O_POLL, 0, 0, -28};
    if (i < 13) return {O_D2, 1, i - 7, n1};
    if (i < 16) return {O_ST, 0, 0, ns};
    if (i < 22) return {O_D2, 2, i - 16, n2};
    if (i < 25) return {O_ST, 0, 0, ns};
    if (i < 29) return {O_B2, 0, i - 25, -1};
    if (i < 33) return {O_B2, 1, i - 29, n1};
    if (i < 36) return {O_ST, 0, 0, ns};
    if (i < 40) return {O_B2, 2, i - 36, n2};
    return {O_ST, 0, 0, ns};
  }
  return p2_op(F, R, 12, ATT_AD, 0);
}
template <int WHICH> constexpr Op op_at(int F, int R) { return WHICH == 0 ? mlp_op(F, R) : WHICH == 1 ? att1_op(F, R) : att2_op(F, R); }
template <int WHICH> constexpr int vm_at(int tau) {
  int s = 0;
  for (int F = tau - 1; F >= -64; --F)
    for (int R = 3; R >= 2; --R) {
      const Op o = op_at<WHICH>(F, R);
      if (o.kind == O_NONE) continue;
      if (o.need <= tau || s >= 63) return s;
      ++s;
    }
  return s;
}
template <int WHICH, int TAU> constexpr int VM = vm_at<WHICH>(TAU);

DI void load_epoch(Ctx& c) {
  asm volatile("s_load_dwordx2 %0, %1, %2" : "=s"(c.E) : "s"(c.ws), "s"(WS_E + c.T * 128) : "memory");
}
DI void flag_store(const Ctx& c, unsigned off, unsigned val) {
  const u64 v = c.E + val;
  __builtin_amdgcn_raw_buffer_store_b64(u32x2{(unsigned)v, (unsigned)(v >> 32)}, c.rws, 0, off, 16);
}
DI void poll_issue(const Ctx& c, u32x2& f, unsigned voff, unsigned off) {
  asm volatile("buffer_load_dwordx2 %0, %1, %2, %3 offen sc1" : "=v"(f) : "v"(voff), "s"(c.rws), "s"(off) : "memory");
}
DI bool flag_ok(const Ctx& c, u32x2 f, unsigned need) {
  const u64 d = (((u64)f[1] << 32) | f[0]) - c.E - need;
  return __builtin_amdgcn_ballot_w64(d >= 8) == 0;
}
DI void poll_check(const Ctx& c, u32x2& f, unsigned voff, unsigned off, unsigned need) {
  asm volatile("" : "+v"(f));
  if (__builtin_expect(!flag_ok(c, f, need), 0)) {
    for (unsigned spin = 0; spin < (1u << 16); ++spin) {
      __builtin_amdgcn_s_sleep(1);
      asm volatile("buffer_load_dwordx2 %0, %1, %2, %3 offen sc1\n\ts_waitcnt vmcnt(0)" : "=&v"(f) : "v"(voff), "s"(c.rws), "s"(off) : "memory");
      if (flag_ok(c, f, need)) break;
    }
  }
}

constexpr unsigned POISON = 0xffffffffu;
DI unsigned inbox(const Ctx& c) { return WS_PART + ((c.T * 8 + c.m) * 64 + c.w) * 3072; }
DI unsigned flag2(const Ctx& c) { return WS_F2 + ((c.T * 8 + c.m) * 4 + c.w) * 128; }
template <int NI, int J> DI void poison(const Ctx& c, u32x4& pz, unsigned vl16) {
  if constexpr (J == 0) { pz = u32x4{POISON, POISON, POISON, POISON}; PIN(pz); }
  asm volatile("buffer_store_dwordx4 %0, %1, %2, %3 offen offset:%4 sc1" ::"v"(pz), "v"(vl16), "s"(c.rws), "s"(inbox(c) + (c.a * 4 + ((J / NI + 1) ^ c.a)) * 12288), "i"((J % NI) * 1024) : "memory");
}
template <int NT> struct P2 {
  static constexpr int AD = NT == 8 ? MLP_AD : ATT_AD, NS = NT == 8 ? 8 : 4, FB = NT == 8 ? 440 : 0, ROT = NT == 8 ? 4 : 0;
  static constexpr unsigned SS = NT == 8 ? S2 : S3;
  static constexpr int sidx(int u) { return NT == 8 ? (u + ROT) % NS : u % 4 == 0 ? 2 : u % 4 == 3 ? 3 : u % 4 - 1; }
  static constexpr unsigned slot(int u) { return sidx(u) * SS; }
  static constexpr int WN = NT == 8 ? 3 : 2;
  static constexpr unsigned win(int u) { return (sidx(u) / WN * WN) * SS; }
  unsigned v128[2], v64, vh[2], vf;
  unsigned lw, lw64, sd, so, sh[2];
  unsigned f1, cur, curh;
  bf16x8 bf[21][4];
  u32x2 pf;
  f32x4 omg;
  DI void init(const Ctx& c) {
    const unsigned l = lane_id(), colb = NT == 8 ? 256 * (c.m >> 1) : 768, kh = c.w >> 1;
    const unsigned lo = 16 * ((l & 15) ^ ((l >> 3) & 2) ^ (4 * (c.a ^ 3)));
    v128[0] = (l >> 4) * 4608 + lo; v128[1] = (l >> 4) * 4608 + (lo ^ 64);
    v64 = (l >> 3) * 2304 + 16 * ((l & 7) ^ (((l >> 1) & 4) | ((l >> 3) & 2)) ^ (2 * (c.a ^ 3))) + 256;
    vh[0] = l * 16 + kh * 2048; vh[1] = l * 16 + (1 - kh) * 2048;
    vf = 8 * (l & 7);
    lw = c.w * 4608; lw64 = c.w * 2304 + S2;
    sd = (1152 * c.a + 16 * c.w) * 1152 + colb;
    so = (144 * c.a + 72 * kh + 16 * (c.w & 1)) * 1152 + colb;
    sh[0] = WS_H + (((4 * c.T + 2 * (c.m & 1) + (c.w & 1)) * 4 + c.a) * 42 + kh) * 4096; sh[1] = sh[0] + kh * 8192;
    f1 = WS_F1 + (c.T * 4 + c.a) * 128;
  }
  template <int S> DI unsigned pvo() const { return S < 3 ? (vf < 48 ? vf : 0) : S == 3 ? 48 + (vf & 8) : vf & 24; }
  template <int S> DI void poll(const Ctx& c) { poll_issue(c, pf, pvo<S>(), S < 4 ? f1 : flag2(c)); }
  template <int S> DI void check(const Ctx& c) { poll_check(c, pf, pvo<S>(), S < 4 ? f1 : flag2(c), S < 3 ? S + 1 : S == 3 ? 3 : 1); }
  template <int KIND, int U, int I> DI void prep(const Ctx& c) {
    if constexpr (KIND == O_D2) {
      constexpr int K = uk(U, NT);
      constexpr unsigned row = K < 18 ? 64 * K : 32 * (K - 18);
      if constexpr (I == 0) cur = dprep<slot(U), row * 1152>(lw, K < 18 ? sd : so);
      if constexpr (I == 4) dprep_l<slot(U)>(lw64);
    }
    if constexpr (KIND == O_B2 && U < RS && (I == 0 || (NT == 8 && U == AD && I == 1))) { curh = sh[uk(U, NT) >= 18] + (uk(U, NT) < 18 ? 2 * uk(U, NT) : 18 + uk(U, NT)) * 4096; asm volatile("" : "+s"(curh)); }
    if constexpr (KIND == O_B2 && U >= RS && (I & 1) == 0) { curh = sh[0] - (c.w >> 1) * 4096 + (2 * uk(U, NT) + (I >> 1)) * 4096; asm volatile("" : "+s"(curh)); }
  }
  template <int KIND, int U, int I> DI void fire(const Ctx& c) {
    if constexpr (KIND == O_D2 && !(ABL & 8)) dfire<(I & 3) * 1152>(I < 4 ? v128[I >> 1] : v64, uk(U, NT) < 18 ? c.rwd : c.rwo, cur);
    if constexpr (KIND == O_B2) ldh<(I & 1) * 1024>(bf[U][I], vh[U < RS ? I >> 1 : 0], c.rws, curh);
    if constexpr (KIND == O_POLL) poll<U>(c);
    if constexpr (KIND == O_GATE) ldg(omg, vh[0] & 255, c.rws, WS_G + (c.a * 32 + 4 * c.T + 2 * (c.m & 1) + (c.w & 1)) * 256);
  }
  static constexpr int tp(int t) { return NT == 8 || t < 6 ? t : t < 9 ? t + 2 : t < 11 ? t - 3 : 11; }
  static DI unsigned rbase(const Ctx& c, unsigned l, int t) {
    const unsigned g = l >> 4, q = (l >> 2) & 3;
    return (c.w >> 1) * 9216 + q * 1152 + g * 256 + 32 * (t ^ ((q & 2) | (g & 1))) + 8 * (l & 3);
  }
};

template <int NT, class Extra> DI void phase2(Ctx& c, P2<NT>& p, bf16x8 (&f)[16], Extra&& extra) {
  using P = P2<NT>;
  constexpr int AD = P::AD, WH = NT == 8 ? 0 : 2;
  const unsigned l = lane_id(), g = l >> 4, q = (l >> 2) & 3, kh = c.w >> 1;
  unsigned rb[4], rb64[4];
#pragma unroll
  for (int t = 0; t < 4; ++t) {
    rb[t] = P::win(0) + P::rbase(c, l, t); PIN(rb[t]);
    if (NT != 8) { rb64[t] = P::win(0) + S2 + kh * 4608 + (q & 1) * 1152 + (2 * g + (q >> 1)) * 128 + 32 * (t ^ ((q & 2) | (g & 1))) + 8 * (l & 3); PIN(rb64[t]); }
  }
  f32x4 acc[NT][4], oth[NT][2];
  bf16x8 f2[NT];
  s16x4 flo;
  auto xoff = [](int t) { return NT == 8 ? 20480 + t * 8192 : t < 6 ? t * 8192 : t < 9 ? (t - 6) * 8192 : 28672 + (t - 9) * 8192; };
  unsigned wa[2] = {0, 0}, ra[2] = {0, 0}, l16 = 0;
  const unsigned pb = inbox(c);
  auto rd = [&](auto U_, auto T_, auto H_) {
    constexpr int t = P::tp(CI(T_)), H = CI(H_);
    constexpr unsigned o = P::slot(CI(U_)) - P::win(CI(U_));
    return t < 8 ? trd(rb[t & 3] + o + 128 * (t >> 2) + H * 4608) : trd(rb64[t & 3] + o + H * 2304);
  };
  if constexpr (NT != 8) {
    wait_bar<VM<2, -1>>();
    STAMP(24);
    tieA(p.bf[0][0], p.bf[0][1], p.bf[0][2], p.bf[0][3]);
    static_for<PD>([&](auto T_) { f[CI(T_)] = cat(rd(IC(0), T_, IC(0)), rd(IC(0), T_, IC(1))); });
  }
  auto send = [&](auto T_, auto R_) {
    constexpr int t = CI(T_), R = CI(R_), it = P::tp(t), dq = it < 8 ? (it >> 1) ^ 3 : (it - 8) ^ 3, idx = it < 8 ? it & 1 : 2;
    if constexpr (R < 2) { acc[t][R] += oth[t][R]; PIN(acc[t][R]); }
    if constexpr (R == 2 && dq != 0) {
      const u32x4 v = {cvtpk(acc[t][0][0], acc[t][0][1]), cvtpk(acc[t][0][2], acc[t][0][3]), cvtpk(acc[t][1][0], acc[t][1][1]), cvtpk(acc[t][1][2], acc[t][1][3])};
      st16(__builtin_elementwise_min(v, u32x4{POISON - 1, POISON - 1, POISON - 1, POISON - 1}), c.rws, l16 + idx * 1024, pb + ((dq ^ c.a) * 4 + c.a) * 12288);
    }
  };
  static_for<21>([&](auto U_) {
    static_for<NT>([&](auto T_) {
      constexpr int U = CI(U_), t = CI(T_), N = U * NT + t, UN = (N + PD) / NT, tn = (N + PD) % NT;
      constexpr int RR = tn == 0 ? 2 : 0;
      const bf16x8 a = f[t], a2 = f2[t];
      static_for<4>([&](auto R_) {
        constexpr int R = CI(R_);
        if constexpr (U < RS) mf<true, U == 0>(acc[t][R], a, p.bf[U][R]); else mf<true, false>(acc[t][R & 1], R < 2 ? a : a2, p.bf[U][R]);
        SBAR();
        if constexpr (tn == 0 && UN <= 20) {
          if constexpr (R < 2 && (P::win(UN) != P::win(UN - 1) || UN == RS)) {
            const unsigned d = P::win(UN) - P::win(UN - 1);
#pragma unroll
            for (int i = 2 * R; i < 2 * R + 2; ++i) {
              rb[i] += d - (UN == RS ? kh * 9216 : 0); PIN(rb[i]);
              if (NT != 8) { rb64[i] += d - (UN == RS ? kh * 4608 : 0); PIN(rb64[i]); }
            }
          }
          if constexpr (R == 2) {
            if constexpr (NT == 8 && UN % 5 == 1) STUB<13 + UN / 5>();
            if constexpr (NT == 8 && UN == 20) STUB<17>();
            if constexpr (NT != 8 && UN % 3 == 2) STUB<46 + UN / 3>();
            wait_bar<(UN == (NT == 8 ? FLG2 : FLGA) ? 0 : VM<WH, P::FB + N>)>();
            STAMP(UN + (NT == 8 ? 28 : 24));
            tieA(p.bf[UN][0], p.bf[UN][1], p.bf[UN][2], p.bf[UN][3]);
          }
        }
        if constexpr (UN < RS || (UN <= 20 && tn != 0)) {
          if constexpr (R == RR) flo = rd(IC(UN), IC(tn), IC(0));
          if constexpr (R == RR + 1) f[tn] = cat(flo, rd(IC(UN), IC(tn), IC(1)));
          if constexpr (UN >= RS && R == 2) flo = rd(IC(UN), IC(tn), IC(2));
          if constexpr (UN >= RS && R == 3) f2[tn] = cat(flo, rd(IC(UN), IC(tn), IC(3)));
        } else if constexpr (UN <= 20) {
          if constexpr (R == 2) f[0] = cat(rd(IC(UN), IC(0), IC(0)), rd(IC(UN), IC(0), IC(1)));
          if constexpr (R == 3) f2[0] = cat(rd(IC(UN), IC(0), IC(2)), rd(IC(UN), IC(0), IC(3)));
        }
        SBAR();
        if constexpr (tn == 0 && R == 3 && UN <= 20) {
          if constexpr (UN == (NT == 8 ? FLG2 : FLGA)) { if (threadIdx.x == 0) flag_store(c, p.f1 + 8 * c.m, 3); }
          if constexpr (dep(UN + AD, NT) >= 0) p.template check<dep(UN + AD, NT)>(c);
          if constexpr (UN == 17) p.template check<4>(c);
          if constexpr (UN == 18) {
            PIN(p.omg);
            l16 = lane_id() * 16; PIN(l16);
            wa[0] = l16 + c.w * 2048 + (NT == 8 ? 0 : 4 * S3); wa[1] = l16 + c.w * 2048 + (NT == 8 ? 32768 : 0); PIN(wa[0]); PIN(wa[1]);
          }
          if constexpr (UN == 20) { const unsigned d = kh ? 0u - 4096 : 4096; ra[0] = wa[0] + d; ra[1] = wa[1] + d; PIN(ra[0]); PIN(ra[1]); }
        }
        extra(IC(N), R_);
        {
          constexpr int gi = N - (RS - 1) * NT - 2, wi = gi < 0 ? -1 : gi < 5 ? (R < 2 ? 2 * gi + R : -1) : (R == 0 ? gi + 5 : -1);
          if constexpr (wi >= 0 && wi < 2 * NT) {
            constexpr int tt = wi / 2, hi = NT == 8 ? tt >= 4 : tt >= 6;
            ldsw<f32x4>(wa[hi] + xoff(tt) - (NT == 8 && hi ? 32768 : 0) + (wi & 1) * 1024, acc[tt][2 + (wi & 1)]);
          }
          constexpr int rt_ = N - (20 * NT - 2);
          if constexpr (rt_ >= 0 && rt_ < NT && R < 2) {
            constexpr int hi = NT == 8 ? rt_ >= 4 : rt_ >= 6;
            oth[rt_][R] = ldsr<f32x4>(ra[hi] + xoff(rt_) - (NT == 8 && hi ? 32768 : 0) + R * 1024);
          }
          if constexpr (U == 20 && t >= 2) send(IC(t - 2), R_);
        }
        if constexpr (R >= 2) {
          constexpr Op o = op_at<WH>(P::FB + N, R), n = op_at<WH>(P::FB + N + 1, 2);
          if constexpr (R == 3 || N == 0) p.template prep<o.kind, o.a, o.b>(c);
          p.template fire<o.kind, o.a, o.b>(c);
          if constexpr (R == 3) p.template prep<n.kind, n.a, n.b>(c);
        }
        SBAR();
      });
    });
  });
  STUB<(NT == 8 ? 18 : 53)>();
  STAMP(NT == 8 ? 49 : 45);
  const unsigned l2 = l16 >> 4;
  STAMP(NT == 8 ? 50 : 46);
  constexpr int NI = NT / 4;
  u32x4 in[3][NI];
  for (unsigned spin = 0; spin < (1u << 16); ++spin) {
    unsigned mx = 0;
#pragma unroll
    for (int s = 1; s < 4; ++s)
#pragma unroll
      for (int i = 0; i < NI; ++i) in[s - 1][i] = __builtin_amdgcn_raw_buffer_load_b128(c.rws, l16 + i * 1024, pb + (c.a * 4 + (s ^ c.a)) * 12288, 16);
    asm volatile("" ::: "memory");
#pragma unroll
    for (int s = 0; s < 3; ++s)
#pragma unroll
      for (int i = 0; i < NI; ++i) mx = max(max(mx, max(in[s][i][0], in[s][i][1])), max(in[s][i][2], in[s][i][3]));
    if (__builtin_amdgcn_ballot_w64(mx == POISON) == 0) break;
  }
  STAMP(NT == 8 ? 51 : 47);
  const unsigned row0 = 256 * c.T + 128 * (c.m & 1) + 64 * (c.w & 1) + 32 * kh;
  const unsigned vo = (l2 & 15) * 2304 + 8 * (l2 >> 4), colb = NT == 8 ? 256 * (c.m >> 1) : 768;
#pragma unroll
  for (int i = 0; i < NI; ++i) {
    const int t = NT - NI + i;
    f32x4 s0 = acc[t][0], s1 = acc[t][1];
    if (t >= NT - 2) { s0 += oth[t][0]; s1 += oth[t][1]; }
#pragma unroll
    for (int s = 0; s < 3; ++s) {
      s0 += f32x4{bflo(in[s][i][0]), bfhi(in[s][i][0]), bflo(in[s][i][1]), bfhi(in[s][i][1])};
      s1 += f32x4{bflo(in[s][i][2]), bfhi(in[s][i][2]), bflo(in[s][i][3]), bfhi(in[s][i][3])};
    }
    s0 *= kh ? p.omg[2] : p.omg[0]; s1 *= kh ? p.omg[3] : p.omg[1];
    const unsigned cb = colb + (i < 2 ? 64 * c.a + 32 * i : 256 + 32 * c.a);
    __builtin_amdgcn_raw_buffer_store_b64(u32x2{cvtpk(s0[0], s0[1]), cvtpk(s0[2], s0[3])}, c.rout, vo, row0 * 1152 + cb, 0);
    __builtin_amdgcn_raw_buffer_store_b64(u32x2{cvtpk(s1[0], s1[1]), cvtpk(s1[2], s1[3])}, c.rout, vo, (row0 + 1) * 1152 + cb, 0);
  }
  if (NT == 8 && c.a == 0 && c.m == 0 && c.w == 0 && l2 == 0) flag_store(c, WS_E + c.T * 128, 16);
  if constexpr (NT == 8) asm volatile(".Lgm_code1:" ::: "memory"); else asm volatile(".Lgm_code2:" ::: "memory");
  STAMP(NT == 8 ? 52 : 48);
}

struct XS {
  unsigned v[2], lw, sx, rd[2], cur, sg;
  DI void init(const Ctx& c, unsigned base) {
    const unsigned l = lane_id(), j = l & 15, g = l >> 4;
    v[0] = (l >> 3) * 2304 + 16 * ((l & 7) ^ g); v[1] = (l >> 3) * 2304 + 16 * ((l & 7) ^ (4 + g));
    lw = base + c.w * 8192; sx = (256 * c.T + 64 * c.w) * 1152;
    rd[0] = lw + j * 128 + 16 * (g ^ (j >> 1)); rd[1] = rd[0] ^ 64;
    PIN(rd[0]); PIN(rd[1]);
  }
  template <int NSL, int C, int I> DI void prep(const Ctx& c) {
    if constexpr (I == 0) sg = sx + c.kg[C] * 128;
    cur = dprep<(C % NSL) * 32768 + I * 1024, (32 * (I >> 2) + 16 * (I & 1) + ((I >> 1) & 1)) * 1152>(lw, sg);
  }
  template <int I> DI void fire(const Ctx& c) { dfire<0>(v[I & 1], c.rx, cur); }
};
template <unsigned LA> DI void touch_pages(const Ctx& c) {
  const unsigned l = lane_id();
  const unsigned o = l < 4 ? WS_H + (((4 * c.T + l) * 4 + c.a) * 42 + 2 * c.m) * 4096 : l < 8 ? WS_PART + ((c.T * 8 + c.m) * 64 + (l - 4) * 16) * 3072 : l < 9 ? WS_G : WS_F1;
  touch<LA>(c.w < 2 ? o : (256 * c.T + 64 * (l & 3)) * 1152, c.w < 2 ? c.rws : c.rout, 0);
}
template <int I> DI void touch_w2(const Ctx& c, unsigned v128) {
  const unsigned idx = (c.T & 3) * 6 + c.m;
  const unsigned so = I < 7 ? (1152 * c.a + 48 * idx) * 1152 + c.w * 2048 + I * 8192 : (144 * c.a + 7 * idx) * 1152 + c.w * 2048;
  asm volatile("s_mov_b32 m0, %3\n\ts_mov_b64 exec, 0xffff\n\tbuffer_load_dword %0, %1, %2 offen sc1 lds\n\ts_mov_b64 exec, -1"
               ::"v"(v128), "s"(I < 7 ? c.rwd : c.rwo), "s"(so), "i"(9 * S1) : "memory");
}

constexpr int slot1(int n) { return n < 9 ? n % 4 : n % 9 < 5 ? 4 + n % 9 : n % 9 % 4; }
constexpr int sig_idx(int F) {
  if ((F + PD) % 16 == 0) return -1;
  int e = 0;
  for (int G = F - F % 72; G < F; ++G) if ((G + PD) % 16 != 0) ++e;
  return e;
}
constexpr int w_ks(int n, int I) { return n < 9 ? 2 * n + (I >> 1) : ((n % 9) * 4 + I) % 18; }
constexpr int w_up(int n, int I) { return n < 9 ? I & 1 : (n % 9) * 4 + I >= 18; }
DI void role_mlp(Ctx& c, XS& xs) {
  const unsigned l = lane_id(), g = l >> 4, q = (l & 15) >> 2, pp = l & 3, r8 = l >> 3;
  const unsigned vW = (8 * (r8 >> 2) + (r8 & 3)) * 9216 + 16 * ((l & 7) ^ (((r8 >> 1) & 3) << 1));
  const unsigned lw = c.w * 1024, sW = (16 * (c.w & 1) + 4 * (c.w >> 1)) * 9216 + (1152 * c.a + 64 * c.m) * 2;
  unsigned sWk[9];
#pragma unroll
  for (int i = 0; i < 9; ++i) sWk[i] = sW + c.kg[i] * (64 * 9216);
  const unsigned sH = WS_H + (((4 * c.T + c.w) * 4 + c.a) * 42 + 2 * c.m) * 4096;
  const unsigned f1 = WS_F1 + (c.T * 4 + c.a) * 128;
  bf16x8 xf[18][4];
  f32x4 ag0[4][4], ag[4][4], au[4][4];
  const float nl2e = -1.4426950408889634f;
  unsigned sp[4][4][2];
  P2<8> p2;
  u32x4 hq[8], pz;
  unsigned vl16 = l * 16;
  unsigned cur = 0;
  auto prep = [&](auto F_, auto R_) {
    constexpr Op o = mlp_op(CI(F_), CI(R_));
    if constexpr (o.kind == O_W) {
      constexpr int pn = o.a / 9, k = w_ks(o.a, o.b);
      cur = dprep<slot1(o.a) * S1 + o.b * 4096, (k & 1) * 32 * 9216 + pn * 768>(lw, sWk[k >> 1]);
    } else if constexpr (o.kind == O_X) xs.template prep<3, o.a, o.b>(c);
    else p2.template prep<o.kind, o.a, o.b>(c);
  };
  auto fire = [&](auto F_, auto R_) {
    constexpr Op o = mlp_op(CI(F_), CI(R_));
    if constexpr (o.kind == O_W && (ABL & 16) && o.a >= 14) {}
    else if constexpr (o.kind == O_W) dfire<0>(vW, w_up(o.a, o.b) ? c.rwu : c.rwg, cur);
    else if constexpr (o.kind == O_X) {
      if constexpr (o.a >= 3 && o.b == 0) asm volatile("s_waitcnt lgkmcnt(4)" ::: "memory");
      xs.template fire<o.b>(c);
    } else if constexpr (o.kind == O_FIN) {
      __builtin_amdgcn_raw_buffer_store_b128(hq[o.b], c.rws, vl16 + (o.b & 3) * 1024, sH + (12 * o.a + (o.b >> 2)) * 4096, 16);
      asm volatile("" ::: "memory");
    } else if constexpr (o.kind == O_PZ) poison<2, o.a>(c, pz, vl16);
    else if constexpr (o.kind == O_TW) touch_w2<o.a>(c, vl16 * 8);
    else if constexpr (o.kind == O_TL) touch_pages<3 * S1>(c);
    else p2.template fire<o.kind, o.a, o.b>(c);
  };
  static_for<5>([&](auto I_) { prep(IC(CI(I_) - 7), IC(2)); fire(IC(CI(I_) - 7), IC(2)); });
  STAMP(56);
  visit_all<0, 19>(c.pc0, c.w);
  STAMP(57);
  load_epoch(c);
  unsigned rdx2[2] = {xs.rd[0] + 65536, xs.rd[1] + 65536}; PIN(rdx2[0]); PIN(rdx2[1]);
  auto rdx = [&](auto C_, auto I_) {
    constexpr int C = CI(C_), s = CI(I_) >> 2, rt = CI(I_) & 3;
    xf[2 * C + s][rt] = ldsr<bf16x8>((C % 3 == 2 ? rdx2[s] : xs.rd[s] + (C % 3) * 32768) + rt * 2048);
  };
  unsigned rb[4];
#pragma unroll
  for (int t = 0; t < 4; ++t) rb[t] = (4 * g + q) * 128 + 32 * (t ^ ((2 * g + (q >> 1)) & 3)) + 8 * pp;
  float e0 = 0.f, e1 = 0.f, e3 = 0.f, h0 = 0.f, h1 = 0.f;
  unsigned e2 = 0;
  bf16x8 f[16];
  s16x4 flo;
  unsigned rb2[3];
  wait_bar<VM<0, -1>>();
  asm volatile("" : "+s"(c.E));
  STAMP(1);
  static_for<4>([&](auto I_) { rdx(IC(0), I_); });
  static_for<PD>([&](auto I_) { f[CI(I_)] = cat(trd(rb[CI(I_)]), trd(rb[CI(I_)] + 2048)); });
  static_for<4>([&](auto I_) { rdx(IC(0), IC(4 + CI(I_))); });
  SBAR();
  static_for<27>([&](auto N_) {
    static_for<16>([&](auto I_) {
      constexpr int N = CI(N_), I = CI(I_), F = N * 16 + I, FP = F % 144, ps = w_up(N, I >> 2), ks = w_ks(N, I >> 2), t = I & 3;
      constexpr int RG = (F + PD) / 16, pos = (F + PD) % 16, RR = pos == 0 ? 2 : 0;
      constexpr unsigned ro = (slot1(RG) % 4) * S1 + (pos >> 2) * 4096;
      const bf16x8 a = f[I];
      static_for<4>([&](auto R_) {
        constexpr int R = CI(R_);
        if constexpr (ps == 0) mf<(ks < 16), ks == 0>(F < 144 ? ag0[t][SN(t, R)] : ag[t][SN(t, R)], a, xf[ks][SN(t, R)]); else mf<(ks < 16), ks == 0>(au[t][SN(t, R)], a, xf[ks][SN(t, R)]);
        SBAR();
        if constexpr (R == 0 && N < 8 && I == XR) wait_vm<VM<0, F>>();
        if constexpr (pos == 0) {
          if constexpr (R < 2 && RG < 27 && slot1(RG) / 4 != slot1(RG - 1) / 4) {
#pragma unroll
            for (int i = 2 * R; i < 2 * R + 2; ++i) { rb[i] += (unsigned)(slot1(RG) / 4 - slot1(RG - 1) / 4) * 4 * S1; PIN(rb[i]); }
          }
          if constexpr (R == 0 && RG == 27) {
            const unsigned l4 = lane_id();
#pragma unroll
            for (int i = 0; i < 3; ++i) { rb2[i] = P2<8>::slot(0) + P2<8>::rbase(c, l4, i); PIN(rb2[i]); }
          }
          if constexpr (R == 2) {
            if constexpr (RG % 2 == 0) STUB<RG / 2 - 1>();
            static_assert((RG != FLG0 && RG != FLG1) || VM<0, F> == 0, "flags follow a full drain");
            wait_bar<VM<0, F>>();
            STAMP(1 + RG);
            if constexpr (RG == 27) tieA(p2.bf[0][0], p2.bf[0][1], p2.bf[0][2], p2.bf[0][3]);
          }
        }
        if constexpr (R == RR) { if constexpr (RG < 27) flo = trd(rb[pos & 3] + ro); else flo = trd(rb2[pos]); }
        if constexpr (R == RR + 1) { if constexpr (RG < 27) f[pos] = cat(flo, trd(rb[pos & 3] + ro + 2048)); else f[pos] = cat(flo, trd(rb2[pos] + 4608)); }
        if constexpr (N < 8 && (I == XR || I == XR + 1)) rdx(IC(N + 1), IC(4 * (I - XR) + R));
        SBAR();
        if constexpr (pos == 0 && R == 3) {
          if constexpr (RG == FLG0 || RG == FLG1) { if (threadIdx.x == 0) flag_store(c, f1 + 8 * c.m, RG == FLG0 ? 1 : 2); }
          if constexpr (RG == PZR + 1) keep(pz);
          if constexpr (RG == FLG0) { if (vl16 == 0) flag_store(c, flag2(c) + 8 * c.a, 1); }
          if constexpr (RG == 19) p2.init(c);
          if constexpr (RG == 25) p2.template check<0>(c);
        }
        if constexpr (R >= 2) { if constexpr (R == 3 || F == 0) prep(IC(F), R_); fire(IC(F), R_); }
        if constexpr (F >= 144 && ps == 1 && sig_idx(F) >= 0 && sig_idx(F) <= 65) {
          constexpr int e = sig_idx(F), d = e - 2;
          if constexpr (R == 0 && d >= 0) e2 = w_add1(e1);
          if constexpr (R == 1) { e3 = e0; if constexpr (e < 64) e0 = w_mul(ag[e / 16][(e / 4) % 4][e % 4]); }
          if constexpr (R == 2 && e >= 1 && e <= 64) e1 = w_exp(e3);
          if constexpr (R == 3 && d >= 0) { if constexpr (d % 2 == 0) sp[d / 16][(d / 4) % 4][(d % 4) / 2] = w_rcplo(e2); else w_rcphi(sp[d / 16][(d / 4) % 4][(d % 4) / 2], e2); }
        }
        if constexpr (F >= 138 && F < 146) {
          constexpr int tt = (F - 138) / 2, rt = 2 * (F & 1) + (R >> 1), k = R & 1;
          sp[tt][rt][k] = w_pklo(ag0[tt][rt][2 * k], nl2e); w_pkhi(sp[tt][rt][k], ag0[tt][rt][2 * k + 1], nl2e);
        }
        if constexpr (F >= 144 && F < 216 && sig_idx(F) >= 0) {
          constexpr int e = sig_idx(F), d = e - 2, pa = (e - 2) / 2, pf = (e - 4) / 2;
          if constexpr (R == 0 && e % 2 == 0 && e >= 2 && pa < 32) w_add1h(sp[pa / 8][(pa / 2) % 4][pa % 2]);
          if constexpr (R == 0 && e % 2 == 1 && e >= 5) h1 = w_mixhi(au[pf / 8][(pf / 2) % 4][2 * (pf % 2) + 1], sp[pf / 8][(pf / 2) % 4][pf % 2]);
          if constexpr (R == 1 && e % 2 == 0 && e >= 4) h0 = w_mixlo(au[pf / 8][(pf / 2) % 4][2 * (pf % 2)], sp[pf / 8][(pf / 2) % 4][pf % 2]);
          if constexpr (R == 1 && e % 2 == 1 && e >= 5) hq[(pf / 16) * 4 + (pf / 2) % 4][2 * ((pf / 8) & 1) + pf % 2] = w_cvt(h0, h1);
          if constexpr (R == 2 && e < 64) w_exph<e & 1>(sp[e / 16][(e / 4) % 4][(e % 4) / 2]);
          if constexpr (R == 3 && d >= 0 && d < 64) w_rcph<d & 1>(sp[d / 16][(d / 4) % 4][(d % 4) / 2]);
        }
        if constexpr (F >= 288 && FP >= FIN0 && FP < FIN0 + 32) {
          constexpr int J = (FP - FIN0) / 4, i = FP % 4, e = J >> 2, rt = J & 3, tt = 2 * e + (i >> 1), k = 2 * (i & 1);
          if constexpr (R == 0) h0 = w_mixlo(au[tt][rt][k], sp[tt][rt][i & 1]);
          if constexpr (R == 1) h1 = w_mixhi(au[tt][rt][k + 1], sp[tt][rt][i & 1]);
          if constexpr (R == 3) hq[J][i] = w_cvt(h0, h1);
        }
        if constexpr (R == 3 && mlp_op(F, 2).kind == O_FIN) keep(hq[mlp_op(F, 2).b]);
        if constexpr (R == 3 && F < 431) prep(IC(F + 1), IC(2));
        SBAR();
      });
    });
  });
  phase2<8>(c, p2, f, [&](auto N_, auto R_) {
    constexpr int N = CI(N_), R = CI(R_);
    if constexpr (N < 16 && R < 3) {
      constexpr int J = N / 2, e = J >> 2, rt = J & 3, tt = 2 * e + (N & 1);
      if constexpr (R == 0) { h0 = w_mixlo(au[tt][rt][0], sp[tt][rt][0]); h1 = w_mixhi(au[tt][rt][1], sp[tt][rt][0]); }
      if constexpr (R == 1) { e0 = w_mixlo(au[tt][rt][2], sp[tt][rt][1]); e1 = w_mixhi(au[tt][rt][3], sp[tt][rt][1]); }
      if constexpr (R == 2) { hq[J][2 * (N & 1)] = w_cvt(h0, h1); hq[J][2 * (N & 1) + 1] = w_cvt(e0, e1); }
    }
    if constexpr (R == 3 && N < 20 && lastfin(N) >= 0) {
      constexpr int J = lastfin(N);
      __builtin_amdgcn_raw_buffer_store_b128(hq[J], c.rws, vl16 + (J & 3) * 1024, sH + (24 + (J >> 2)) * 4096, 16);
      asm volatile("" ::: "memory");
    }
    if constexpr (R == 1 && N >= 1 && N < 21 && lastfin(N - 1) >= 0) keep(hq[lastfin(N - 1)]);
  });
}

DI u32x2 pk4(const f32x4& v) { return u32x2{cvtpk(v[0], v[1]), cvtpk(v[2], v[3])}; }
DI void sw16(float& a, float& b) { auto r = __builtin_amdgcn_permlane16_swap(__float_as_uint(a), __float_as_uint(b), false, false); a = __uint_as_float(r[0]); b = __uint_as_float(r[1]); }
DI void sw32(float& a, float& b) { auto r = __builtin_amdgcn_permlane32_swap(__float_as_uint(a), __float_as_uint(b), false, false); a = __uint_as_float(r[0]); b = __uint_as_float(r[1]); }
template <bool MX> DI float xred(float (&v)[4]) {
  sw16(v[0], v[1]); sw16(v[2], v[3]);
  float a = MX ? max2(v[0], v[1]) : v[0] + v[1], b = MX ? max2(v[2], v[3]) : v[2] + v[3];
  sw32(a, b);
  return MX ? max2(a, b) : a + b;
}
DI void xbc(float t, float (&v)[4]) {
  float a = t, b = t;
  sw32(a, b);
  v[0] = a; v[1] = a; v[2] = b; v[3] = b;
  sw16(v[0], v[1]); sw16(v[2], v[3]);
}

DI void role_att(Ctx& c, XS& xs) {
  const unsigned mp = c.m & 1;
  u32x2 vp[4][5], qp[4][5];
  u32x4 kt[6][4];
  f32x4 gate, bprev, bown, bprev0;
  unsigned mpv[2];
  P2<12> p2;
  {
    const unsigned l = lane_id(), g = l >> 4, q = (l & 15) >> 2, pp = l & 3;
    const unsigned lc = (l & 15) ^ (g << 1), hb = 144 * (2 * c.a + mp);
    const unsigned vA = g * 3456 + hb + (lc < 10 ? 1152 + 16 * (lc < 9 ? lc : 8) : 2304 + 16 * (lc - 10));
    const unsigned vB = g * 3456 + hb + (lc < 4 ? 2304 + 16 * (lc < 3 ? 6 + lc : 8) : 16 * (lc - 4));
    const u64 maskB = __builtin_amdgcn_ballot_w64(lc < 13);
    const unsigned lw = c.w * 1152;
    unsigned sWk[9];
#pragma unroll
    for (int i = 0; i < 9; ++i) sWk[i] = (8 * c.w + 64 * c.kg[i]) * 3456;
    const int hr = 256 * (int)c.T + ((c.w & 1) ? -16 : -15) + 2 * (int)(l >> 3);
    const unsigned vHx = (unsigned)(hr < 0 ? 0 : hr) * 1152 + 16 * ((l & 7) ^ (4 * (c.w & 1) + g));
    const unsigned lHx = A_HX + c.w * 1024;
    unsigned rh[2] = {A_HX + (l & 15) * 128 + 16 * (g ^ ((l & 15) >> 1)), 0}; rh[1] = rh[0] ^ 64; PIN(rh[0]); PIN(rh[1]);
    bf16x8 xf[18][4];
    unsigned cur = 0, l8 = 0;
    u32x4 pz;
    auto prep = [&](auto F_, auto R_) {
      constexpr Op o = att1_op(CI(F_), CI(R_));
      if constexpr (o.kind == O_W) cur = dprep<(o.a % 4) * S2 + o.b * 4608, (32 * (o.b >> 1) + 4 * (o.b & 1)) * 3456>(lw, sWk[o.a % 9]);
      else if constexpr (o.kind == O_X) xs.template prep<2, o.a, o.b>(c);
      else if constexpr (o.kind == O_HX) {
        if constexpr (o.a < 4) cur = dprep<o.a * 4096, 0>(lHx, (c.w < 2 ? c.kg[2 * o.a] : c.kg[2 * o.a + 1]) * 128);
        else cur = dprep<16384, 0>(lHx, c.w < 2 ? c.kg[8] * 128 : (c.w & 1) * 1024);
      } else p2.template prep<o.kind, o.a, o.b>(c);
    };
    auto fire = [&](auto F_, auto R_) {
      constexpr Op o = att1_op(CI(F_), CI(R_));
      if constexpr (o.kind == O_W) { if constexpr (o.a < 9) dfire<0>(vA, c.rqkv, cur); else dfire_m(vB, c.rqkv, cur, maskB); }
      else if constexpr (o.kind == O_X) {
        if constexpr (o.a >= 2 && o.b == 0) asm volatile("s_waitcnt lgkmcnt(4)" ::: "memory");
        xs.template fire<o.b>(c);
      } else if constexpr (o.kind == O_HX) {
        if constexpr (o.a < 4) dfire<0>(vHx, c.rx, cur); else dfire<0>(c.w < 2 ? vHx : l * 16, c.w < 2 ? c.rx : c.rgw, cur);
      } else if constexpr (o.kind == O_TL) touch_pages<A_GW + 2048>(c);
      else if constexpr (o.kind == O_PZ) poison<3, o.a>(c, pz, 2 * l8);
      else p2.template fire<o.kind, o.a, o.b>(c);
    };
    auto rdx = [&](auto C_, auto I_) {
      constexpr int C = CI(C_), s = CI(I_) >> 2, rt = CI(I_) & 3;
      xf[2 * C + s][rt] = ldsr<bf16x8>(xs.rd[s] + (C % 2) * 32768 + rt * 2048);
    };
    static_for<6>([&](auto I_) { prep(IC(CI(I_) - 8), IC(2)); fire(IC(CI(I_) - 8), IC(2)); });
    visit_all<32, 22>(c.pc0, c.w);
    STAMP(57);
    load_epoch(c);
    unsigned rb[4], rx1, rx2;
#pragma unroll
    for (int t = 0; t < 4; ++t) rb[t] = g * 1152 + q * 256 + 32 * (t ^ q) + 8 * pp;
    rx1 = g * 1152 + q * 256 + 32 * (c.w ^ q) + 8 * pp;
    rx2 = g * 1152 + q * 256 + 128 + 32 * ((c.w < 2 ? c.w + 1 : 3) ^ q) + 8 * pp;
    const unsigned gsrc = A_GW + 2 * (32 * (l >> 5) + 8 * ((l >> 2) & 3) + 4 * ((l >> 4) & 1) + (l & 3));
    const unsigned gdst = (l >> 2) * 1152 + (l & 3) * 256 + 16 * (13 ^ ((l & 3) << 1));
    u16 gwv = 0;
    f32x4 acc[2][8][4], ah[4];
    u32x2 hp;
    unsigned wk = 0, wv = 0;
    bf16x8 f[16], hx, ha, hc;
    s16x4 flo, hlo;
    wait_bar<VM<1, -1>>();
    asm volatile("" : "+s"(c.E));
    STAMP(1);
    static_for<4>([&](auto I_) { rdx(IC(0), I_); });
    static_for<PD>([&](auto I_) { f[CI(I_)] = cat(trd(rb[CI(I_)]), trd(rb[CI(I_)] + 4608)); });
    static_for<4>([&](auto I_) { rdx(IC(0), IC(4 + CI(I_))); });
    SBAR();
    auto pass = [&](auto PS_) {
      constexpr int PS = CI(PS_), NT = PS == 0 ? 8 : 7, FC = 2 * NT;
      static_for<9>([&](auto N_) {
        static_for<FC>([&](auto I_) {
          constexpr int N = CI(N_), I = CI(I_), F = 144 * PS + N * FC + I, ks = 2 * N + I / NT, t = I % NT;
          constexpr int RG = rgA(F), pos = posA(F);
          constexpr int NTN = RG < 9 ? 8 : 7, tn = pos % NTN, RR = pos == 0 ? 2 : 0;
          constexpr unsigned ro = (RG % 2) * S2 + (pos / NTN) * 9216 + 128 * (tn >> 2);
          constexpr unsigned rc = ((9 * PS + N) % 2) * S2 + (I / NT) * 9216;
          constexpr bool SW = PS == 0 ? t >= 5 : t < 2;
          const bf16x8 a = f[I];
          static_for<5>([&](auto R_) {
            constexpr int R = CI(R_);
            if constexpr (R < 4) { if constexpr (SW) mfs<(ks < 16), ks == 0>(acc[PS][t][R], a, xf[ks][R]); else mf<(ks < 16), ks == 0>(acc[PS][t][R], a, xf[ks][R]); }
            else if constexpr (PS == 0) {
              if constexpr (t == 4) mf<false, ks == 0>(ah[1], a, hx);
              if constexpr (t == 5) mf<false, ks == 0>(ah[0], ha, hx);
              if constexpr (t == 7) mfs<false, ks == 0>(ah[2], hc, hx);
            } else if constexpr (t == 4) mfs<false, ks == 0>(ah[3], ha, hx);
            SBAR();
            if constexpr (R == 0 && PS == 0 && I == XR && N < 8) wait_vm<VM<1, F>>();
            if constexpr (pos == 0 && RG < 18) {
              if constexpr (R < 2 && RG % 2 == 0) {
                constexpr unsigned d = RG % 4 == 0 ? 0u - 2 * S2 : 2 * S2;
#pragma unroll
                for (int i = 2 * R; i < 2 * R + 2; ++i) { rb[i] += d; PIN(rb[i]); }
                if (R == 0) { rx1 += d; PIN(rx1); } else if (RG < 9) { rx2 += d; PIN(rx2); }
              }
              if constexpr (R == 0 && RG == 9) { rx1 ^= 32 * (c.w & 2); PIN(rx1); }
              if constexpr (R == 2) {
                if constexpr (RG % 2 == 1) STUB<32 + RG / 2>();
                static_assert(RG != 16 || VM<1, F> == 0, "flags follow a full drain");
                wait_bar<VM<1, F>>();
                STAMP(1 + RG);
              }
            }
            if constexpr (R == RR && RG < 18) flo = trd(rb[tn & 3] + ro);
            if constexpr (R == RR + 1 && RG < 18) f[pos] = cat(flo, trd(rb[tn & 3] + ro + 4608));
            if constexpr (PS == 0 && N < 8 && (I == XR || I == XR + 1) && R < 4) rdx(IC(N + 1), IC(4 * (I - XR) + R));
            if constexpr (t == 0 && R == 3) hx = ldsr<bf16x8>(rh[ks & 1] + (ks >> 1) * 2048);
            if constexpr (t == 1 && R == 3) hlo = trd(rx1 + rc);
            if constexpr (t == 2 && R == 3) ha = cat(hlo, trd(rx1 + rc + 4608));
            if constexpr (PS == 0 && t == 3 && R == 3) hlo = trd(rx2 + rc);
            if constexpr (PS == 0 && t == 4 && R == 3) hc = cat(hlo, trd(rx2 + rc + 4608));
            if constexpr (RG + 3 >= 9 && RG + 3 <= 17 && R == 1) {
              if constexpr (pos == 6) gwv = ldsr<u16>(gsrc + c.kg[RG + 3 - 9] * 128);
              if constexpr (pos == 9) ldsw<u16>(gdst + ((RG + 3) % 4) * S2, gwv);
            }
            SBAR();
            if constexpr (F == 136 && R == 0) { l8 = lane_id() * 8; PIN(l8); wk = A_KX + (4 * c.w + 2) * 2560 + l8; PIN(wk); }
            if constexpr (F >= 138 && F < 146 && R < 4) {
              constexpr int tt = F - 138;
              if constexpr (tt < 5) ldsw<u32x2>(wk + R * 2560 + tt * 512, pk4(acc[0][tt][R])); else { vp[R][tt - 5] = pk4(acc[0][tt][R]); PIN(vp[R][tt - 5]); }
            }
            if constexpr (F == 146 && R < 2) { const u32x2 h = pk4(ah[R]); const unsigned o = A_KX + (R == 0 ? c.w : 4) * 512; ldsw<u32x2>(o + l8, h); ldsw<u32x2>(o + 2560 + (l8 ^ 64), h); }
            if constexpr (F == 146 && R == 2) { hp = pk4(ah[2]); PIN(hp); }
            if constexpr (pos == 0 && R == 3) {
              if constexpr (RG == 15) { keep(pz); p2.init(c); wv = A_VX + (c.w < 3 ? (c.w + 1) * 5120 : A_GW + 2304 - A_VX) + (c.w < 3 ? l8 : 0); PIN(wv); }
              if constexpr (RG == 16) { if (l8 == 0) flag_store(c, flag2(c) + 8 * c.a, 1); }
            }
            if constexpr (RG == 15 && pos >= 4 && pos < 10 && R == 2) { if (c.w < 3) ldsw<u32x2>(wv + (((pos - 4) / 3) * 5 + (pos - 4) % 3) * 512, vp[2 + (pos - 4) / 3][(pos - 4) % 3]); }
            if constexpr (RG == 15 && pos == 11 && R == 2) { const unsigned o = A_VX + (c.w < 2 ? c.w : 2) * 512; ldsw<u32x2>(o + l8, hp); ldsw<u32x2>(o + 2560 + (l8 ^ 256), hp); }
            if constexpr (RG == 16 && pos >= 9 && pos < 13 && R == 2) {
              constexpr int r = pos - 9;
              const int key = (int)(l8 >> 7) * 4 + r, jj = (int)(l8 >> 3) & 15;
              bprev[r] = key >= jj + 9 ? 0.f : -1e30f;
              bown[r] = (key <= jj && key >= jj - 7) ? 0.f : -1e30f;
              bprev0[r] = (c.T == 0 && c.w == 0) ? -1e30f : bprev[r];
              if constexpr (r % 2 == 0) mpv[r / 2] = key >= jj + 9 ? 0xffffu : 0u; else { mpv[r / 2] |= key >= jj + 9 ? 0xffff0000u : 0u; PIN(mpv[r / 2]); }
              if constexpr (r == 3) { PIN(bprev); PIN(bown); PIN(bprev0); }
            }
            if constexpr (F == 256 && R == 0) { wk -= 5120; PIN(wk); }
            if constexpr (F >= 257 && F < 265 && (R == 0 || R == 1 || R == 3)) {
              constexpr int n = 3 * (F - 257) + (R == 3 ? 2 : R), i = n / 4, h = n % 4;
              const u32x2 k0 = ldsr<u32x2>(wk + i * 2560 + h * 512), k1 = ldsr<u32x2>(wk + i * 2560 + h * 512 + 512);
              kt[i][h] = u32x4{k0[0], k0[1], k1[0], k1[1]};
              asm volatile("" ::: "memory");
            }
            if constexpr (F >= 265 && R < 4) {
              constexpr int tt = F - 265;
              if constexpr (tt < 2) { vp[R][3 + tt] = pk4(acc[1][tt][R]); PIN(vp[R][3 + tt]); if constexpr (R >= 2) { if (c.w < 3) ldsw<u32x2>(wv + ((R - 2) * 5 + 3 + tt) * 512, vp[R][3 + tt]); } }
              else { qp[R][tt - 2] = pk4(acc[1][tt][R]); PIN(qp[R][tt - 2]); }
            }
            if constexpr (R == 2 || R == 3) { if constexpr (R == 3 || F == 0) prep(IC(F), R_); fire(IC(F), R_); }
            if constexpr (R == 4) prep(IC(F + 1), IC(2));
            SBAR();
          });
        });
      });
    };
    pass(IC(0));
    pass(IC(1));
    STUB<41>();
    STAMP(19);
    { const u32x2 h = pk4(ah[3]); const unsigned o = A_VX + (3 + (c.w & 1)) * 512; ldsw<u32x2>(o + l8, h); ldsw<u32x2>(o + 2560 + (l8 ^ 256), h); }
#pragma unroll
    for (int rt = 0; rt < 4; ++rt) { qp[rt][3] = pk4(acc[1][5][rt]); PIN(qp[rt][3]); }
    asm volatile("s_waitcnt lgkmcnt(0)\n\ts_barrier" ::: "memory");
#pragma unroll
    for (int rt = 0; rt < 4; ++rt) {
      qp[rt][4] = pk4(acc[1][6][rt]);
      gate[rt] = __builtin_amdgcn_exp2f(fminf(1.4426950408889634f * acc[1][6][rt][0], 80.0f));
    }
  }
  STAMP(20);
  auto pre = [&](auto A_, auto N_) { static_for<CI(N_)>([&](auto I_) { constexpr Op o = att2_op(CI(A_) + CI(I_) - 50, 2); p2.template prep<o.kind, o.a, o.b>(c); p2.template fire<o.kind, o.a, o.b>(c); }); };
  const unsigned l = lane_id(), g = (l >> 4) & 3, j = l & 15;
  {
    u32x6 vt[5][2];
    const unsigned ra = A_VX + c.w * 5120 + l * 8;
#pragma unroll
    for (int pa = 0; pa < 2; ++pa)
#pragma unroll
      for (int t = 0; t < 5; ++t) {
        const u32x2 v2 = ldsr<u32x2>(ra + (pa * 5 + t) * 512);
        vt[t][pa] = u32x6{v2[0], v2[1], vp[pa][t][0], vp[pa][t][1], vp[2 + pa][t][0], vp[2 + pa][t][1]};
      }
    const f32x4 omg = {__builtin_amdgcn_rcpf(1.0f + gate[0]), __builtin_amdgcn_rcpf(1.0f + gate[1]), __builtin_amdgcn_rcpf(1.0f + gate[2]), __builtin_amdgcn_rcpf(1.0f + gate[3])};
    if (mp == 0 && g == 2) st16(__builtin_bit_cast(u32x4, omg), c.rws, j * 16, WS_G + (c.a * 32 + 4 * c.T + c.w) * 256);
#pragma unroll
    for (int rt = 0; rt < 4; ++rt) gate[rt] = __shfl(gate[rt], 32 + j);
    constexpr float SC2 = 0.23570226039551584f * 1.4426950408889634f;
    const unsigned sH = WS_H + (((4 * c.T + c.w) * 4 + c.a) * 42 + 36 + 3 * mp) * 4096;
    u32x4 qf[4], P[4];
    f32x4 sp[4], so[4], hi[4], lo[4];
    auto mkq = [&](auto RT_) {
      constexpr int rt = CI(RT_);
#pragma unroll
      for (int h = 0; h < 4; ++h) {
#pragma unroll
        for (int i = 0; i < 4; ++i) { const int col = 16 * (h + (i >> 1)) + 4 * (int)g + 2 * (i & 1); qf[h][i] = (col >= 18 * h && col < 18 * h + 18) ? qp[rt][h + (i >> 1)][i & 1] : 0u; }
        PIN(qf[h]);
      }
    };
    auto qk = [&](auto RT_, auto H_, auto W_) {
      constexpr int rt = CI(RT_), h = CI(H_);
      if constexpr (CI(W_) == 0) sp[h] = mfc(kt[rt][h], qf[h], rt < 2 ? bprev0 : bprev);
      else so[h] = mfc(kt[rt + 2][h], qf[h], bown);
    };
    auto pv = [&](auto RT_, auto T_, auto W_) {
      constexpr int rt = CI(RT_), t = CI(T_) + CI(W_), pa = rt & 1, sn = rt >> 1;
      const f32x4 r = mfz(__builtin_shufflevector(vt[t][pa], vt[t][pa], 2 * sn, 2 * sn + 1, 2 * sn + 2, 2 * sn + 3), P[CI(T_)]);
      if constexpr (CI(W_) == 0) hi[CI(T_)] = r; else lo[CI(T_)] = r;
    };
    auto outp = [&](auto RT_) {
      constexpr int rt = CI(RT_);
      u32x2 o[5];
      PIN(lo[0]);
#pragma unroll
      for (int t = 0; t < 5; ++t)
#pragma unroll
        for (int i = 0; i < 2; ++i) {
          const unsigned vh = t < 4 ? cvtpk(hi[t < 4 ? t : 0][2 * i], hi[t < 4 ? t : 0][2 * i + 1]) : 0u;
          o[t][i] = (4 * (int)g + 2 * i < 2 * t) ? cvtpk(lo[t > 0 ? t - 1 : 0][2 * i], lo[t > 0 ? t - 1 : 0][2 * i + 1]) : vh;
        }
      st16(u32x4{o[0][0], o[0][1], o[1][0], o[1][1]}, c.rws, l * 16 + rt * 1024, sH);
      st16(u32x4{o[2][0], o[2][1], o[3][0], o[3][1]}, c.rws, l * 16 + rt * 1024, sH + 4096);
      st16(u32x4{o[4][0], o[4][1], 0u, 0u}, c.rws, l * 16 + rt * 1024, sH + 8192);
    };
    mkq(IC(0));
    asm volatile("s_nop 1" ::: "memory");
    static_for<8>([&](auto I_) { qk(IC(0), IC(CI(I_) >> 1), IC(CI(I_) & 1)); });
    SBAR();
    mkq(IC(1));
    static_for<4>([&](auto RT_) {
      constexpr int rt = CI(RT_);
      float s[4][4], red[4], bc[4];
      STUB<42 + rt>();
      SBAR();
      static_for<4>([&](auto H_) {
        constexpr int h = CI(H_);
        if constexpr (rt > 0) pv(IC(rt - 1), H_, IC(0));
        s[h][0] = max2(sp[h][0], so[h][0]); s[h][1] = max2(sp[h][1], so[h][1]); s[h][2] = max2(sp[h][2], so[h][2]);
        SBAR();
        if constexpr (rt > 0) pv(IC(rt - 1), H_, IC(1));
        s[h][3] = max2(sp[h][3], so[h][3]);
        red[h] = max2(max3(s[h][0], s[h][1], s[h][2]), s[h][3]);
        SBAR();
      });
      xbc(-SC2 * xred<true>(red), bc);
      if constexpr (rt == 0) pre(IC(6), IC(7));
      if constexpr (rt == 2) { wait_vm<VM<2, -28>>(); p2.template check<0>(c); }
      if constexpr (rt > 0) outp(IC(rt - 1));
      SBAR();
      static_for<4>([&](auto H_) {
        constexpr int h = CI(H_);
        if constexpr (rt < 3) qk(IC(rt + 1), H_, IC(0));
        s[h][0] = __builtin_amdgcn_exp2f(__builtin_fmaf(s[h][0], SC2, bc[h])); s[h][1] = __builtin_amdgcn_exp2f(__builtin_fmaf(s[h][1], SC2, bc[h]));
        SBAR();
        if constexpr (rt < 3) qk(IC(rt + 1), H_, IC(1));
        s[h][2] = __builtin_amdgcn_exp2f(__builtin_fmaf(s[h][2], SC2, bc[h])); s[h][3] = __builtin_amdgcn_exp2f(__builtin_fmaf(s[h][3], SC2, bc[h]));
        SBAR();
      });
#pragma unroll
      for (int h = 0; h < 4; ++h) red[h] = (s[h][0] + s[h][1]) + (s[h][2] + s[h][3]);
      xbc(gate[rt] * __builtin_amdgcn_rcpf(xred<false>(red)), bc);
      if constexpr (rt == 1) pre(IC(16), IC(6));
      if constexpr (rt == 2) pre(IC(25), IC(8));
      if constexpr (rt == 3) pre(IC(36), IC(4));
      if constexpr (rt == 0) {
#pragma unroll
        for (int i = 0; i < 10; ++i) PIN(vt[i >> 1][i & 1]);
      }
      if constexpr (rt < 2) mkq(IC(rt + 2));
#pragma unroll
      for (int h = 0; h < 4; ++h) {
        const unsigned p0 = cvtpk(s[h][0] * bc[h], s[h][1] * bc[h]), p1 = cvtpk(s[h][2] * bc[h], s[h][3] * bc[h]);
        P[h] = u32x4{p0 & mpv[0], p1 & mpv[1], p0 & ~mpv[0], p1 & ~mpv[1]};
        PIN(P[h]);
      }
      asm volatile("s_nop 1" ::: "memory");
    });
    static_for<8>([&](auto I_) { pv(IC(3), IC(CI(I_) >> 1), IC(CI(I_) & 1)); });
    asm volatile("s_nop 7" ::: "memory");
    SBAR();
    outp(IC(3));
  }
  STAMP(21);
  bf16x8 f[16];
  phase2<12>(c, p2, f, [](auto, auto) {});
}

#endif
#define STAMP_OFF (64u << 20)
__global__ __launch_bounds__(256, 1) void fused_kernel(const u16* x, const u16* wqkv, const u16* wo, const u16* wg, const u16* wu, const u16* wd,
                                                        const u16* gw, char* ws, u16* out) {
#if defined(__HIP_DEVICE_COMPILE__)
  __shared__ __attribute__((aligned(16))) char smem[LDS_BYTES];
  asm volatile("" ::"v"((unsigned)(size_t)smem));
  Ctx c;
  STAMP(0);
  const unsigned bid = blockIdx.x, xcd = bid & 7, idx = bid >> 3;
  c.w = __builtin_amdgcn_readfirstlane(threadIdx.x >> 6);
  {
    u64 pc; unsigned len;
    asm volatile("s_getpc_b64 %0\n.Lgm_code0:\n\ts_mov_b32 %1, .Lgm_code1-.Lgm_code0\n\ts_max_u32 %1, %1, .Lgm_code2-.Lgm_code0" : "=s"(pc), "=s"(len) :: "scc");
    c.pc0 = pc;
    asm volatile("s_branch .Lgm_go\n.Lgm_ret:\n\ts_setpc_b64 s[100:101]\n.Lgm_go:");
    const rsrc_t rc = __builtin_amdgcn_make_buffer_rsrc((void*)pc, (short)0, len, 0x00020000);
    if (idx < 16) touch<3 * S1>((threadIdx.x & 15) * 128, rc, idx * 8192 + c.w * 2048);
  }
  asm volatile("" ::"s"(x), "s"(out));
  STAMP(54);
  c.rx = __builtin_amdgcn_make_buffer_rsrc((void*)x, (short)0, SEQ * DM * 2, 0x00020000);
  c.rqkv = __builtin_amdgcn_make_buffer_rsrc((void*)wqkv, (short)0, DM * 3 * DM * 2, 0x00020000);
  c.rwo = __builtin_amdgcn_make_buffer_rsrc((void*)wo, (short)0, DM * DM * 2, 0x00020000);
  c.rwg = __builtin_amdgcn_make_buffer_rsrc((void*)wg, (short)0, DM * FF * 2, 0x00020000);
  c.rwu = __builtin_amdgcn_make_buffer_rsrc((void*)wu, (short)0, DM * FF * 2, 0x00020000);
  c.rwd = __builtin_amdgcn_make_buffer_rsrc((void*)wd, (short)0, DM * FF * 2, 0x00020000);
  c.rgw = __builtin_amdgcn_make_buffer_rsrc((void*)gw, (short)0, DM * 2, 0x00020000);
  c.rws = __builtin_amdgcn_make_buffer_rsrc((void*)ws, (short)0, WS_BYTES, 0x00020000);
  c.rout = __builtin_amdgcn_make_buffer_rsrc((void*)out, (short)0, SEQ * DM * 2, 0x00020000);
  c.ws = (u64)ws;
  c.a = xcd & 3; c.T = 4 * (xcd >> 2) + (idx >> 3);
  c.m = (idx & 3) == 3 ? 6 + ((idx >> 2) & 1) : 3 * ((idx >> 2) & 1) + (idx & 3);
#define ROTM 1
  const unsigned rot = ROTM == 0 ? 0 : ROTM == 1 ? c.m + 2 * (c.T & 3) : ROTM == 2 ? 3 * ((c.m + c.T) % 3) : 4 * ((c.m + c.T) & 1);
  c.kg[0] = ROTM >= 4 ? 0 : rot >= 9 ? rot - 9 : rot;
  XS xs; xs.init(c, c.m < 6 ? M_X : A_X);
  static_for<8>([&](auto I_) { xs.template prep<1, 0, CI(I_)>(c); xs.template fire<CI(I_)>(c); });
  STAMP(55);
  SBAR();
#pragma unroll
  for (int i = 1; i < 9; ++i) {
    if (ROTM == 4) c.kg[i] = 1 + ((i + c.m + 2 * c.T) & 7);
    else if (ROTM == 5) c.kg[i] = i == 1 ? 1 : i == 2 ? 2 + (c.m + 2 * (c.T & 3)) % 7 : c.kg[i - 1] == 8 ? 2 : c.kg[i - 1] + 1;
    else c.kg[i] = c.kg[i - 1] == 8 ? 0 : c.kg[i - 1] + 1;
  }
  if (__builtin_expect(c.m < 6, 1)) role_mlp(c, xs); else role_att(c, xs);
  if (bid == 8) { const u64 e = rt_(); const u64 d = 4 * (c.t1 - c.t0); while (rt_() - e < d) __builtin_amdgcn_s_sleep(4); }
#endif
}

extern "C" void kernel_launch(void* const* d_in, const int* in_sizes, int n_in, void* d_out, int out_size, void* d_ws, size_t ws_size,
                              hipStream_t stream) {
  fused_kernel<<<256, 256, 0, stream>>>((const u16*)d_in[0], (const u16*)d_in[1], (const u16*)d_in[2], (const u16*)d_in[4], (const u16*)d_in[5],
                                        (const u16*)d_in[6], (const u16*)d_in[7], (char*)d_ws, (u16*)d_out);
}
```

```cpp
#include <hip/hip_runtime.h>
#include <utility>

typedef unsigned short u16;
typedef unsigned long long u64;
typedef short s16x4 __attribute__((ext_vector_type(4)));
typedef __bf16 bf16x8 __attribute__((ext_vector_type(8)));
typedef __bf16 bf16x2 __attribute__((ext_vector_type(2)));
typedef float f32x2 __attribute__((ext_vector_type(2)));
typedef float f32x4 __attribute__((ext_vector_type(4)));
typedef unsigned u32x2 __attribute__((ext_vector_type(2)));
typedef unsigned u32x4 __attribute__((ext_vector_type(4)));
typedef unsigned u32x6 __attribute__((ext_vector_type(6)));
typedef unsigned u32x10 __attribute__((ext_vector_type(10)));
typedef __amdgpu_buffer_rsrc_t rsrc_t;
#define LAS __attribute__((address_space(3)))
#define DI __device__ __forceinline__
#define SBAR() __builtin_amdgcn_sched_barrier(0)
#define SNAKE 0
#define SN(t, R) ((SNAKE && ((t) & 1)) ? 3 - (R) : (R))
#define ABL 0
#define DPOL ""
#define IC(x) std::integral_constant<int, (x)>{}
#define CI(x) decltype(x)::value

constexpr int SEQ = 2048, DM = 576, FF = 4608;
constexpr unsigned WS_H = 0;
constexpr unsigned WS_PART = 24u << 20;
constexpr unsigned WS_G = 40u << 20;
constexpr unsigned WS_F1 = 41u << 20;
constexpr unsigned WS_F2 = WS_F1 + 8192;
constexpr unsigned WS_E = WS_F2 + 65536;
constexpr unsigned WS_BYTES = 42u << 20;
constexpr unsigned LDS_BYTES = 163840;
constexpr unsigned S1 = 16384;
constexpr unsigned S2 = 18432;
constexpr unsigned S3 = S2 + 9216;
constexpr unsigned M_X = 65536;
constexpr unsigned A_X = 4 * S2;
constexpr unsigned A_HX = A_X + 65536;
constexpr unsigned A_GW = A_HX + 18432;
constexpr unsigned A_KX = 83968;
constexpr unsigned A_VX = A_KX + 18 * 2560;
static_assert(A_GW + 2048 <= LDS_BYTES && A_VX <= A_HX && A_VX + 20480 <= LDS_BYTES && 3 * S3 <= A_KX, "lds");

#if defined(__HIP_DEVICE_COMPILE__)
template <int... Is> using iseq = std::integer_sequence<int, Is...>;
template <class Fn, int... Is> DI void static_for_(Fn&& f, iseq<Is...>) { (f(std::integral_constant<int, Is>{}), ...); }
template <int N, class Fn> DI void static_for(Fn&& f) { static_for_(f, std::make_integer_sequence<int, N>{}); }

template <int N> DI void wait_bar() { asm volatile("s_waitcnt vmcnt(%0) lgkmcnt(0)\n\ts_barrier" ::"i"(N) : "memory"); }
template <int N> DI void wait_vm() { asm volatile("s_waitcnt vmcnt(%0)" ::"i"(N) : "memory"); }
template <class T> DI void PIN(T& x) { asm volatile("" : "+v"(x)); }
DI void tieA(bf16x8& a, bf16x8& b, bf16x8& c, bf16x8& d) { asm volatile("" : "+a"(a), "+a"(b), "+a"(c), "+a"(d)); }
template <unsigned LO, unsigned SO> DI unsigned dprep(unsigned lb, unsigned sb) {
  unsigned t;
  asm volatile("s_add_u32 m0, %1, %3\n\ts_add_u32 %0, %2, %4" : "=s"(t) : "s"(lb), "s"(sb), "i"(LO), "i"(SO) : "scc");
  return t;
}
template <unsigned LO> DI void dprep_l(unsigned lb) { asm volatile("s_add_u32 m0, %0, %1\n\ts_nop 0" ::"s"(lb), "i"(LO) : "scc"); }
template <unsigned IMM> DI void dfire(unsigned voff, rsrc_t srd, unsigned so) {
  asm volatile("buffer_load_dwordx4 %0, %1, %2 offen offset:%3" DPOL " lds" ::"v"(voff), "s"(srd), "s"(so), "i"(IMM) : "memory");
}
DI void dfire_m(unsigned voff, rsrc_t srd, unsigned so, u64 mask) {
  asm volatile("s_mov_b64 exec, %3\n\tbuffer_load_dwordx4 %0, %1, %2 offen" DPOL " lds\n\ts_mov_b64 exec, -1" ::"v"(voff), "s"(srd), "s"(so), "s"(mask) : "memory");
}
template <unsigned IMM> DI void ldh(bf16x8& d, unsigned voff, rsrc_t srd, unsigned so) {
  if constexpr (ABL & 4) asm volatile("" : "=a"(d)); else
  asm volatile("buffer_load_dwordx4 %0, %1, %2, %3 offen offset:%4 sc1" : "=a"(d) : "v"(voff), "s"(srd), "s"(so), "i"(IMM) : "memory");
}
DI void ldg(f32x4& d, unsigned voff, rsrc_t srd, unsigned so) { asm volatile("buffer_load_dwordx4 %0, %1, %2, %3 offen sc1" : "=v"(d) : "v"(voff), "s"(srd), "s"(so) : "memory"); }
template <unsigned LA> DI void touch(unsigned voff, rsrc_t srd, unsigned so) {
  asm volatile("s_mov_b32 m0, %3\n\ts_nop 0\n\tbuffer_load_dword %0, %1, %2 offen sc1 lds" ::"v"(voff), "s"(srd), "s"(so), "i"(LA) : "memory");
}
DI void st16(const u32x4& v, rsrc_t srd, unsigned voff, unsigned so) {
  __builtin_amdgcn_raw_buffer_store_b128(v, srd, voff, so, 16);
  asm volatile("s_nop 1" ::"v"(v) : "memory");
}
DI void keep(const u32x4& v) { asm volatile("" ::"v"(v)); }
template <bool AG, bool ZERO> DI void mf(f32x4& c, const bf16x8& a, const bf16x8& b) {
  if constexpr (ZERO) {
    if constexpr (AG) asm volatile("v_mfma_f32_16x16x32_bf16 %0, %1, %2, 0" : "=&v"(c) : "v"(a), "a"(b));
    else asm volatile("v_mfma_f32_16x16x32_bf16 %0, %1, %2, 0" : "=&v"(c) : "v"(a), "v"(b));
  } else {
    if constexpr (AG) asm volatile("v_mfma_f32_16x16x32_bf16 %0, %1, %2, %0" : "+v"(c) : "v"(a), "a"(b));
    else asm volatile("v_mfma_f32_16x16x32_bf16 %0, %1, %2, %0" : "+v"(c) : "v"(a), "v"(b));
  }
}
template <bool AG, bool ZERO> DI void mfs(f32x4& c, const bf16x8& a, const bf16x8& b) {
  if constexpr (ZERO) {
    if constexpr (AG) asm volatile("v_mfma_f32_16x16x32_bf16 %0, %2, %1, 0" : "=&v"(c) : "v"(a), "a"(b));
    else asm volatile("v_mfma_f32_16x16x32_bf16 %0, %2, %1, 0" : "=&v"(c) : "v"(a), "v"(b));
  } else {
    if constexpr (AG) asm volatile("v_mfma_f32_16x16x32_bf16 %0, %2, %1, %0" : "+v"(c) : "v"(a), "a"(b));
    else asm volatile("v_mfma_f32_16x16x32_bf16 %0, %2, %1, %0" : "+v"(c) : "v"(a), "v"(b));
  }
}
DI f32x4 mfc(const u32x4& a, const u32x4& b, const f32x4& c) { f32x4 d; asm volatile("v_mfma_f32_16x16x32_bf16 %0, %1, %2, %3" : "=&v"(d) : "a"(a), "v"(b), "v"(c)); return d; }
template <class T> DI void PINA(T& x) { asm volatile("" : "+a"(x)); }
DI f32x4 mfz(const u32x4& a, const u32x4& b) { f32x4 d; asm volatile("v_mfma_f32_16x16x32_bf16 %0, %1, %2, 0" : "=&v"(d) : "v"(a), "v"(b)); return d; }
DI float max2(float a, float b) { float r; asm("v_max_f32 %0, %1, %2" : "=v"(r) : "v"(a), "v"(b)); return r; }
DI float max3(float a, float b, float c) { float r; asm("v_max3_f32 %0, %1, %2, %3" : "=v"(r) : "v"(a), "v"(b), "v"(c)); return r; }
DI s16x4 trd(unsigned a) { return __builtin_amdgcn_ds_read_tr16_b64_v4i16((LAS s16x4*)a); }
DI bf16x8 cat(s16x4 lo, s16x4 hi) { return __builtin_bit_cast(bf16x8, __builtin_shufflevector(lo, hi, 0, 1, 2, 3, 4, 5, 6, 7)); }
DI unsigned cvtpk(float lo, float hi) { f32x2 v = {lo, hi}; return __builtin_bit_cast(unsigned, __builtin_convertvector(v, bf16x2)); }
DI float w_mul(float a) { if constexpr (ABL & 32) { asm volatile("" :: "v"(a)); return a; } float r; asm volatile("v_mul_f32 %0, 0xbfb8aa3b, %1" : "=v"(r) : "v"(a)); return r; }
DI float w_exp(float a) { if constexpr (ABL & 32) return a; float r; asm volatile("v_exp_f32 %0, %1" : "=v"(r) : "v"(a)); return r; }
DI unsigned w_add1(float a) { if constexpr (ABL & 32) return 0; unsigned r; asm volatile("v_fma_mixlo_f16 %0, %1, 1.0, 1.0" : "=v"(r) : "v"(a)); return r; }
DI unsigned w_rcplo(unsigned a) { if constexpr (ABL & 32) return 0x38003800u; unsigned r; asm volatile("v_rcp_f16 %0, %1" : "=v"(r) : "v"(a)); return r; }
DI void w_rcphi(unsigned& d, unsigned a) { if constexpr (ABL & 32) return; asm volatile("v_rcp_f16_sdwa %0, %1 dst_sel:WORD_1 dst_unused:UNUSED_PRESERVE src0_sel:WORD_0" : "+v"(d) : "v"(a)); }
DI unsigned w_pklo(float a, float k) { unsigned r; asm volatile("v_fma_mixlo_f16 %0, %1, %2, 0" : "=v"(r) : "v"(a), "s"(k)); return r; }
DI void w_pkhi(unsigned& d, float a, float k) { asm volatile("v_fma_mixhi_f16 %0, %1, %2, 0" : "+v"(d) : "v"(a), "s"(k)); }
template <int H> DI void w_exph(unsigned& d) { if constexpr (ABL & 32) return;
  if constexpr (H == 0) asm volatile("v_exp_f16_sdwa %0, %0 dst_sel:WORD_0 dst_unused:UNUSED_PRESERVE src0_sel:WORD_0" : "+v"(d));
  else asm volatile("v_exp_f16_sdwa %0, %0 dst_sel:WORD_1 dst_unused:UNUSED_PRESERVE src0_sel:WORD_1" : "+v"(d));
}
template <int H> DI void w_rcph(unsigned& d) { if constexpr (ABL & 32) return;
  if constexpr (H == 0) asm volatile("v_rcp_f16_sdwa %0, %0 dst_sel:WORD_0 dst_unused:UNUSED_PRESERVE src0_sel:WORD_0" : "+v"(d));
  else asm volatile("v_rcp_f16_sdwa %0, %0 dst_sel:WORD_1 dst_unused:UNUSED_PRESERVE src0_sel:WORD_1" : "+v"(d));
}
DI void w_add1h(unsigned& d) { asm volatile("v_pk_add_f16 %0, %0, 1.0 op_sel_hi:[1,0]" : "+v"(d)); }
DI float w_mixlo(float u, unsigned s) { float r; asm volatile("v_fma_mix_f32 %0, %1, %2, 0 op_sel_hi:[0,1,0]" : "=v"(r) : "v"(u), "v"(s)); return r; }
DI float w_mixhi(float u, unsigned s) { float r; asm volatile("v_fma_mix_f32 %0, %1, %2, 0 op_sel:[0,1,0] op_sel_hi:[0,1,0]" : "=v"(r) : "v"(u), "v"(s)); return r; }
DI unsigned w_cvt(float lo, float hi) { unsigned r; asm volatile("v_cvt_pk_bf16_f32 %0, %1, %2" : "=v"(r) : "v"(lo), "v"(hi)); return r; }
DI float w_scale(float a, float s) { float r; asm volatile("v_mul_f32 %0, %1, %2" : "=v"(r) : "v"(a), "v"(s)); return r; }
DI float bflo(unsigned v) { return __uint_as_float(v << 16); }
DI float bfhi(unsigned v) { return __uint_as_float(v & 0xffff0000u); }
template <class T> DI T ldsr(unsigned a) { return *(const LAS T*)a; }
template <class T> DI void ldsw(unsigned a, T v) { *(LAS T*)a = v; }
DI unsigned lane_id() { unsigned l; asm volatile("v_mbcnt_lo_u32_b32 %0, -1, 0\n\tv_mbcnt_hi_u32_b32 %0, -1, %0" : "=v"(l)); return l; }

template <int N> DI void STUB() { asm volatile(".Lgm_s%c0:\n\ts_cbranch_execz .Lgm_ret" ::"i"(N)); }
template <int N> DI void visit(u64 pc0) {
  asm volatile("s_add_u32 s98, %0, .Lgm_s%c2-.Lgm_code0\n\ts_addc_u32 s99, %1, 0\n\ts_mov_b64 exec, 0\n\ts_swappc_b64 s[100:101], s[98:99]\n\ts_mov_b64 exec, -1"
               ::"s"((unsigned)pc0), "s"((unsigned)(pc0 >> 32)), "i"(N) : "s98", "s99", "s100", "s101", "scc", "memory");
}
template <int B, int CNT> DI void visit_all(u64 pc0, unsigned w) {
  static_for<4>([&](auto W_) { if (w == CI(W_)) static_for<(CNT + 3 - CI(W_)) / 4>([&](auto I_) { visit<B + CI(W_) + 4 * CI(I_)>(pc0); }); });
}
#define STAMP(i)

struct Ctx {
  rsrc_t rx, rqkv, rwo, rwg, rwu, rwd, rgw, rws, rout;
  unsigned w, a, T, m;
  unsigned kg[9];
  u64 E, ws, pc0;
};

enum { O_NONE, O_W, O_X, O_HX, O_FIN, O_POLL, O_D2, O_B2, O_GATE, O_ST, O_TW, O_TL, O_PZ };
constexpr int NEVER = 1 << 20;
struct Op { int kind, a, b, need; };
constexpr int PD = 3;
#define MLPAD 3
constexpr int MLP_AD = MLPAD, ATT_AD = 3;
constexpr Op NOP_ = {O_NONE, 0, 0, 0};
constexpr int uk(int U, int NT) { const int o = NT == 8 ? 12 : 6; return U < o ? U : U < o + 3 ? U - o + 18 : U - 3; }
constexpr int dep(int U, int NT) { return U == 15 ? 2 : NT == 8 ? (U == 6 ? 1 : U == 12 ? 3 : -1) : (U == 6 ? 3 : U == 9 ? 1 : -1); }
constexpr int FLG2 = 4, FLGA = 1;
constexpr int RS = 19;
constexpr Op p2_op(int N, int R, int NT, int AD, int FB) {
  const int ND = NT == 8 ? 4 : 6, RG = (N + PD) / NT, pos = (N + PD) % NT, U = RG + AD;
  if (RG > 20 || pos == 0) return NOP_;
  int k = -1;
  if (RG == 0) k = 2 * (pos - PD) + (R - 2);
  else if (NT == 8 && RG == FLG2 - 1) k = 2 * (pos - 1) + (R - 2);
  else if (NT == 8) k = R == 2 ? (pos == 1 ? 0 : pos) : pos == 1 ? 1 : pos == 7 ? 8 : -1;
  else k = R == 2 ? pos - 1 : -1;
  if (NT != 8 && R == 3 && pos == 1 && dep(U + 1, NT) >= 0) return {O_POLL, dep(U + 1, NT), 0, FB + (RG + 1) * NT - PD};
  if (k < 0) return NOP_;
  if (U <= 20 && k < ND) return {O_D2, U, k, FB + U * NT - PD};
  if (U <= 20 && k < ND + 4) return {O_B2, U, k - ND, FB + U * NT - PD};
  if (NT == 8 && dep(U + 2, NT) >= 0 && k == ND + 4) return {O_POLL, dep(U + 2, NT), 0, FB + (RG + 2) * NT - PD};
  if (RG == 14 && k == ND + 4) return {O_POLL, 4, 0, FB + 17 * NT - PD};
  if (RG == 15 && k == ND + 4) return {O_GATE, 0, 0, FB + 18 * NT - PD};
  return NOP_;
}
constexpr int FIN0 = 36, ST0 = 72, FLG0 = 18, FLG1 = 25;
#define PZR 16
constexpr int lastfin(int N) { return N == 1 ? 0 : N == 3 ? 1 : N == 7 ? 2 : N == 8 ? 3 : N == 10 ? 4 : N == 11 ? 5 : N == 15 ? 6 : N == 16 ? 7 : -1; }
constexpr int bar1(int n) { return n == 0 ? -1 : 16 * n - PD; }
constexpr Op opW(int n, int i) { return {O_W, n, i, bar1(n)}; }
constexpr int XR = 10;
constexpr Op opX(int c, int i) { return {O_X, c, i, c == 0 ? -1 : 16 * (c - 1) + XR}; }
constexpr Op mlp_op(int F, int R) {
  if (F < 0) {
    const int i = F + 15;
    if (R != 2 || i < 0 || i >= 13) return NOP_;
    return i < 8 ? opX(0, i) : i < 12 ? opW(0, i - 8) : Op{O_TL, 0, 0, bar1(1)};
  }
  if (F >= 440) {
    const int N = F - 440;
    if (N < 5) return R == 2 ? Op{N < 4 ? O_D2 : O_B2, MLP_AD, N & 3, 440 + MLP_AD * 8 - PD} : N == 0 || N == 2 || N == 4 ? Op{O_B2, MLP_AD, N / 2 + 1, 440 + MLP_AD * 8 - PD} : Op{O_FIN, 2, N / 2, 440 + FLG2 * 8 - PD};
    if (R == 3 && N < 20 && lastfin(N) >= 0) return {O_FIN, 2, lastfin(N), 440 + FLG2 * 8 - PD};
    return p2_op(N, R, 8, MLP_AD, 440);
  }
  if (F >= 432) return NOP_;
  const int RG = (F + PD) / 16, pos = (F + PD) % 16, r = R - 2;
  if (pos == 0) return NOP_;
  if (F < 4) return opX(1, 2 * F + r);
  if (F < 6) return opW(1, 2 * (F - 4) + r);
  if (R == 3) return NOP_;
  if (RG <= 5) {
    const int n = F - 6 - RG, c = 2 + n / 12, i = n % 12;
    const int cap = RG + 3;
    if (RG <= 3) { if (c <= cap) return i < 8 ? opX(c, i) : opW(c, i - 8); return NOP_; }
    if (RG == 4 && pos <= 8) return pos <= 4 ? opX(6, pos + 3) : opW(6, pos - 5);
    if (RG == 4) return opX(7, pos - 9);
    if (pos == 1) return opX(7, 7);
    if (pos <= 5) return opW(7, pos - 2);
    return pos <= 13 ? opX(8, pos - 6) : opW(8, pos - 14);
  }
  if (RG == 6 && pos <= 2) return opW(8, pos + 1);
  const int k8 = pos >= 3 && pos < 15 && (pos - 3) % 3 != 2 ? (pos - 3) - (pos - 3) / 3 : -1;
  if (RG >= 6 && RG <= 8 && k8 >= 0) return opW(9 + 2 * (RG - 6) + k8 / 4, k8 % 4);
  if (F >= 144 + ST0 && F < 144 + ST0 + 32 && F % 4 == 2) return {O_FIN, 0, (F - 144 - ST0) / 4, bar1(FLG0)};
  if (F >= 288 + ST0 && F < 288 + ST0 + 16 && F % 2 == 0) return {O_FIN, 1, (F - 288 - ST0) / 2, bar1(FLG1)};
  if (RG >= 9 && RG <= 12 && (pos == 1 || pos == 15)) return {O_TW, 2 * (RG - 9) + (pos == 15), 0, NEVER};
  if (RG >= 9 && RG <= 10 && k8 >= 0) return opW(15 + 2 * (RG - 9) + k8 / 4, k8 % 4);
  if (RG >= 11 && RG <= 12 && pos % 4 == 2) return opW(RG + 8, pos / 4);
  if (RG == 13 && pos <= 4) return opW(21, pos - 1);
  if (RG >= 18 && RG <= 19 && k8 >= 0) return opW(22 + 2 * (RG - 18) + k8 / 4, k8 % 4);
  if (RG == 20 && k8 >= 0) return k8 < 4 ? opW(26, k8) : Op{O_D2, 0, k8 - 4, bar1(27)};
  if (RG >= 21 && RG <= 22 && pos <= 4) return {O_D2, RG - 20, pos - 1, 440 + (RG - 20) * 8 - PD};
  if (RG == PZR && pos % 2 == 1 && pos < 12) return {O_PZ, pos / 2, 0, bar1(FLG0)};
  if (RG == 22 && pos == 7) return {O_POLL, 0, 0, bar1(25)};
  if (RG >= 25 && RG <= 26) {
    const int k = (RG - 25) * 15 + pos - 1;
    if (k < 24 && k % 2 == 0) { const int U = k / 8; return {O_B2, U, (k / 2) % 4, U == 0 ? bar1(27) : 440 + U * 8 - PD}; }
    if (MLP_AD > 3 && k < 16 && k % 2 == 1) { const int i = k / 2; return i < 4 ? Op{O_D2, 3, i, 440 + 24 - PD} : NOP_; }
    if (MLP_AD > 3 && k >= 24 && k < 28) return {O_B2, 3, k - 24, 440 + 24 - PD};
  }
  return NOP_;
}
static_assert(MLP_AD == 3 || MLP_AD == 4, "pre-issue table");
constexpr int barA(int n) { return n == 0 ? -1 : n <= 9 ? 16 * n - PD : 144 + 14 * (n - 9) - PD; }
constexpr int rgA(int F) { return F + PD < 144 ? (F + PD) / 16 : 9 + (F + PD - 144) / 14; }
constexpr int posA(int F) { return F + PD < 144 ? (F + PD) % 16 : (F + PD - 144) % 14; }
struct AttSeq {
  Op op[270] = {};
  constexpr AttSeq() {
    constexpr int seq[27][2] = {{O_X, 2}, {O_W, 2}, {O_HX, 1}, {O_X, 3}, {O_W, 3}, {O_HX, 2}, {O_W, 4}, {O_X, 4}, {O_HX, 3}, {O_HX, 4}, {O_W, 5}, {O_X, 5}, {O_W, 6}, {O_X, 6},
                                {O_W, 7}, {O_X, 7}, {O_W, 8}, {O_X, 8}, {O_W, 9}, {O_W, 10}, {O_W, 11}, {O_W, 12}, {O_W, 13}, {O_W, 14}, {O_W, 15}, {O_W, 16}, {O_W, 17}};
    int F = 6;
    for (int e = 0; e < 27; ++e) {
      const int k = seq[e][0], a = seq[e][1], n = k == O_X ? 8 : k == O_W ? 4 : 1, rg = k == O_X ? a - 2 : k == O_W ? a - 3 : 0;
      for (int i = 0; i < n; ++i) {
        while (posA(F) == 0 || rgA(F) < rg || (F >= 138 && F < 148)) ++F;
        op[F++] = k == O_X ? opX(a, i) : k == O_W ? Op{O_W, a, i, barA(a)} : Op{O_HX, a, 0, barA(a < 3 ? 2 * a : 5)};
      }
    }
    int z = 0;
    for (int G = 150; G < 270; ++G) {
      const int RG = rgA(G), pos = posA(G);
      if (RG >= 10 && RG <= 14 && (pos == 7 || pos == 10) && z < 9) op[G] = {O_PZ, z++, 0, barA(16)};
      if (RG == 16 && pos >= 2 && pos < 8) op[G] = {O_D2, 0, pos - 2, NEVER};
    }
  }
};
constexpr AttSeq ATT_SEQ{};
constexpr Op att1_op(int F, int R) {
  if (F < 0) {
    const int i = F + 16;
    if (R != 2 || i < 0 || i >= 14) return NOP_;
    return i < 8 ? opX(0, i) : i < 12 ? Op{O_W, 0, i - 8, -1} : i == 12 ? Op{O_HX, 0, 0, -1} : Op{O_TL, 0, 0, NEVER};
  }
  if (F < 4) return opX(1, 2 * F + R - 2);
  if (F < 6) return {O_W, 1, 2 * (F - 4) + R - 2, barA(1)};
  return R == 2 && F < 270 ? ATT_SEQ.op[F] : NOP_;
}
constexpr Op att2_op(int F, int R) {
  if (F < 0) {
    const int i = F + 50;
    if (R != 2 || i < 0 || i >= 43) return NOP_;
    constexpr int n1 = 12 - PD, n2 = 24 - PD, ns = FLGA * 12 - PD;
    if (i < 6) return {O_D2, 0, i, -1};
    if (i == 6) return {O_POLL, 0, 0, -28};
    if (i < 13) return {O_D2, 1, i - 7, n1};
    if (i < 16) return {O_ST, 0, 0, ns};
    if (i < 22) return {O_D2, 2, i - 16, n2};
    if (i < 25) return {O_ST, 0, 0, ns};
    if (i < 29) return {O_B2, 0, i - 25, -1};
    if (i < 33) return {O_B2, 1, i - 29, n1};
    if (i < 36) return {O_ST, 0, 0, ns};
    if (i < 40) return {O_B2, 2, i - 36, n2};
    return {O_ST, 0, 0, ns};
  }
  return p2_op(F, R, 12, ATT_AD, 0);
}
template <int WHICH> constexpr Op op_at(int F, int R) { return WHICH == 0 ? mlp_op(F, R) : WHICH == 1 ? att1_op(F, R) : att2_op(F, R); }
template <int WHICH> constexpr int vm_at(int tau) {
  int s = 0;
  for (int F = tau - 1; F >= -64; --F)
    for (int R = 3; R >= 2; --R) {
      const Op o = op_at<WHICH>(F, R);
      if (o.kind == O_NONE) continue;
      if (o.need <= tau || s >= 63) return s;
      ++s;
    }
  return s;
}
template <int WHICH, int TAU> constexpr int VM = vm_at<WHICH>(TAU);

DI void load_epoch(Ctx& c) {
  asm volatile("s_load_dwordx2 %0, %1, %2" : "=s"(c.E) : "s"(c.ws), "s"(WS_E + c.T * 128) : "memory");
}
DI void flag_store(const Ctx& c, unsigned off, unsigned val) {
  const u64 v = c.E + val;
  __builtin_amdgcn_raw_buffer_store_b64(u32x2{(unsigned)v, (unsigned)(v >> 32)}, c.rws, 0, off, 16);
}
DI void poll_issue(const Ctx& c, u32x2& f, unsigned voff, unsigned off) {
  asm volatile("buffer_load_dwordx2 %0, %1, %2, %3 offen sc1" : "=v"(f) : "v"(voff), "s"(c.rws), "s"(off) : "memory");
}
DI bool flag_ok(const Ctx& c, u32x2 f, unsigned need) {
  const u64 d = (((u64)f[1] << 32) | f[0]) - c.E - need;
  return __builtin_amdgcn_ballot_w64(d >= 8) == 0;
}
DI void poll_check(const Ctx& c, u32x2& f, unsigned voff, unsigned off, unsigned need) {
  asm volatile("" : "+v"(f));
  if (__builtin_expect(!flag_ok(c, f, need), 0)) {
    for (unsigned spin = 0; spin < (1u << 16); ++spin) {
      __builtin_amdgcn_s_sleep(1);
      asm volatile("buffer_load_dwordx2 %0, %1, %2, %3 offen sc1\n\ts_waitcnt vmcnt(0)" : "=&v"(f) : "v"(voff), "s"(c.rws), "s"(off) : "memory");
      if (flag_ok(c, f, need)) break;
    }
  }
}

constexpr unsigned POISON = 0xffffffffu;
DI unsigned inbox(const Ctx& c) { return WS_PART + ((c.T * 8 + c.m) * 64 + c.w) * 3072; }
DI unsigned flag2(const Ctx& c) { return WS_F2 + ((c.T * 8 + c.m) * 4 + c.w) * 128; }
template <int NI, int J> DI void poison(const Ctx& c, u32x4& pz, unsigned vl16) {
  if constexpr (J == 0) { pz = u32x4{POISON, POISON, POISON, POISON}; PIN(pz); }
  asm volatile("buffer_store_dwordx4 %0, %1, %2, %3 offen offset:%4 sc1" ::"v"(pz), "v"(vl16), "s"(c.rws), "s"(inbox(c) + (c.a * 4 + ((J / NI + 1) ^ c.a)) * 12288), "i"((J % NI) * 1024) : "memory");
}
template <int NT> struct P2 {
  static constexpr int AD = NT == 8 ? MLP_AD : ATT_AD, NS = NT == 8 ? 8 : 4, FB = NT == 8 ? 440 : 0, ROT = NT == 8 ? 4 : 0;
  static constexpr unsigned SS = NT == 8 ? S2 : S3;
  static constexpr int sidx(int u) { return NT == 8 ? (u + ROT) % NS : u % 4 == 0 ? 2 : u % 4 == 3 ? 3 : u % 4 - 1; }
  static constexpr unsigned slot(int u) { return sidx(u) * SS; }
  static constexpr int WN = NT == 8 ? 3 : 2;
  static constexpr unsigned win(int u) { return (sidx(u) / WN * WN) * SS; }
  unsigned v128[2], v64, vh[2], vf;
  unsigned lw, lw64, sd, so, sh[2];
  unsigned f1, cur, curh;
  bf16x8 bf[21][4];
  u32x2 pf;
  f32x4 omg;
  DI void init(const Ctx& c) {
    const unsigned l = lane_id(), colb = NT == 8 ? 256 * (c.m >> 1) : 768, kh = c.w >> 1;
    const unsigned lo = 16 * ((l & 15) ^ ((l >> 3) & 2) ^ (4 * (c.a ^ 3)));
    v128[0] = (l >> 4) * 4608 + lo; v128[1] = (l >> 4) * 4608 + (lo ^ 64);
    v64 = (l >> 3) * 2304 + 16 * ((l & 7) ^ (((l >> 1) & 4) | ((l >> 3) & 2)) ^ (2 * (c.a ^ 3))) + 256;
    vh[0] = l * 16 + kh * 2048; vh[1] = l * 16 + (1 - kh) * 2048;
    vf = 8 * (l & 7);
    lw = c.w * 4608; lw64 = c.w * 2304 + S2;
    sd = (1152 * c.a + 16 * c.w) * 1152 + colb;
    so = (144 * c.a + 72 * kh + 16 * (c.w & 1)) * 1152 + colb;
    sh[0] = WS_H + (((4 * c.T + 2 * (c.m & 1) + (c.w & 1)) * 4 + c.a) * 42 + kh) * 4096; sh[1] = sh[0] + kh * 8192;
    f1 = WS_F1 + (c.T * 4 + c.a) * 128;
  }
  template <int S> DI unsigned pvo() const { return S < 3 ? (vf < 48 ? vf : 0) : S == 3 ? 48 + (vf & 8) : vf & 24; }
  template <int S> DI void poll(const Ctx& c) { poll_issue(c, pf, pvo<S>(), S < 4 ? f1 : flag2(c)); }
  template <int S> DI void check(const Ctx& c) { poll_check(c, pf, pvo<S>(), S < 4 ? f1 : flag2(c), S < 3 ? S + 1 : S == 3 ? 3 : 1); }
  template <int KIND, int U, int I> DI void prep(const Ctx& c) {
    if constexpr (KIND == O_D2) {
      constexpr int K = uk(U, NT);
      constexpr unsigned row = K < 18 ? 64 * K : 32 * (K - 18);
      if constexpr (I == 0) cur = dprep<slot(U), row * 1152>(lw, K < 18 ? sd : so);
      if constexpr (I == 4) dprep_l<slot(U)>(lw64);
    }
    if constexpr (KIND == O_B2 && U < RS && (I == 0 || (NT == 8 && U == AD && I == 1))) { curh = sh[uk(U, NT) >= 18] + (uk(U, NT) < 18 ? 2 * uk(U, NT) : 18 + uk(U, NT)) * 4096; asm volatile("" : "+s"(curh)); }
    if constexpr (KIND == O_B2 && U >= RS && (I & 1) == 0) { curh = sh[0] - (c.w >> 1) * 4096 + (2 * uk(U, NT) + (I >> 1)) * 4096; asm volatile("" : "+s"(curh)); }
  }
  template <int KIND, int U, int I> DI void fire(const Ctx& c) {
    if constexpr (KIND == O_D2 && !(ABL & 8)) dfire<(I & 3) * 1152>(I < 4 ? v128[I >> 1] : v64, uk(U, NT) < 18 ? c.rwd : c.rwo, cur);
    if constexpr (KIND == O_B2) ldh<(I & 1) * 1024>(bf[U][I], vh[U < RS ? I >> 1 : 0], c.rws, curh);
    if constexpr (KIND == O_POLL) poll<U>(c);
    if constexpr (KIND == O_GATE) ldg(omg, vh[0] & 255, c.rws, WS_G + (c.a * 32 + 4 * c.T + 2 * (c.m & 1) + (c.w & 1)) * 256);
  }
  static constexpr int tp(int t) { return NT == 8 || t < 6 ? t : t < 9 ? t + 2 : t < 11 ? t - 3 : 11; }
  static DI unsigned rbase(const Ctx& c, unsigned l, int t) {
    const unsigned g = l >> 4, q = (l >> 2) & 3;
    return (c.w >> 1) * 9216 + q * 1152 + g * 256 + 32 * (t ^ ((q & 2) | (g & 1))) + 8 * (l & 3);
  }
};

template <int NT, class Extra> DI void phase2(Ctx& c, P2<NT>& p, bf16x8 (&f)[16], Extra&& extra) {
  using P = P2<NT>;
  constexpr int AD = P::AD, WH = NT == 8 ? 0 : 2;
  const unsigned l = lane_id(), g = l >> 4, q = (l >> 2) & 3, kh = c.w >> 1;
  unsigned rb[4], rb64[4];
#pragma unroll
  for (int t = 0; t < 4; ++t) {
    rb[t] = P::win(0) + P::rbase(c, l, t); PIN(rb[t]);
    if (NT != 8) { rb64[t] = P::win(0) + S2 + kh * 4608 + (q & 1) * 1152 + (2 * g + (q >> 1)) * 128 + 32 * (t ^ ((q & 2) | (g & 1))) + 8 * (l & 3); PIN(rb64[t]); }
  }
  f32x4 acc[NT][4], oth[NT][2];
  bf16x8 f2[NT];
  s16x4 flo;
  auto xoff = [](int t) { return NT == 8 ? 20480 + t * 8192 : t < 6 ? t * 8192 : t < 9 ? (t - 6) * 8192 : 28672 + (t - 9) * 8192; };
  unsigned wa[2] = {0, 0}, ra[2] = {0, 0}, l16 = 0;
  const unsigned pb = inbox(c);
  auto rd = [&](auto U_, auto T_, auto H_) {
    constexpr int t = P::tp(CI(T_)), H = CI(H_);
    constexpr unsigned o = P::slot(CI(U_)) - P::win(CI(U_));
    return t < 8 ? trd(rb[t & 3] + o + 128 * (t >> 2) + H * 4608) : trd(rb64[t & 3] + o + H * 2304);
  };
  if constexpr (NT != 8) {
    wait_bar<VM<2, -1>>();
    STAMP(24);
    tieA(p.bf[0][0], p.bf[0][1], p.bf[0][2], p.bf[0][3]);
    static_for<PD>([&](auto T_) { f[CI(T_)] = cat(rd(IC(0), T_, IC(0)), rd(IC(0), T_, IC(1))); });
  }
  auto send = [&](auto T_, auto R_) {
    constexpr int t = CI(T_), R = CI(R_), it = P::tp(t), dq = it < 8 ? (it >> 1) ^ 3 : (it - 8) ^ 3, idx = it < 8 ? it & 1 : 2;
    if constexpr (R < 2) { acc[t][R] += oth[t][R]; PIN(acc[t][R]); }
    if constexpr (R == 2 && dq != 0) {
      const u32x4 v = {cvtpk(acc[t][0][0], acc[t][0][1]), cvtpk(acc[t][0][2], acc[t][0][3]), cvtpk(acc[t][1][0], acc[t][1][1]), cvtpk(acc[t][1][2], acc[t][1][3])};
      st16(__builtin_elementwise_min(v, u32x4{POISON - 1, POISON - 1, POISON - 1, POISON - 1}), c.rws, l16 + idx * 1024, pb + ((dq ^ c.a) * 4 + c.a) * 12288);
    }
  };
  static_for<21>([&](auto U_) {
    static_for<NT>([&](auto T_) {
      constexpr int U = CI(U_), t = CI(T_), N = U * NT + t, UN = (N + PD) / NT, tn = (N + PD) % NT;
      constexpr int RR = tn == 0 ? 2 : 0;
      const bf16x8 a = f[t], a2 = f2[t];
      static_for<4>([&](auto R_) {
        constexpr int R = CI(R_);
        if constexpr (U < RS) mf<true, U == 0>(acc[t][R], a, p.bf[U][R]); else mf<true, false>(acc[t][R & 1], R < 2 ? a : a2, p.bf[U][R]);
        SBAR();
        if constexpr (tn == 0 && UN <= 20) {
          if constexpr (R < 2 && (P::win(UN) != P::win(UN - 1) || UN == RS)) {
            const unsigned d = P::win(UN) - P::win(UN - 1);
#pragma unroll
            for (int i = 2 * R; i < 2 * R + 2; ++i) {
              rb[i] += d - (UN == RS ? kh * 9216 : 0); PIN(rb[i]);
              if (NT != 8) { rb64[i] += d - (UN == RS ? kh * 4608 : 0); PIN(rb64[i]); }
            }
          }
          if constexpr (R == 2) {
            if constexpr (NT == 8 && UN % 5 == 1) STUB<13 + UN / 5>();
            if constexpr (NT == 8 && UN == 20) STUB<17>();
            if constexpr (NT != 8 && UN % 3 == 2) STUB<46 + UN / 3>();
            wait_bar<(UN == (NT == 8 ? FLG2 : FLGA) ? 0 : VM<WH, P::FB + N>)>();
            STAMP(UN + (NT == 8 ? 28 : 24));
            tieA(p.bf[UN][0], p.bf[UN][1], p.bf[UN][2], p.bf[UN][3]);
          }
        }
        if constexpr (UN < RS || (UN <= 20 && tn != 0)) {
          if constexpr (R == RR) flo = rd(IC(UN), IC(tn), IC(0));
          if constexpr (R == RR + 1) f[tn] = cat(flo, rd(IC(UN), IC(tn), IC(1)));
          if constexpr (UN >= RS && R == 2) flo = rd(IC(UN), IC(tn), IC(2));
          if constexpr (UN >= RS && R == 3) f2[tn] = cat(flo, rd(IC(UN), IC(tn), IC(3)));
        } else if constexpr (UN <= 20) {
          if constexpr (R == 2) f[0] = cat(rd(IC(UN), IC(0), IC(0)), rd(IC(UN), IC(0), IC(1)));
          if constexpr (R == 3) f2[0] = cat(rd(IC(UN), IC(0), IC(2)), rd(IC(UN), IC(0), IC(3)));
        }
        SBAR();
        if constexpr (tn == 0 && R == 3 && UN <= 20) {
          if constexpr (UN == (NT == 8 ? FLG2 : FLGA)) { if (threadIdx.x == 0) flag_store(c, p.f1 + 8 * c.m, 3); }
          if constexpr (dep(UN + AD, NT) >= 0) p.template check<dep(UN + AD, NT)>(c);
          if constexpr (UN == 17) p.template check<4>(c);
          if constexpr (UN == 18) {
            PIN(p.omg);
            l16 = lane_id() * 16; PIN(l16);
            wa[0] = l16 + c.w * 2048 + (NT == 8 ? 0 : 4 * S3); wa[1] = l16 + c.w * 2048 + (NT == 8 ? 32768 : 0); PIN(wa[0]); PIN(wa[1]);
          }
          if constexpr (UN == 20) { const unsigned d = kh ? 0u - 4096 : 4096; ra[0] = wa[0] + d; ra[1] = wa[1] + d; PIN(ra[0]); PIN(ra[1]); }
        }
        extra(IC(N), R_);
        {
          constexpr int gi = N - (RS - 1) * NT - 2, wi = gi < 0 ? -1 : gi < 5 ? (R < 2 ? 2 * gi + R : -1) : (R == 0 ? gi + 5 : -1);
          if constexpr (wi >= 0 && wi < 2 * NT) {
            constexpr int tt = wi / 2, hi = NT == 8 ? tt >= 4 : tt >= 6;
            ldsw<f32x4>(wa[hi] + xoff(tt) - (NT == 8 && hi ? 32768 : 0) + (wi & 1) * 1024, acc[tt][2 + (wi & 1)]);
          }
          constexpr int rt_ = N - (20 * NT - 2);
          if constexpr (rt_ >= 0 && rt_ < NT && R < 2) {
            constexpr int hi = NT == 8 ? rt_ >= 4 : rt_ >= 6;
            oth[rt_][R] = ldsr<f32x4>(ra[hi] + xoff(rt_) - (NT == 8 && hi ? 32768 : 0) + R * 1024);
          }
          if constexpr (U == 20 && t >= 2) send(IC(t - 2), R_);
        }
        if constexpr (R >= 2) {
          constexpr Op o = op_at<WH>(P::FB + N, R), n = op_at<WH>(P::FB + N + 1, 2);
          if constexpr (R == 3 || N == 0) p.template prep<o.kind, o.a, o.b>(c);
          p.template fire<o.kind, o.a, o.b>(c);
          if constexpr (R == 3) p.template prep<n.kind, n.a, n.b>(c);
        }
        SBAR();
      });
    });
  });
  STUB<(NT == 8 ? 18 : 53)>();
  STAMP(NT == 8 ? 49 : 45);
  const unsigned l2 = l16 >> 4;
  STAMP(NT == 8 ? 50 : 46);
  constexpr int NI = NT / 4;
  u32x4 in[3][NI];
  for (unsigned spin = 0; spin < (1u << 16); ++spin) {
    unsigned mx = 0;
#pragma unroll
    for (int s = 1; s < 4; ++s)
#pragma unroll
      for (int i = 0; i < NI; ++i) in[s - 1][i] = __builtin_amdgcn_raw_buffer_load_b128(c.rws, l16 + i * 1024, pb + (c.a * 4 + (s ^ c.a)) * 12288, 16);
    asm volatile("" ::: "memory");
#pragma unroll
    for (int s = 0; s < 3; ++s)
#pragma unroll
      for (int i = 0; i < NI; ++i) mx = max(max(mx, max(in[s][i][0], in[s][i][1])), max(in[s][i][2], in[s][i][3]));
    if (__builtin_amdgcn_ballot_w64(mx == POISON) == 0) break;
  }
  STAMP(NT == 8 ? 51 : 47);
  const unsigned row0 = 256 * c.T + 128 * (c.m & 1) + 64 * (c.w & 1) + 32 * kh;
  const unsigned vo = (l2 & 15) * 2304 + 8 * (l2 >> 4), colb = NT == 8 ? 256 * (c.m >> 1) : 768;
#pragma unroll
  for (int i = 0; i < NI; ++i) {
    const int t = NT - NI + i;
    f32x4 s0 = acc[t][0], s1 = acc[t][1];
    if (t >= NT - 2) { s0 += oth[t][0]; s1 += oth[t][1]; }
#pragma unroll
    for (int s = 0; s < 3; ++s) {
      s0 += f32x4{bflo(in[s][i][0]), bfhi(in[s][i][0]), bflo(in[s][i][1]), bfhi(in[s][i][1])};
      s1 += f32x4{bflo(in[s][i][2]), bfhi(in[s][i][2]), bflo(in[s][i][3]), bfhi(in[s][i][3])};
    }
    s0 *= kh ? p.omg[2] : p.omg[0]; s1 *= kh ? p.omg[3] : p.omg[1];
    const unsigned cb = colb + (i < 2 ? 64 * c.a + 32 * i : 256 + 32 * c.a);
    __builtin_amdgcn_raw_buffer_store_b64(u32x2{cvtpk(s0[0], s0[1]), cvtpk(s0[2], s0[3])}, c.rout, vo, row0 * 1152 + cb, 0);
    __builtin_amdgcn_raw_buffer_store_b64(u32x2{cvtpk(s1[0], s1[1]), cvtpk(s1[2], s1[3])}, c.rout, vo, (row0 + 1) * 1152 + cb, 0);
  }
  if (NT == 8 && c.a == 0 && c.m == 0 && c.w == 0 && l2 == 0) flag_store(c, WS_E + c.T * 128, 16);
  if constexpr (NT == 8) asm volatile(".Lgm_code1:" ::: "memory"); else asm volatile(".Lgm_code2:" ::: "memory");
  STAMP(NT == 8 ? 52 : 48);
}

struct XS {
  unsigned v[2], lw, sx, rd[2], cur, sg;
  DI void init(const Ctx& c, unsigned base) {
    const unsigned l = lane_id(), j = l & 15, g = l >> 4;
    v[0] = (l >> 3) * 2304 + 16 * ((l & 7) ^ g); v[1] = (l >> 3) * 2304 + 16 * ((l & 7) ^ (4 + g));
    lw = base + c.w * 8192; sx = (256 * c.T + 64 * c.w) * 1152;
    rd[0] = lw + j * 128 + 16 * (g ^ (j >> 1)); rd[1] = rd[0] ^ 64;
    PIN(rd[0]); PIN(rd[1]);
  }
  template <int NSL, int C, int I> DI void prep(const Ctx& c) {
    if constexpr (I == 0) sg = sx + c.kg[C] * 128;
    cur = dprep<(C % NSL) * 32768 + I * 1024, (32 * (I >> 2) + 16 * (I & 1) + ((I >> 1) & 1)) * 1152>(lw, sg);
  }
  template <int I> DI void fire(const Ctx& c) { dfire<0>(v[I & 1], c.rx, cur); }
};
template <unsigned LA> DI void touch_pages(const Ctx& c) {
  const unsigned l = lane_id();
  const unsigned o = l < 4 ? WS_H + (((4 * c.T + l) * 4 + c.a) * 42 + 2 * c.m) * 4096 : l < 8 ? WS_PART + ((c.T * 8 + c.m) * 64 + (l - 4) * 16) * 3072 : l < 9 ? WS_G : WS_F1;
  touch<LA>(c.w < 2 ? o : (256 * c.T + 64 * (l & 3)) * 1152, c.w < 2 ? c.rws : c.rout, 0);
}
template <int I> DI void touch_w2(const Ctx& c, unsigned v128) {
  const unsigned idx = (c.T & 3) * 6 + c.m;
  const unsigned so = I < 7 ? (1152 * c.a + 48 * idx) * 1152 + c.w * 2048 + I * 8192 : (144 * c.a + 7 * idx) * 1152 + c.w * 2048;
  asm volatile("s_mov_b32 m0, %3\n\ts_mov_b64 exec, 0xffff\n\tbuffer_load_dword %0, %1, %2 offen sc1 lds\n\ts_mov_b64 exec, -1"
               ::"v"(v128), "s"(I < 7 ? c.rwd : c.rwo), "s"(so), "i"(9 * S1) : "memory");
}

constexpr int slot1(int n) { return n < 9 ? n % 4 : n % 9 < 5 ? 4 + n % 9 : n % 9 % 4; }
constexpr int sig_idx(int F) {
  if ((F + PD) % 16 == 0) return -1;
  int e = 0;
  for (int G = F - F % 72; G < F; ++G) if ((G + PD) % 16 != 0) ++e;
  return e;
}
constexpr int w_ks(int n, int I) { return n < 9 ? 2 * n + (I >> 1) : ((n % 9) * 4 + I) % 18; }
constexpr int w_up(int n, int I) { return n < 9 ? I & 1 : (n % 9) * 4 + I >= 18; }
DI void role_mlp(Ctx& c, XS& xs) {
  const unsigned l = lane_id(), g = l >> 4, q = (l & 15) >> 2, pp = l & 3, r8 = l >> 3;
  const unsigned vW = (8 * (r8 >> 2) + (r8 & 3)) * 9216 + 16 * ((l & 7) ^ (((r8 >> 1) & 3) << 1));
  const unsigned lw = c.w * 1024, sW = (16 * (c.w & 1) + 4 * (c.w >> 1)) * 9216 + (1152 * c.a + 64 * c.m) * 2;
  unsigned sWk[9];
#pragma unroll
  for (int i = 0; i < 9; ++i) sWk[i] = sW + c.kg[i] * (64 * 9216);
  const unsigned sH = WS_H + (((4 * c.T + c.w) * 4 + c.a) * 42 + 2 * c.m) * 4096;
  const unsigned f1 = WS_F1 + (c.T * 4 + c.a) * 128;
  bf16x8 xf[18][4];
  f32x4 ag0[4][4], ag[4][4], au[4][4];
  const float nl2e = -1.4426950408889634f;
  unsigned sp[4][4][2];
  P2<8> p2;
  u32x4 hq[8], pz;
  unsigned vl16 = l * 16;
  unsigned cur = 0;
  auto prep = [&](auto F_, auto R_) {
    constexpr Op o = mlp_op(CI(F_), CI(R_));
    if constexpr (o.kind == O_W) {
      constexpr int pn = o.a / 9, k = w_ks(o.a, o.b);
      cur = dprep<slot1(o.a) * S1 + o.b * 4096, (k & 1) * 32 * 9216 + pn * 768>(lw, sWk[k >> 1]);
    } else if constexpr (o.kind == O_X) xs.template prep<3, o.a, o.b>(c);
    else p2.template prep<o.kind, o.a, o.b>(c);
  };
  auto fire = [&](auto F_, auto R_) {
    constexpr Op o = mlp_op(CI(F_), CI(R_));
    if constexpr (o.kind == O_W && (ABL & 16) && o.a >= 14) {}
    else if constexpr (o.kind == O_W) dfire<0>(vW, w_up(o.a, o.b) ? c.rwu : c.rwg, cur);
    else if constexpr (o.kind == O_X) {
      if constexpr (o.a >= 3 && o.b == 0) asm volatile("s_waitcnt lgkmcnt(4)" ::: "memory");
      xs.template fire<o.b>(c);
    } else if constexpr (o.kind == O_FIN) {
      __builtin_amdgcn_raw_buffer_store_b128(hq[o.b], c.rws, vl16 + (o.b & 3) * 1024, sH + (12 * o.a + (o.b >> 2)) * 4096, 16);
      asm volatile("" ::: "memory");
    } else if constexpr (o.kind == O_PZ) poison<2, o.a>(c, pz, vl16);
    else if constexpr (o.kind == O_TW) touch_w2<o.a>(c, vl16 * 8);
    else if constexpr (o.kind == O_TL) touch_pages<3 * S1>(c);
    else p2.template fire<o.kind, o.a, o.b>(c);
  };
  static_for<5>([&](auto I_) { prep(IC(CI(I_) - 7), IC(2)); fire(IC(CI(I_) - 7), IC(2)); });
  STAMP(56);
  visit_all<0, 19>(c.pc0, c.w);
  STAMP(57);
  load_epoch(c);
  unsigned rdx2[2] = {xs.rd[0] + 65536, xs.rd[1] + 65536}; PIN(rdx2[0]); PIN(rdx2[1]);
  auto rdx = [&](auto C_, auto I_) {
    constexpr int C = CI(C_), s = CI(I_) >> 2, rt = CI(I_) & 3;
    xf[2 * C + s][rt] = ldsr<bf16x8>((C % 3 == 2 ? rdx2[s] : xs.rd[s] + (C % 3) * 32768) + rt * 2048);
  };
  unsigned rb[4];
#pragma unroll
  for (int t = 0; t < 4; ++t) rb[t] = (4 * g + q) * 128 + 32 * (t ^ ((2 * g + (q >> 1)) & 3)) + 8 * pp;
  float e0 = 0.f, e1 = 0.f, e3 = 0.f, h0 = 0.f, h1 = 0.f;
  unsigned e2 = 0;
  bf16x8 f[16];
  s16x4 flo;
  unsigned rb2[3];
  wait_bar<VM<0, -1>>();
  asm volatile("" : "+s"(c.E));
  STAMP(1);
  static_for<4>([&](auto I_) { rdx(IC(0), I_); });
  static_for<PD>([&](auto I_) { f[CI(I_)] = cat(trd(rb[CI(I_)]), trd(rb[CI(I_)] + 2048)); });
  static_for<4>([&](auto I_) { rdx(IC(0), IC(4 + CI(I_))); });
  SBAR();
  static_for<27>([&](auto N_) {
    static_for<16>([&](auto I_) {
      constexpr int N = CI(N_), I = CI(I_), F = N * 16 + I, FP = F % 144, ps = w_up(N, I >> 2), ks = w_ks(N, I >> 2), t = I & 3;
      constexpr int RG = (F + PD) / 16, pos = (F + PD) % 16, RR = pos == 0 ? 2 : 0;
      constexpr unsigned ro = (slot1(RG) % 4) * S1 + (pos >> 2) * 4096;
      const bf16x8 a = f[I];
      static_for<4>([&](auto R_) {
        constexpr int R = CI(R_);
        if constexpr (ps == 0) mf<(ks < 16), ks == 0>(F < 144 ? ag0[t][SN(t, R)] : ag[t][SN(t, R)], a, xf[ks][SN(t, R)]); else mf<(ks < 16), ks == 0>(au[t][SN(t, R)], a, xf[ks][SN(t, R)]);
        SBAR();
        if constexpr (R == 0 && N < 8 && I == XR) wait_vm<VM<0, F>>();
        if constexpr (pos == 0) {
          if constexpr (R < 2 && RG < 27 && slot1(RG) / 4 != slot1(RG - 1) / 4) {
#pragma unroll
            for (int i = 2 * R; i < 2 * R + 2; ++i) { rb[i] += (unsigned)(slot1(RG) / 4 - slot1(RG - 1) / 4) * 4 * S1; PIN(rb[i]); }
          }
          if constexpr (R == 0 && RG == 27) {
            const unsigned l4 = lane_id();
#pragma unroll
            for (int i = 0; i < 3; ++i) { rb2[i] = P2<8>::slot(0) + P2<8>::rbase(c, l4, i); PIN(rb2[i]); }
          }
          if constexpr (R == 2) {
            if constexpr (RG % 2 == 0) STUB<RG / 2 - 1>();
            static_assert((RG != FLG0 && RG != FLG1) || VM<0, F> == 0, "flags follow a full drain");
            wait_bar<VM<0, F>>();
            STAMP(1 + RG);
            if constexpr (RG == 27) tieA(p2.bf[0][0], p2.bf[0][1], p2.bf[0][2], p2.bf[0][3]);
          }
        }
        if constexpr (R == RR) { if constexpr (RG < 27) flo = trd(rb[pos & 3] + ro); else flo = trd(rb2[pos]); }
        if constexpr (R == RR + 1) { if constexpr (RG < 27) f[pos] = cat(flo, trd(rb[pos & 3] + ro + 2048)); else f[pos] = cat(flo, trd(rb2[pos] + 4608)); }
        if constexpr (N < 8 && (I == XR || I == XR + 1)) rdx(IC(N + 1), IC(4 * (I - XR) + R));
        SBAR();
        if constexpr (pos == 0 && R == 3) {
          if constexpr (RG == FLG0 || RG == FLG1) { if (threadIdx.x == 0) flag_store(c, f1 + 8 * c.m, RG == FLG0 ? 1 : 2); }
          if constexpr (RG == PZR + 1) keep(pz);
          if constexpr (RG == FLG0) { if (vl16 == 0) flag_store(c, flag2(c) + 8 * c.a, 1); }
          if constexpr (RG == 19) p2.init(c);
          if constexpr (RG == 25) p2.template check<0>(c);
        }
        if constexpr (R >= 2) { if constexpr (R == 3 || F == 0) prep(IC(F), R_); fire(IC(F), R_); }
        if constexpr (F >= 144 && ps == 1 && sig_idx(F) >= 0 && sig_idx(F) <= 65) {
          constexpr int e = sig_idx(F), d = e - 2;
          if constexpr (R == 0 && d >= 0) e2 = w_add1(e1);
          if constexpr (R == 1) { e3 = e0; if constexpr (e < 64) e0 = w_mul(ag[e / 16][(e / 4) % 4][e % 4]); }
          if constexpr (R == 2 && e >= 1 && e <= 64) e1 = w_exp(e3);
          if constexpr (R == 3 && d >= 0) { if constexpr (d % 2 == 0) sp[d / 16][(d / 4) % 4][(d % 4) / 2] = w_rcplo(e2); else w_rcphi(sp[d / 16][(d / 4) % 4][(d % 4) / 2], e2); }
        }
        if constexpr (F >= 138 && F < 146) {
          constexpr int tt = (F - 138) / 2, rt = 2 * (F & 1) + (R >> 1), k = R & 1;
          sp[tt][rt][k] = w_pklo(ag0[tt][rt][2 * k], nl2e); w_pkhi(sp[tt][rt][k], ag0[tt][rt][2 * k + 1], nl2e);
        }
        if constexpr (F >= 144 && F < 216 && sig_idx(F) >= 0) {
          constexpr int e = sig_idx(F), d = e - 2, pa = (e - 2) / 2, pf = (e - 4) / 2;
          if constexpr (R == 0 && e % 2 == 0 && e >= 2 && pa < 32) w_add1h(sp[pa / 8][(pa / 2) % 4][pa % 2]);
          if constexpr (R == 0 && e % 2 == 1 && e >= 5) h1 = w_mixhi(au[pf / 8][(pf / 2) % 4][2 * (pf % 2) + 1], sp[pf / 8][(pf / 2) % 4][pf % 2]);
          if constexpr (R == 1 && e % 2 == 0 && e >= 4) h0 = w_mixlo(au[pf / 8][(pf / 2) % 4][2 * (pf % 2)], sp[pf / 8][(pf / 2) % 4][pf % 2]);
          if constexpr (R == 1 && e % 2 == 1 && e >= 5) hq[(pf / 16) * 4 + (pf / 2) % 4][2 * ((pf / 8) & 1) + pf % 2] = w_cvt(h0, h1);
          if constexpr (R == 2 && e < 64) w_exph<e & 1>(sp[e / 16][(e / 4) % 4][(e % 4) / 2]);
          if constexpr (R == 3 && d >= 0 && d < 64) w_rcph<d & 1>(sp[d / 16][(d / 4) % 4][(d % 4) / 2]);
        }
        if constexpr (F >= 288 && FP >= FIN0 && FP < FIN0 + 32) {
          constexpr int J = (FP - FIN0) / 4, i = FP % 4, e = J >> 2, rt = J & 3, tt = 2 * e + (i >> 1), k = 2 * (i & 1);
          if constexpr (R == 0) h0 = w_mixlo(au[tt][rt][k], sp[tt][rt][i & 1]);
          if constexpr (R == 1) h1 = w_mixhi(au[tt][rt][k + 1], sp[tt][rt][i & 1]);
          if constexpr (R == 3) hq[J][i] = w_cvt(h0, h1);
        }
        if constexpr (R == 3 && mlp_op(F, 2).kind == O_FIN) keep(hq[mlp_op(F, 2).b]);
        if constexpr (R == 3 && F < 431) prep(IC(F + 1), IC(2));
        SBAR();
      });
    });
  });
  phase2<8>(c, p2, f, [&](auto N_, auto R_) {
    constexpr int N = CI(N_), R = CI(R_);
    if constexpr (N < 16 && R < 3) {
      constexpr int J = N / 2, e = J >> 2, rt = J & 3, tt = 2 * e + (N & 1);
      if constexpr (R == 0) { h0 = w_mixlo(au[tt][rt][0], sp[tt][rt][0]); h1 = w_mixhi(au[tt][rt][1], sp[tt][rt][0]); }
      if constexpr (R == 1) { e0 = w_mixlo(au[tt][rt][2], sp[tt][rt][1]); e1 = w_mixhi(au[tt][rt][3], sp[tt][rt][1]); }
      if constexpr (R == 2) { hq[J][2 * (N & 1)] = w_cvt(h0, h1); hq[J][2 * (N & 1) + 1] = w_cvt(e0, e1); }
    }
    if constexpr (R == 3 && N < 20 && lastfin(N) >= 0) {
      constexpr int J = lastfin(N);
      __builtin_amdgcn_raw_buffer_store_b128(hq[J], c.rws, vl16 + (J & 3) * 1024, sH + (24 + (J >> 2)) * 4096, 16);
      asm volatile("" ::: "memory");
    }
    if constexpr (R == 1 && N >= 1 && N < 21 && lastfin(N - 1) >= 0) keep(hq[lastfin(N - 1)]);
  });
}

DI u32x2 pk4(const f32x4& v) { return u32x2{cvtpk(v[0], v[1]), cvtpk(v[2], v[3])}; }
DI void sw16(float& a, float& b) { auto r = __builtin_amdgcn_permlane16_swap(__float_as_uint(a), __float_as_uint(b), false, false); a = __uint_as_float(r[0]); b = __uint_as_float(r[1]); }
DI void sw32(float& a, float& b) { auto r = __builtin_amdgcn_permlane32_swap(__float_as_uint(a), __float_as_uint(b), false, false); a = __uint_as_float(r[0]); b = __uint_as_float(r[1]); }
template <bool MX> DI float xred(float (&v)[4]) {
  sw16(v[0], v[1]); sw16(v[2], v[3]);
  float a = MX ? max2(v[0], v[1]) : v[0] + v[1], b = MX ? max2(v[2], v[3]) : v[2] + v[3];
  sw32(a, b);
  return MX ? max2(a, b) : a + b;
}
DI void xbc(float t, float (&v)[4]) {
  float a = t, b = t;
  sw32(a, b);
  v[0] = a; v[1] = a; v[2] = b; v[3] = b;
  sw16(v[0], v[1]); sw16(v[2], v[3]);
}

DI void role_att(Ctx& c, XS& xs) {
  const unsigned mp = c.m & 1;
  u32x2 vp[4][5], qp[4][5];
  u32x4 kt[6][4];
  f32x4 gate, bprev, bown, bprev0;
  unsigned mpv[2];
  P2<12> p2;
  {
    const unsigned l = lane_id(), g = l >> 4, q = (l & 15) >> 2, pp = l & 3;
    const unsigned lc = (l & 15) ^ (g << 1), hb = 144 * (2 * c.a + mp);
    const unsigned vA = g * 3456 + hb + (lc < 10 ? 1152 + 16 * (lc < 9 ? lc : 8) : 2304 + 16 * (lc - 10));
    const unsigned vB = g * 3456 + hb + (lc < 4 ? 2304 + 16 * (lc < 3 ? 6 + lc : 8) : 16 * (lc - 4));
    const u64 maskB = __builtin_amdgcn_ballot_w64(lc < 13);
    const unsigned lw = c.w * 1152;
    unsigned sWk[9];
#pragma unroll
    for (int i = 0; i < 9; ++i) sWk[i] = (8 * c.w + 64 * c.kg[i]) * 3456;
    const int hr = 256 * (int)c.T + ((c.w & 1) ? -16 : -15) + 2 * (int)(l >> 3);
    const unsigned vHx = (unsigned)(hr < 0 ? 0 : hr) * 1152 + 16 * ((l & 7) ^ (4 * (c.w & 1) + g));
    const unsigned lHx = A_HX + c.w * 1024;
    unsigned rh[2] = {A_HX + (l & 15) * 128 + 16 * (g ^ ((l & 15) >> 1)), 0}; rh[1] = rh[0] ^ 64; PIN(rh[0]); PIN(rh[1]);
    bf16x8 xf[18][4];
    unsigned cur = 0, l8 = 0;
    u32x4 pz;
    auto prep = [&](auto F_, auto R_) {
      constexpr Op o = att1_op(CI(F_), CI(R_));
      if constexpr (o.kind == O_W) cur = dprep<(o.a % 4) * S2 + o.b * 4608, (32 * (o.b >> 1) + 4 * (o.b & 1)) * 3456>(lw, sWk[o.a % 9]);
      else if constexpr (o.kind == O_X) xs.template prep<2, o.a, o.b>(c);
      else if constexpr (o.kind == O_HX) {
        if constexpr (o.a < 4) cur = dprep<o.a * 4096, 0>(lHx, (c.w < 2 ? c.kg[2 * o.a] : c.kg[2 * o.a + 1]) * 128);
        else cur = dprep<16384, 0>(lHx, c.w < 2 ? c.kg[8] * 128 : (c.w & 1) * 1024);
      } else p2.template prep<o.kind, o.a, o.b>(c);
    };
    auto fire = [&](auto F_, auto R_) {
      constexpr Op o = att1_op(CI(F_), CI(R_));
      if constexpr (o.kind == O_W) { if constexpr (o.a < 9) dfire<0>(vA, c.rqkv, cur); else dfire_m(vB, c.rqkv, cur, maskB); }
      else if constexpr (o.kind == O_X) {
        if constexpr (o.a >= 2 && o.b == 0) asm volatile("s_waitcnt lgkmcnt(4)" ::: "memory");
        xs.template fire<o.b>(c);
      } else if constexpr (o.kind == O_HX) {
        if constexpr (o.a < 4) dfire<0>(vHx, c.rx, cur); else dfire<0>(c.w < 2 ? vHx : l * 16, c.w < 2 ? c.rx : c.rgw, cur);
      } else if constexpr (o.kind == O_TL) touch_pages<A_GW + 2048>(c);
      else if constexpr (o.kind == O_PZ) poison<3, o.a>(c, pz, 2 * l8);
      else p2.template fire<o.kind, o.a, o.b>(c);
    };
    auto rdx = [&](auto C_, auto I_) {
      constexpr int C = CI(C_), s = CI(I_) >> 2, rt = CI(I_) & 3;
      xf[2 * C + s][rt] = ldsr<bf16x8>(xs.rd[s] + (C % 2) * 32768 + rt * 2048);
    };
    static_for<6>([&](auto I_) { prep(IC(CI(I_) - 8), IC(2)); fire(IC(CI(I_) - 8), IC(2)); });
    visit_all<32, 22>(c.pc0, c.w);
    STAMP(57);
    load_epoch(c);
    unsigned rb[4], rx1, rx2;
#pragma unroll
    for (int t = 0; t < 4; ++t) rb[t] = g * 1152 + q * 256 + 32 * (t ^ q) + 8 * pp;
    rx1 = g * 1152 + q * 256 + 32 * (c.w ^ q) + 8 * pp;
    rx2 = g * 1152 + q * 256 + 128 + 32 * ((c.w < 2 ? c.w + 1 : 3) ^ q) + 8 * pp;
    const unsigned gsrc = A_GW + 2 * (32 * (l >> 5) + 8 * ((l >> 2) & 3) + 4 * ((l >> 4) & 1) + (l & 3));
    const unsigned gdst = (l >> 2) * 1152 + (l & 3) * 256 + 16 * (13 ^ ((l & 3) << 1));
    u16 gwv = 0;
    f32x4 acc[2][8][4], ah[4];
    u32x2 hp;
    unsigned wk = 0, wv = 0;
    bf16x8 f[16], hx, ha, hc;
    s16x4 flo, hlo;
    wait_bar<VM<1, -1>>();
    asm volatile("" : "+s"(c.E));
    STAMP(1);
    static_for<4>([&](auto I_) { rdx(IC(0), I_); });
    static_for<PD>([&](auto I_) { f[CI(I_)] = cat(trd(rb[CI(I_)]), trd(rb[CI(I_)] + 4608)); });
    static_for<4>([&](auto I_) { rdx(IC(0), IC(4 + CI(I_))); });
    SBAR();
    auto pass = [&](auto PS_) {
      constexpr int PS = CI(PS_), NT = PS == 0 ? 8 : 7, FC = 2 * NT;
      static_for<9>([&](auto N_) {
        static_for<FC>([&](auto I_) {
          constexpr int N = CI(N_), I = CI(I_), F = 144 * PS + N * FC + I, ks = 2 * N + I / NT, t = I % NT;
          constexpr int RG = rgA(F), pos = posA(F);
          constexpr int NTN = RG < 9 ? 8 : 7, tn = pos % NTN, RR = pos == 0 ? 2 : 0;
          constexpr unsigned ro = (RG % 2) * S2 + (pos / NTN) * 9216 + 128 * (tn >> 2);
          constexpr unsigned rc = ((9 * PS + N) % 2) * S2 + (I / NT) * 9216;
          constexpr bool SW = PS == 0 ? t >= 5 : t < 2;
          const bf16x8 a = f[I];
          static_for<5>([&](auto R_) {
            constexpr int R = CI(R_);
            if constexpr (R < 4) { if constexpr (SW) mfs<(ks < 16), ks == 0>(acc[PS][t][R], a, xf[ks][R]); else mf<(ks < 16), ks == 0>(acc[PS][t][R], a, xf[ks][R]); }
            else if constexpr (PS == 0) {
              if constexpr (t == 4) mf<false, ks == 0>(ah[1], a, hx);
              if constexpr (t == 5) mf<false, ks == 0>(ah[0], ha, hx);
              if constexpr (t == 7) mfs<false, ks == 0>(ah[2], hc, hx);
            } else if constexpr (t == 4) mfs<false, ks == 0>(ah[3], ha, hx);
            SBAR();
            if constexpr (R == 0 && PS == 0 && I == XR && N < 8) wait_vm<VM<1, F>>();
            if constexpr (pos == 0 && RG < 18) {
              if constexpr (R < 2 && RG % 2 == 0) {
                constexpr unsigned d = RG % 4 == 0 ? 0u - 2 * S2 : 2 * S2;
#pragma unroll
                for (int i = 2 * R; i < 2 * R + 2; ++i) { rb[i] += d; PIN(rb[i]); }
                if (R == 0) { rx1 += d; PIN(rx1); } else if (RG < 9) { rx2 += d; PIN(rx2); }
              }
              if constexpr (R == 0 && RG == 9) { rx1 ^= 32 * (c.w & 2); PIN(rx1); }
              if constexpr (R == 2) {
                if constexpr (RG % 2 == 1) STUB<32 + RG / 2>();
                static_assert(RG != 16 || VM<1, F> == 0, "flags follow a full drain");
                wait_bar<VM<1, F>>();
                STAMP(1 + RG);
              }
            }
            if constexpr (R == RR && RG < 18) flo = trd(rb[tn & 3] + ro);
            if constexpr (R == RR + 1 && RG < 18) f[pos] = cat(flo, trd(rb[tn & 3] + ro + 4608));
            if constexpr (PS == 0 && N < 8 && (I == XR || I == XR + 1) && R < 4) rdx(IC(N + 1), IC(4 * (I - XR) + R));
            if constexpr (t == 0 && R == 3) hx = ldsr<bf16x8>(rh[ks & 1] + (ks >> 1) * 2048);
            if constexpr (t == 1 && R == 3) hlo = trd(rx1 + rc);
            if constexpr (t == 2 && R == 3) ha = cat(hlo, trd(rx1 + rc + 4608));
            if constexpr (PS == 0 && t == 3 && R == 3) hlo = trd(rx2 + rc);
            if constexpr (PS == 0 && t == 4 && R == 3) hc = cat(hlo, trd(rx2 + rc + 4608));
            if constexpr (RG + 3 >= 9 && RG + 3 <= 17 && R == 1) {
              if constexpr (pos == 6) gwv = ldsr<u16>(gsrc + c.kg[RG + 3 - 9] * 128);
              if constexpr (pos == 9) ldsw<u16>(gdst + ((RG + 3) % 4) * S2, gwv);
            }
            SBAR();
            if constexpr (F == 136 && R == 0) { l8 = lane_id() * 8; PIN(l8); wk = A_KX + (4 * c.w + 2) * 2560 + l8; PIN(wk); }
            if constexpr (F >= 138 && F < 146 && R < 4) {
              constexpr int tt = F - 138;
              if constexpr (tt < 5) ldsw<u32x2>(wk + R * 2560 + tt * 512, pk4(acc[0][tt][R])); else { vp[R][tt - 5] = pk4(acc[0][tt][R]); PIN(vp[R][tt - 5]); }
            }
            if constexpr (F == 146 && R < 2) { const u32x2 h = pk4(ah[R]); const unsigned o = A_KX + (R == 0 ? c.w : 4) * 512; ldsw<u32x2>(o + l8, h); ldsw<u32x2>(o + 2560 + (l8 ^ 64), h); }
            if constexpr (F == 146 && R == 2) { hp = pk4(ah[2]); PIN(hp); }
            if constexpr (pos == 0 && R == 3) {
              if constexpr (RG == 15) { keep(pz); p2.init(c); wv = A_VX + (c.w < 3 ? (c.w + 1) * 5120 : A_GW + 2304 - A_VX) + (c.w < 3 ? l8 : 0); PIN(wv); }
              if constexpr (RG == 16) { if (l8 == 0) flag_store(c, flag2(c) + 8 * c.a, 1); }
            }
            if constexpr (RG == 15 && pos >= 4 && pos < 10 && R == 2) { if (c.w < 3) ldsw<u32x2>(wv + (((pos - 4) / 3) * 5 + (pos - 4) % 3) * 512, vp[2 + (pos - 4) / 3][(pos - 4) % 3]); }
            if constexpr (RG == 15 && pos == 11 && R == 2) { const unsigned o = A_VX + (c.w < 2 ? c.w : 2) * 512; ldsw<u32x2>(o + l8, hp); ldsw<u32x2>(o + 2560 + (l8 ^ 256), hp); }
            if constexpr (RG == 16 && pos >= 9 && pos < 13 && R == 2) {
              constexpr int r = pos - 9;
              const int key = (int)(l8 >> 7) * 4 + r, jj = (int)(l8 >> 3) & 15;
              bprev[r] = key >= jj + 9 ? 0.f : -1e30f;
              bown[r] = (key <= jj && key >= jj - 7) ? 0.f : -1e30f;
              bprev0[r] = (c.T == 0 && c.w == 0) ? -1e30f : bprev[r];
              if constexpr (r % 2 == 0) mpv[r / 2] = key >= jj + 9 ? 0xffffu : 0u; else { mpv[r / 2] |= key >= jj + 9 ? 0xffff0000u : 0u; PIN(mpv[r / 2]); }
              if constexpr (r == 3) { PIN(bprev); PIN(bown); PIN(bprev0); }
            }
            if constexpr (F == 256 && R == 0) { wk -= 5120; PIN(wk); }
            if constexpr (F >= 257 && F < 265 && (R == 0 || R == 1 || R == 3)) {
              constexpr int n = 3 * (F - 257) + (R == 3 ? 2 : R), i = n / 4, h = n % 4;
              const u32x2 k0 = ldsr<u32x2>(wk + i * 2560 + h * 512), k1 = ldsr<u32x2>(wk + i * 2560 + h * 512 + 512);
              kt[i][h] = u32x4{k0[0], k0[1], k1[0], k1[1]};
              asm volatile("" ::: "memory");
            }
            if constexpr (F >= 265 && R < 4) {
              constexpr int tt = F - 265;
              if constexpr (tt < 2) { vp[R][3 + tt] = pk4(acc[1][tt][R]); PIN(vp[R][3 + tt]); if constexpr (R >= 2) { if (c.w < 3) ldsw<u32x2>(wv + ((R - 2) * 5 + 3 + tt) * 512, vp[R][3 + tt]); } }
              else { qp[R][tt - 2] = pk4(acc[1][tt][R]); PIN(qp[R][tt - 2]); }
            }
            if constexpr (R == 2 || R == 3) { if constexpr (R == 3 || F == 0) prep(IC(F), R_); fire(IC(F), R_); }
            if constexpr (R == 4) prep(IC(F + 1), IC(2));
            SBAR();
          });
        });
      });
    };
    pass(IC(0));
    pass(IC(1));
    STUB<41>();
    STAMP(19);
    { const u32x2 h = pk4(ah[3]); const unsigned o = A_VX + (3 + (c.w & 1)) * 512; ldsw<u32x2>(o + l8, h); ldsw<u32x2>(o + 2560 + (l8 ^ 256), h); }
#pragma unroll
    for (int rt = 0; rt < 4; ++rt) { qp[rt][3] = pk4(acc[1][5][rt]); PIN(qp[rt][3]); }
    asm volatile("s_waitcnt lgkmcnt(0)\n\ts_barrier" ::: "memory");
#pragma unroll
    for (int rt = 0; rt < 4; ++rt) {
      qp[rt][4] = pk4(acc[1][6][rt]);
      gate[rt] = __builtin_amdgcn_exp2f(fminf(1.4426950408889634f * acc[1][6][rt][0], 80.0f));
    }
  }
  STAMP(20);
  auto pre = [&](auto A_, auto N_) { static_for<CI(N_)>([&](auto I_) { constexpr Op o = att2_op(CI(A_) + CI(I_) - 50, 2); p2.template prep<o.kind, o.a, o.b>(c); p2.template fire<o.kind, o.a, o.b>(c); }); };
  const unsigned l = lane_id(), g = (l >> 4) & 3, j = l & 15;
  {
    u32x6 vt[5][2];
    const unsigned ra = A_VX + c.w * 5120 + l * 8;
#pragma unroll
    for (int pa = 0; pa < 2; ++pa)
#pragma unroll
      for (int t = 0; t < 5; ++t) {
        const u32x2 v2 = ldsr<u32x2>(ra + (pa * 5 + t) * 512);
        vt[t][pa] = u32x6{v2[0], v2[1], vp[pa][t][0], vp[pa][t][1], vp[2 + pa][t][0], vp[2 + pa][t][1]};
      }
    const f32x4 omg = {__builtin_amdgcn_rcpf(1.0f + gate[0]), __builtin_amdgcn_rcpf(1.0f + gate[1]), __builtin_amdgcn_rcpf(1.0f + gate[2]), __builtin_amdgcn_rcpf(1.0f + gate[3])};
    if (mp == 0 && g == 2) st16(__builtin_bit_cast(u32x4, omg), c.rws, j * 16, WS_G + (c.a * 32 + 4 * c.T + c.w) * 256);
#pragma unroll
    for (int rt = 0; rt < 4; ++rt) gate[rt] = __shfl(gate[rt], 32 + j);
    constexpr float SC2 = 0.23570226039551584f * 1.4426950408889634f;
    const unsigned sH = WS_H + (((4 * c.T + c.w) * 4 + c.a) * 42 + 36 + 3 * mp) * 4096;
    u32x4 qf[4], P[4];
    f32x4 sp[4], so[4], hi[4], lo[4];
    auto mkq = [&](auto RT_) {
      constexpr int rt = CI(RT_);
#pragma unroll
      for (int h = 0; h < 4; ++h) {
#pragma unroll
        for (int i = 0; i < 4; ++i) { const int col = 16 * (h + (i >> 1)) + 4 * (int)g + 2 * (i & 1); qf[h][i] = (col >= 18 * h && col < 18 * h + 18) ? qp[rt][h + (i >> 1)][i & 1] : 0u; }
        PIN(qf[h]);
      }
    };
    auto qk = [&](auto RT_, auto H_, auto W_) {
      constexpr int rt = CI(RT_), h = CI(H_);
      if constexpr (CI(W_) == 0) sp[h] = mfc(kt[rt][h], qf[h], rt < 2 ? bprev0 : bprev);
      else so[h] = mfc(kt[rt + 2][h], qf[h], bown);
    };
    auto pv = [&](auto RT_, auto T_, auto W_) {
      constexpr int rt = CI(RT_), t = CI(T_) + CI(W_), pa = rt & 1, sn = rt >> 1;
      const f32x4 r = mfz(__builtin_shufflevector(vt[t][pa], vt[t][pa], 2 * sn, 2 * sn + 1, 2 * sn + 2, 2 * sn + 3), P[CI(T_)]);
      if constexpr (CI(W_) == 0) hi[CI(T_)] = r; else lo[CI(T_)] = r;
    };
    auto outp = [&](auto RT_) {
      constexpr int rt = CI(RT_);
      u32x2 o[5];
      PIN(lo[0]);
#pragma unroll
      for (int t = 0; t < 5; ++t)
#pragma unroll
        for (int i = 0; i < 2; ++i) {
          const unsigned vh = t < 4 ? cvtpk(hi[t < 4 ? t : 0][2 * i], hi[t < 4 ? t : 0][2 * i + 1]) : 0u;
          o[t][i] = (4 * (int)g + 2 * i < 2 * t) ? cvtpk(lo[t > 0 ? t - 1 : 0][2 * i], lo[t > 0 ? t - 1 : 0][2 * i + 1]) : vh;
        }
      st16(u32x4{o[0][0], o[0][1], o[1][0], o[1][1]}, c.rws, l * 16 + rt * 1024, sH);
      st16(u32x4{o[2][0], o[2][1], o[3][0], o[3][1]}, c.rws, l * 16 + rt * 1024, sH + 4096);
      st16(u32x4{o[4][0], o[4][1], 0u, 0u}, c.rws, l * 16 + rt * 1024, sH + 8192);
    };
    mkq(IC(0));
    asm volatile("s_nop 1" ::: "memory");
    static_for<8>([&](auto I_) { qk(IC(0), IC(CI(I_) >> 1), IC(CI(I_) & 1)); });
    SBAR();
    mkq(IC(1));
    static_for<4>([&](auto RT_) {
      constexpr int rt = CI(RT_);
      float s[4][4], red[4], bc[4];
      STUB<42 + rt>();
      SBAR();
      static_for<4>([&](auto H_) {
        constexpr int h = CI(H_);
        if constexpr (rt > 0) pv(IC(rt - 1), H_, IC(0));
        s[h][0] = max2(sp[h][0], so[h][0]); s[h][1] = max2(sp[h][1], so[h][1]); s[h][2] = max2(sp[h][2], so[h][2]);
        SBAR();
        if constexpr (rt > 0) pv(IC(rt - 1), H_, IC(1));
        s[h][3] = max2(sp[h][3], so[h][3]);
        red[h] = max2(max3(s[h][0], s[h][1], s[h][2]), s[h][3]);
        SBAR();
      });
      xbc(-SC2 * xred<true>(red), bc);
      if constexpr (rt == 0) pre(IC(6), IC(7));
      if constexpr (rt == 2) { wait_vm<VM<2, -28>>(); p2.template check<0>(c); }
      if constexpr (rt > 0) outp(IC(rt - 1));
      SBAR();
      static_for<4>([&](auto H_) {
        constexpr int h = CI(H_);
        if constexpr (rt < 3) qk(IC(rt + 1), H_, IC(0));
        s[h][0] = __builtin_amdgcn_exp2f(__builtin_fmaf(s[h][0], SC2, bc[h])); s[h][1] = __builtin_amdgcn_exp2f(__builtin_fmaf(s[h][1], SC2, bc[h]));
        SBAR();
        if constexpr (rt < 3) qk(IC(rt + 1), H_, IC(1));
        s[h][2] = __builtin_amdgcn_exp2f(__builtin_fmaf(s[h][2], SC2, bc[h])); s[h][3] = __builtin_amdgcn_exp2f(__builtin_fmaf(s[h][3], SC2, bc[h]));
        SBAR();
      });
#pragma unroll
      for (int h = 0; h < 4; ++h) red[h] = (s[h][0] + s[h][1]) + (s[h][2] + s[h][3]);
      xbc(gate[rt] * __builtin_amdgcn_rcpf(xred<false>(red)), bc);
      if constexpr (rt == 1) pre(IC(16), IC(6));
      if constexpr (rt == 2) pre(IC(25), IC(8));
      if constexpr (rt == 3) pre(IC(36), IC(4));
      if constexpr (rt == 0) {
#pragma unroll
        for (int i = 0; i < 10; ++i) PIN(vt[i >> 1][i & 1]);
      }
      if constexpr (rt < 2) mkq(IC(rt + 2));
#pragma unroll
      for (int h = 0; h < 4; ++h) {
        const unsigned p0 = cvtpk(s[h][0] * bc[h], s[h][1] * bc[h]), p1 = cvtpk(s[h][2] * bc[h], s[h][3] * bc[h]);
        P[h] = u32x4{p0 & mpv[0], p1 & mpv[1], p0 & ~mpv[0], p1 & ~mpv[1]};
        PIN(P[h]);
      }
      asm volatile("s_nop 1" ::: "memory");
    });
    static_for<8>([&](auto I_) { pv(IC(3), IC(CI(I_) >> 1), IC(CI(I_) & 1)); });
    asm volatile("s_nop 7" ::: "memory");
    SBAR();
    outp(IC(3));
  }
  STAMP(21);
  bf16x8 f[16];
  phase2<12>(c, p2, f, [](auto, auto) {});
}

#endif
#define STAMP_OFF (64u << 20)
__global__ __launch_bounds__(256, 1) void fused_kernel(const u16* x, const u16* wqkv, const u16* wo, const u16* wg, const u16* wu, const u16* wd,
                                                        const u16* gw, char* ws, u16* out) {
#if defined(__HIP_DEVICE_COMPILE__)
  __shared__ __attribute__((aligned(16))) char smem[LDS_BYTES];
  asm volatile("" ::"v"((unsigned)(size_t)smem));
  Ctx c;
  STAMP(0);
  const unsigned bid = blockIdx.x, xcd = bid & 7, idx = bid >> 3;
  c.w = __builtin_amdgcn_readfirstlane(threadIdx.x >> 6);
  {
    u64 pc; unsigned len;
    asm volatile("s_getpc_b64 %0\n.Lgm_code0:\n\ts_mov_b32 %1, .Lgm_code1-.Lgm_code0\n\ts_max_u32 %1, %1, .Lgm_code2-.Lgm_code0" : "=s"(pc), "=s"(len) :: "scc");
    c.pc0 = pc;
    asm volatile("s_branch .Lgm_go\n.Lgm_ret:\n\ts_setpc_b64 s[100:101]\n.Lgm_go:");
    const rsrc_t rc = __builtin_amdgcn_make_buffer_rsrc((void*)pc, (short)0, len, 0x00020000);
    if (idx < 16) touch<3 * S1>((threadIdx.x & 15) * 128, rc, idx * 8192 + c.w * 2048);
  }
  asm volatile("" ::"s"(x), "s"(out));
  STAMP(54);
  c.rx = __builtin_amdgcn_make_buffer_rsrc((void*)x, (short)0, SEQ * DM * 2, 0x00020000);
  c.rqkv = __builtin_amdgcn_make_buffer_rsrc((void*)wqkv, (short)0, DM * 3 * DM * 2, 0x00020000);
  c.rwo = __builtin_amdgcn_make_buffer_rsrc((void*)wo, (short)0, DM * DM * 2, 0x00020000);
  c.rwg = __builtin_amdgcn_make_buffer_rsrc((void*)wg, (short)0, DM * FF * 2, 0x00020000);
  c.rwu = __builtin_amdgcn_make_buffer_rsrc((void*)wu, (short)0, DM * FF * 2, 0x00020000);
  c.rwd = __builtin_amdgcn_make_buffer_rsrc((void*)wd, (short)0, DM * FF * 2, 0x00020000);
  c.rgw = __builtin_amdgcn_make_buffer_rsrc((void*)gw, (short)0, DM * 2, 0x00020000);
  c.rws = __builtin_amdgcn_make_buffer_rsrc((void*)ws, (short)0, WS_BYTES, 0x00020000);
  c.rout = __builtin_amdgcn_make_buffer_rsrc((void*)out, (short)0, SEQ * DM * 2, 0x00020000);
  c.ws = (u64)ws;
  c.a = xcd & 3; c.T = 4 * (xcd >> 2) + (idx >> 3);
  c.m = (idx & 3) == 3 ? 6 + ((idx >> 2) & 1) : 3 * ((idx >> 2) & 1) + (idx & 3);
#define ROTM 1
  const unsigned rot = ROTM == 0 ? 0 : ROTM == 1 ? c.m + 2 * (c.T & 3) : ROTM == 2 ? 3 * ((c.m + c.T) % 3) : 4 * ((c.m + c.T) & 1);
  c.kg[0] = ROTM >= 4 ? 0 : rot >= 9 ? rot - 9 : rot;
  XS xs; xs.init(c, c.m < 6 ? M_X : A_X);
  static_for<8>([&](auto I_) { xs.template prep<1, 0, CI(I_)>(c); xs.template fire<CI(I_)>(c); });
  STAMP(55);
  SBAR();
#pragma unroll
  for (int i = 1; i < 9; ++i) {
    if (ROTM == 4) c.kg[i] = 1 + ((i + c.m + 2 * c.T) & 7);
    else if (ROTM == 5) c.kg[i] = i == 1 ? 1 : i == 2 ? 2 + (c.m + 2 * (c.T & 3)) % 7 : c.kg[i - 1] == 8 ? 2 : c.kg[i - 1] + 1;
    else c.kg[i] = c.kg[i - 1] == 8 ? 0 : c.kg[i - 1] + 1;
  }
  if (__builtin_expect(c.m < 6, 1)) role_mlp(c, xs); else role_att(c, xs);
#endif
}

extern "C" void kernel_launch(void* const* d_in, const int* in_sizes, int n_in, void* d_out, int out_size, void* d_ws, size_t ws_size,
                              hipStream_t stream) {
  fused_kernel<<<256, 256, 0, stream>>>((const u16*)d_in[0], (const u16*)d_in[1], (const u16*)d_in[2], (const u16*)d_in[4], (const u16*)d_in[5],
                                        (const u16*)d_in[6], (const u16*)d_in[7], (char*)d_ws, (u16*)d_out);
}
```

```cpp
#include <hip/hip_runtime.h>
#include <utility>

typedef unsigned short u16;
typedef unsigned long long u64;
typedef short s16x4 __attribute__((ext_vector_type(4)));
typedef __bf16 bf16x8 __attribute__((ext_vector_type(8)));
typedef __bf16 bf16x2 __attribute__((ext_vector_type(2)));
typedef float f32x2 __attribute__((ext_vector_type(2)));
typedef float f32x4 __attribute__((ext_vector_type(4)));
typedef unsigned u32x2 __attribute__((ext_vector_type(2)));
typedef unsigned u32x4 __attribute__((ext_vector_type(4)));
typedef unsigned u32x6 __attribute__((ext_vector_type(6)));
typedef unsigned u32x10 __attribute__((ext_vector_type(10)));
typedef __amdgpu_buffer_rsrc_t rsrc_t;
#define LAS __attribute__((address_space(3)))
#define DI __device__ __forceinline__
#define SBAR() __builtin_amdgcn_sched_barrier(0)
#define SNAKE 0
#define SN(t, R) ((SNAKE && ((t) & 1)) ? 3 - (R) : (R))
#define ABL 0
#define DPOL ""
#define IC(x) std::integral_constant<int, (x)>{}
#define CI(x) decltype(x)::value

constexpr int SEQ = 2048, DM = 576, FF = 4608;
constexpr unsigned WS_H = 0;
constexpr unsigned WS_PART = 24u << 20;
constexpr unsigned WS_G = 40u << 20;
constexpr unsigned WS_F1 = 41u << 20;
constexpr unsigned WS_F2 = WS_F1 + 8192;
constexpr unsigned WS_E = WS_F2 + 65536;
constexpr unsigned WS_BYTES = 42u << 20;
constexpr unsigned LDS_BYTES = 163840;
constexpr unsigned S1 = 16384;
constexpr unsigned S2 = 18432;
constexpr unsigned S3 = S2 + 9216;
constexpr unsigned M_X = 65536;
constexpr unsigned A_X = 4 * S2;
constexpr unsigned A_HX = A_X + 65536;
constexpr unsigned A_GW = A_HX + 18432;
constexpr unsigned A_KX = 83968;
constexpr unsigned A_VX = A_KX + 18 * 2560;
static_assert(A_GW + 2048 <= LDS_BYTES && A_VX <= A_HX && A_VX + 20480 <= LDS_BYTES && 3 * S3 <= A_KX, "lds");

#if defined(__HIP_DEVICE_COMPILE__)
template <int... Is> using iseq = std::integer_sequence<int, Is...>;
template <class Fn, int... Is> DI void static_for_(Fn&& f, iseq<Is...>) { (f(std::integral_constant<int, Is>{}), ...); }
template <int N, class Fn> DI void static_for(Fn&& f) { static_for_(f, std::make_integer_sequence<int, N>{}); }

template <int N> DI void wait_bar() { asm volatile("s_waitcnt vmcnt(%0) lgkmcnt(0)\n\ts_barrier" ::"i"(N) : "memory"); }
template <int N> DI void wait_vm() { asm volatile("s_waitcnt vmcnt(%0)" ::"i"(N) : "memory"); }
template <class T> DI void PIN(T& x) { asm volatile("" : "+v"(x)); }
DI void tieA(bf16x8& a, bf16x8& b, bf16x8& c, bf16x8& d) { asm volatile("" : "+a"(a), "+a"(b), "+a"(c), "+a"(d)); }
template <unsigned LO, unsigned SO> DI unsigned dprep(unsigned lb, unsigned sb) {
  unsigned t;
  asm volatile("s_add_u32 m0, %1, %3\n\ts_add_u32 %0, %2, %4" : "=s"(t) : "s"(lb), "s"(sb), "i"(LO), "i"(SO) : "scc");
  return t;
}
template <unsigned LO> DI void dprep_l(unsigned lb) { asm volatile("s_add_u32 m0, %0, %1\n\ts_nop 0" ::"s"(lb), "i"(LO) : "scc"); }
template <unsigned IMM> DI void dfire(unsigned voff, rsrc_t srd, unsigned so) {
  asm volatile("buffer_load_dwordx4 %0, %1, %2 offen offset:%3" DPOL " lds" ::"v"(voff), "s"(srd), "s"(so), "i"(IMM) : "memory");
}
DI void dfire_m(unsigned voff, rsrc_t srd, unsigned so, u64 mask) {
  asm volatile("s_mov_b64 exec, %3\n\tbuffer_load_dwordx4 %0, %1, %2 offen" DPOL " lds\n\ts_mov_b64 exec, -1" ::"v"(voff), "s"(srd), "s"(so), "s"(mask) : "memory");
}
template <unsigned IMM> DI void ldh(bf16x8& d, unsigned voff, rsrc_t srd, unsigned so) {
  if constexpr (ABL & 4) asm volatile("" : "=a"(d)); else
  asm volatile("buffer_load_dwordx4 %0, %1, %2, %3 offen offset:%4 sc1" : "=a"(d) : "v"(voff), "s"(srd), "s"(so), "i"(IMM) : "memory");
}
DI void ldg(f32x4& d, unsigned voff, rsrc_t srd, unsigned so) { asm volatile("buffer_load_dwordx4 %0, %1, %2, %3 offen sc1" : "=v"(d) : "v"(voff), "s"(srd), "s"(so) : "memory"); }
template <unsigned LA> DI void touch(unsigned voff, rsrc_t srd, unsigned so) {
  asm volatile("s_mov_b32 m0, %3\n\ts_nop 0\n\tbuffer_load_dword %0, %1, %2 offen sc1 lds" ::"v"(voff), "s"(srd), "s"(so), "i"(LA) : "memory");
}
DI void st16(const u32x4& v, rsrc_t srd, unsigned voff, unsigned so) {
  __builtin_amdgcn_raw_buffer_store_b128(v, srd, voff, so, 16);
  asm volatile("s_nop 1" ::"v"(v) : "memory");
}
DI void keep(const u32x4& v) { asm volatile("" ::"v"(v)); }
template <bool AG, bool ZERO> DI void mf(f32x4& c, const bf16x8& a, const bf16x8& b) {
  if constexpr (ZERO) {
    if constexpr (AG) asm volatile("v_mfma_f32_16x16x32_bf16 %0, %1, %2, 0" : "=&v"(c) : "v"(a), "a"(b));
    else asm volatile("v_mfma_f32_16x16x32_bf16 %0, %1, %2, 0" : "=&v"(c) : "v"(a), "v"(b));
  } else {
    if constexpr (AG) asm volatile("v_mfma_f32_16x16x32_bf16 %0, %1, %2, %0" : "+v"(c) : "v"(a), "a"(b));
    else asm volatile("v_mfma_f32_16x16x32_bf16 %0, %1, %2, %0" : "+v"(c) : "v"(a), "v"(b));
  }
}
template <bool AG, bool ZERO> DI void mfs(f32x4& c, const bf16x8& a, const bf16x8& b) {
  if constexpr (ZERO) {
    if constexpr (AG) asm volatile("v_mfma_f32_16x16x32_bf16 %0, %2, %1, 0" : "=&v"(c) : "v"(a), "a"(b));
    else asm volatile("v_mfma_f32_16x16x32_bf16 %0, %2, %1, 0" : "=&v"(c) : "v"(a), "v"(b));
  } else {
    if constexpr (AG) asm volatile("v_mfma_f32_16x16x32_bf16 %0, %2, %1, %0" : "+v"(c) : "v"(a), "a"(b));
    else asm volatile("v_mfma_f32_16x16x32_bf16 %0, %2, %1, %0" : "+v"(c) : "v"(a), "v"(b));
  }
}
DI f32x4 mfc(const u32x4& a, const u32x4& b, const f32x4& c) { f32x4 d; asm volatile("v_mfma_f32_16x16x32_bf16 %0, %1, %2, %3" : "=&v"(d) : "a"(a), "v"(b), "v"(c)); return d; }
template <class T> DI void PINA(T& x) { asm volatile("" : "+a"(x)); }
DI f32x4 mfz(const u32x4& a, const u32x4& b) { f32x4 d; asm volatile("v_mfma_f32_16x16x32_bf16 %0, %1, %2, 0" : "=&v"(d) : "v"(a), "v"(b)); return d; }
DI float max2(float a, float b) { float r; asm("v_max_f32 %0, %1, %2" : "=v"(r) : "v"(a), "v"(b)); return r; }
DI float max3(float a, float b, float c) { float r; asm("v_max3_f32 %0, %1, %2, %3" : "=v"(r) : "v"(a), "v"(b), "v"(c)); return r; }
DI s16x4 trd(unsigned a) { return __builtin_amdgcn_ds_read_tr16_b64_v4i16((LAS s16x4*)a); }
DI bf16x8 cat(s16x4 lo, s16x4 hi) { return __builtin_bit_cast(bf16x8, __builtin_shufflevector(lo, hi, 0, 1, 2, 3, 4, 5, 6, 7)); }
DI unsigned cvtpk(float lo, float hi) { f32x2 v = {lo, hi}; return __builtin_bit_cast(unsigned, __builtin_convertvector(v, bf16x2)); }
DI float w_mul(float a) { if constexpr (ABL & 32) { asm volatile("" :: "v"(a)); return a; } float r; asm volatile("v_mul_f32 %0, 0xbfb8aa3b, %1" : "=v"(r) : "v"(a)); return r; }
DI float w_exp(float a) { if constexpr (ABL & 32) return a; float r; asm volatile("v_exp_f32 %0, %1" : "=v"(r) : "v"(a)); return r; }
DI unsigned w_add1(float a) { if constexpr (ABL & 32) return 0; unsigned r; asm volatile("v_fma_mixlo_f16 %0, %1, 1.0, 1.0" : "=v"(r) : "v"(a)); return r; }
DI unsigned w_rcplo(unsigned a) { if constexpr (ABL & 32) return 0x38003800u; unsigned r; asm volatile("v_rcp_f16 %0, %1" : "=v"(r) : "v"(a)); return r; }
DI void w_rcphi(unsigned& d, unsigned a) { if constexpr (ABL & 32) return; asm volatile("v_rcp_f16_sdwa %0, %1 dst_sel:WORD_1 dst_unused:UNUSED_PRESERVE src0_sel:WORD_0" : "+v"(d) : "v"(a)); }
DI unsigned w_pklo(float a, float k) { unsigned r; asm volatile("v_fma_mixlo_f16 %0, %1, %2, 0" : "=v"(r) : "v"(a), "s"(k)); return r; }
DI void w_pkhi(unsigned& d, float a, float k) { asm volatile("v_fma_mixhi_f16 %0, %1, %2, 0" : "+v"(d) : "v"(a), "s"(k)); }
template <int H> DI void w_exph(unsigned& d) { if constexpr (ABL & 32) return;
  if constexpr (H == 0) asm volatile("v_exp_f16_sdwa %0, %0 dst_sel:WORD_0 dst_unused:UNUSED_PRESERVE src0_sel:WORD_0" : "+v"(d));
  else asm volatile("v_exp_f16_sdwa %0, %0 dst_sel:WORD_1 dst_unused:UNUSED_PRESERVE src0_sel:WORD_1" : "+v"(d));
}
template <int H> DI void w_rcph(unsigned& d) { if constexpr (ABL & 32) return;
  if constexpr (H == 0) asm volatile("v_rcp_f16_sdwa %0, %0 dst_sel:WORD_0 dst_unused:UNUSED_PRESERVE src0_sel:WORD_0" : "+v"(d));
  else asm volatile("v_rcp_f16_sdwa %0, %0 dst_sel:WORD_1 dst_unused:UNUSED_PRESERVE src0_sel:WORD_1" : "+v"(d));
}
DI void w_add1h(unsigned& d) { asm volatile("v_pk_add_f16 %0, %0, 1.0 op_sel_hi:[1,0]" : "+v"(d)); }
DI float w_mixlo(float u, unsigned s) { float r; asm volatile("v_fma_mix_f32 %0, %1, %2, 0 op_sel_hi:[0,1,0]" : "=v"(r) : "v"(u), "v"(s)); return r; }
DI float w_mixhi(float u, unsigned s) { float r; asm volatile("v_fma_mix_f32 %0, %1, %2, 0 op_sel:[0,1,0] op_sel_hi:[0,1,0]" : "=v"(r) : "v"(u), "v"(s)); return r; }
DI unsigned w_cvt(float lo, float hi) { unsigned r; asm volatile("v_cvt_pk_bf16_f32 %0, %1, %2" : "=v"(r) : "v"(lo), "v"(hi)); return r; }
DI float w_scale(float a, float s) { float r; asm volatile("v_mul_f32 %0, %1, %2" : "=v"(r) : "v"(a), "v"(s)); return r; }
DI float bflo(unsigned v) { return __uint_as_float(v << 16); }
DI float bfhi(unsigned v) { return __uint_as_float(v & 0xffff0000u); }
template <class T> DI T ldsr(unsigned a) { return *(const LAS T*)a; }
template <class T> DI void ldsw(unsigned a, T v) { *(LAS T*)a = v; }
DI unsigned lane_id() { unsigned l; asm volatile("v_mbcnt_lo_u32_b32 %0, -1, 0\n\tv_mbcnt_hi_u32_b32 %0, -1, %0" : "=v"(l)); return l; }

template <int N> DI void STUB() { asm volatile(".Lgm_s%c0:\n\ts_cbranch_execz .Lgm_ret" ::"i"(N)); }
template <int N> DI void visit(u64 pc0) {
  asm volatile("s_add_u32 s98, %0, .Lgm_s%c2-.Lgm_code0\n\ts_addc_u32 s99, %1, 0\n\ts_mov_b64 exec, 0\n\ts_swappc_b64 s[100:101], s[98:99]\n\ts_mov_b64 exec, -1"
               ::"s"((unsigned)pc0), "s"((unsigned)(pc0 >> 32)), "i"(N) : "s98", "s99", "s100", "s101", "scc", "memory");
}
template <int B, int CNT> DI void visit_all(u64 pc0, unsigned w) {
  static_for<4>([&](auto W_) { if (w == CI(W_)) static_for<(CNT + 3 - CI(W_)) / 4>([&](auto I_) { visit<B + CI(W_) + 4 * CI(I_)>(pc0); }); });
}
#define STAMP(i)

struct Ctx {
  rsrc_t rx, rqkv, rwo, rwg, rwu, rwd, rgw, rws, rout;
  unsigned w, a, T, m;
  unsigned kg[9];
  u64 E, ws, pc0;
};

enum { O_NONE, O_W, O_X, O_HX, O_FIN, O_POLL, O_D2, O_B2, O_GATE, O_ST, O_TW, O_TL, O_PZ };
constexpr int NEVER = 1 << 20;
struct Op { int kind, a, b, need; };
constexpr int PD = 3;
#define MLPAD 3
constexpr int MLP_AD = MLPAD, ATT_AD = 3;
constexpr Op NOP_ = {O_NONE, 0, 0, 0};
constexpr int uk(int U, int NT) { const int o = NT == 8 ? 12 : 6; return U < o ? U : U < o + 3 ? U - o + 18 : U - 3; }
constexpr int dep(int U, int NT) { return U == 15 ? 2 : NT == 8 ? (U == 6 ? 1 : U == 12 ? 3 : -1) : (U == 6 ? 3 : U == 9 ? 1 : -1); }
constexpr int FLG2 = 4, FLGA = 1;
constexpr int RS = 19;
constexpr Op p2_op(int N, int R, int NT, int AD, int FB) {
  const int ND = NT == 8 ? 4 : 6, RG = (N + PD) / NT, pos = (N + PD) % NT, U = RG + AD;
  if (RG > 20 || pos == 0) return NOP_;
  int k = -1;
  if (RG == 0) k = 2 * (pos - PD) + (R - 2);
  else if (NT == 8 && RG == FLG2 - 1) k = 2 * (pos - 1) + (R - 2);
  else if (NT == 8) k = R == 2 ? (pos == 1 ? 0 : pos) : pos == 1 ? 1 : pos == 7 ? 8 : -1;
  else k = R == 2 ? pos - 1 : -1;
  if (NT != 8 && R == 3 && pos == 1 && dep(U + 1, NT) >= 0) return {O_POLL, dep(U + 1, NT), 0, FB + (RG + 1) * NT - PD};
  if (k < 0) return NOP_;
  if (U <= 20 && k < ND) return {O_D2, U, k, FB + U * NT - PD};
  if (U <= 20 && k < ND + 4) return {O_B2, U, k - ND, FB + U * NT - PD};
  if (NT == 8 && dep(U + 2, NT) >= 0 && k == ND + 4) return {O_POLL, dep(U + 2, NT), 0, FB + (RG + 2) * NT - PD};
  if (RG == 14 && k == ND + 4) return {O_POLL, 4, 0, FB + 17 * NT - PD};
  if (RG == 15 && k == ND + 4) return {O_GATE, 0, 0, FB + 18 * NT - PD};
  return NOP_;
}
constexpr int FIN0 = 36, ST0 = 72, FLG0 = 18, FLG1 = 25;
#define PZR 16
constexpr int lastfin(int N) { return N == 1 ? 0 : N == 3 ? 1 : N == 7 ? 2 : N == 8 ? 3 : N == 10 ? 4 : N == 11 ? 5 : N == 15 ? 6 : N == 16 ? 7 : -1; }
constexpr int bar1(int n) { return n == 0 ? -1 : 16 * n - PD; }
constexpr Op opW(int n, int i) { return {O_W, n, i, bar1(n)}; }
constexpr int XR = 10;
constexpr Op opX(int c, int i) { return {O_X, c, i, c == 0 ? -1 : 16 * (c - 1) + XR}; }
constexpr Op mlp_op(int F, int R) {
  if (F < 0) {
    const int i = F + 19;
    if (R != 2 || i < 0 || i >= 17) return NOP_;
    return i < 8 ? opX(0, i) : i < 16 ? opW((i - 8) / 4, i % 4) : Op{O_TL, 0, 0, bar1(1)};
  }
  if (F >= 440) {
    const int N = F - 440;
    if (N < 5) return R == 2 ? Op{N < 4 ? O_D2 : O_B2, MLP_AD, N & 3, 440 + MLP_AD * 8 - PD} : N == 0 || N == 2 || N == 4 ? Op{O_B2, MLP_AD, N / 2 + 1, 440 + MLP_AD * 8 - PD} : Op{O_FIN, 2, N / 2, 440 + FLG2 * 8 - PD};
    if (R == 3 && N < 20 && lastfin(N) >= 0) return {O_FIN, 2, lastfin(N), 440 + FLG2 * 8 - PD};
    return p2_op(N, R, 8, MLP_AD, 440);
  }
  if (F >= 432) return NOP_;
  const int RG = (F + PD) / 16, pos = (F + PD) % 16, r = R - 2;
  if (pos == 0) return NOP_;
  if (F < 4) return opX(1, 2 * F + r);
  if (F < 6 || R == 3) return NOP_;
  if (RG <= 5) {
    const int n = F - 6 - RG, c = 2 + n / 12, i = n % 12;
    const int cap = RG + 3;
    if (RG <= 3) { if (c <= cap) return i < 8 ? opX(c, i) : opW(c, i - 8); return NOP_; }
    if (RG == 4 && pos <= 8) return pos <= 4 ? opX(6, pos + 3) : opW(6, pos - 5);
    if (RG == 4) return opX(7, pos - 9);
    if (pos == 1) return opX(7, 7);
    if (pos <= 5) return opW(7, pos - 2);
    return pos <= 13 ? opX(8, pos - 6) : opW(8, pos - 14);
  }
  if (RG == 6 && pos <= 2) return opW(8, pos + 1);
  const int k8 = pos >= 3 && pos < 15 && (pos - 3) % 3 != 2 ? (pos - 3) - (pos - 3) / 3 : -1;
  if (RG >= 6 && RG <= 8 && k8 >= 0) return opW(9 + 2 * (RG - 6) + k8 / 4, k8 % 4);
  if (F >= 144 + ST0 && F < 144 + ST0 + 32 && F % 4 == 2) return {O_FIN, 0, (F - 144 - ST0) / 4, bar1(FLG0)};
  if (F >= 288 + ST0 && F < 288 + ST0 + 16 && F % 2 == 0) return {O_FIN, 1, (F - 288 - ST0) / 2, bar1(FLG1)};
  if (RG >= 9 && RG <= 12 && (pos == 1 || pos == 15)) return {O_TW, 2 * (RG - 9) + (pos == 15), 0, NEVER};
  if (RG >= 9 && RG <= 10 && k8 >= 0) return opW(15 + 2 * (RG - 9) + k8 / 4, k8 % 4);
  if (RG >= 11 && RG <= 12 && pos % 4 == 2) return opW(RG + 8, pos / 4);
  if (RG == 13 && pos <= 4) return opW(21, pos - 1);
  if (RG >= 18 && RG <= 19 && k8 >= 0) return opW(22 + 2 * (RG - 18) + k8 / 4, k8 % 4);
  if (RG == 20 && k8 >= 0) return k8 < 4 ? opW(26, k8) : Op{O_D2, 0, k8 - 4, bar1(27)};
  if (RG >= 21 && RG <= 22 && pos <= 4) return {O_D2, RG - 20, pos - 1, 440 + (RG - 20) * 8 - PD};
  if (RG == PZR && pos % 2 == 1 && pos < 12) return {O_PZ, pos / 2, 0, bar1(FLG0)};
  if (RG == 22 && pos == 7) return {O_POLL, 0, 0, bar1(25)};
  if (RG >= 25 && RG <= 26) {
    const int k = (RG - 25) * 15 + pos - 1;
    if (k < 24 && k % 2 == 0) { const int U = k / 8; return {O_B2, U, (k / 2) % 4, U == 0 ? bar1(27) : 440 + U * 8 - PD}; }
    if (MLP_AD > 3 && k < 16 && k % 2 == 1) { const int i = k / 2; return i < 4 ? Op{O_D2, 3, i, 440 + 24 - PD} : NOP_; }
    if (MLP_AD > 3 && k >= 24 && k < 28) return {O_B2, 3, k - 24, 440 + 24 - PD};
  }
  return NOP_;
}
static_assert(MLP_AD == 3 || MLP_AD == 4, "pre-issue table");
constexpr int barA(int n) { return n == 0 ? -1 : n <= 9 ? 16 * n - PD : 144 + 14 * (n - 9) - PD; }
constexpr int rgA(int F) { return F + PD < 144 ? (F + PD) / 16 : 9 + (F + PD - 144) / 14; }
constexpr int posA(int F) { return F + PD < 144 ? (F + PD) % 16 : (F + PD - 144) % 14; }
struct AttSeq {
  Op op[270] = {};
  constexpr AttSeq() {
    constexpr int seq[27][2] = {{O_X, 2}, {O_W, 2}, {O_HX, 1}, {O_X, 3}, {O_W, 3}, {O_HX, 2}, {O_W, 4}, {O_X, 4}, {O_HX, 3}, {O_HX, 4}, {O_W, 5}, {O_X, 5}, {O_W, 6}, {O_X, 6},
                                {O_W, 7}, {O_X, 7}, {O_W, 8}, {O_X, 8}, {O_W, 9}, {O_W, 10}, {O_W, 11}, {O_W, 12}, {O_W, 13}, {O_W, 14}, {O_W, 15}, {O_W, 16}, {O_W, 17}};
    int F = 4;
    for (int e = 0; e < 27; ++e) {
      const int k = seq[e][0], a = seq[e][1], n = k == O_X ? 8 : k == O_W ? 4 : 1, rg = k == O_X ? a - 2 : k == O_W ? a - 3 : 0;
      for (int i = 0; i < n; ++i) {
        while (posA(F) == 0 || rgA(F) < rg || (F >= 138 && F < 148)) ++F;
        op[F++] = k == O_X ? opX(a, i) : k == O_W ? Op{O_W, a, i, barA(a)} : Op{O_HX, a, 0, barA(a < 3 ? 2 * a : 5)};
      }
    }
    int z = 0;
    for (int G = 150; G < 270; ++G) {
      const int RG = rgA(G), pos = posA(G);
      if (RG >= 10 && RG <= 14 && (pos == 7 || pos == 10) && z < 9) op[G] = {O_PZ, z++, 0, barA(16)};
      if (RG == 16 && pos >= 2 && pos < 8) op[G] = {O_D2, 0, pos - 2, NEVER};
    }
  }
};
constexpr AttSeq ATT_SEQ{};
constexpr Op att1_op(int F, int R) {
  if (F < 0) {
    const int i = F + 20;
    if (R != 2 || i < 0 || i >= 18) return NOP_;
    return i < 8 ? opX(0, i) : i < 12 ? Op{O_W, 0, i - 8, -1} : i == 12 ? Op{O_HX, 0, 0, -1} : i < 17 ? Op{O_W, 1, i - 13, barA(1)} : Op{O_TL, 0, 0, NEVER};
  }
  if (F < 4) return opX(1, 2 * F + R - 2);
  return R == 2 && F < 270 ? ATT_SEQ.op[F] : NOP_;
}
constexpr Op att2_op(int F, int R) {
  if (F < 0) {
    const int i = F + 50;
    if (R != 2 || i < 0 || i >= 43) return NOP_;
    constexpr int n1 = 12 - PD, n2 = 24 - PD, ns = FLGA * 12 - PD;
    if (i < 6) return {O_D2, 0, i, -1};
    if (i == 6) return {O_POLL, 0, 0, -28};
    if (i < 13) return {O_D2, 1, i - 7, n1};
    if (i < 16) return {O_ST, 0, 0, ns};
    if (i < 22) return {O_D2, 2, i - 16, n2};
    if (i < 25) return {O_ST, 0, 0, ns};
    if (i < 29) return {O_B2, 0, i - 25, -1};
    if (i < 33) return {O_B2, 1, i - 29, n1};
    if (i < 36) return {O_ST, 0, 0, ns};
    if (i < 40) return {O_B2, 2, i - 36, n2};
    return {O_ST, 0, 0, ns};
  }
  return p2_op(F, R, 12, ATT_AD, 0);
}
template <int WHICH> constexpr Op op_at(int F, int R) { return WHICH == 0 ? mlp_op(F, R) : WHICH == 1 ? att1_op(F, R) : att2_op(F, R); }
template <int WHICH> constexpr int vm_at(int tau) {
  int s = 0;
  for (int F = tau - 1; F >= -64; --F)
    for (int R = 3; R >= 2; --R) {
      const Op o = op_at<WHICH>(F, R);
      if (o.kind == O_NONE) continue;
      if (o.need <= tau || s >= 63) return s;
      ++s;
    }
  return s;
}
template <int WHICH, int TAU> constexpr int VM = vm_at<WHICH>(TAU);

DI void load_epoch(Ctx& c) {
  asm volatile("s_load_dwordx2 %0, %1, %2" : "=s"(c.E) : "s"(c.ws), "s"(WS_E + c.T * 128) : "memory");
}
DI void flag_store(const Ctx& c, unsigned off, unsigned val) {
  const u64 v = c.E + val;
  __builtin_amdgcn_raw_buffer_store_b64(u32x2{(unsigned)v, (unsigned)(v >> 32)}, c.rws, 0, off, 16);
}
DI void poll_issue(const Ctx& c, u32x2& f, unsigned voff, unsigned off) {
  asm volatile("buffer_load_dwordx2 %0, %1, %2, %3 offen sc1" : "=v"(f) : "v"(voff), "s"(c.rws), "s"(off) : "memory");
}
DI bool flag_ok(const Ctx& c, u32x2 f, unsigned need) {
  const u64 d = (((u64)f[1] << 32) | f[0]) - c.E - need;
  return __builtin_amdgcn_ballot_w64(d >= 8) == 0;
}
DI void poll_check(const Ctx& c, u32x2& f, unsigned voff, unsigned off, unsigned need) {
  asm volatile("" : "+v"(f));
  if (__builtin_expect(!flag_ok(c, f, need), 0)) {
    for (unsigned spin = 0; spin < (1u << 16); ++spin) {
      __builtin_amdgcn_s_sleep(1);
      asm volatile("buffer_load_dwordx2 %0, %1, %2, %3 offen sc1\n\ts_waitcnt vmcnt(0)" : "=&v"(f) : "v"(voff), "s"(c.rws), "s"(off) : "memory");
      if (flag_ok(c, f, need)) break;
    }
  }
}

constexpr unsigned POISON = 0xffffffffu;
DI unsigned inbox(const Ctx& c) { return WS_PART + ((c.T * 8 + c.m) * 64 + c.w) * 3072; }
DI unsigned flag2(const Ctx& c) { return WS_F2 + ((c.T * 8 + c.m) * 4 + c.w) * 128; }
template <int NI, int J> DI void poison(const Ctx& c, u32x4& pz, unsigned vl16) {
  if constexpr (J == 0) { pz = u32x4{POISON, POISON, POISON, POISON}; PIN(pz); }
  asm volatile("buffer_store_dwordx4 %0, %1, %2, %3 offen offset:%4 sc1" ::"v"(pz), "v"(vl16), "s"(c.rws), "s"(inbox(c) + (c.a * 4 + ((J / NI + 1) ^ c.a)) * 12288), "i"((J % NI) * 1024) : "memory");
}
template <int NT> struct P2 {
  static constexpr int AD = NT == 8 ? MLP_AD : ATT_AD, NS = NT == 8 ? 8 : 4, FB = NT == 8 ? 440 : 0, ROT = NT == 8 ? 4 : 0;
  static constexpr unsigned SS = NT == 8 ? S2 : S3;
  static constexpr int sidx(int u) { return NT == 8 ? (u + ROT) % NS : u % 4 == 0 ? 2 : u % 4 == 3 ? 3 : u % 4 - 1; }
  static constexpr unsigned slot(int u) { return sidx(u) * SS; }
  static constexpr int WN = NT == 8 ? 3 : 2;
  static constexpr unsigned win(int u) { return (sidx(u) / WN * WN) * SS; }
  unsigned v128[2], v64, vh[2], vf;
  unsigned lw, lw64, sd, so, sh[2];
  unsigned f1, cur, curh;
  bf16x8 bf[21][4];
  u32x2 pf;
  f32x4 omg;
  DI void init(const Ctx& c) {
    const unsigned l = lane_id(), colb = NT == 8 ? 256 * (c.m >> 1) : 768, kh = c.w >> 1;
    const unsigned lo = 16 * ((l & 15) ^ ((l >> 3) & 2) ^ (4 * (c.a ^ 3)));
    v128[0] = (l >> 4) * 4608 + lo; v128[1] = (l >> 4) * 4608 + (lo ^ 64);
    v64 = (l >> 3) * 2304 + 16 * ((l & 7) ^ (((l >> 1) & 4) | ((l >> 3) & 2)) ^ (2 * (c.a ^ 3))) + 256;
    vh[0] = l * 16 + kh * 2048; vh[1] = l * 16 + (1 - kh) * 2048;
    vf = 8 * (l & 7);
    lw = c.w * 4608; lw64 = c.w * 2304 + S2;
    sd = (1152 * c.a + 16 * c.w) * 1152 + colb;
    so = (144 * c.a + 72 * kh + 16 * (c.w & 1)) * 1152 + colb;
    sh[0] = WS_H + (((4 * c.T + 2 * (c.m & 1) + (c.w & 1)) * 4 + c.a) * 42 + kh) * 4096; sh[1] = sh[0] + kh * 8192;
    f1 = WS_F1 + (c.T * 4 + c.a) * 128;
  }
  template <int S> DI unsigned pvo() const { return S < 3 ? (vf < 48 ? vf : 0) : S == 3 ? 48 + (vf & 8) : vf & 24; }
  template <int S> DI void poll(const Ctx& c) { poll_issue(c, pf, pvo<S>(), S < 4 ? f1 : flag2(c)); }
  template <int S> DI void check(const Ctx& c) { poll_check(c, pf, pvo<S>(), S < 4 ? f1 : flag2(c), S < 3 ? S + 1 : S == 3 ? 3 : 1); }
  template <int KIND, int U, int I> DI void prep(const Ctx& c) {
    if constexpr (KIND == O_D2) {
      constexpr int K = uk(U, NT);
      constexpr unsigned row = K < 18 ? 64 * K : 32 * (K - 18);
      if constexpr (I == 0) cur = dprep<slot(U), row * 1152>(lw, K < 18 ? sd : so);
      if constexpr (I == 4) dprep_l<slot(U)>(lw64);
    }
    if constexpr (KIND == O_B2 && U < RS && (I == 0 || (NT == 8 && U == AD && I == 1))) { curh = sh[uk(U, NT) >= 18] + (uk(U, NT) < 18 ? 2 * uk(U, NT) : 18 + uk(U, NT)) * 4096; asm volatile("" : "+s"(curh)); }
    if constexpr (KIND == O_B2 && U >= RS && (I & 1) == 0) { curh = sh[0] - (c.w >> 1) * 4096 + (2 * uk(U, NT) + (I >> 1)) * 4096; asm volatile("" : "+s"(curh)); }
  }
  template <int KIND, int U, int I> DI void fire(const Ctx& c) {
    if constexpr (KIND == O_D2 && !(ABL & 8)) dfire<(I & 3) * 1152>(I < 4 ? v128[I >> 1] : v64, uk(U, NT) < 18 ? c.rwd : c.rwo, cur);
    if constexpr (KIND == O_B2) ldh<(I & 1) * 1024>(bf[U][I], vh[U < RS ? I >> 1 : 0], c.rws, curh);
    if constexpr (KIND == O_POLL) poll<U>(c);
    if constexpr (KIND == O_GATE) ldg(omg, vh[0] & 255, c.rws, WS_G + (c.a * 32 + 4 * c.T + 2 * (c.m & 1) + (c.w & 1)) * 256);
  }
  static constexpr int tp(int t) { return NT == 8 || t < 6 ? t : t < 9 ? t + 2 : t < 11 ? t - 3 : 11; }
  static DI unsigned rbase(const Ctx& c, unsigned l, int t) {
    const unsigned g = l >> 4, q = (l >> 2) & 3;
    return (c.w >> 1) * 9216 + q * 1152 + g * 256 + 32 * (t ^ ((q & 2) | (g & 1))) + 8 * (l & 3);
  }
};

template <int NT, class Extra> DI void phase2(Ctx& c, P2<NT>& p, bf16x8 (&f)[16], Extra&& extra) {
  using P = P2<NT>;
  constexpr int AD = P::AD, WH = NT == 8 ? 0 : 2;
  const unsigned l = lane_id(), g = l >> 4, q = (l >> 2) & 3, kh = c.w >> 1;
  unsigned rb[4], rb64[4];
#pragma unroll
  for (int t = 0; t < 4; ++t) {
    rb[t] = P::win(0) + P::rbase(c, l, t); PIN(rb[t]);
    if (NT != 8) { rb64[t] = P::win(0) + S2 + kh * 4608 + (q & 1) * 1152 + (2 * g + (q >> 1)) * 128 + 32 * (t ^ ((q & 2) | (g & 1))) + 8 * (l & 3); PIN(rb64[t]); }
  }
  f32x4 acc[NT][4], oth[NT][2];
  bf16x8 f2[NT];
  s16x4 flo;
  auto xoff = [](int t) { return NT == 8 ? 20480 + t * 8192 : t < 6 ? t * 8192 : t < 9 ? (t - 6) * 8192 : 28672 + (t - 9) * 8192; };
  unsigned wa[2] = {0, 0}, ra[2] = {0, 0}, l16 = 0;
  const unsigned pb = inbox(c);
  auto rd = [&](auto U_, auto T_, auto H_) {
    constexpr int t = P::tp(CI(T_)), H = CI(H_);
    constexpr unsigned o = P::slot(CI(U_)) - P::win(CI(U_));
    return t < 8 ? trd(rb[t & 3] + o + 128 * (t >> 2) + H * 4608) : trd(rb64[t & 3] + o + H * 2304);
  };
  if constexpr (NT != 8) {
    wait_bar<VM<2, -1>>();
    STAMP(24);
    tieA(p.bf[0][0], p.bf[0][1], p.bf[0][2], p.bf[0][3]);
    static_for<PD>([&](auto T_) { f[CI(T_)] = cat(rd(IC(0), T_, IC(0)), rd(IC(0), T_, IC(1))); });
  }
  auto send = [&](auto T_, auto R_) {
    constexpr int t = CI(T_), R = CI(R_), it = P::tp(t), dq = it < 8 ? (it >> 1) ^ 3 : (it - 8) ^ 3, idx = it < 8 ? it & 1 : 2;
    if constexpr (R < 2) { acc[t][R] += oth[t][R]; PIN(acc[t][R]); }
    if constexpr (R == 2 && dq != 0) {
      const u32x4 v = {cvtpk(acc[t][0][0], acc[t][0][1]), cvtpk(acc[t][0][2], acc[t][0][3]), cvtpk(acc[t][1][0], acc[t][1][1]), cvtpk(acc[t][1][2], acc[t][1][3])};
      st16(__builtin_elementwise_min(v, u32x4{POISON - 1, POISON - 1, POISON - 1, POISON - 1}), c.rws, l16 + idx * 1024, pb + ((dq ^ c.a) * 4 + c.a) * 12288);
    }
  };
  static_for<21>([&](auto U_) {
    static_for<NT>([&](auto T_) {
      constexpr int U = CI(U_), t = CI(T_), N = U * NT + t, UN = (N + PD) / NT, tn = (N + PD) % NT;
      constexpr int RR = tn == 0 ? 2 : 0;
      const bf16x8 a = f[t], a2 = f2[t];
      static_for<4>([&](auto R_) {
        constexpr int R = CI(R_);
        if constexpr (U < RS) mf<true, U == 0>(acc[t][R], a, p.bf[U][R]); else mf<true, false>(acc[t][R & 1], R < 2 ? a : a2, p.bf[U][R]);
        SBAR();
        if constexpr (tn == 0 && UN <= 20) {
          if constexpr (R < 2 && (P::win(UN) != P::win(UN - 1) || UN == RS)) {
            const unsigned d = P::win(UN) - P::win(UN - 1);
#pragma unroll
            for (int i = 2 * R; i < 2 * R + 2; ++i) {
              rb[i] += d - (UN == RS ? kh * 9216 : 0); PIN(rb[i]);
              if (NT != 8) { rb64[i] += d - (UN == RS ? kh * 4608 : 0); PIN(rb64[i]); }
            }
          }
          if constexpr (R == 2) {
            if constexpr (NT == 8 && UN % 5 == 1) STUB<13 + UN / 5>();
            if constexpr (NT == 8 && UN == 20) STUB<17>();
            if constexpr (NT != 8 && UN % 3 == 2) STUB<46 + UN / 3>();
            wait_bar<(UN == (NT == 8 ? FLG2 : FLGA) ? 0 : VM<WH, P::FB + N>)>();
            STAMP(UN + (NT == 8 ? 28 : 24));
            tieA(p.bf[UN][0], p.bf[UN][1], p.bf[UN][2], p.bf[UN][3]);
          }
        }
        if constexpr (UN < RS || (UN <= 20 && tn != 0)) {
          if constexpr (R == RR) flo = rd(IC(UN), IC(tn), IC(0));
          if constexpr (R == RR + 1) f[tn] = cat(flo, rd(IC(UN), IC(tn), IC(1)));
          if constexpr (UN >= RS && R == 2) flo = rd(IC(UN), IC(tn), IC(2));
          if constexpr (UN >= RS && R == 3) f2[tn] = cat(flo, rd(IC(UN), IC(tn), IC(3)));
        } else if constexpr (UN <= 20) {
          if constexpr (R == 2) f[0] = cat(rd(IC(UN), IC(0), IC(0)), rd(IC(UN), IC(0), IC(1)));
          if constexpr (R == 3) f2[0] = cat(rd(IC(UN), IC(0), IC(2)), rd(IC(UN), IC(0), IC(3)));
        }
        SBAR();
        if constexpr (tn == 0 && R == 3 && UN <= 20) {
          if constexpr (UN == (NT == 8 ? FLG2 : FLGA)) { if (threadIdx.x == 0) flag_store(c, p.f1 + 8 * c.m, 3); }
          if constexpr (dep(UN + AD, NT) >= 0) p.template check<dep(UN + AD, NT)>(c);
          if constexpr (UN == 17) p.template check<4>(c);
          if constexpr (UN == 18) {
            PIN(p.omg);
            l16 = lane_id() * 16; PIN(l16);
            wa[0] = l16 + c.w * 2048 + (NT == 8 ? 0 : 4 * S3); wa[1] = l16 + c.w * 2048 + (NT == 8 ? 32768 : 0); PIN(wa[0]); PIN(wa[1]);
          }
          if constexpr (UN == 20) { const unsigned d = kh ? 0u - 4096 : 4096; ra[0] = wa[0] + d; ra[1] = wa[1] + d; PIN(ra[0]); PIN(ra[1]); }
        }
        extra(IC(N), R_);
        {
          constexpr int gi = N - (RS - 1) * NT - 2, wi = gi < 0 ? -1 : gi < 5 ? (R < 2 ? 2 * gi + R : -1) : (R == 0 ? gi + 5 : -1);
          if constexpr (wi >= 0 && wi < 2 * NT) {
            constexpr int tt = wi / 2, hi = NT == 8 ? tt >= 4 : tt >= 6;
            ldsw<f32x4>(wa[hi] + xoff(tt) - (NT == 8 && hi ? 32768 : 0) + (wi & 1) * 1024, acc[tt][2 + (wi & 1)]);
          }
          constexpr int rt_ = N - (20 * NT - 2);
          if constexpr (rt_ >= 0 && rt_ < NT && R < 2) {
            constexpr int hi = NT == 8 ? rt_ >= 4 : rt_ >= 6;
            oth[rt_][R] = ldsr<f32x4>(ra[hi] + xoff(rt_) - (NT == 8 && hi ? 32768 : 0) + R * 1024);
          }
          if constexpr (U == 20 && t >= 2) send(IC(t - 2), R_);
        }
        if constexpr (R >= 2) {
          constexpr Op o = op_at<WH>(P::FB + N, R), n = op_at<WH>(P::FB + N + 1, 2);
          if constexpr (R == 3 || N == 0) p.template prep<o.kind, o.a, o.b>(c);
          p.template fire<o.kind, o.a, o.b>(c);
          if constexpr (R == 3) p.template prep<n.kind, n.a, n.b>(c);
        }
        SBAR();
      });
    });
  });
  STUB<(NT == 8 ? 18 : 53)>();
  STAMP(NT == 8 ? 49 : 45);
  const unsigned l2 = l16 >> 4;
  STAMP(NT == 8 ? 50 : 46);
  constexpr int NI = NT / 4;
  u32x4 in[3][NI];
  for (unsigned spin = 0; spin < (1u << 16); ++spin) {
    unsigned mx = 0;
#pragma unroll
    for (int s = 1; s < 4; ++s)
#pragma unroll
      for (int i = 0; i < NI; ++i) in[s - 1][i] = __builtin_amdgcn_raw_buffer_load_b128(c.rws, l16 + i * 1024, pb + (c.a * 4 + (s ^ c.a)) * 12288, 16);
    asm volatile("" ::: "memory");
#pragma unroll
    for (int s = 0; s < 3; ++s)
#pragma unroll
      for (int i = 0; i < NI; ++i) mx = max(max(mx, max(in[s][i][0], in[s][i][1])), max(in[s][i][2], in[s][i][3]));
    if (__builtin_amdgcn_ballot_w64(mx == POISON) == 0) break;
  }
  STAMP(NT == 8 ? 51 : 47);
  const unsigned row0 = 256 * c.T + 128 * (c.m & 1) + 64 * (c.w & 1) + 32 * kh;
  const unsigned vo = (l2 & 15) * 2304 + 8 * (l2 >> 4), colb = NT == 8 ? 256 * (c.m >> 1) : 768;
#pragma unroll
  for (int i = 0; i < NI; ++i) {
    const int t = NT - NI + i;
    f32x4 s0 = acc[t][0], s1 = acc[t][1];
    if (t >= NT - 2) { s0 += oth[t][0]; s1 += oth[t][1]; }
#pragma unroll
    for (int s = 0; s < 3; ++s) {
      s0 += f32x4{bflo(in[s][i][0]), bfhi(in[s][i][0]), bflo(in[s][i][1]), bfhi(in[s][i][1])};
      s1 += f32x4{bflo(in[s][i][2]), bfhi(in[s][i][2]), bflo(in[s][i][3]), bfhi(in[s][i][3])};
    }
    s0 *= kh ? p.omg[2] : p.omg[0]; s1 *= kh ? p.omg[3] : p.omg[1];
    const unsigned cb = colb + (i < 2 ? 64 * c.a + 32 * i : 256 + 32 * c.a);
    __builtin_amdgcn_raw_buffer_store_b64(u32x2{cvtpk(s0[0], s0[1]), cvtpk(s0[2], s0[3])}, c.rout, vo, row0 * 1152 + cb, 0);
    __builtin_amdgcn_raw_buffer_store_b64(u32x2{cvtpk(s1[0], s1[1]), cvtpk(s1[2], s1[3])}, c.rout, vo, (row0 + 1) * 1152 + cb, 0);
  }
  if (NT == 8 && c.a == 0 && c.m == 0 && c.w == 0 && l2 == 0) flag_store(c, WS_E + c.T * 128, 16);
  if constexpr (NT == 8) asm volatile(".Lgm_code1:" ::: "memory"); else asm volatile(".Lgm_code2:" ::: "memory");
  STAMP(NT == 8 ? 52 : 48);
}

struct XS {
  unsigned v[2], lw, sx, rd[2], cur, sg;
  DI void init(const Ctx& c, unsigned base) {
    const unsigned l = lane_id(), j = l & 15, g = l >> 4;
    v[0] = (l >> 3) * 2304 + 16 * ((l & 7) ^ g); v[1] = (l >> 3) * 2304 + 16 * ((l & 7) ^ (4 + g));
    lw = base + c.w * 8192; sx = (256 * c.T + 64 * c.w) * 1152;
    rd[0] = lw + j * 128 + 16 * (g ^ (j >> 1)); rd[1] = rd[0] ^ 64;
    PIN(rd[0]); PIN(rd[1]);
  }
  template <int NSL, int C, int I> DI void prep(const Ctx& c) {
    if constexpr (I == 0) sg = sx + c.kg[C] * 128;
    cur = dprep<(C % NSL) * 32768 + I * 1024, (32 * (I >> 2) + 16 * (I & 1) + ((I >> 1) & 1)) * 1152>(lw, sg);
  }
  template <int I> DI void fire(const Ctx& c) { dfire<0>(v[I & 1], c.rx, cur); }
};
template <unsigned LA> DI void touch_pages(const Ctx& c) {
  const unsigned l = lane_id();
  const unsigned o = l < 4 ? WS_H + (((4 * c.T + l) * 4 + c.a) * 42 + 2 * c.m) * 4096 : l < 8 ? WS_PART + ((c.T * 8 + c.m) * 64 + (l - 4) * 16) * 3072 : l < 9 ? WS_G : WS_F1;
  touch<LA>(c.w < 2 ? o : (256 * c.T + 64 * (l & 3)) * 1152, c.w < 2 ? c.rws : c.rout, 0);
}
template <int I> DI void touch_w2(const Ctx& c, unsigned v128) {
  const unsigned idx = (c.T & 3) * 6 + c.m;
  const unsigned so = I < 7 ? (1152 * c.a + 48 * idx) * 1152 + c.w * 2048 + I * 8192 : (144 * c.a + 7 * idx) * 1152 + c.w * 2048;
  asm volatile("s_mov_b32 m0, %3\n\ts_mov_b64 exec, 0xffff\n\tbuffer_load_dword %0, %1, %2 offen sc1 lds\n\ts_mov_b64 exec, -1"
               ::"v"(v128), "s"(I < 7 ? c.rwd : c.rwo), "s"(so), "i"(9 * S1) : "memory");
}

constexpr int slot1(int n) { return n < 9 ? n % 4 : n % 9 < 5 ? 4 + n % 9 : n % 9 % 4; }
constexpr int sig_idx(int F) {
  if ((F + PD) % 16 == 0) return -1;
  int e = 0;
  for (int G = F - F % 72; G < F; ++G) if ((G + PD) % 16 != 0) ++e;
  return e;
}
constexpr int w_ks(int n, int I) { return n < 9 ? 2 * n + (I >> 1) : ((n % 9) * 4 + I) % 18; }
constexpr int w_up(int n, int I) { return n < 9 ? I & 1 : (n % 9) * 4 + I >= 18; }
DI void role_mlp(Ctx& c, XS& xs) {
  const unsigned l = lane_id(), g = l >> 4, q = (l & 15) >> 2, pp = l & 3, r8 = l >> 3;
  const unsigned vW = (8 * (r8 >> 2) + (r8 & 3)) * 9216 + 16 * ((l & 7) ^ (((r8 >> 1) & 3) << 1));
  const unsigned lw = c.w * 1024, sW = (16 * (c.w & 1) + 4 * (c.w >> 1)) * 9216 + (1152 * c.a + 64 * c.m) * 2;
  unsigned sWk[9];
#pragma unroll
  for (int i = 0; i < 9; ++i) sWk[i] = sW + c.kg[i] * (64 * 9216);
  const unsigned sH = WS_H + (((4 * c.T + c.w) * 4 + c.a) * 42 + 2 * c.m) * 4096;
  const unsigned f1 = WS_F1 + (c.T * 4 + c.a) * 128;
  bf16x8 xf[18][4];
  f32x4 ag0[4][4], ag[4][4], au[4][4];
  const float nl2e = -1.4426950408889634f;
  unsigned sp[4][4][2];
  P2<8> p2;
  u32x4 hq[8], pz;
  unsigned vl16 = l * 16;
  unsigned cur = 0;
  auto prep = [&](auto F_, auto R_) {
    constexpr Op o = mlp_op(CI(F_), CI(R_));
    if constexpr (o.kind == O_W) {
      constexpr int pn = o.a / 9, k = w_ks(o.a, o.b);
      cur = dprep<slot1(o.a) * S1 + o.b * 4096, (k & 1) * 32 * 9216 + pn * 768>(lw, sWk[k >> 1]);
    } else if constexpr (o.kind == O_X) xs.template prep<3, o.a, o.b>(c);
    else p2.template prep<o.kind, o.a, o.b>(c);
  };
  auto fire = [&](auto F_, auto R_) {
    constexpr Op o = mlp_op(CI(F_), CI(R_));
    if constexpr (o.kind == O_W && (ABL & 16) && o.a >= 14) {}
    else if constexpr (o.kind == O_W) dfire<0>(vW, w_up(o.a, o.b) ? c.rwu : c.rwg, cur);
    else if constexpr (o.kind == O_X) {
      if constexpr (o.a >= 3 && o.b == 0) asm volatile("s_waitcnt lgkmcnt(4)" ::: "memory");
      xs.template fire<o.b>(c);
    } else if constexpr (o.kind == O_FIN) {
      __builtin_amdgcn_raw_buffer_store_b128(hq[o.b], c.rws, vl16 + (o.b & 3) * 1024, sH + (12 * o.a + (o.b >> 2)) * 4096, 16);
      asm volatile("" ::: "memory");
    } else if constexpr (o.kind == O_PZ) poison<2, o.a>(c, pz, vl16);
    else if constexpr (o.kind == O_TW) touch_w2<o.a>(c, vl16 * 8);
    else if constexpr (o.kind == O_TL) touch_pages<3 * S1>(c);
    else p2.template fire<o.kind, o.a, o.b>(c);
  };
  static_for<9>([&](auto I_) { prep(IC(CI(I_) - 11), IC(2)); fire(IC(CI(I_) - 11), IC(2)); });
  STAMP(56);
  visit_all<0, 19>(c.pc0, c.w);
  STAMP(57);
  load_epoch(c);
  unsigned rdx2[2] = {xs.rd[0] + 65536, xs.rd[1] + 65536}; PIN(rdx2[0]); PIN(rdx2[1]);
  auto rdx = [&](auto C_, auto I_) {
    constexpr int C = CI(C_), s = CI(I_) >> 2, rt = CI(I_) & 3;
    xf[2 * C + s][rt] = ldsr<bf16x8>((C % 3 == 2 ? rdx2[s] : xs.rd[s] + (C % 3) * 32768) + rt * 2048);
  };
  unsigned rb[4];
#pragma unroll
  for (int t = 0; t < 4; ++t) rb[t] = (4 * g + q) * 128 + 32 * (t ^ ((2 * g + (q >> 1)) & 3)) + 8 * pp;
  float e0 = 0.f, e1 = 0.f, e3 = 0.f, h0 = 0.f, h1 = 0.f;
  unsigned e2 = 0;
  bf16x8 f[16];
  s16x4 flo;
  unsigned rb2[3];
  wait_bar<VM<0, -1>>();
  asm volatile("" : "+s"(c.E));
  STAMP(1);
  static_for<4>([&](auto I_) { rdx(IC(0), I_); });
  static_for<PD>([&](auto I_) { f[CI(I_)] = cat(trd(rb[CI(I_)]), trd(rb[CI(I_)] + 2048)); });
  static_for<4>([&](auto I_) { rdx(IC(0), IC(4 + CI(I_))); });
  SBAR();
  static_for<27>([&](auto N_) {
    static_for<16>([&](auto I_) {
      constexpr int N = CI(N_), I = CI(I_), F = N * 16 + I, FP = F % 144, ps = w_up(N, I >> 2), ks = w_ks(N, I >> 2), t = I & 3;
      constexpr int RG = (F + PD) / 16, pos = (F + PD) % 16, RR = pos == 0 ? 2 : 0;
      constexpr unsigned ro = (slot1(RG) % 4) * S1 + (pos >> 2) * 4096;
      const bf16x8 a = f[I];
      static_for<4>([&](auto R_) {
        constexpr int R = CI(R_);
        if constexpr (ps == 0) mf<(ks < 16), ks == 0>(F < 144 ? ag0[t][SN(t, R)] : ag[t][SN(t, R)], a, xf[ks][SN(t, R)]); else mf<(ks < 16), ks == 0>(au[t][SN(t, R)], a, xf[ks][SN(t, R)]);
        SBAR();
        if constexpr (R == 0 && N < 8 && I == XR) wait_vm<VM<0, F>>();
        if constexpr (pos == 0) {
          if constexpr (R < 2 && RG < 27 && slot1(RG) / 4 != slot1(RG - 1) / 4) {
#pragma unroll
            for (int i = 2 * R; i < 2 * R + 2; ++i) { rb[i] += (unsigned)(slot1(RG) / 4 - slot1(RG - 1) / 4) * 4 * S1; PIN(rb[i]); }
          }
          if constexpr (R == 0 && RG == 27) {
            const unsigned l4 = lane_id();
#pragma unroll
            for (int i = 0; i < 3; ++i) { rb2[i] = P2<8>::slot(0) + P2<8>::rbase(c, l4, i); PIN(rb2[i]); }
          }
          if constexpr (R == 2) {
            if constexpr (RG % 2 == 0) STUB<RG / 2 - 1>();
            static_assert((RG != FLG0 && RG != FLG1) || VM<0, F> == 0, "flags follow a full drain");
            wait_bar<VM<0, F>>();
            STAMP(1 + RG);
            if constexpr (RG == 27) tieA(p2.bf[0][0], p2.bf[0][1], p2.bf[0][2], p2.bf[0][3]);
          }
        }
        if constexpr (R == RR) { if constexpr (RG < 27) flo = trd(rb[pos & 3] + ro); else flo = trd(rb2[pos]); }
        if constexpr (R == RR + 1) { if constexpr (RG < 27) f[pos] = cat(flo, trd(rb[pos & 3] + ro + 2048)); else f[pos] = cat(flo, trd(rb2[pos] + 4608)); }
        if constexpr (N < 8 && (I == XR || I == XR + 1)) rdx(IC(N + 1), IC(4 * (I - XR) + R));
        SBAR();
        if constexpr (pos == 0 && R == 3) {
          if constexpr (RG == FLG0 || RG == FLG1) { if (threadIdx.x == 0) flag_store(c, f1 + 8 * c.m, RG == FLG0 ? 1 : 2); }
          if constexpr (RG == PZR + 1) keep(pz);
          if constexpr (RG == FLG0) { if (vl16 == 0) flag_store(c, flag2(c) + 8 * c.a, 1); }
          if constexpr (RG == 19) p2.init(c);
          if constexpr (RG == 25) p2.template check<0>(c);
        }
        if constexpr (R >= 2) { if constexpr (R == 3 || F == 0) prep(IC(F), R_); fire(IC(F), R_); }
        if constexpr (F >= 144 && ps == 1 && sig_idx(F) >= 0 && sig_idx(F) <= 65) {
          constexpr int e = sig_idx(F), d = e - 2;
          if constexpr (R == 0 && d >= 0) e2 = w_add1(e1);
          if constexpr (R == 1) { e3 = e0; if constexpr (e < 64) e0 = w_mul(ag[e / 16][(e / 4) % 4][e % 4]); }
          if constexpr (R == 2 && e >= 1 && e <= 64) e1 = w_exp(e3);
          if constexpr (R == 3 && d >= 0) { if constexpr (d % 2 == 0) sp[d / 16][(d / 4) % 4][(d % 4) / 2] = w_rcplo(e2); else w_rcphi(sp[d / 16][(d / 4) % 4][(d % 4) / 2], e2); }
        }
        if constexpr (F >= 138 && F < 146) {
          constexpr int tt = (F - 138) / 2, rt = 2 * (F & 1) + (R >> 1), k = R & 1;
          sp[tt][rt][k] = w_pklo(ag0[tt][rt][2 * k], nl2e); w_pkhi(sp[tt][rt][k], ag0[tt][rt][2 * k + 1], nl2e);
        }
        if constexpr (F >= 144 && F < 216 && sig_idx(F) >= 0) {
          constexpr int e = sig_idx(F), d = e - 2, pa = (e - 2) / 2, pf = (e - 4) / 2;
          if constexpr (R == 0 && e % 2 == 0 && e >= 2 && pa < 32) w_add1h(sp[pa / 8][(pa / 2) % 4][pa % 2]);
          if constexpr (R == 0 && e % 2 == 1 && e >= 5) h1 = w_mixhi(au[pf / 8][(pf / 2) % 4][2 * (pf % 2) + 1], sp[pf / 8][(pf / 2) % 4][pf % 2]);
          if constexpr (R == 1 && e % 2 == 0 && e >= 4) h0 = w_mixlo(au[pf / 8][(pf / 2) % 4][2 * (pf % 2)], sp[pf / 8][(pf / 2) % 4][pf % 2]);
          if constexpr (R == 1 && e % 2 == 1 && e >= 5) hq[(pf / 16) * 4 + (pf / 2) % 4][2 * ((pf / 8) & 1) + pf % 2] = w_cvt(h0, h1);
          if constexpr (R == 2 && e < 64) w_exph<e & 1>(sp[e / 16][(e / 4) % 4][(e % 4) / 2]);
          if constexpr (R == 3 && d >= 0 && d < 64) w_rcph<d & 1>(sp[d / 16][(d / 4) % 4][(d % 4) / 2]);
        }
        if constexpr (F >= 288 && FP >= FIN0 && FP < FIN0 + 32) {
          constexpr int J = (FP - FIN0) / 4, i = FP % 4, e = J >> 2, rt = J & 3, tt = 2 * e + (i >> 1), k = 2 * (i & 1);
          if constexpr (R == 0) h0 = w_mixlo(au[tt][rt][k], sp[tt][rt][i & 1]);
          if constexpr (R == 1) h1 = w_mixhi(au[tt][rt][k + 1], sp[tt][rt][i & 1]);
          if constexpr (R == 3) hq[J][i] = w_cvt(h0, h1);
        }
        if constexpr (R == 3 && mlp_op(F, 2).kind == O_FIN) keep(hq[mlp_op(F, 2).b]);
        if constexpr (R == 3 && F < 431) prep(IC(F + 1), IC(2));
        SBAR();
      });
    });
  });
  phase2<8>(c, p2, f, [&](auto N_, auto R_) {
    constexpr int N = CI(N_), R = CI(R_);
    if constexpr (N < 16 && R < 3) {
      constexpr int J = N / 2, e = J >> 2, rt = J & 3, tt = 2 * e + (N & 1);
      if constexpr (R == 0) { h0 = w_mixlo(au[tt][rt][0], sp[tt][rt][0]); h1 = w_mixhi(au[tt][rt][1], sp[tt][rt][0]); }
      if constexpr (R == 1) { e0 = w_mixlo(au[tt][rt][2], sp[tt][rt][1]); e1 = w_mixhi(au[tt][rt][3], sp[tt][rt][1]); }
      if constexpr (R == 2) { hq[J][2 * (N & 1)] = w_cvt(h0, h1); hq[J][2 * (N & 1) + 1] = w_cvt(e0, e1); }
    }
    if constexpr (R == 3 && N < 20 && lastfin(N) >= 0) {
      constexpr int J = lastfin(N);
      __builtin_amdgcn_raw_buffer_store_b128(hq[J], c.rws, vl16 + (J & 3) * 1024, sH + (24 + (J >> 2)) * 4096, 16);
      asm volatile("" ::: "memory");
    }
    if constexpr (R == 1 && N >= 1 && N < 21 && lastfin(N - 1) >= 0) keep(hq[lastfin(N - 1)]);
  });
}

DI u32x2 pk4(const f32x4& v) { return u32x2{cvtpk(v[0], v[1]), cvtpk(v[2], v[3])}; }
DI void sw16(float& a, float& b) { auto r = __builtin_amdgcn_permlane16_swap(__float_as_uint(a), __float_as_uint(b), false, false); a = __uint_as_float(r[0]); b = __uint_as_float(r[1]); }
DI void sw32(float& a, float& b) { auto r = __builtin_amdgcn_permlane32_swap(__float_as_uint(a), __float_as_uint(b), false, false); a = __uint_as_float(r[0]); b = __uint_as_float(r[1]); }
template <bool MX> DI float xred(float (&v)[4]) {
  sw16(v[0], v[1]); sw16(v[2], v[3]);
  float a = MX ? max2(v[0], v[1]) : v[0] + v[1], b = MX ? max2(v[2], v[3]) : v[2] + v[3];
  sw32(a, b);
  return MX ? max2(a, b) : a + b;
}
DI void xbc(float t, float (&v)[4]) {
  float a = t, b = t;
  sw32(a, b);
  v[0] = a; v[1] = a; v[2] = b; v[3] = b;
  sw16(v[0], v[1]); sw16(v[2], v[3]);
}

DI void role_att(Ctx& c, XS& xs) {
  const unsigned mp = c.m & 1;
  u32x2 vp[4][5], qp[4][5];
  u32x4 kt[6][4];
  f32x4 gate, bprev, bown, bprev0;
  unsigned mpv[2];
  P2<12> p2;
  {
    const unsigned l = lane_id(), g = l >> 4, q = (l & 15) >> 2, pp = l & 3;
    const unsigned lc = (l & 15) ^ (g << 1), hb = 144 * (2 * c.a + mp);
    const unsigned vA = g * 3456 + hb + (lc < 10 ? 1152 + 16 * (lc < 9 ? lc : 8) : 2304 + 16 * (lc - 10));
    const unsigned vB = g * 3456 + hb + (lc < 4 ? 2304 + 16 * (lc < 3 ? 6 + lc : 8) : 16 * (lc - 4));
    const u64 maskB = __builtin_amdgcn_ballot_w64(lc < 13);
    const unsigned lw = c.w * 1152;
    unsigned sWk[9];
#pragma unroll
    for (int i = 0; i < 9; ++i) sWk[i] = (8 * c.w + 64 * c.kg[i]) * 3456;
    const int hr = 256 * (int)c.T + ((c.w & 1) ? -16 : -15) + 2 * (int)(l >> 3);
    const unsigned vHx = (unsigned)(hr < 0 ? 0 : hr) * 1152 + 16 * ((l & 7) ^ (4 * (c.w & 1) + g));
    const unsigned lHx = A_HX + c.w * 1024;
    unsigned rh[2] = {A_HX + (l & 15) * 128 + 16 * (g ^ ((l & 15) >> 1)), 0}; rh[1] = rh[0] ^ 64; PIN(rh[0]); PIN(rh[1]);
    bf16x8 xf[18][4];
    unsigned cur = 0, l8 = 0;
    u32x4 pz;
    auto prep = [&](auto F_, auto R_) {
      constexpr Op o = att1_op(CI(F_), CI(R_));
      if constexpr (o.kind == O_W) cur = dprep<(o.a % 4) * S2 + o.b * 4608, (32 * (o.b >> 1) + 4 * (o.b & 1)) * 3456>(lw, sWk[o.a % 9]);
      else if constexpr (o.kind == O_X) xs.template prep<2, o.a, o.b>(c);
      else if constexpr (o.kind == O_HX) {
        if constexpr (o.a < 4) cur = dprep<o.a * 4096, 0>(lHx, (c.w < 2 ? c.kg[2 * o.a] : c.kg[2 * o.a + 1]) * 128);
        else cur = dprep<16384, 0>(lHx, c.w < 2 ? c.kg[8] * 128 : (c.w & 1) * 1024);
      } else p2.template prep<o.kind, o.a, o.b>(c);
    };
    auto fire = [&](auto F_, auto R_) {
      constexpr Op o = att1_op(CI(F_), CI(R_));
      if constexpr (o.kind == O_W) { if constexpr (o.a < 9) dfire<0>(vA, c.rqkv, cur); else dfire_m(vB, c.rqkv, cur, maskB); }
      else if constexpr (o.kind == O_X) {
        if constexpr (o.a >= 2 && o.b == 0) asm volatile("s_waitcnt lgkmcnt(4)" ::: "memory");
        xs.template fire<o.b>(c);
      } else if constexpr (o.kind == O_HX) {
        if constexpr (o.a < 4) dfire<0>(vHx, c.rx, cur); else dfire<0>(c.w < 2 ? vHx : l * 16, c.w < 2 ? c.rx : c.rgw, cur);
      } else if constexpr (o.kind == O_TL) touch_pages<A_GW + 2048>(c);
      else if constexpr (o.kind == O_PZ) poison<3, o.a>(c, pz, 2 * l8);
      else p2.template fire<o.kind, o.a, o.b>(c);
    };
    auto rdx = [&](auto C_, auto I_) {
      constexpr int C = CI(C_), s = CI(I_) >> 2, rt = CI(I_) & 3;
      xf[2 * C + s][rt] = ldsr<bf16x8>(xs.rd[s] + (C % 2) * 32768 + rt * 2048);
    };
    static_for<10>([&](auto I_) { prep(IC(CI(I_) - 12), IC(2)); fire(IC(CI(I_) - 12), IC(2)); });
    visit_all<32, 22>(c.pc0, c.w);
    STAMP(57);
    load_epoch(c);
    unsigned rb[4], rx1, rx2;
#pragma unroll
    for (int t = 0; t < 4; ++t) rb[t] = g * 1152 + q * 256 + 32 * (t ^ q) + 8 * pp;
    rx1 = g * 1152 + q * 256 + 32 * (c.w ^ q) + 8 * pp;
    rx2 = g * 1152 + q * 256 + 128 + 32 * ((c.w < 2 ? c.w + 1 : 3) ^ q) + 8 * pp;
    const unsigned gsrc = A_GW + 2 * (32 * (l >> 5) + 8 * ((l >> 2) & 3) + 4 * ((l >> 4) & 1) + (l & 3));
    const unsigned gdst = (l >> 2) * 1152 + (l & 3) * 256 + 16 * (13 ^ ((l & 3) << 1));
    u16 gwv = 0;
    f32x4 acc[2][8][4], ah[4];
    u32x2 hp;
    unsigned wk = 0, wv = 0;
    bf16x8 f[16], hx, ha, hc;
    s16x4 flo, hlo;
    wait_bar<VM<1, -1>>();
    asm volatile("" : "+s"(c.E));
    STAMP(1);
    static_for<4>([&](auto I_) { rdx(IC(0), I_); });
    static_for<PD>([&](auto I_) { f[CI(I_)] = cat(trd(rb[CI(I_)]), trd(rb[CI(I_)] + 4608)); });
    static_for<4>([&](auto I_) { rdx(IC(0), IC(4 + CI(I_))); });
    SBAR();
    auto pass = [&](auto PS_) {
      constexpr int PS = CI(PS_), NT = PS == 0 ? 8 : 7, FC = 2 * NT;
      static_for<9>([&](auto N_) {
        static_for<FC>([&](auto I_) {
          constexpr int N = CI(N_), I = CI(I_), F = 144 * PS + N * FC + I, ks = 2 * N + I / NT, t = I % NT;
          constexpr int RG = rgA(F), pos = posA(F);
          constexpr int NTN = RG < 9 ? 8 : 7, tn = pos % NTN, RR = pos == 0 ? 2 : 0;
          constexpr unsigned ro = (RG % 2) * S2 + (pos / NTN) * 9216 + 128 * (tn >> 2);
          constexpr unsigned rc = ((9 * PS + N) % 2) * S2 + (I / NT) * 9216;
          constexpr bool SW = PS == 0 ? t >= 5 : t < 2;
          const bf16x8 a = f[I];
          static_for<5>([&](auto R_) {
            constexpr int R = CI(R_);
            if constexpr (R < 4) { if constexpr (SW) mfs<(ks < 16), ks == 0>(acc[PS][t][R], a, xf[ks][R]); else mf<(ks < 16), ks == 0>(acc[PS][t][R], a, xf[ks][R]); }
            else if constexpr (PS == 0) {
              if constexpr (t == 4) mf<false, ks == 0>(ah[1], a, hx);
              if constexpr (t == 5) mf<false, ks == 0>(ah[0], ha, hx);
              if constexpr (t == 7) mfs<false, ks == 0>(ah[2], hc, hx);
            } else if constexpr (t == 4) mfs<false, ks == 0>(ah[3], ha, hx);
            SBAR();
            if constexpr (R == 0 && PS == 0 && I == XR && N < 8) wait_vm<VM<1, F>>();
            if constexpr (pos == 0 && RG < 18) {
              if constexpr (R < 2 && RG % 2 == 0) {
                constexpr unsigned d = RG % 4 == 0 ? 0u - 2 * S2 : 2 * S2;
#pragma unroll
                for (int i = 2 * R; i < 2 * R + 2; ++i) { rb[i] += d; PIN(rb[i]); }
                if (R == 0) { rx1 += d; PIN(rx1); } else if (RG < 9) { rx2 += d; PIN(rx2); }
              }
              if constexpr (R == 0 && RG == 9) { rx1 ^= 32 * (c.w & 2); PIN(rx1); }
              if constexpr (R == 2) {
                if constexpr (RG % 2 == 1) STUB<32 + RG / 2>();
                static_assert(RG != 16 || VM<1, F> == 0, "flags follow a full drain");
                wait_bar<VM<1, F>>();
                STAMP(1 + RG);
              }
            }
            if constexpr (R == RR && RG < 18) flo = trd(rb[tn & 3] + ro);
            if constexpr (R == RR + 1 && RG < 18) f[pos] = cat(flo, trd(rb[tn & 3] + ro + 4608));
            if constexpr (PS == 0 && N < 8 && (I == XR || I == XR + 1) && R < 4) rdx(IC(N + 1), IC(4 * (I - XR) + R));
            if constexpr (t == 0 && R == 3) hx = ldsr<bf16x8>(rh[ks & 1] + (ks >> 1) * 2048);
            if constexpr (t == 1 && R == 3) hlo = trd(rx1 + rc);
            if constexpr (t == 2 && R == 3) ha = cat(hlo, trd(rx1 + rc + 4608));
            if constexpr (PS == 0 && t == 3 && R == 3) hlo = trd(rx2 + rc);
            if constexpr (PS == 0 && t == 4 && R == 3) hc = cat(hlo, trd(rx2 + rc + 4608));
            if constexpr (RG + 3 >= 9 && RG + 3 <= 17 && R == 1) {
              if constexpr (pos == 6) gwv = ldsr<u16>(gsrc + c.kg[RG + 3 - 9] * 128);
              if constexpr (pos == 9) ldsw<u16>(gdst + ((RG + 3) % 4) * S2, gwv);
            }
            SBAR();
            if constexpr (F == 136 && R == 0) { l8 = lane_id() * 8; PIN(l8); wk = A_KX + (4 * c.w + 2) * 2560 + l8; PIN(wk); }
            if constexpr (F >= 138 && F < 146 && R < 4) {
              constexpr int tt = F - 138;
              if constexpr (tt < 5) ldsw<u32x2>(wk + R * 2560 + tt * 512, pk4(acc[0][tt][R])); else { vp[R][tt - 5] = pk4(acc[0][tt][R]); PIN(vp[R][tt - 5]); }
            }
            if constexpr (F == 146 && R < 2) { const u32x2 h = pk4(ah[R]); const unsigned o = A_KX + (R == 0 ? c.w : 4) * 512; ldsw<u32x2>(o + l8, h); ldsw<u32x2>(o + 2560 + (l8 ^ 64), h); }
            if constexpr (F == 146 && R == 2) { hp = pk4(ah[2]); PIN(hp); }
            if constexpr (pos == 0 && R == 3) {
              if constexpr (RG == 15) { keep(pz); p2.init(c); wv = A_VX + (c.w < 3 ? (c.w + 1) * 5120 : A_GW + 2304 - A_VX) + (c.w < 3 ? l8 : 0); PIN(wv); }
              if constexpr (RG == 16) { if (l8 == 0) flag_store(c, flag2(c) + 8 * c.a, 1); }
            }
            if constexpr (RG == 15 && pos >= 4 && pos < 10 && R == 2) { if (c.w < 3) ldsw<u32x2>(wv + (((pos - 4) / 3) * 5 + (pos - 4) % 3) * 512, vp[2 + (pos - 4) / 3][(pos - 4) % 3]); }
            if constexpr (RG == 15 && pos == 11 && R == 2) { const unsigned o = A_VX + (c.w < 2 ? c.w : 2) * 512; ldsw<u32x2>(o + l8, hp); ldsw<u32x2>(o + 2560 + (l8 ^ 256), hp); }
            if constexpr (RG == 16 && pos >= 9 && pos < 13 && R == 2) {
              constexpr int r = pos - 9;
              const int key = (int)(l8 >> 7) * 4 + r, jj = (int)(l8 >> 3) & 15;
              bprev[r] = key >= jj + 9 ? 0.f : -1e30f;
              bown[r] = (key <= jj && key >= jj - 7) ? 0.f : -1e30f;
              bprev0[r] = (c.T == 0 && c.w == 0) ? -1e30f : bprev[r];
              if constexpr (r % 2 == 0) mpv[r / 2] = key >= jj + 9 ? 0xffffu : 0u; else { mpv[r / 2] |= key >= jj + 9 ? 0xffff0000u : 0u; PIN(mpv[r / 2]); }
              if constexpr (r == 3) { PIN(bprev); PIN(bown); PIN(bprev0); }
            }
            if constexpr (F == 256 && R == 0) { wk -= 5120; PIN(wk); }
            if constexpr (F >= 257 && F < 265 && (R == 0 || R == 1 || R == 3)) {
              constexpr int n = 3 * (F - 257) + (R == 3 ? 2 : R), i = n / 4, h = n % 4;
              const u32x2 k0 = ldsr<u32x2>(wk + i * 2560 + h * 512), k1 = ldsr<u32x2>(wk + i * 2560 + h * 512 + 512);
              kt[i][h] = u32x4{k0[0], k0[1], k1[0], k1[1]};
              asm volatile("" ::: "memory");
            }
            if constexpr (F >= 265 && R < 4) {
              constexpr int tt = F - 265;
              if constexpr (tt < 2) { vp[R][3 + tt] = pk4(acc[1][tt][R]); PIN(vp[R][3 + tt]); if constexpr (R >= 2) { if (c.w < 3) ldsw<u32x2>(wv + ((R - 2) * 5 + 3 + tt) * 512, vp[R][3 + tt]); } }
              else { qp[R][tt - 2] = pk4(acc[1][tt][R]); PIN(qp[R][tt - 2]); }
            }
            if constexpr (R == 2 || R == 3) { if constexpr (R == 3 || F == 0) prep(IC(F), R_); fire(IC(F), R_); }
            if constexpr (R == 4) prep(IC(F + 1), IC(2));
            SBAR();
          });
        });
      });
    };
    pass(IC(0));
    pass(IC(1));
    STUB<41>();
    STAMP(19);
    { const u32x2 h = pk4(ah[3]); const unsigned o = A_VX + (3 + (c.w & 1)) * 512; ldsw<u32x2>(o + l8, h); ldsw<u32x2>(o + 2560 + (l8 ^ 256), h); }
#pragma unroll
    for (int rt = 0; rt < 4; ++rt) { qp[rt][3] = pk4(acc[1][5][rt]); PIN(qp[rt][3]); }
    asm volatile("s_waitcnt lgkmcnt(0)\n\ts_barrier" ::: "memory");
#pragma unroll
    for (int rt = 0; rt < 4; ++rt) {
      qp[rt][4] = pk4(acc[1][6][rt]);
      gate[rt] = __builtin_amdgcn_exp2f(fminf(1.4426950408889634f * acc[1][6][rt][0], 80.0f));
    }
  }
  STAMP(20);
  auto pre = [&](auto A_, auto N_) { static_for<CI(N_)>([&](auto I_) { constexpr Op o = att2_op(CI(A_) + CI(I_) - 50, 2); p2.template prep<o.kind, o.a, o.b>(c); p2.template fire<o.kind, o.a, o.b>(c); }); };
  const unsigned l = lane_id(), g = (l >> 4) & 3, j = l & 15;
  {
    u32x6 vt[5][2];
    const unsigned ra = A_VX + c.w * 5120 + l * 8;
#pragma unroll
    for (int pa = 0; pa < 2; ++pa)
#pragma unroll
      for (int t = 0; t < 5; ++t) {
        const u32x2 v2 = ldsr<u32x2>(ra + (pa * 5 + t) * 512);
        vt[t][pa] = u32x6{v2[0], v2[1], vp[pa][t][0], vp[pa][t][1], vp[2 + pa][t][0], vp[2 + pa][t][1]};
      }
    const f32x4 omg = {__builtin_amdgcn_rcpf(1.0f + gate[0]), __builtin_amdgcn_rcpf(1.0f + gate[1]), __builtin_amdgcn_rcpf(1.0f + gate[2]), __builtin_amdgcn_rcpf(1.0f + gate[3])};
    if (mp == 0 && g == 2) st16(__builtin_bit_cast(u32x4, omg), c.rws, j * 16, WS_G + (c.a * 32 + 4 * c.T + c.w) * 256);
#pragma unroll
    for (int rt = 0; rt < 4; ++rt) gate[rt] = __shfl(gate[rt], 32 + j);
    constexpr float SC2 = 0.23570226039551584f * 1.4426950408889634f;
    const unsigned sH = WS_H + (((4 * c.T + c.w) * 4 + c.a) * 42 + 36 + 3 * mp) * 4096;
    u32x4 qf[4], P[4];
    f32x4 sp[4], so[4], hi[4], lo[4];
    auto mkq = [&](auto RT_) {
      constexpr int rt = CI(RT_);
#pragma unroll
      for (int h = 0; h < 4; ++h) {
#pragma unroll
        for (int i = 0; i < 4; ++i) { const int col = 16 * (h + (i >> 1)) + 4 * (int)g + 2 * (i & 1); qf[h][i] = (col >= 18 * h && col < 18 * h + 18) ? qp[rt][h + (i >> 1)][i & 1] : 0u; }
        PIN(qf[h]);
      }
    };
    auto qk = [&](auto RT_, auto H_, auto W_) {
      constexpr int rt = CI(RT_), h = CI(H_);
      if constexpr (CI(W_) == 0) sp[h] = mfc(kt[rt][h], qf[h], rt < 2 ? bprev0 : bprev);
      else so[h] = mfc(kt[rt + 2][h], qf[h], bown);
    };
    auto pv = [&](auto RT_, auto T_, auto W_) {
      constexpr int rt = CI(RT_), t = CI(T_) + CI(W_), pa = rt & 1, sn = rt >> 1;
      const f32x4 r = mfz(__builtin_shufflevector(vt[t][pa], vt[t][pa], 2 * sn, 2 * sn + 1, 2 * sn + 2, 2 * sn + 3), P[CI(T_)]);
      if constexpr (CI(W_) == 0) hi[CI(T_)] = r; else lo[CI(T_)] = r;
    };
    auto outp = [&](auto RT_) {
      constexpr int rt = CI(RT_);
      u32x2 o[5];
      PIN(lo[0]);
#pragma unroll
      for (int t = 0; t < 5; ++t)
#pragma unroll
        for (int i = 0; i < 2; ++i) {
          const unsigned vh = t < 4 ? cvtpk(hi[t < 4 ? t : 0][2 * i], hi[t < 4 ? t : 0][2 * i + 1]) : 0u;
          o[t][i] = (4 * (int)g + 2 * i < 2 * t) ? cvtpk(lo[t > 0 ? t - 1 : 0][2 * i], lo[t > 0 ? t - 1 : 0][2 * i + 1]) : vh;
        }
      st16(u32x4{o[0][0], o[0][1], o[1][0], o[1][1]}, c.rws, l * 16 + rt * 1024, sH);
      st16(u32x4{o[2][0], o[2][1], o[3][0], o[3][1]}, c.rws, l * 16 + rt * 1024, sH + 4096);
      st16(u32x4{o[4][0], o[4][1], 0u, 0u}, c.rws, l * 16 + rt * 1024, sH + 8192);
    };
    mkq(IC(0));
    asm volatile("s_nop 1" ::: "memory");
    static_for<8>([&](auto I_) { qk(IC(0), IC(CI(I_) >> 1), IC(CI(I_) & 1)); });
    SBAR();
    mkq(IC(1));
    static_for<4>([&](auto RT_) {
      constexpr int rt = CI(RT_);
      float s[4][4], red[4], bc[4];
      STUB<42 + rt>();
      SBAR();
      static_for<4>([&](auto H_) {
        constexpr int h = CI(H_);
        if constexpr (rt > 0) pv(IC(rt - 1), H_, IC(0));
        s[h][0] = max2(sp[h][0], so[h][0]); s[h][1] = max2(sp[h][1], so[h][1]); s[h][2] = max2(sp[h][2], so[h][2]);
        SBAR();
        if constexpr (rt > 0) pv(IC(rt - 1), H_, IC(1));
        s[h][3] = max2(sp[h][3], so[h][3]);
        red[h] = max2(max3(s[h][0], s[h][1], s[h][2]), s[h][3]);
        SBAR();
      });
      xbc(-SC2 * xred<true>(red), bc);
      if constexpr (rt == 0) pre(IC(6), IC(7));
      if constexpr (rt == 2) { wait_vm<VM<2, -28>>(); p2.template check<0>(c); }
      if constexpr (rt > 0) outp(IC(rt - 1));
      SBAR();
      static_for<4>([&](auto H_) {
        constexpr int h = CI(H_);
        if constexpr (rt < 3) qk(IC(rt + 1), H_, IC(0));
        s[h][0] = __builtin_amdgcn_exp2f(__builtin_fmaf(s[h][0], SC2, bc[h])); s[h][1] = __builtin_amdgcn_exp2f(__builtin_fmaf(s[h][1], SC2, bc[h]));
        SBAR();
        if constexpr (rt < 3) qk(IC(rt + 1), H_, IC(1));
        s[h][2] = __builtin_amdgcn_exp2f(__builtin_fmaf(s[h][2], SC2, bc[h])); s[h][3] = __builtin_amdgcn_exp2f(__builtin_fmaf(s[h][3], SC2, bc[h]));
        SBAR();
      });
#pragma unroll
      for (int h = 0; h < 4; ++h) red[h] = (s[h][0] + s[h][1]) + (s[h][2] + s[h][3]);
      xbc(gate[rt] * __builtin_amdgcn_rcpf(xred<false>(red)), bc);
      if constexpr (rt == 1) pre(IC(16), IC(6));
      if constexpr (rt == 2) pre(IC(25), IC(8));
      if constexpr (rt == 3) pre(IC(36), IC(4));
      if constexpr (rt == 0) {
#pragma unroll
        for (int i = 0; i < 10; ++i) PIN(vt[i >> 1][i & 1]);
      }
      if constexpr (rt < 2) mkq(IC(rt + 2));
#pragma unroll
      for (int h = 0; h < 4; ++h) {
        const unsigned p0 = cvtpk(s[h][0] * bc[h], s[h][1] * bc[h]), p1 = cvtpk(s[h][2] * bc[h], s[h][3] * bc[h]);
        P[h] = u32x4{p0 & mpv[0], p1 & mpv[1], p0 & ~mpv[0], p1 & ~mpv[1]};
        PIN(P[h]);
      }
      asm volatile("s_nop 1" ::: "memory");
    });
    static_for<8>([&](auto I_) { pv(IC(3), IC(CI(I_) >> 1), IC(CI(I_) & 1)); });
    asm volatile("s_nop 7" ::: "memory");
    SBAR();
    outp(IC(3));
  }
  STAMP(21);
  bf16x8 f[16];
  phase2<12>(c, p2, f, [](auto, auto) {});
}

#endif
#define STAMP_OFF (64u << 20)
__global__ __launch_bounds__(256, 1) void fused_kernel(const u16* x, const u16* wqkv, const u16* wo, const u16* wg, const u16* wu, const u16* wd,
                                                        const u16* gw, char* ws, u16* out) {
#if defined(__HIP_DEVICE_COMPILE__)
  __shared__ __attribute__((aligned(16))) char smem[LDS_BYTES];
  asm volatile("" ::"v"((unsigned)(size_t)smem));
  Ctx c;
  STAMP(0);
  const unsigned bid = blockIdx.x, xcd = bid & 7, idx = bid >> 3;
  c.w = __builtin_amdgcn_readfirstlane(threadIdx.x >> 6);
  {
    u64 pc; unsigned len;
    asm volatile("s_getpc_b64 %0\n.Lgm_code0:\n\ts_mov_b32 %1, .Lgm_code1-.Lgm_code0\n\ts_max_u32 %1, %1, .Lgm_code2-.Lgm_code0" : "=s"(pc), "=s"(len) :: "scc");
    c.pc0 = pc;
    asm volatile("s_branch .Lgm_go\n.Lgm_ret:\n\ts_setpc_b64 s[100:101]\n.Lgm_go:");
    const rsrc_t rc = __builtin_amdgcn_make_buffer_rsrc((void*)pc, (short)0, len, 0x00020000);
    if (idx < 16) touch<3 * S1>((threadIdx.x & 15) * 128, rc, idx * 8192 + c.w * 2048);
  }
  asm volatile("" ::"s"(x), "s"(out));
  STAMP(54);
  c.rx = __builtin_amdgcn_make_buffer_rsrc((void*)x, (short)0, SEQ * DM * 2, 0x00020000);
  c.rqkv = __builtin_amdgcn_make_buffer_rsrc((void*)wqkv, (short)0, DM * 3 * DM * 2, 0x00020000);
  c.rwo = __builtin_amdgcn_make_buffer_rsrc((void*)wo, (short)0, DM * DM * 2, 0x00020000);
  c.rwg = __builtin_amdgcn_make_buffer_rsrc((void*)wg, (short)0, DM * FF * 2, 0x00020000);
  c.rwu = __builtin_amdgcn_make_buffer_rsrc((void*)wu, (short)0, DM * FF * 2, 0x00020000);
  c.rwd = __builtin_amdgcn_make_buffer_rsrc((void*)wd, (short)0, DM * FF * 2, 0x00020000);
  c.rgw = __builtin_amdgcn_make_buffer_rsrc((void*)gw, (short)0, DM * 2, 0x00020000);
  c.rws = __builtin_amdgcn_make_buffer_rsrc((void*)ws, (short)0, WS_BYTES, 0x00020000);
  c.rout = __builtin_amdgcn_make_buffer_rsrc((void*)out, (short)0, SEQ * DM * 2, 0x00020000);
  c.ws = (u64)ws;
  c.a = xcd & 3; c.T = 4 * (xcd >> 2) + (idx >> 3);
  c.m = (idx & 3) == 3 ? 6 + ((idx >> 2) & 1) : 3 * ((idx >> 2) & 1) + (idx & 3);
#define ROTM 1
  const unsigned rot = ROTM == 0 ? 0 : ROTM == 1 ? c.m + 2 * (c.T & 3) : ROTM == 2 ? 3 * ((c.m + c.T) % 3) : 4 * ((c.m + c.T) & 1);
  c.kg[0] = ROTM >= 4 ? 0 : rot >= 9 ? rot - 9 : rot;
  XS xs; xs.init(c, c.m < 6 ? M_X : A_X);
  static_for<8>([&](auto I_) { xs.template prep<1, 0, CI(I_)>(c); xs.template fire<CI(I_)>(c); });
  STAMP(55);
  SBAR();
#pragma unroll
  for (int i = 1; i < 9; ++i) {
    if (ROTM == 4) c.kg[i] = 1 + ((i + c.m + 2 * c.T) & 7);
    else if (ROTM == 5) c.kg[i] = i == 1 ? 1 : i == 2 ? 2 + (c.m + 2 * (c.T & 3)) % 7 : c.kg[i - 1] == 8 ? 2 : c.kg[i - 1] + 1;
    else c.kg[i] = c.kg[i - 1] == 8 ? 0 : c.kg[i - 1] + 1;
  }
  if (__builtin_expect(c.m < 6, 1)) role_mlp(c, xs); else role_att(c, xs);
#endif
}

extern "C" void kernel_launch(void* const* d_in, const int* in_sizes, int n_in, void* d_out, int out_size, void* d_ws, size_t ws_size,
                              hipStream_t stream) {
  fused_kernel<<<256, 256, 0, stream>>>((const u16*)d_in[0], (const u16*)d_in[1], (const u16*)d_in[2], (const u16*)d_in[4], (const u16*)d_in[5],
                                        (const u16*)d_in[6], (const u16*)d_in[7], (char*)d_ws, (u16*)d_out);
}
```

```cpp
#include <hip/hip_runtime.h>
#include <utility>

typedef unsigned short u16;
typedef unsigned long long u64;
typedef short s16x4 __attribute__((ext_vector_type(4)));
typedef __bf16 bf16x8 __attribute__((ext_vector_type(8)));
typedef __bf16 bf16x2 __attribute__((ext_vector_type(2)));
typedef float f32x2 __attribute__((ext_vector_type(2)));
typedef float f32x4 __attribute__((ext_vector_type(4)));
typedef unsigned u32x2 __attribute__((ext_vector_type(2)));
typedef unsigned u32x4 __attribute__((ext_vector_type(4)));
typedef unsigned u32x6 __attribute__((ext_vector_type(6)));
typedef __amdgpu_buffer_rsrc_t rsrc_t;
#define LAS __attribute__((address_space(3)))
#define DI __device__ __forceinline__
#define SBAR() __builtin_amdgcn_sched_barrier(0)
#define IC(x) std::integral_constant<int, (x)>{}
#define CI(x) decltype(x)::value

constexpr int SEQ = 2048, DM = 576, FF = 4608;
constexpr unsigned WS_H = 0;
constexpr unsigned WS_PART = 24u << 20;
constexpr unsigned WS_G = 40u << 20;
constexpr unsigned WS_F1 = 41u << 20;
constexpr unsigned WS_F2 = WS_F1 + 8192;
constexpr unsigned WS_E = WS_F2 + 65536;
constexpr unsigned WS_BYTES = 42u << 20;
constexpr unsigned LDS_BYTES = 163840;
constexpr unsigned S1 = 16384;
constexpr unsigned S2 = 18432;
constexpr unsigned S3 = S2 + 9216;
constexpr unsigned M_X = 65536;
constexpr unsigned A_X = 4 * S2;
constexpr unsigned A_HX = A_X + 65536;
constexpr unsigned A_GW = A_HX + 18432;
constexpr unsigned A_KX = 83968;
constexpr unsigned A_VX = A_KX + 18 * 2560;
static_assert(A_GW + 2048 <= LDS_BYTES && A_VX <= A_HX && A_VX + 20480 <= LDS_BYTES && 3 * S3 <= A_KX, "lds");

#if defined(__HIP_DEVICE_COMPILE__)
template <int... Is> using iseq = std::integer_sequence<int, Is...>;
template <class Fn, int... Is> DI void static_for_(Fn&& f, iseq<Is...>) { (f(std::integral_constant<int, Is>{}), ...); }
template <int N, class Fn> DI void static_for(Fn&& f) { static_for_(f, std::make_integer_sequence<int, N>{}); }

template <int N> DI void wait_bar() { asm volatile("s_waitcnt vmcnt(%0) lgkmcnt(0)\n\ts_barrier" ::"i"(N) : "memory"); }
template <int N> DI void wait_vm() { asm volatile("s_waitcnt vmcnt(%0)" ::"i"(N) : "memory"); }
template <class T> DI void PIN(T& x) { asm volatile("" : "+v"(x)); }
DI void tieA(bf16x8& a, bf16x8& b, bf16x8& c, bf16x8& d) { asm volatile("" : "+a"(a), "+a"(b), "+a"(c), "+a"(d)); }
template <unsigned LO, unsigned SO> DI unsigned dprep(unsigned lb, unsigned sb) {
  unsigned t;
  asm volatile("s_add_u32 m0, %1, %3\n\ts_add_u32 %0, %2, %4" : "=s"(t) : "s"(lb), "s"(sb), "i"(LO), "i"(SO) : "scc");
  return t;
}
template <unsigned LO> DI void dprep_l(unsigned lb) { asm volatile("s_add_u32 m0, %0, %1\n\ts_nop 0" ::"s"(lb), "i"(LO) : "scc"); }
template <unsigned IMM> DI void dfire(unsigned voff, rsrc_t srd, unsigned so) {
  asm volatile("buffer_load_dwordx4 %0, %1, %2 offen offset:%3 lds" ::"v"(voff), "s"(srd), "s"(so), "i"(IMM) : "memory");
}
DI void dfire_m(unsigned voff, rsrc_t srd, unsigned so, u64 mask) {
  asm volatile("s_mov_b64 exec, %3\n\tbuffer_load_dwordx4 %0, %1, %2 offen lds\n\ts_mov_b64 exec, -1" ::"v"(voff), "s"(srd), "s"(so), "s"(mask) : "memory");
}
template <unsigned IMM> DI void ldh(bf16x8& d, unsigned voff, rsrc_t srd, unsigned so) {
  asm volatile("buffer_load_dwordx4 %0, %1, %2, %3 offen offset:%4 sc1" : "=a"(d) : "v"(voff), "s"(srd), "s"(so), "i"(IMM) : "memory");
}
DI void ldg(f32x4& d, unsigned voff, rsrc_t srd, unsigned so) { asm volatile("buffer_load_dwordx4 %0, %1, %2, %3 offen sc1" : "=v"(d) : "v"(voff), "s"(srd), "s"(so) : "memory"); }
template <unsigned LA> DI void touch(unsigned voff, rsrc_t srd, unsigned so) {
  asm volatile("s_mov_b32 m0, %3\n\ts_nop 0\n\tbuffer_load_dword %0, %1, %2 offen sc1 lds" ::"v"(voff), "s"(srd), "s"(so), "i"(LA) : "memory");
}
DI void st16(const u32x4& v, rsrc_t srd, unsigned voff, unsigned so) {
  __builtin_amdgcn_raw_buffer_store_b128(v, srd, voff, so, 16);
  asm volatile("s_nop 1" ::"v"(v) : "memory");
}
DI void keep(const u32x4& v) { asm volatile("" ::"v"(v)); }
template <bool AG, bool ZERO> DI void mf(f32x4& c, const bf16x8& a, const bf16x8& b) {
  if constexpr (ZERO) {
    if constexpr (AG) asm volatile("v_mfma_f32_16x16x32_bf16 %0, %1, %2, 0" : "=&v"(c) : "v"(a), "a"(b));
    else asm volatile("v_mfma_f32_16x16x32_bf16 %0, %1, %2, 0" : "=&v"(c) : "v"(a), "v"(b));
  } else {
    if constexpr (AG) asm volatile("v_mfma_f32_16x16x32_bf16 %0, %1, %2, %0" : "+v"(c) : "v"(a), "a"(b));
    else asm volatile("v_mfma_f32_16x16x32_bf16 %0, %1, %2, %0" : "+v"(c) : "v"(a), "v"(b));
  }
}
template <bool AG, bool ZERO> DI void mfs(f32x4& c, const bf16x8& a, const bf16x8& b) {
  if constexpr (ZERO) {
    if constexpr (AG) asm volatile("v_mfma_f32_16x16x32_bf16 %0, %2, %1, 0" : "=&v"(c) : "v"(a), "a"(b));
    else asm volatile("v_mfma_f32_16x16x32_bf16 %0, %2, %1, 0" : "=&v"(c) : "v"(a), "v"(b));
  } else {
    if constexpr (AG) asm volatile("v_mfma_f32_16x16x32_bf16 %0, %2, %1, %0" : "+v"(c) : "v"(a), "a"(b));
    else asm volatile("v_mfma_f32_16x16x32_bf16 %0, %2, %1, %0" : "+v"(c) : "v"(a), "v"(b));
  }
}
DI f32x4 mfc(const u32x4& a, const u32x4& b, const f32x4& c) { f32x4 d; asm volatile("v_mfma_f32_16x16x32_bf16 %0, %1, %2, %3" : "=&v"(d) : "a"(a), "v"(b), "v"(c)); return d; }
DI f32x4 mfz(const u32x4& a, const u32x4& b) { f32x4 d; asm volatile("v_mfma_f32_16x16x32_bf16 %0, %1, %2, 0" : "=&v"(d) : "v"(a), "v"(b)); return d; }
DI float max2(float a, float b) { float r; asm("v_max_f32 %0, %1, %2" : "=v"(r) : "v"(a), "v"(b)); return r; }
DI float max3(float a, float b, float c) { float r; asm("v_max3_f32 %0, %1, %2, %3" : "=v"(r) : "v"(a), "v"(b), "v"(c)); return r; }
DI s16x4 trd(unsigned a) { return __builtin_amdgcn_ds_read_tr16_b64_v4i16((LAS s16x4*)a); }
DI bf16x8 cat(s16x4 lo, s16x4 hi) { return __builtin_bit_cast(bf16x8, __builtin_shufflevector(lo, hi, 0, 1, 2, 3, 4, 5, 6, 7)); }
DI unsigned cvtpk(float lo, float hi) { f32x2 v = {lo, hi}; return __builtin_bit_cast(unsigned, __builtin_convertvector(v, bf16x2)); }
DI float w_mul(float a) { float r; asm volatile("v_mul_f32 %0, 0xbfb8aa3b, %1" : "=v"(r) : "v"(a)); return r; }
DI float w_exp(float a) { float r; asm volatile("v_exp_f32 %0, %1" : "=v"(r) : "v"(a)); return r; }
DI unsigned w_add1(float a) { unsigned r; asm volatile("v_fma_mixlo_f16 %0, %1, 1.0, 1.0" : "=v"(r) : "v"(a)); return r; }
DI unsigned w_rcplo(unsigned a) { unsigned r; asm volatile("v_rcp_f16 %0, %1" : "=v"(r) : "v"(a)); return r; }
DI void w_rcphi(unsigned& d, unsigned a) { asm volatile("v_rcp_f16_sdwa %0, %1 dst_sel:WORD_1 dst_unused:UNUSED_PRESERVE src0_sel:WORD_0" : "+v"(d) : "v"(a)); }
DI unsigned w_pklo(float a, float k) { unsigned r; asm volatile("v_fma_mixlo_f16 %0, %1, %2, 0" : "=v"(r) : "v"(a), "s"(k)); return r; }
DI void w_pkhi(unsigned& d, float a, float k) { asm volatile("v_fma_mixhi_f16 %0, %1, %2, 0" : "+v"(d) : "v"(a), "s"(k)); }
template <int H> DI void w_exph(unsigned& d) {
  if constexpr (H == 0) asm volatile("v_exp_f16_sdwa %0, %0 dst_sel:WORD_0 dst_unused:UNUSED_PRESERVE src0_sel:WORD_0" : "+v"(d));
  else asm volatile("v_exp_f16_sdwa %0, %0 dst_sel:WORD_1 dst_unused:UNUSED_PRESERVE src0_sel:WORD_1" : "+v"(d));
}
template <int H> DI void w_rcph(unsigned& d) {
  if constexpr (H == 0) asm volatile("v_rcp_f16_sdwa %0, %0 dst_sel:WORD_0 dst_unused:UNUSED_PRESERVE src0_sel:WORD_0" : "+v"(d));
  else asm volatile("v_rcp_f16_sdwa %0, %0 dst_sel:WORD_1 dst_unused:UNUSED_PRESERVE src0_sel:WORD_1" : "+v"(d));
}
DI void w_add1h(unsigned& d) { asm volatile("v_pk_add_f16 %0, %0, 1.0 op_sel_hi:[1,0]" : "+v"(d)); }
DI float w_mixlo(float u, unsigned s) { float r; asm volatile("v_fma_mix_f32 %0, %1, %2, 0 op_sel_hi:[0,1,0]" : "=v"(r) : "v"(u), "v"(s)); return r; }
DI float w_mixhi(float u, unsigned s) { float r; asm volatile("v_fma_mix_f32 %0, %1, %2, 0 op_sel:[0,1,0] op_sel_hi:[0,1,0]" : "=v"(r) : "v"(u), "v"(s)); return r; }
DI unsigned w_cvt(float lo, float hi) { unsigned r; asm volatile("v_cvt_pk_bf16_f32 %0, %1, %2" : "=v"(r) : "v"(lo), "v"(hi)); return r; }
DI float bflo(unsigned v) { return __uint_as_float(v << 16); }
DI float bfhi(unsigned v) { return __uint_as_float(v & 0xffff0000u); }
template <class T> DI T ldsr(unsigned a) { return *(const LAS T*)a; }
template <class T> DI void ldsw(unsigned a, T v) { *(LAS T*)a = v; }
DI unsigned lane_id() { unsigned l; asm volatile("v_mbcnt_lo_u32_b32 %0, -1, 0\n\tv_mbcnt_hi_u32_b32 %0, -1, %0" : "=v"(l)); return l; }

template <int N> DI void STUB() { asm volatile(".Lgm_s%c0:\n\ts_cbranch_execz .Lgm_ret" ::"i"(N)); }
template <int N> DI void visit(u64 pc0) {
  asm volatile("s_add_u32 s98, %0, .Lgm_s%c2-.Lgm_code0\n\ts_addc_u32 s99, %1, 0\n\ts_mov_b64 exec, 0\n\ts_swappc_b64 s[100:101], s[98:99]\n\ts_mov_b64 exec, -1"
               ::"s"((unsigned)pc0), "s"((unsigned)(pc0 >> 32)), "i"(N) : "s98", "s99", "s100", "s101", "scc", "memory");
}
template <int B, int CNT> DI void visit_all(u64 pc0, unsigned w) {
  static_for<4>([&](auto W_) { if (w == CI(W_)) static_for<(CNT + 3 - CI(W_)) / 4>([&](auto I_) { visit<B + CI(W_) + 4 * CI(I_)>(pc0); }); });
}

struct Ctx {
  rsrc_t rx, rqkv, rwo, rwg, rwu, rwd, rgw, rws, rout;
  unsigned w, a, T, m;
  unsigned kg[9];
  u64 E, ws, pc0;
};

enum { O_NONE, O_W, O_X, O_HX, O_FIN, O_POLL, O_D2, O_B2, O_GATE, O_ST, O_TW, O_TL, O_PZ };
constexpr int NEVER = 1 << 20;
struct Op { int kind, a, b, need; };
constexpr int PD = 3;
constexpr int MLP_AD = 3, ATT_AD = 3;
constexpr Op NOP_ = {O_NONE, 0, 0, 0};
constexpr int uk(int U, int NT) { const int o = NT == 8 ? 12 : 6; return U < o ? U : U < o + 3 ? U - o + 18 : U - 3; }
constexpr int dep(int U, int NT) { return U == 15 ? 2 : NT == 8 ? (U == 6 ? 1 : U == 12 ? 3 : -1) : (U == 6 ? 3 : U == 9 ? 1 : -1); }
constexpr int FLG2 = 4, FLGA = 1;
constexpr int RS = 19;
constexpr Op p2_op(int N, int R, int NT, int AD, int FB) {
  const int ND = NT == 8 ? 4 : 6, NO = ND + 4, RG = (N + PD) / NT, pos = (N + PD) % NT, FL = NT == 8 ? FLG2 : FLGA;
  if (RG > 20 || pos == 0 || RG == FL - 1) return NOP_;
  if (NT != 8 && R == 3 && pos == 1 && dep(RG + AD + 1, NT) >= 0) return {O_POLL, dep(RG + AD + 1, NT), 0, FB + (RG + 1) * NT - PD};
  int k;
  const bool busy = RG == FL || RG == FL + 1;
  if (busy) { const int d = NT == 8 ? 1 : 2, nb = NT == 8 ? 5 : 4; k = pos < d ? (R == 2 ? pos - 1 : -1) : pos < d + nb ? d - 1 + 2 * (pos - d) + (R - 2) : (R == 2 ? pos - 1 + nb : -1); }
  else if (NT == 8) k = R == 2 ? (pos == 1 ? 0 : pos) : pos == 1 ? 1 : pos == 7 ? 8 : -1;
  else k = R == 2 ? pos - 1 : -1;
  if (k < 0) return NOP_;
  const int n = busy ? (FL - 1 + AD) * NO + (RG - FL) * (3 * NO / 2) + k : (RG + AD) * NO + k, U = n / NO, i = n % NO;
  if (k < (busy ? 3 * NO / 2 : NO)) return U > 20 ? NOP_ : Op{i < ND ? O_D2 : O_B2, U, i < ND ? i : i - ND, FB + U * NT - PD};
  if (k != NO || busy) return NOP_;
  if (NT == 8 && dep(RG + AD + 2, NT) >= 0) return {O_POLL, dep(RG + AD + 2, NT), 0, FB + (RG + 2) * NT - PD};
  if (RG == 14) return {O_POLL, 4, 0, FB + 17 * NT - PD};
  if (RG == 15) return {O_GATE, 0, 0, FB + 18 * NT - PD};
  return NOP_;
}
constexpr int FIN0 = 36, ST0 = 72, FLG0 = 18, FLG1 = 25;
constexpr int PZR = 16;
constexpr int lastfin(int N) { return N == 1 ? 0 : N == 3 ? 1 : N == 7 ? 2 : N == 8 ? 3 : N == 10 ? 4 : N == 11 ? 5 : N == 15 ? 6 : N == 16 ? 7 : -1; }
constexpr int bar1(int n) { return n == 0 ? -1 : 16 * n - PD; }
constexpr Op opW(int n, int i) { return {O_W, n, i, bar1(n)}; }
constexpr int XR = 10;
constexpr Op opX(int c, int i) { return {O_X, c, i, c == 0 ? -1 : 16 * (c - 1) + XR}; }
constexpr Op mlp_op(int F, int R) {
  if (F < 0) {
    const int i = F + 15;
    if (R != 2 || i < 0 || i >= 13) return NOP_;
    return i < 8 ? opX(0, i) : i < 12 ? opW(0, i - 8) : Op{O_TL, 0, 0, bar1(1)};
  }
  if (F >= 440) {
    const int N = F - 440;
    if (N < 5) return R == 2 ? Op{N < 4 ? O_D2 : O_B2, MLP_AD, N & 3, 440 + MLP_AD * 8 - PD} : N == 0 || N == 2 || N == 4 ? Op{O_B2, MLP_AD, N / 2 + 1, 440 + MLP_AD * 8 - PD} : Op{O_FIN, 2, N / 2, 440 + FLG2 * 8 - PD};
    if (R == 3 && N < 20 && lastfin(N) >= 0) return {O_FIN, 2, lastfin(N), 440 + FLG2 * 8 - PD};
    return p2_op(N, R, 8, MLP_AD, 440);
  }
  if (F >= 432) return NOP_;
  const int RG = (F + PD) / 16, pos = (F + PD) % 16, r = R - 2;
  if (pos == 0) return NOP_;
  if (F < 4) return opX(1, 2 * F + r);
  if (F < 6) return opW(1, 2 * (F - 4) + r);
  if (R == 3) return NOP_;
  if (RG <= 5) {
    const int n = F - 6 - RG, c = 2 + n / 12, i = n % 12;
    const int cap = RG + 3;
    if (RG <= 3) { if (c <= cap) return i < 8 ? opX(c, i) : opW(c, i - 8); return NOP_; }
    if (RG == 4 && pos <= 8) return pos <= 4 ? opX(6, pos + 3) : opW(6, pos - 5);
    if (RG == 4) return opX(7, pos - 9);
    if (pos == 1) return opX(7, 7);
    if (pos <= 5) return opW(7, pos - 2);
    return pos <= 13 ? opX(8, pos - 6) : opW(8, pos - 14);
  }
  if (RG == 6 && pos <= 2) return opW(8, pos + 1);
  const int k8 = pos >= 3 && pos < 15 && (pos - 3) % 3 != 2 ? (pos - 3) - (pos - 3) / 3 : -1;
  if (RG == 6 && k8 >= 0) return opW(9 + k8 / 4, k8 % 4);
  if (RG == 7 && pos <= 12) return opW(pos <= 8 ? 11 + (pos - 1) / 4 : 14, (pos - 1) % 4);
  if (RG == 8 && pos <= 12) return opW(pos <= 4 ? 13 : 14 + (pos - 1) / 4, (pos - 1) % 4);
  if (F >= 144 + ST0 && F < 144 + ST0 + 32 && F % 4 == 2) return {O_FIN, 0, (F - 144 - ST0) / 4, bar1(FLG0)};
  if (F >= 288 + ST0 && F < 288 + ST0 + 16 && F % 2 == 0) return {O_FIN, 1, (F - 288 - ST0) / 2, bar1(FLG1)};
  if (RG >= 9 && RG <= 12 && (pos == 9 || pos == 15)) return {O_TW, 2 * (RG - 9) + (pos == 15), 0, NEVER};
  if (RG == 9 && pos >= 6 && pos % 2 == 0 && pos < 14) return opW(17, (pos - 6) / 2);
  if (RG >= 10 && RG <= 12 && pos % 4 == 2) return opW(RG + 8, pos / 4);
  if (RG == 13 && pos <= 4) return opW(21, pos - 1);
  if (RG >= 18 && RG <= 19 && k8 >= 0) return opW(22 + 2 * (RG - 18) + k8 / 4, k8 % 4);
  if (RG == 20 && k8 >= 0) return k8 < 4 ? opW(26, k8) : Op{O_D2, 0, k8 - 4, bar1(27)};
  if (RG >= 21 && RG <= 22 && pos <= 4) return {O_D2, RG - 20, pos - 1, 440 + (RG - 20) * 8 - PD};
  if (RG == PZR && pos % 2 == 1 && pos < 12) return {O_PZ, pos / 2, 0, bar1(FLG0)};
  if (RG == 22 && pos == 7) return {O_POLL, 0, 0, bar1(25)};
  if (RG >= 25 && RG <= 26) {
    const int k = (RG - 25) * 15 + pos - 1;
    if (k < 24 && k % 2 == 0) { const int U = k / 8; return {O_B2, U, (k / 2) % 4, U == 0 ? bar1(27) : 440 + U * 8 - PD}; }
  }
  return NOP_;
}
constexpr int barA(int n) { return n == 0 ? -1 : n <= 9 ? 16 * n - PD : 144 + 14 * (n - 9) - PD; }
constexpr int rgA(int F) { return F + PD < 144 ? (F + PD) / 16 : 9 + (F + PD - 144) / 14; }
constexpr int posA(int F) { return F + PD < 144 ? (F + PD) % 16 : (F + PD - 144) % 14; }
struct AttSeq {
  Op op[270] = {};
  constexpr AttSeq() {
    constexpr int seq[27][2] = {{O_X, 2}, {O_W, 2}, {O_HX, 1}, {O_X, 3}, {O_W, 3}, {O_HX, 2}, {O_W, 4}, {O_X, 4}, {O_HX, 3}, {O_HX, 4}, {O_W, 5}, {O_X, 5}, {O_W, 6}, {O_X, 6},
                                {O_W, 7}, {O_X, 7}, {O_W, 8}, {O_X, 8}, {O_W, 9}, {O_W, 10}, {O_W, 11}, {O_W, 12}, {O_W, 13}, {O_W, 14}, {O_W, 15}, {O_W, 16}, {O_W, 17}};
    int F = 6;
    for (int e = 0; e < 27; ++e) {
      const int k = seq[e][0], a = seq[e][1], n = k == O_X ? 8 : k == O_W ? 4 : 1, rg = k == O_X ? a - 2 : k == O_W ? a - 3 : 0;
      for (int i = 0; i < n; ++i) {
        while (posA(F) == 0 || rgA(F) < rg || (F >= 130 && F < 148)) ++F;
        op[F++] = k == O_X ? opX(a, i) : k == O_W ? Op{O_W, a, i, barA(a)} : Op{O_HX, a, 0, barA(a < 3 ? 2 * a : 5)};
      }
    }
    int z = 0;
    for (int G = 150; G < 270; ++G) {
      const int RG = rgA(G), pos = posA(G);
      if (RG >= 10 && RG <= 14 && (pos == 7 || pos == 10) && z < 9) op[G] = {O_PZ, z++, 0, barA(16)};
      if (RG == 16 && pos >= 2 && pos < 8) op[G] = {O_D2, 0, pos - 2, NEVER};
    }
  }
};
constexpr AttSeq ATT_SEQ{};
constexpr Op att1_op(int F, int R) {
  if (F < 0) {
    const int i = F + 16;
    if (R != 2 || i < 0 || i >= 14) return NOP_;
    return i < 8 ? opX(0, i) : i < 12 ? Op{O_W, 0, i - 8, -1} : i == 12 ? Op{O_HX, 0, 0, -1} : Op{O_TL, 0, 0, NEVER};
  }
  if (F < 4) return opX(1, 2 * F + R - 2);
  if (F < 6) return {O_W, 1, 2 * (F - 4) + R - 2, barA(1)};
  return R == 2 && F < 270 ? ATT_SEQ.op[F] : NOP_;
}
constexpr Op att2_op(int F, int R) {
  if (F < 0) {
    const int i = F + 50;
    if (R != 2 || i < 0 || i >= 43) return NOP_;
    constexpr int n1 = 12 - PD, n2 = 24 - PD, ns = FLGA * 12 - PD;
    if (i < 6) return {O_D2, 0, i, -1};
    if (i == 6) return {O_POLL, 0, 0, -28};
    if (i < 13) return {O_D2, 1, i - 7, n1};
    if (i < 16) return {O_ST, 0, 0, ns};
    if (i < 22) return {O_D2, 2, i - 16, n2};
    if (i < 25) return {O_ST, 0, 0, ns};
    if (i < 29) return {O_B2, 0, i - 25, -1};
    if (i < 33) return {O_B2, 1, i - 29, n1};
    if (i < 36) return {O_ST, 0, 0, ns};
    if (i < 40) return {O_B2, 2, i - 36, n2};
    return {O_ST, 0, 0, ns};
  }
  return p2_op(F, R, 12, ATT_AD, 0);
}
template <int WHICH> constexpr Op op_at(int F, int R) { return WHICH == 0 ? mlp_op(F, R) : WHICH == 1 ? att1_op(F, R) : att2_op(F, R); }
template <int WHICH> constexpr int vm_at(int tau) {
  int s = 0;
  for (int F = tau - 1; F >= -64; --F)
    for (int R = 3; R >= 2; --R) {
      const Op o = op_at<WHICH>(F, R);
      if (o.kind == O_NONE) continue;
      if (o.need <= tau || s >= 63) return s;
      ++s;
    }
  return s;
}
template <int WHICH, int TAU> constexpr int VM = vm_at<WHICH>(TAU);

DI void load_epoch(Ctx& c) {
  asm volatile("s_load_dwordx2 %0, %1, %2" : "=s"(c.E) : "s"(c.ws), "s"(WS_E + c.T * 128) : "memory");
}
DI void flag_store(const Ctx& c, unsigned off, unsigned val) {
  const u64 v = c.E + val;
  __builtin_amdgcn_raw_buffer_store_b64(u32x2{(unsigned)v, (unsigned)(v >> 32)}, c.rws, 0, off, 16);
}
DI void poll_issue(const Ctx& c, u32x2& f, unsigned voff, unsigned off) {
  asm volatile("buffer_load_dwordx2 %0, %1, %2, %3 offen sc1" : "=v"(f) : "v"(voff), "s"(c.rws), "s"(off) : "memory");
}
DI bool flag_ok(const Ctx& c, u32x2 f, unsigned need) {
  const u64 d = (((u64)f[1] << 32) | f[0]) - c.E - need;
  return __builtin_amdgcn_ballot_w64(d >= 8) == 0;
}
DI void poll_check(const Ctx& c, u32x2& f, unsigned voff, unsigned off, unsigned need) {
  asm volatile("" : "+v"(f));
  if (__builtin_expect(!flag_ok(c, f, need), 0)) {
    for (unsigned spin = 0; spin < (1u << 16); ++spin) {
      __builtin_amdgcn_s_sleep(1);
      asm volatile("buffer_load_dwordx2 %0, %1, %2, %3 offen sc1\n\ts_waitcnt vmcnt(0)" : "=&v"(f) : "v"(voff), "s"(c.rws), "s"(off) : "memory");
      if (flag_ok(c, f, need)) break;
    }
  }
}

constexpr unsigned POISON = 0xffffffffu;
DI unsigned inbox(const Ctx& c) { return WS_PART + ((c.T * 8 + c.m) * 64 + c.w) * 3072; }
DI unsigned flag2(const Ctx& c) { return WS_F2 + ((c.T * 8 + c.m) * 4 + c.w) * 128; }
template <int NI, int J> DI void poison(const Ctx& c, u32x4& pz, unsigned vl16) {
  if constexpr (J == 0) { pz = u32x4{POISON, POISON, POISON, POISON}; PIN(pz); }
  asm volatile("buffer_store_dwordx4 %0, %1, %2, %3 offen offset:%4 sc1" ::"v"(pz), "v"(vl16), "s"(c.rws), "s"(inbox(c) + (c.a * 4 + ((J / NI + 1) ^ c.a)) * 12288), "i"((J % NI) * 1024) : "memory");
}
template <int NT> struct P2 {
  static constexpr int AD = NT == 8 ? MLP_AD : ATT_AD, NS = NT == 8 ? 8 : 4, FB = NT == 8 ? 440 : 0, ROT = NT == 8 ? 4 : 0;
  static constexpr unsigned SS = NT == 8 ? S2 : S3;
  static constexpr int sidx(int u) { return NT == 8 ? (u + ROT) % NS : u % 4 == 0 ? 2 : u % 4 == 3 ? 3 : u % 4 - 1; }
  static constexpr unsigned slot(int u) { return sidx(u) * SS; }
  static constexpr int WN = NT == 8 ? 3 : 2;
  static constexpr unsigned win(int u) { return (sidx(u) / WN * WN) * SS; }
  unsigned v128[2], v64, vh[2], vf;
  unsigned lw, lw64, sd, so, sh[2];
  unsigned f1, cur, curh;
  bf16x8 bf[21][4];
  u32x2 pf;
  f32x4 omg;
  DI void init(const Ctx& c) {
    const unsigned l = lane_id(), colb = NT == 8 ? 256 * (c.m >> 1) : 768, kh = c.w >> 1;
    const unsigned lo = 16 * ((l & 15) ^ ((l >> 3) & 2) ^ (4 * (c.a ^ 3)));
    v128[0] = (l >> 4) * 4608 + lo; v128[1] = (l >> 4) * 4608 + (lo ^ 64);
    v64 = (l >> 3) * 2304 + 16 * ((l & 7) ^ (((l >> 1) & 4) | ((l >> 3) & 2)) ^ (2 * (c.a ^ 3))) + 256;
    vh[0] = l * 16 + kh * 2048; vh[1] = l * 16 + (1 - kh) * 2048;
    vf = 8 * (l & 7);
    lw = c.w * 4608; lw64 = c.w * 2304 + S2;
    sd = (1152 * c.a + 16 * c.w) * 1152 + colb;
    so = (144 * c.a + 72 * kh + 16 * (c.w & 1)) * 1152 + colb;
    sh[0] = WS_H + (((4 * c.T + 2 * (c.m & 1) + (c.w & 1)) * 4 + c.a) * 42 + kh) * 4096; sh[1] = sh[0] + kh * 8192;
    f1 = WS_F1 + (c.T * 4 + c.a) * 128;
  }
  template <int S> DI unsigned pvo() const { return S < 3 ? (vf < 48 ? vf : 0) : S == 3 ? 48 + (vf & 8) : vf & 24; }
  template <int S> DI void poll(const Ctx& c) { poll_issue(c, pf, pvo<S>(), S < 4 ? f1 : flag2(c)); }
  template <int S> DI void check(const Ctx& c) { poll_check(c, pf, pvo<S>(), S < 4 ? f1 : flag2(c), S < 3 ? S + 1 : S == 3 ? 3 : 1); }
  template <int KIND, int U, int I> DI void prep(const Ctx& c) {
    if constexpr (KIND == O_D2) {
      constexpr int K = uk(U, NT);
      constexpr unsigned row = K < 18 ? 64 * K : 32 * (K - 18);
      if constexpr (I == 0) cur = dprep<slot(U), row * 1152>(lw, K < 18 ? sd : so);
      if constexpr (I == 4) dprep_l<slot(U)>(lw64);
    }
    if constexpr (KIND == O_B2 && U < RS && (I == 0 || (NT == 8 && U == AD && I == 1))) { curh = sh[uk(U, NT) >= 18] + (uk(U, NT) < 18 ? 2 * uk(U, NT) : 18 + uk(U, NT)) * 4096; asm volatile("" : "+s"(curh)); }
    if constexpr (KIND == O_B2 && U >= RS && (I & 1) == 0) { curh = sh[0] - (c.w >> 1) * 4096 + (2 * uk(U, NT) + (I >> 1)) * 4096; asm volatile("" : "+s"(curh)); }
  }
  template <int KIND, int U, int I> DI void fire(const Ctx& c) {
    if constexpr (KIND == O_D2) dfire<(I & 3) * 1152>(I < 4 ? v128[I >> 1] : v64, uk(U, NT) < 18 ? c.rwd : c.rwo, cur);
    if constexpr (KIND == O_B2) ldh<(I & 1) * 1024>(bf[U][I], vh[U < RS ? I >> 1 : 0], c.rws, curh);
    if constexpr (KIND == O_POLL) poll<U>(c);
    if constexpr (KIND == O_GATE) ldg(omg, vh[0] & 255, c.rws, WS_G + (c.a * 32 + 4 * c.T + 2 * (c.m & 1) + (c.w & 1)) * 256);
  }
  static constexpr int tp(int t) { return NT == 8 || t < 6 ? t : t < 9 ? t + 2 : t < 11 ? t - 3 : 11; }
  static DI unsigned rbase(const Ctx& c, unsigned l, int t) {
    const unsigned g = l >> 4, q = (l >> 2) & 3;
    return (c.w >> 1) * 9216 + q * 1152 + g * 256 + 32 * (t ^ ((q & 2) | (g & 1))) + 8 * (l & 3);
  }
};

template <int NT, class Extra> DI void phase2(Ctx& c, P2<NT>& p, bf16x8 (&f)[16], Extra&& extra) {
  using P = P2<NT>;
  constexpr int AD = P::AD, WH = NT == 8 ? 0 : 2;
  const unsigned l = lane_id(), g = l >> 4, q = (l >> 2) & 3, kh = c.w >> 1;
  unsigned rb[4], rb64[4];
#pragma unroll
  for (int t = 0; t < 4; ++t) {
    rb[t] = P::win(0) + P::rbase(c, l, t); PIN(rb[t]);
    if (NT != 8) { rb64[t] = P::win(0) + S2 + kh * 4608 + (q & 1) * 1152 + (2 * g + (q >> 1)) * 128 + 32 * (t ^ ((q & 2) | (g & 1))) + 8 * (l & 3); PIN(rb64[t]); }
  }
  f32x4 acc[NT][4], oth[NT][2];
  bf16x8 f2[NT];
  s16x4 flo;
  auto xoff = [](int t) { return NT == 8 ? 20480 + t * 8192 : t < 6 ? t * 8192 : t < 9 ? (t - 6) * 8192 : 28672 + (t - 9) * 8192; };
  unsigned wa[2] = {0, 0}, ra[2] = {0, 0}, l16 = 0;
  const unsigned pb = inbox(c);
  auto rd = [&](auto U_, auto T_, auto H_) {
    constexpr int t = P::tp(CI(T_)), H = CI(H_);
    constexpr unsigned o = P::slot(CI(U_)) - P::win(CI(U_));
    return t < 8 ? trd(rb[t & 3] + o + 128 * (t >> 2) + H * 4608) : trd(rb64[t & 3] + o + H * 2304);
  };
  if constexpr (NT != 8) {
    wait_bar<VM<2, -1>>();
    tieA(p.bf[0][0], p.bf[0][1], p.bf[0][2], p.bf[0][3]);
    static_for<PD>([&](auto T_) { f[CI(T_)] = cat(rd(IC(0), T_, IC(0)), rd(IC(0), T_, IC(1))); });
  }
  auto send = [&](auto T_, auto R_) {
    constexpr int t = CI(T_), R = CI(R_), it = P::tp(t), dq = it < 8 ? (it >> 1) ^ 3 : (it - 8) ^ 3, idx = it < 8 ? it & 1 : 2;
    if constexpr (R < 2) { acc[t][R] += oth[t][R]; PIN(acc[t][R]); }
    if constexpr (R == 2 && dq != 0) {
      const u32x4 v = {cvtpk(acc[t][0][0], acc[t][0][1]), cvtpk(acc[t][0][2], acc[t][0][3]), cvtpk(acc[t][1][0], acc[t][1][1]), cvtpk(acc[t][1][2], acc[t][1][3])};
      st16(__builtin_elementwise_min(v, u32x4{POISON - 1, POISON - 1, POISON - 1, POISON - 1}), c.rws, l16 + idx * 1024, pb + ((dq ^ c.a) * 4 + c.a) * 12288);
    }
  };
  static_for<21>([&](auto U_) {
    static_for<NT>([&](auto T_) {
      constexpr int U = CI(U_), t = CI(T_), N = U * NT + t, UN = (N + PD) / NT, tn = (N + PD) % NT;
      constexpr int RR = tn == 0 ? 2 : 0;
      const bf16x8 a = f[t], a2 = f2[t];
      static_for<4>([&](auto R_) {
        constexpr int R = CI(R_);
        constexpr int rs = (t & 1) ? 3 - R : R, sk = (R == 1 || R == 2);
        if constexpr (U < RS) mf<true, U == 0>(acc[t][rs], a, p.bf[U][rs]); else mf<true, false>(acc[t][R >> 1], sk ? a2 : a, p.bf[U][2 * sk + (R >> 1)]);
        SBAR();
        if constexpr (tn == 0 && UN <= 20) {
          if constexpr (R < 2 && (P::win(UN) != P::win(UN - 1) || UN == RS)) {
            const unsigned d = P::win(UN) - P::win(UN - 1);
#pragma unroll
            for (int i = 2 * R; i < 2 * R + 2; ++i) {
              rb[i] += d - (UN == RS ? kh * 9216 : 0); PIN(rb[i]);
              if (NT != 8) { rb64[i] += d - (UN == RS ? kh * 4608 : 0); PIN(rb64[i]); }
            }
          }
          if constexpr (R == 2) {
            if constexpr (NT == 8 && UN % 5 == 1) STUB<13 + UN / 5>();
            if constexpr (NT == 8 && UN == 20) STUB<17>();
            if constexpr (NT != 8 && UN % 3 == 2) STUB<46 + UN / 3>();
            wait_bar<(UN == (NT == 8 ? FLG2 : FLGA) ? 0 : VM<WH, P::FB + N>)>();
            tieA(p.bf[UN][0], p.bf[UN][1], p.bf[UN][2], p.bf[UN][3]);
          }
        }
        if constexpr (UN < RS || (UN <= 20 && tn != 0)) {
          if constexpr (R == RR) flo = rd(IC(UN), IC(tn), IC(0));
          if constexpr (R == RR + 1) f[tn] = cat(flo, rd(IC(UN), IC(tn), IC(1)));
          if constexpr (UN >= RS && R == 2) flo = rd(IC(UN), IC(tn), IC(2));
          if constexpr (UN >= RS && R == 3) f2[tn] = cat(flo, rd(IC(UN), IC(tn), IC(3)));
        } else if constexpr (UN <= 20) {
          if constexpr (R == 2) f[0] = cat(rd(IC(UN), IC(0), IC(0)), rd(IC(UN), IC(0), IC(1)));
          if constexpr (R == 3) f2[0] = cat(rd(IC(UN), IC(0), IC(2)), rd(IC(UN), IC(0), IC(3)));
        }
        SBAR();
        if constexpr (tn == 0 && R == 3 && UN <= 20) {
          if constexpr (UN == (NT == 8 ? FLG2 : FLGA)) { if (threadIdx.x == 0) flag_store(c, p.f1 + 8 * c.m, 3); }
          if constexpr (dep(UN + AD, NT) >= 0) p.template check<dep(UN + AD, NT)>(c);
          if constexpr (UN == 17) p.template check<4>(c);
          if constexpr (UN == 18) {
            PIN(p.omg);
            l16 = lane_id() * 16; PIN(l16);
            wa[0] = l16 + c.w * 2048 + (NT == 8 ? 0 : 4 * S3); wa[1] = l16 + c.w * 2048 + (NT == 8 ? 32768 : 0); PIN(wa[0]); PIN(wa[1]);
          }
          if constexpr (UN == 20) { const unsigned d = kh ? 0u - 4096 : 4096; ra[0] = wa[0] + d; ra[1] = wa[1] + d; PIN(ra[0]); PIN(ra[1]); }
        }
        extra(IC(N), R_);
        {
          constexpr int gi = N - (RS - 1) * NT - 2, wi = gi < 0 ? -1 : gi < 5 ? (R < 2 ? 2 * gi + R : -1) : (R == 0 ? gi + 5 : -1);
          if constexpr (wi >= 0 && wi < 2 * NT) {
            constexpr int tt = wi / 2, hi = NT == 8 ? tt >= 4 : tt >= 6;
            ldsw<f32x4>(wa[hi] + xoff(tt) - (NT == 8 && hi ? 32768 : 0) + (wi & 1) * 1024, acc[tt][2 + (wi & 1)]);
          }
          constexpr int rt_ = N - (20 * NT - 2);
          if constexpr (rt_ >= 0 && rt_ < NT && R < 2) {
            constexpr int hi = NT == 8 ? rt_ >= 4 : rt_ >= 6;
            oth[rt_][R] = ldsr<f32x4>(ra[hi] + xoff(rt_) - (NT == 8 && hi ? 32768 : 0) + R * 1024);
          }
          if constexpr (U == 20 && t >= 2) send(IC(t - 2), R_);
        }
        if constexpr (R >= 2) {
          constexpr Op o = op_at<WH>(P::FB + N, R), n = op_at<WH>(P::FB + N + 1, 2);
          if constexpr (R == 3 || N == 0) p.template prep<o.kind, o.a, o.b>(c);
          p.template fire<o.kind, o.a, o.b>(c);
          if constexpr (R == 3) p.template prep<n.kind, n.a, n.b>(c);
        }
        SBAR();
      });
    });
  });
  STUB<(NT == 8 ? 18 : 53)>();
  const unsigned l2 = l16 >> 4;
  constexpr int NI = NT / 4;
  u32x4 in[3][NI];
  for (unsigned spin = 0; spin < (1u << 16); ++spin) {
    unsigned mx = 0;
#pragma unroll
    for (int s = 1; s < 4; ++s)
#pragma unroll
      for (int i = 0; i < NI; ++i) in[s - 1][i] = __builtin_amdgcn_raw_buffer_load_b128(c.rws, l16 + i * 1024, pb + (c.a * 4 + (s ^ c.a)) * 12288, 16);
    asm volatile("" ::: "memory");
#pragma unroll
    for (int s = 0; s < 3; ++s)
#pragma unroll
      for (int i = 0; i < NI; ++i) mx = max(max(mx, max(in[s][i][0], in[s][i][1])), max(in[s][i][2], in[s][i][3]));
    if (__builtin_amdgcn_ballot_w64(mx == POISON) == 0) break;
  }
  const unsigned row0 = 256 * c.T + 128 * (c.m & 1) + 64 * (c.w & 1) + 32 * kh;
  const unsigned vo = (l2 & 15) * 2304 + 8 * (l2 >> 4), colb = NT == 8 ? 256 * (c.m >> 1) : 768;
#pragma unroll
  for (int i = 0; i < NI; ++i) {
    const int t = NT - NI + i;
    f32x4 s0 = acc[t][0], s1 = acc[t][1];
    if (t >= NT - 2) { s0 += oth[t][0]; s1 += oth[t][1]; }
#pragma unroll
    for (int s = 0; s < 3; ++s) {
      s0 += f32x4{bflo(in[s][i][0]), bfhi(in[s][i][0]), bflo(in[s][i][1]), bfhi(in[s][i][1])};
      s1 += f32x4{bflo(in[s][i][2]), bfhi(in[s][i][2]), bflo(in[s][i][3]), bfhi(in[s][i][3])};
    }
    s0 *= kh ? p.omg[2] : p.omg[0]; s1 *= kh ? p.omg[3] : p.omg[1];
    const unsigned cb = colb + (i < 2 ? 64 * c.a + 32 * i : 256 + 32 * c.a);
    __builtin_amdgcn_raw_buffer_store_b64(u32x2{cvtpk(s0[0], s0[1]), cvtpk(s0[2], s0[3])}, c.rout, vo, row0 * 1152 + cb, 0);
    __builtin_amdgcn_raw_buffer_store_b64(u32x2{cvtpk(s1[0], s1[1]), cvtpk(s1[2], s1[3])}, c.rout, vo, (row0 + 1) * 1152 + cb, 0);
  }
  if (NT == 8 && c.a == 0 && c.m == 0 && c.w == 0 && l2 == 0) flag_store(c, WS_E + c.T * 128, 16);
  if constexpr (NT == 8) asm volatile(".Lgm_code1:" ::: "memory"); else asm volatile(".Lgm_code2:" ::: "memory");
}

struct XS {
  unsigned v[2], lw, sx, rd[2], cur, sg;
  DI void init(const Ctx& c, unsigned base) {
    const unsigned l = lane_id(), j = l & 15, g = l >> 4;
    v[0] = (l >> 3) * 2304 + 16 * ((l & 7) ^ g); v[1] = (l >> 3) * 2304 + 16 * ((l & 7) ^ (4 + g));
    lw = base + c.w * 8192; sx = (256 * c.T + 64 * c.w) * 1152;
    rd[0] = lw + j * 128 + 16 * (g ^ (j >> 1)); rd[1] = rd[0] ^ 64;
    PIN(rd[0]); PIN(rd[1]);
  }
  template <int NSL, int C, int I> DI void prep(const Ctx& c) {
    if constexpr (I == 0) sg = sx + c.kg[C] * 128;
    cur = dprep<(C % NSL) * 32768 + I * 1024, (32 * (I >> 2) + 16 * (I & 1) + ((I >> 1) & 1)) * 1152>(lw, sg);
  }
  template <int I> DI void fire(const Ctx& c) { dfire<0>(v[I & 1], c.rx, cur); }
};
template <unsigned LA> DI void touch_pages(const Ctx& c) {
  const unsigned l = lane_id(), own = c.m < 6 ? 2 * c.m : 18 + 3 * c.m;
  const unsigned o = l < 4 ? WS_H + (((4 * c.T + l) * 4 + c.a) * 42 + own) * 4096 : l < 8 ? WS_PART + ((c.T * 8 + c.m) * 64 + (l - 4) * 20) * 3072 : l < 9 ? WS_G + 32768 : WS_F1 + 4096;
  touch<LA>(c.w < 2 ? o : (256 * c.T + 64 * (l & 3)) * 1152, c.w < 2 ? c.rws : c.rout, 0);
}
template <int I> DI void touch_w2(const Ctx& c, unsigned v128) {
  const unsigned idx = (c.T & 3) * 6 + c.m;
  const unsigned so = I < 7 ? (1152 * c.a + 48 * idx) * 1152 + c.w * 2048 + I * 8192 : (144 * c.a + 7 * idx) * 1152 + c.w * 2048;
  asm volatile("s_mov_b32 m0, %3\n\ts_mov_b64 exec, 0xffff\n\tbuffer_load_dword %0, %1, %2 offen sc1 lds\n\ts_mov_b64 exec, -1"
               ::"v"(v128), "s"(I < 7 ? c.rwd : c.rwo), "s"(so), "i"(9 * S1) : "memory");
}

constexpr int slot1(int n) { return n < 9 ? n % 4 : n % 9 < 5 ? 4 + n % 9 : n % 9 % 4; }
constexpr int sig_idx(int F) {
  if ((F + PD) % 16 == 0) return -1;
  int e = 0;
  for (int G = F - F % 72; G < F; ++G) if ((G + PD) % 16 != 0) ++e;
  return e;
}
constexpr int w_ks(int n, int I) { return n < 9 ? 2 * n + (I & 1) : ((n % 9) * 4 + I) % 18; }
constexpr int w_up(int n, int I) { return n < 9 ? I >> 1 : (n % 9) * 4 + I >= 18; }
DI void role_mlp(Ctx& c, XS& xs) {
  const unsigned l = lane_id(), g = l >> 4, q = (l & 15) >> 2, pp = l & 3, r8 = l >> 3;
  const unsigned vW = (8 * (r8 >> 2) + (r8 & 3)) * 9216 + 16 * ((l & 7) ^ (((r8 >> 1) & 3) << 1));
  const unsigned lw = c.w * 1024, sW = (16 * (c.w & 1) + 4 * (c.w >> 1)) * 9216 + (1152 * c.a + 64 * c.m) * 2;
  unsigned sWk[9];
#pragma unroll
  for (int i = 0; i < 9; ++i) sWk[i] = sW + c.kg[i] * (64 * 9216);
  const unsigned sH = WS_H + (((4 * c.T + c.w) * 4 + c.a) * 42 + 2 * c.m) * 4096;
  const unsigned f1 = WS_F1 + (c.T * 4 + c.a) * 128;
  bf16x8 xf[18][4];
  f32x4 ag0[4][4], ag[4][4], au[4][4];
  const float nl2e = -1.4426950408889634f;
  unsigned sp[4][4][2];
  P2<8> p2;
  u32x4 hq[8], pz;
  unsigned vl16 = l * 16;
  unsigned cur = 0;
  auto prep = [&](auto F_, auto R_) {
    constexpr Op o = mlp_op(CI(F_), CI(R_));
    if constexpr (o.kind == O_W) {
      constexpr int pn = o.a / 9, k = w_ks(o.a, o.b);
      cur = dprep<slot1(o.a) * S1 + o.b * 4096, (k & 1) * 32 * 9216 + pn * 768>(lw, sWk[k >> 1]);
    } else if constexpr (o.kind == O_X) xs.template prep<3, o.a, o.b>(c);
    else p2.template prep<o.kind, o.a, o.b>(c);
  };
  auto fire = [&](auto F_, auto R_) {
    constexpr Op o = mlp_op(CI(F_), CI(R_));
    if constexpr (o.kind == O_W) dfire<0>(vW, w_up(o.a, o.b) ? c.rwu : c.rwg, cur);
    else if constexpr (o.kind == O_X) {
      if constexpr (o.a >= 3 && o.b == 0) asm volatile("s_waitcnt lgkmcnt(4)" ::: "memory");
      xs.template fire<o.b>(c);
    } else if constexpr (o.kind == O_FIN) {
      __builtin_amdgcn_raw_buffer_store_b128(hq[o.b], c.rws, vl16 + (o.b & 3) * 1024, sH + (12 * o.a + (o.b >> 2)) * 4096, 16);
      asm volatile("" ::: "memory");
    } else if constexpr (o.kind == O_PZ) poison<2, o.a>(c, pz, vl16);
    else if constexpr (o.kind == O_TW) touch_w2<o.a>(c, vl16 * 8);
    else if constexpr (o.kind == O_TL) touch_pages<3 * S1>(c);
    else p2.template fire<o.kind, o.a, o.b>(c);
  };
  static_for<5>([&](auto I_) { prep(IC(CI(I_) - 7), IC(2)); fire(IC(CI(I_) - 7), IC(2)); });
  visit_all<0, 19>(c.pc0, c.w);
  load_epoch(c);
  unsigned rdx2[2] = {xs.rd[0] + 65536, xs.rd[1] + 65536}; PIN(rdx2[0]); PIN(rdx2[1]);
  auto rdx = [&](auto C_, auto I_) {
    constexpr int C = CI(C_), s = CI(I_) >> 2, rt = CI(I_) & 3;
    xf[2 * C + s][rt] = ldsr<bf16x8>((C % 3 == 2 ? rdx2[s] : xs.rd[s] + (C % 3) * 32768) + rt * 2048);
  };
  unsigned rb[4];
#pragma unroll
  for (int t = 0; t < 4; ++t) rb[t] = (4 * g + q) * 128 + 32 * (t ^ ((2 * g + (q >> 1)) & 3)) + 8 * pp;
  float e0 = 0.f, e1 = 0.f, h0 = 0.f, h1 = 0.f;
  unsigned e2 = 0;
  bf16x8 f[16];
  s16x4 flo;
  unsigned rb2[3];
  wait_bar<VM<0, -1>>();
  asm volatile("" : "+s"(c.E));
  rdx(IC(0), IC(0)); rdx(IC(0), IC(4));
  static_for<PD>([&](auto I_) { constexpr unsigned o = (CI(I_) & 1) * 4096; f[CI(I_)] = cat(trd(rb[CI(I_) >> 1] + o), trd(rb[CI(I_) >> 1] + o + 2048)); });
  static_for<6>([&](auto I_) { rdx(IC(0), IC(1 + CI(I_) / 2 + 4 * (CI(I_) & 1))); });
  SBAR();
  static_for<27>([&](auto N_) {
    static_for<16>([&](auto I_) {
      constexpr int N = CI(N_), I = CI(I_), F = N * 16 + I, FP = F % 144, ps = w_up(N, 2 * (I >> 3)), k0 = w_ks(N, 2 * (I >> 3)), t = (I >> 1) & 3;
      constexpr int RG = (F + PD) / 16, pos = (F + PD) % 16, RR = pos == 0 ? 2 : 0;
      constexpr unsigned ro = (slot1(RG) % 4) * S1 + (2 * (pos >> 3) + (pos & 1)) * 4096;
      const bf16x8 a[2] = {f[I & ~1], f[I | 1]};
      static_for<4>([&](auto R_) {
        constexpr int M = CI(R_), R = M == 1 ? 2 : M == 2 ? 1 : M;
        constexpr int rt0 = 2 * (I & 1) + (M >> 1), sk = (M == 1 || M == 2), ks = k0 + sk;
        mf<(ks < 16), (k0 == 0 && (M & 1) == 0)>(ps == 1 ? au[t][rt0] : F < 144 ? ag0[t][rt0] : ag[t][rt0], a[sk], xf[ks][rt0]);
        SBAR();
        if constexpr (R == 0 && N < 8 && I == XR) wait_vm<VM<0, F>>();
        if constexpr (pos == 0) {
          if constexpr (R < 2 && RG < 27 && slot1(RG) / 4 != slot1(RG - 1) / 4) {
#pragma unroll
            for (int i = 2 * R; i < 2 * R + 2; ++i) { rb[i] += (unsigned)(slot1(RG) / 4 - slot1(RG - 1) / 4) * 4 * S1; PIN(rb[i]); }
          }
          if constexpr (R == 0 && RG == 27) {
            const unsigned l4 = lane_id();
#pragma unroll
            for (int i = 0; i < 3; ++i) { rb2[i] = P2<8>::slot(0) + P2<8>::rbase(c, l4, i); PIN(rb2[i]); }
          }
          if constexpr (R == 2) {
            if constexpr (RG % 2 == 0) STUB<RG / 2 - 1>();
            static_assert((RG != FLG0 && RG != FLG1) || VM<0, F> == 0, "flags follow a full drain");
            wait_bar<VM<0, F>>();
            if constexpr (RG == 27) tieA(p2.bf[0][0], p2.bf[0][1], p2.bf[0][2], p2.bf[0][3]);
          }
        }
        if constexpr (R == RR) { if constexpr (RG < 27) flo = trd(rb[(pos >> 1) & 3] + ro); else flo = trd(rb2[pos]); }
        if constexpr (R == RR + 1) { if constexpr (RG < 27) f[pos] = cat(flo, trd(rb[(pos >> 1) & 3] + ro + 2048)); else f[pos] = cat(flo, trd(rb2[pos] + 4608)); }
        if constexpr (N < 8 && (I == XR || I == XR + 1)) rdx(IC(N + 1), IC(4 * (I - XR) + R));
        SBAR();
        if constexpr (pos == 0 && R == 3) {
          if constexpr (RG == FLG0 || RG == FLG1) { if (threadIdx.x == 0) flag_store(c, f1 + 8 * c.m, RG == FLG0 ? 1 : 2); }
          if constexpr (RG == PZR + 1) keep(pz);
          if constexpr (RG == FLG0) { if (vl16 == 0) flag_store(c, flag2(c) + 8 * c.a, 1); }
          if constexpr (RG == 19) p2.init(c);
          if constexpr (RG == 25) p2.template check<0>(c);
        }
        if constexpr (R >= 2) { if constexpr (R == 3 || F == 0) prep(IC(F), IC(R)); fire(IC(F), IC(R)); }
        if constexpr (F >= 144 && ps == 1 && sig_idx(F) >= 0 && sig_idx(F) <= 65) {
          constexpr int e = sig_idx(F), d = e - 2;
          if constexpr (R == 0 && d >= 0) e2 = w_add1(e1);
          if constexpr (R == 1 && e < 64) e0 = w_mul(ag[e / 16][(e / 4) % 4][e % 4]);
          if constexpr (R == 2 && e >= 1 && e <= 64) e1 = w_exp(e0);
          if constexpr (R == 3 && d >= 0) { if constexpr (d % 2 == 0) sp[d / 16][(d / 4) % 4][(d % 4) / 2] = w_rcplo(e2); else w_rcphi(sp[d / 16][(d / 4) % 4][(d % 4) / 2], e2); }
        }
        if constexpr (F >= 134 && F < 142) {
          constexpr int tt = (F - 134) / 2, rt = 2 * (F & 1) + (R >> 1), k = R & 1;
          sp[tt][rt][k] = w_pklo(ag0[tt][rt][2 * k], nl2e); w_pkhi(sp[tt][rt][k], ag0[tt][rt][2 * k + 1], nl2e);
        }
        if constexpr (F >= 144 && F < 216 && sig_idx(F) >= 0) {
          constexpr int e = sig_idx(F), d = e - 2, pa = (e - 2) / 2, pf = (e - 4) / 2;
          if constexpr (R == 0 && e % 2 == 0 && e >= 2 && pa < 32) w_add1h(sp[pa / 8][(pa / 2) % 4][pa % 2]);
          if constexpr (R == 0 && e % 2 == 1 && e >= 5) h1 = w_mixhi(au[pf / 8][(pf / 2) % 4][2 * (pf % 2) + 1], sp[pf / 8][(pf / 2) % 4][pf % 2]);
          if constexpr (R == 1 && e % 2 == 0 && e >= 4) h0 = w_mixlo(au[pf / 8][(pf / 2) % 4][2 * (pf % 2)], sp[pf / 8][(pf / 2) % 4][pf % 2]);
          if constexpr (R == 1 && e % 2 == 1 && e >= 5) hq[(pf / 16) * 4 + (pf / 2) % 4][2 * ((pf / 8) & 1) + pf % 2] = w_cvt(h0, h1);
          if constexpr (R == 2 && e < 64) w_exph<e & 1>(sp[e / 16][(e / 4) % 4][(e % 4) / 2]);
          if constexpr (R == 3 && d >= 0 && d < 64) w_rcph<d & 1>(sp[d / 16][(d / 4) % 4][(d % 4) / 2]);
        }
        if constexpr (F >= 288 && FP >= FIN0 && FP < FIN0 + 32) {
          constexpr int J = (FP - FIN0) / 4, i = FP % 4, e = J >> 2, rt = J & 3, tt = 2 * e + (i >> 1), k = 2 * (i & 1);
          if constexpr (R == 0) h0 = w_mixlo(au[tt][rt][k], sp[tt][rt][i & 1]);
          if constexpr (R == 1) h1 = w_mixhi(au[tt][rt][k + 1], sp[tt][rt][i & 1]);
          if constexpr (R == 3) hq[J][i] = w_cvt(h0, h1);
        }
        if constexpr (R == 3 && mlp_op(F, 2).kind == O_FIN) keep(hq[mlp_op(F, 2).b]);
        if constexpr (R == 3 && F < 431) prep(IC(F + 1), IC(2));
        SBAR();
      });
    });
  });
  phase2<8>(c, p2, f, [&](auto N_, auto R_) {
    constexpr int N = CI(N_), R = CI(R_);
    if constexpr (N < 16 && R < 3) {
      constexpr int J = N / 2, e = J >> 2, rt = J & 3, tt = 2 * e + (N & 1);
      if constexpr (R == 0) { h0 = w_mixlo(au[tt][rt][0], sp[tt][rt][0]); h1 = w_mixhi(au[tt][rt][1], sp[tt][rt][0]); }
      if constexpr (R == 1) { e0 = w_mixlo(au[tt][rt][2], sp[tt][rt][1]); e1 = w_mixhi(au[tt][rt][3], sp[tt][rt][1]); }
      if constexpr (R == 2) { hq[J][2 * (N & 1)] = w_cvt(h0, h1); hq[J][2 * (N & 1) + 1] = w_cvt(e0, e1); }
    }
    if constexpr (R == 3 && N < 20 && lastfin(N) >= 0) {
      constexpr int J = lastfin(N);
      __builtin_amdgcn_raw_buffer_store_b128(hq[J], c.rws, vl16 + (J & 3) * 1024, sH + (24 + (J >> 2)) * 4096, 16);
      asm volatile("" ::: "memory");
    }
    if constexpr (R == 1 && N >= 1 && N < 21 && lastfin(N - 1) >= 0) keep(hq[lastfin(N - 1)]);
  });
}

DI u32x2 pk4(const f32x4& v) { return u32x2{cvtpk(v[0], v[1]), cvtpk(v[2], v[3])}; }
DI void sw16(float& a, float& b) { auto r = __builtin_amdgcn_permlane16_swap(__float_as_uint(a), __float_as_uint(b), false, false); a = __uint_as_float(r[0]); b = __uint_as_float(r[1]); }
DI void sw32(float& a, float& b) { auto r = __builtin_amdgcn_permlane32_swap(__float_as_uint(a), __float_as_uint(b), false, false); a = __uint_as_float(r[0]); b = __uint_as_float(r[1]); }
template <bool MX> DI float xred(float (&v)[4]) {
  sw16(v[0], v[1]); sw16(v[2], v[3]);
  float a = MX ? max2(v[0], v[1]) : v[0] + v[1], b = MX ? max2(v[2], v[3]) : v[2] + v[3];
  sw32(a, b);
  return MX ? max2(a, b) : a + b;
}
DI void xbc(float t, float (&v)[4]) {
  float a = t, b = t;
  sw32(a, b);
  v[0] = a; v[1] = a; v[2] = b; v[3] = b;
  sw16(v[0], v[1]); sw16(v[2], v[3]);
}

DI void role_att(Ctx& c, XS& xs) {
  const unsigned mp = c.m & 1;
  u32x2 vp[4][5], qp[4][5];
  u32x4 kt[6][4];
  f32x4 gate, bprev, bown, bprev0;
  unsigned mpv[2];
  P2<12> p2;
  {
    const unsigned l = lane_id(), g = l >> 4, q = (l & 15) >> 2, pp = l & 3;
    const unsigned lc = (l & 15) ^ (g << 1), hb = 144 * (2 * c.a + mp);
    const unsigned vA = g * 3456 + hb + (lc < 10 ? 1152 + 16 * (lc < 9 ? lc : 8) : 2304 + 16 * (lc - 10));
    const unsigned vB = g * 3456 + hb + (lc < 4 ? 2304 + 16 * (lc < 3 ? 6 + lc : 8) : 16 * (lc - 4));
    const u64 maskB = __builtin_amdgcn_ballot_w64(lc < 13);
    const unsigned lw = c.w * 1152;
    unsigned sWk[9];
#pragma unroll
    for (int i = 0; i < 9; ++i) sWk[i] = (8 * c.w + 64 * c.kg[i]) * 3456;
    const int hr = 256 * (int)c.T + ((c.w & 1) ? -16 : -15) + 2 * (int)(l >> 3);
    const unsigned vHx = (unsigned)(hr < 0 ? 0 : hr) * 1152 + 16 * ((l & 7) ^ (4 * (c.w & 1) + g));
    const unsigned lHx = A_HX + c.w * 1024;
    unsigned rh[2] = {A_HX + (l & 15) * 128 + 16 * (g ^ ((l & 15) >> 1)), 0}; rh[1] = rh[0] ^ 64; PIN(rh[0]); PIN(rh[1]);
    bf16x8 xf[18][4];
    unsigned cur = 0, l8 = 0;
    u32x4 pz;
    auto prep = [&](auto F_, auto R_) {
      constexpr Op o = att1_op(CI(F_), CI(R_));
      if constexpr (o.kind == O_W) cur = dprep<(o.a % 4) * S2 + o.b * 4608, (32 * (o.b >> 1) + 4 * (o.b & 1)) * 3456>(lw, sWk[o.a % 9]);
      else if constexpr (o.kind == O_X) xs.template prep<2, o.a, o.b>(c);
      else if constexpr (o.kind == O_HX) {
        if constexpr (o.a < 4) cur = dprep<o.a * 4096, 0>(lHx, (c.w < 2 ? c.kg[2 * o.a] : c.kg[2 * o.a + 1]) * 128);
        else cur = dprep<16384, 0>(lHx, c.w < 2 ? c.kg[8] * 128 : (c.w & 1) * 1024);
      } else p2.template prep<o.kind, o.a, o.b>(c);
    };
    auto fire = [&](auto F_, auto R_) {
      constexpr Op o = att1_op(CI(F_), CI(R_));
      if constexpr (o.kind == O_W) { if constexpr (o.a < 9) dfire<0>(vA, c.rqkv, cur); else dfire_m(vB, c.rqkv, cur, maskB); }
      else if constexpr (o.kind == O_X) {
        if constexpr (o.a >= 2 && o.b == 0) asm volatile("s_waitcnt lgkmcnt(4)" ::: "memory");
        xs.template fire<o.b>(c);
      } else if constexpr (o.kind == O_HX) {
        if constexpr (o.a < 4) dfire<0>(vHx, c.rx, cur); else dfire<0>(c.w < 2 ? vHx : l * 16, c.w < 2 ? c.rx : c.rgw, cur);
      } else if constexpr (o.kind == O_TL) touch_pages<A_GW + 2048>(c);
      else if constexpr (o.kind == O_PZ) poison<3, o.a>(c, pz, 2 * l8);
      else p2.template fire<o.kind, o.a, o.b>(c);
    };
    auto rdx = [&](auto C_, auto I_) {
      constexpr int C = CI(C_), s = CI(I_) >> 2, rt = CI(I_) & 3;
      xf[2 * C + s][rt] = ldsr<bf16x8>(xs.rd[s] + (C % 2) * 32768 + rt * 2048);
    };
    static_for<6>([&](auto I_) { prep(IC(CI(I_) - 8), IC(2)); fire(IC(CI(I_) - 8), IC(2)); });
    visit_all<32, 22>(c.pc0, c.w);
    load_epoch(c);
    unsigned rb[4], rx1, rx2;
#pragma unroll
    for (int t = 0; t < 4; ++t) rb[t] = g * 1152 + q * 256 + 32 * (t ^ q) + 8 * pp;
    rx1 = g * 1152 + q * 256 + 32 * (c.w ^ q) + 8 * pp;
    rx2 = g * 1152 + q * 256 + 128 + 32 * ((c.w < 2 ? c.w + 1 : 3) ^ q) + 8 * pp;
    const unsigned gsrc = A_GW + 2 * (32 * (l >> 5) + 8 * ((l >> 2) & 3) + 4 * ((l >> 4) & 1) + (l & 3));
    const unsigned gdst = (l >> 2) * 1152 + (l & 3) * 256 + 16 * (13 ^ ((l & 3) << 1));
    u16 gwv = 0;
    f32x4 acc[2][8][4], ah[4];
    u32x2 hp;
    unsigned wk = 0, wv = 0;
    bf16x8 f[16], hx[2], ha[2], hc[2];
    s16x4 flo, hlo;
    wait_bar<VM<1, -1>>();
    asm volatile("" : "+s"(c.E));
    rdx(IC(0), IC(0)); rdx(IC(0), IC(4));
    static_for<PD>([&](auto I_) { constexpr unsigned o = (CI(I_) & 1) * 9216; f[CI(I_)] = cat(trd(rb[CI(I_) >> 1] + o), trd(rb[CI(I_) >> 1] + o + 4608)); });
    static_for<6>([&](auto I_) { rdx(IC(0), IC(1 + CI(I_) / 2 + 4 * (CI(I_) & 1))); });
    SBAR();
    auto pass = [&](auto PS_) {
      constexpr int PS = CI(PS_), NT = PS == 0 ? 8 : 7, FC = 2 * NT;
      static_for<9>([&](auto N_) {
        static_for<FC>([&](auto I_) {
          constexpr int N = CI(N_), I = CI(I_), F = 144 * PS + N * FC + I, t = I >> 1;
          constexpr int RG = rgA(F), pos = posA(F);
          constexpr int tn = pos >> 1, RR = pos == 0 ? 2 : 0;
          constexpr unsigned ro = (RG % 2) * S2 + (pos & 1) * 9216 + 128 * (tn >> 2);
          constexpr unsigned rc = ((9 * PS + N) % 2) * S2 + ((I >> 1) & 1) * 9216 + (I & 1) * 4608;
          constexpr bool SW = PS == 0 ? t >= 5 : t < 2;
          const bf16x8 a[2] = {f[I & ~1], f[I | 1]};
          static_for<5>([&](auto R_) {
            constexpr int M = CI(R_), R = M == 1 ? 2 : M == 2 ? 1 : M;
            if constexpr (M < 4) {
              constexpr int rt0 = 2 * (I & 1) + (M >> 1), sk = (M == 1 || M == 2), ks = 2 * N + sk;
              if constexpr (SW) mfs<(ks < 16), (N == 0 && (M & 1) == 0)>(acc[PS][t][rt0], a[sk], xf[ks][rt0]); else mf<(ks < 16), (N == 0 && (M & 1) == 0)>(acc[PS][t][rt0], a[sk], xf[ks][rt0]);
            } else if constexpr (PS == 0) {
              if constexpr (I == 7) { mf<false, N == 0>(ah[0], ha[1], hx[1]); mf<false, false>(ah[0], ha[0], hx[0]); }
              if constexpr (I == 9) { mf<false, N == 0>(ah[1], a[0], hx[0]); mf<false, false>(ah[1], a[1], hx[1]); }
              if constexpr (I == 12) { mfs<false, N == 0>(ah[2], hc[1], hx[1]); mfs<false, false>(ah[2], hc[0], hx[0]); }
            } else if constexpr (I == 7) { mfs<false, N == 0>(ah[3], ha[1], hx[1]); mfs<false, false>(ah[3], ha[0], hx[0]); }
            SBAR();
            if constexpr (R == 0 && PS == 0 && I == XR && N < 8) wait_vm<VM<1, F>>();
            if constexpr (pos == 0 && RG < 18) {
              if constexpr (R < 2 && RG % 2 == 0) {
                constexpr unsigned d = RG % 4 == 0 ? 0u - 2 * S2 : 2 * S2;
#pragma unroll
                for (int i = 2 * R; i < 2 * R + 2; ++i) { rb[i] += d; PIN(rb[i]); }
                if (R == 0) { rx1 += d; PIN(rx1); } else if (RG < 9) { rx2 += d; PIN(rx2); }
              }
              if constexpr (R == 0 && RG == 9) { rx1 ^= 32 * (c.w & 2); PIN(rx1); }
              if constexpr (R == 2) {
                if constexpr (RG % 2 == 1) STUB<32 + RG / 2>();
                static_assert(RG != 16 || VM<1, F> == 0, "flags follow a full drain");
                wait_bar<VM<1, F>>();
              }
            }
            if constexpr (R == RR && RG < 18) flo = trd(rb[tn & 3] + ro);
            if constexpr (R == RR + 1 && RG < 18) f[pos] = cat(flo, trd(rb[tn & 3] + ro + 4608));
            if constexpr (PS == 0 && N < 8 && (I == XR || I == XR + 1) && R < 4) rdx(IC(N + 1), IC(4 * (I - XR) + R));
            if constexpr (I < 2 && R == 3) hx[I] = ldsr<bf16x8>(rh[I] + N * 2048);
            if constexpr (I >= 2 && I < 6 && R == 3) { if constexpr (I % 2 == 0) hlo = trd(rx1 + rc); else ha[(I >> 1) & 1] = cat(hlo, trd(rx1 + rc)); }
            if constexpr (PS == 0 && I >= 6 && I < 10 && R == 3) { if constexpr (I % 2 == 0) hlo = trd(rx2 + rc); else hc[(I >> 1) & 1] = cat(hlo, trd(rx2 + rc)); }
            if constexpr (RG + 3 >= 9 && RG + 3 <= 17 && R == 1) {
              if constexpr (pos == 6) gwv = ldsr<u16>(gsrc + c.kg[RG + 3 - 9] * 128);
              if constexpr (pos == 9) ldsw<u16>(gdst + ((RG + 3) % 4) * S2, gwv);
            }
            SBAR();
            if constexpr (F == 128 && R == 0) { l8 = lane_id() * 8; PIN(l8); wk = A_KX + (4 * c.w + 2) * 2560 + l8; PIN(wk); }
            if constexpr (F >= 130 && F < 146 && (R == 0 || R == 2)) {
              constexpr int tt = (F - 130) / 2, rt = 2 * (F & 1) + R / 2;
              if constexpr (tt < 5) ldsw<u32x2>(wk + rt * 2560 + tt * 512, pk4(acc[0][tt][rt])); else { vp[rt][tt - 5] = pk4(acc[0][tt][rt]); PIN(vp[rt][tt - 5]); }
            }
            if constexpr (F == 146 && R < 2) { const u32x2 h = pk4(ah[R]); const unsigned o = A_KX + (R == 0 ? c.w : 4) * 512; ldsw<u32x2>(o + l8, h); ldsw<u32x2>(o + 2560 + (l8 ^ 64), h); }
            if constexpr (F == 146 && R == 2) { hp = pk4(ah[2]); PIN(hp); }
            if constexpr (pos == 0 && R == 3) {
              if constexpr (RG == 15) { keep(pz); p2.init(c); wv = A_VX + (c.w < 3 ? (c.w + 1) * 5120 : A_GW + 2304 - A_VX) + (c.w < 3 ? l8 : 0); PIN(wv); }
              if constexpr (RG == 16) { if (l8 == 0) flag_store(c, flag2(c) + 8 * c.a, 1); }
            }
            if constexpr (RG == 15 && pos >= 4 && pos < 10 && R == 2) { if (c.w < 3) ldsw<u32x2>(wv + (((pos - 4) / 3) * 5 + (pos - 4) % 3) * 512, vp[2 + (pos - 4) / 3][(pos - 4) % 3]); }
            if constexpr (RG == 15 && pos == 11 && R == 2) { const unsigned o = A_VX + (c.w < 2 ? c.w : 2) * 512; ldsw<u32x2>(o + l8, hp); ldsw<u32x2>(o + 2560 + (l8 ^ 256), hp); }
            if constexpr (RG == 16 && pos >= 9 && pos < 13 && R == 2) {
              constexpr int r = pos - 9;
              const int key = (int)(l8 >> 7) * 4 + r, jj = (int)(l8 >> 3) & 15;
              bprev[r] = key >= jj + 9 ? 0.f : -1e30f;
              bown[r] = (key <= jj && key >= jj - 7) ? 0.f : -1e30f;
              bprev0[r] = (c.T == 0 && c.w == 0) ? -1e30f : bprev[r];
              if constexpr (r % 2 == 0) mpv[r / 2] = key >= jj + 9 ? 0xffffu : 0u; else { mpv[r / 2] |= key >= jj + 9 ? 0xffff0000u : 0u; PIN(mpv[r / 2]); }
              if constexpr (r == 3) { PIN(bprev); PIN(bown); PIN(bprev0); }
            }
            if constexpr (F == 256 && R == 0) { wk -= 5120; PIN(wk); }
            if constexpr (F >= 257 && F < 265 && (R == 0 || R == 1 || R == 3)) {
              constexpr int n = 3 * (F - 257) + (R == 3 ? 2 : R), i = n / 4, h = n % 4;
              const u32x2 k0 = ldsr<u32x2>(wk + i * 2560 + h * 512), k1 = ldsr<u32x2>(wk + i * 2560 + h * 512 + 512);
              kt[i][h] = u32x4{k0[0], k0[1], k1[0], k1[1]};
              asm volatile("" ::: "memory");
            }
            if constexpr (F >= 258 && R == 2) {
              constexpr int tt = (F - 258) / 2;
#pragma unroll
              for (int rt = 2 * (F & 1); rt < 2 * (F & 1) + 2; ++rt) {
                if constexpr (tt < 2) { vp[rt][3 + tt] = pk4(acc[1][tt][rt]); PIN(vp[rt][3 + tt]); }
                else { qp[rt][tt - 2] = pk4(acc[1][tt][rt]); PIN(qp[rt][tt - 2]); }
              }
              if constexpr (tt < 2 && (F & 1)) { if (c.w < 3) { ldsw<u32x2>(wv + (3 + tt) * 512, vp[2][3 + tt]); ldsw<u32x2>(wv + (8 + tt) * 512, vp[3][3 + tt]); } }
            }
            if constexpr (F == 267 && R == 0) { const u32x2 h = pk4(ah[3]); const unsigned o = A_VX + (3 + (c.w & 1)) * 512; ldsw<u32x2>(o + l8, h); ldsw<u32x2>(o + 2560 + (l8 ^ 256), h); }
            if constexpr (R == 2 || R == 3) { if constexpr (R == 3 || F == 0) prep(IC(F), IC(R)); fire(IC(F), IC(R)); }
            if constexpr (R == 4) prep(IC(F + 1), IC(2));
            SBAR();
          });
        });
      });
    };
    pass(IC(0));
    pass(IC(1));
    STUB<41>();
    asm volatile("s_nop 6\n\ts_waitcnt lgkmcnt(0)\n\ts_barrier" ::: "memory");
#pragma unroll
    for (int rt = 0; rt < 4; ++rt) {
      qp[rt][4] = pk4(acc[1][6][rt]);
      gate[rt] = __builtin_amdgcn_exp2f(fminf(1.4426950408889634f * acc[1][6][rt][0], 80.0f));
    }
  }
  auto pre = [&](auto A_, auto N_) { static_for<CI(N_)>([&](auto I_) { constexpr Op o = att2_op(CI(A_) + CI(I_) - 50, 2); p2.template prep<o.kind, o.a, o.b>(c); p2.template fire<o.kind, o.a, o.b>(c); }); };
  const unsigned l = lane_id(), g = (l >> 4) & 3, j = l & 15;
  {
    u32x6 vt[5][2];
    const unsigned ra = A_VX + c.w * 5120 + l * 8;
#pragma unroll
    for (int pa = 0; pa < 2; ++pa)
#pragma unroll
      for (int t = 0; t < 5; ++t) {
        const u32x2 v2 = ldsr<u32x2>(ra + (pa * 5 + t) * 512);
        vt[t][pa] = u32x6{v2[0], v2[1], vp[pa][t][0], vp[pa][t][1], vp[2 + pa][t][0], vp[2 + pa][t][1]};
      }
    const f32x4 omg = {__builtin_amdgcn_rcpf(1.0f + gate[0]), __builtin_amdgcn_rcpf(1.0f + gate[1]), __builtin_amdgcn_rcpf(1.0f + gate[2]), __builtin_amdgcn_rcpf(1.0f + gate[3])};
    if (mp == 0 && g == 2) st16(__builtin_bit_cast(u32x4, omg), c.rws, j * 16, WS_G + (c.a * 32 + 4 * c.T + c.w) * 256);
#pragma unroll
    for (int rt = 0; rt < 4; ++rt) gate[rt] = __shfl(gate[rt], 32 + j);
    constexpr float SC2 = 0.23570226039551584f * 1.4426950408889634f;
    const unsigned sH = WS_H + (((4 * c.T + c.w) * 4 + c.a) * 42 + 36 + 3 * mp) * 4096;
    u32x4 qf[4], P[4];
    f32x4 sp[4], so[4], hi[4], lo[4];
    auto mkq = [&](auto RT_) {
      constexpr int rt = CI(RT_);
#pragma unroll
      for (int h = 0; h < 4; ++h) {
#pragma unroll
        for (int i = 0; i < 4; ++i) { const int col = 16 * (h + (i >> 1)) + 4 * (int)g + 2 * (i & 1); qf[h][i] = (col >= 18 * h && col < 18 * h + 18) ? qp[rt][h + (i >> 1)][i & 1] : 0u; }
        PIN(qf[h]);
      }
    };
    auto qk = [&](auto RT_, auto H_, auto W_) {
      constexpr int rt = CI(RT_), h = CI(H_);
      if constexpr (CI(W_) == 0) sp[h] = mfc(kt[rt][h], qf[h], rt < 2 ? bprev0 : bprev);
      else so[h] = mfc(kt[rt + 2][h], qf[h], bown);
    };
    auto pv = [&](auto RT_, auto T_, auto W_) {
      constexpr int rt = CI(RT_), t = CI(T_) + CI(W_), pa = rt & 1, sn = rt >> 1;
      const f32x4 r = mfz(__builtin_shufflevector(vt[t][pa], vt[t][pa], 2 * sn, 2 * sn + 1, 2 * sn + 2, 2 * sn + 3), P[CI(T_)]);
      if constexpr (CI(W_) == 0) hi[CI(T_)] = r; else lo[CI(T_)] = r;
    };
    auto outp = [&](auto RT_) {
      constexpr int rt = CI(RT_);
      u32x2 o[5];
      PIN(lo[0]);
#pragma unroll
      for (int t = 0; t < 5; ++t)
#pragma unroll
        for (int i = 0; i < 2; ++i) {
          const unsigned vh = t < 4 ? cvtpk(hi[t < 4 ? t : 0][2 * i], hi[t < 4 ? t : 0][2 * i + 1]) : 0u;
          o[t][i] = (4 * (int)g + 2 * i < 2 * t) ? cvtpk(lo[t > 0 ? t - 1 : 0][2 * i], lo[t > 0 ? t - 1 : 0][2 * i + 1]) : vh;
        }
      st16(u32x4{o[0][0], o[0][1], o[1][0], o[1][1]}, c.rws, l * 16 + rt * 1024, sH);
      st16(u32x4{o[2][0], o[2][1], o[3][0], o[3][1]}, c.rws, l * 16 + rt * 1024, sH + 4096);
      st16(u32x4{o[4][0], o[4][1], 0u, 0u}, c.rws, l * 16 + rt * 1024, sH + 8192);
    };
    mkq(IC(0));
    asm volatile("s_nop 1" ::: "memory");
    static_for<8>([&](auto I_) { qk(IC(0), IC(CI(I_) >> 1), IC(CI(I_) & 1)); });
    SBAR();
    mkq(IC(1));
    static_for<4>([&](auto RT_) {
      constexpr int rt = CI(RT_);
      float s[4][4], red[4], bc[4];
      STUB<42 + rt>();
      SBAR();
      static_for<4>([&](auto H_) {
        constexpr int h = CI(H_);
        if constexpr (rt > 0) pv(IC(rt - 1), H_, IC(0));
        s[h][0] = max2(sp[h][0], so[h][0]); s[h][1] = max2(sp[h][1], so[h][1]); s[h][2] = max2(sp[h][2], so[h][2]);
        SBAR();
        if constexpr (rt > 0) pv(IC(rt - 1), H_, IC(1));
        s[h][3] = max2(sp[h][3], so[h][3]);
        red[h] = max2(max3(s[h][0], s[h][1], s[h][2]), s[h][3]);
        SBAR();
      });
      xbc(-SC2 * xred<true>(red), bc);
      if constexpr (rt == 0) pre(IC(6), IC(7));
      if constexpr (rt == 2) { wait_vm<VM<2, -28>>(); p2.template check<0>(c); }
      if constexpr (rt > 0) outp(IC(rt - 1));
      SBAR();
      static_for<4>([&](auto H_) {
        constexpr int h = CI(H_);
        if constexpr (rt < 3) qk(IC(rt + 1), H_, IC(0));
        s[h][0] = __builtin_amdgcn_exp2f(__builtin_fmaf(s[h][0], SC2, bc[h])); s[h][1] = __builtin_amdgcn_exp2f(__builtin_fmaf(s[h][1], SC2, bc[h]));
        SBAR();
        if constexpr (rt < 3) qk(IC(rt + 1), H_, IC(1));
        s[h][2] = __builtin_amdgcn_exp2f(__builtin_fmaf(s[h][2], SC2, bc[h])); s[h][3] = __builtin_amdgcn_exp2f(__builtin_fmaf(s[h][3], SC2, bc[h]));
        SBAR();
      });
#pragma unroll
      for (int h = 0; h < 4; ++h) red[h] = (s[h][0] + s[h][1]) + (s[h][2] + s[h][3]);
      xbc(gate[rt] * __builtin_amdgcn_rcpf(xred<false>(red)), bc);
      if constexpr (rt == 1) pre(IC(16), IC(6));
      if constexpr (rt == 2) pre(IC(25), IC(8));
      if constexpr (rt == 3) pre(IC(36), IC(4));
      if constexpr (rt == 0) {
#pragma unroll
        for (int i = 0; i < 10; ++i) PIN(vt[i >> 1][i & 1]);
      }
      if constexpr (rt < 2) mkq(IC(rt + 2));
#pragma unroll
      for (int h = 0; h < 4; ++h) {
        const unsigned p0 = cvtpk(s[h][0] * bc[h], s[h][1] * bc[h]), p1 = cvtpk(s[h][2] * bc[h], s[h][3] * bc[h]);
        P[h] = u32x4{p0 & mpv[0], p1 & mpv[1], p0 & ~mpv[0], p1 & ~mpv[1]};
        PIN(P[h]);
      }
      asm volatile("s_nop 1" ::: "memory");
    });
    static_for<8>([&](auto I_) { pv(IC(3), IC(CI(I_) >> 1), IC(CI(I_) & 1)); });
    asm volatile("s_nop 7" ::: "memory");
    SBAR();
    outp(IC(3));
  }
  bf16x8 f[16];
  phase2<12>(c, p2, f, [](auto, auto) {});
}

#endif
__global__ __launch_bounds__(256, 1) void fused_kernel(const u16* x, const u16* wqkv, const u16* wo, const u16* wg, const u16* wu, const u16* wd,
                                                        const u16* gw, char* ws, u16* out) {
#if defined(__HIP_DEVICE_COMPILE__)
  __shared__ __attribute__((aligned(16))) char smem[LDS_BYTES];
  asm volatile("" ::"v"((unsigned)(size_t)smem));
  Ctx c;
  const unsigned bid = blockIdx.x, xcd = bid & 7, idx = bid >> 3;
  c.w = __builtin_amdgcn_readfirstlane(threadIdx.x >> 6);
  {
    u64 pc; unsigned len;
    asm volatile("s_getpc_b64 %0\n.Lgm_code0:\n\ts_mov_b32 %1, .Lgm_code1-.Lgm_code0\n\ts_max_u32 %1, %1, .Lgm_code2-.Lgm_code0" : "=s"(pc), "=s"(len) :: "scc");
    c.pc0 = pc;
    asm volatile("s_branch .Lgm_go\n.Lgm_ret:\n\ts_setpc_b64 s[100:101]\n.Lgm_go:");
    const rsrc_t rc = __builtin_amdgcn_make_buffer_rsrc((void*)pc, (short)0, len, 0x00020000);
    if (idx < 16) touch<3 * S1>((threadIdx.x & 15) * 128, rc, idx * 8192 + c.w * 2048);
  }
  asm volatile("" ::"s"(x), "s"(out));
  c.rx = __builtin_amdgcn_make_buffer_rsrc((void*)x, (short)0, SEQ * DM * 2, 0x00020000);
  c.rqkv = __builtin_amdgcn_make_buffer_rsrc((void*)wqkv, (short)0, DM * 3 * DM * 2, 0x00020000);
  c.rwo = __builtin_amdgcn_make_buffer_rsrc((void*)wo, (short)0, DM * DM * 2, 0x00020000);
  c.rwg = __builtin_amdgcn_make_buffer_rsrc((void*)wg, (short)0, DM * FF * 2, 0x00020000);
  c.rwu = __builtin_amdgcn_make_buffer_rsrc((void*)wu, (short)0, DM * FF * 2, 0x00020000);
  c.rwd = __builtin_amdgcn_make_buffer_rsrc((void*)wd, (short)0, DM * FF * 2, 0x00020000);
  c.rgw = __builtin_amdgcn_make_buffer_rsrc((void*)gw, (short)0, DM * 2, 0x00020000);
  c.rws = __builtin_amdgcn_make_buffer_rsrc((void*)ws, (short)0, WS_BYTES, 0x00020000);
  c.rout = __builtin_amdgcn_make_buffer_rsrc((void*)out, (short)0, SEQ * DM * 2, 0x00020000);
  c.ws = (u64)ws;
  c.a = xcd & 3; c.T = 4 * (xcd >> 2) + (idx >> 3);
  c.m = (idx & 3) == 3 ? 6 + ((idx >> 2) & 1) : 3 * ((idx >> 2) & 1) + (idx & 3);
  const unsigned rot = c.m + 2 * (c.T & 3);
  c.kg[0] = rot >= 9 ? rot - 9 : rot;
  XS xs; xs.init(c, c.m < 6 ? M_X : A_X);
  static_for<8>([&](auto I_) { xs.template prep<1, 0, CI(I_)>(c); xs.template fire<CI(I_)>(c); });
  SBAR();
#pragma unroll
  for (int i = 1; i < 9; ++i) c.kg[i] = c.kg[i - 1] == 8 ? 0 : c.kg[i - 1] + 1;
  if (__builtin_expect(c.m < 6, 1)) role_mlp(c, xs); else role_att(c, xs);
#endif
}

extern "C" void kernel_launch(void* const* d_in, const int* in_sizes, int n_in, void* d_out, int out_size, void* d_ws, size_t ws_size,
                              hipStream_t stream) {
  fused_kernel<<<256, 256, 0, stream>>>((const u16*)d_in[0], (const u16*)d_in[1], (const u16*)d_in[2], (const u16*)d_in[4], (const u16*)d_in[5],
                                        (const u16*)d_in[6], (const u16*)d_in[7], (char*)d_ws, (u16*)d_out);
}
```

```cpp
#include <hip/hip_runtime.h>
#include <utility>

typedef unsigned short u16;
typedef unsigned long long u64;
typedef short s16x4 __attribute__((ext_vector_type(4)));
typedef __bf16 bf16x8 __attribute__((ext_vector_type(8)));
typedef __bf16 bf16x2 __attribute__((ext_vector_type(2)));
typedef float f32x2 __attribute__((ext_vector_type(2)));
typedef float f32x4 __attribute__((ext_vector_type(4)));
typedef unsigned u32x2 __attribute__((ext_vector_type(2)));
typedef unsigned u32x4 __attribute__((ext_vector_type(4)));
typedef unsigned u32x6 __attribute__((ext_vector_type(6)));
typedef __amdgpu_buffer_rsrc_t rsrc_t;
#define LAS __attribute__((address_space(3)))
#define DI __device__ __forceinline__
#define SBAR() __builtin_amdgcn_sched_barrier(0)
#define IC(x) std::integral_constant<int, (x)>{}
#define CI(x) decltype(x)::value

constexpr int SEQ = 2048, DM = 576, FF = 4608;
constexpr unsigned WS_H = 0;
constexpr unsigned WS_PART = 24u << 20;
constexpr unsigned WS_G = 40u << 20;
constexpr unsigned WS_F1 = 41u << 20;
constexpr unsigned WS_F2 = WS_F1 + 8192;
constexpr unsigned WS_E = WS_F2 + 65536;
constexpr unsigned WS_BYTES = 42u << 20;
constexpr unsigned LDS_BYTES = 163840;
constexpr unsigned S1 = 16384;
constexpr unsigned S2 = 18432;
constexpr unsigned S3 = S2 + 9216;
constexpr unsigned M_X = 65536;
constexpr unsigned A_X = 4 * S2;
constexpr unsigned A_HX = A_X + 65536;
constexpr unsigned A_GW = A_HX + 18432;
constexpr unsigned A_KX = 83968;
constexpr unsigned A_VX = A_KX + 18 * 2560;
static_assert(A_GW + 2048 <= LDS_BYTES && A_VX <= A_HX && A_VX + 20480 <= LDS_BYTES && 3 * S3 <= A_KX, "lds");

#if defined(__HIP_DEVICE_COMPILE__)
template <int... Is> using iseq = std::integer_sequence<int, Is...>;
template <class Fn, int... Is> DI void static_for_(Fn&& f, iseq<Is...>) { (f(std::integral_constant<int, Is>{}), ...); }
template <int N, class Fn> DI void static_for(Fn&& f) { static_for_(f, std::make_integer_sequence<int, N>{}); }

template <int N> DI void wait_bar() { asm volatile("s_waitcnt vmcnt(%0) lgkmcnt(0)\n\ts_barrier" ::"i"(N) : "memory"); }
template <int N> DI void wait_vm() { asm volatile("s_waitcnt vmcnt(%0)" ::"i"(N) : "memory"); }
template <class T> DI void PIN(T& x) { asm volatile("" : "+v"(x)); }
DI void tieA(bf16x8& a, bf16x8& b, bf16x8& c, bf16x8& d) { asm volatile("" : "+a"(a), "+a"(b), "+a"(c), "+a"(d)); }
template <unsigned LO, unsigned SO> DI unsigned dprep(unsigned lb, unsigned sb) {
  unsigned t;
  asm volatile("s_add_u32 m0, %1, %3\n\ts_add_u32 %0, %2, %4" : "=s"(t) : "s"(lb), "s"(sb), "i"(LO), "i"(SO) : "scc");
  return t;
}
template <unsigned LO> DI void dprep_l(unsigned lb) { asm volatile("s_add_u32 m0, %0, %1\n\ts_nop 0" ::"s"(lb), "i"(LO) : "scc"); }
template <unsigned IMM> DI void dfire(unsigned voff, rsrc_t srd, unsigned so) {
  asm volatile("buffer_load_dwordx4 %0, %1, %2 offen offset:%3 lds" ::"v"(voff), "s"(srd), "s"(so), "i"(IMM) : "memory");
}
DI void dfire_m(unsigned voff, rsrc_t srd, unsigned so, u64 mask) {
  asm volatile("s_mov_b64 exec, %3\n\tbuffer_load_dwordx4 %0, %1, %2 offen lds\n\ts_mov_b64 exec, -1" ::"v"(voff), "s"(srd), "s"(so), "s"(mask) : "memory");
}
template <unsigned IMM> DI void ldh(bf16x8& d, unsigned voff, rsrc_t srd, unsigned so) {
  asm volatile("buffer_load_dwordx4 %0, %1, %2, %3 offen offset:%4 sc1" : "=a"(d) : "v"(voff), "s"(srd), "s"(so), "i"(IMM) : "memory");
}
DI void ldg(f32x4& d, unsigned voff, rsrc_t srd, unsigned so) { asm volatile("buffer_load_dwordx4 %0, %1, %2, %3 offen sc1" : "=v"(d) : "v"(voff), "s"(srd), "s"(so) : "memory"); }
template <unsigned LA> DI void touch(unsigned voff, rsrc_t srd, unsigned so) {
  asm volatile("s_mov_b32 m0, %3\n\ts_nop 0\n\tbuffer_load_dword %0, %1, %2 offen sc1 lds" ::"v"(voff), "s"(srd), "s"(so), "i"(LA) : "memory");
}
DI void st16(const u32x4& v, rsrc_t srd, unsigned voff, unsigned so) {
  __builtin_amdgcn_raw_buffer_store_b128(v, srd, voff, so, 16);
  asm volatile("s_nop 1" ::"v"(v) : "memory");
}
DI void keep(const u32x4& v) { asm volatile("" ::"v"(v)); }
template <bool AG, bool ZERO> DI void mf(f32x4& c, const bf16x8& a, const bf16x8& b) {
  if constexpr (ZERO) {
    if constexpr (AG) asm volatile("v_mfma_f32_16x16x32_bf16 %0, %1, %2, 0" : "=&v"(c) : "v"(a), "a"(b));
    else asm volatile("v_mfma_f32_16x16x32_bf16 %0, %1, %2, 0" : "=&v"(c) : "v"(a), "v"(b));
  } else {
    if constexpr (AG) asm volatile("v_mfma_f32_16x16x32_bf16 %0, %1, %2, %0" : "+v"(c) : "v"(a), "a"(b));
    else asm volatile("v_mfma_f32_16x16x32_bf16 %0, %1, %2, %0" : "+v"(c) : "v"(a), "v"(b));
  }
}
template <bool AG, bool ZERO> DI void mfs(f32x4& c, const bf16x8& a, const bf16x8& b) {
  if constexpr (ZERO) {
    if constexpr (AG) asm volatile("v_mfma_f32_16x16x32_bf16 %0, %2, %1, 0" : "=&v"(c) : "v"(a), "a"(b));
    else asm volatile("v_mfma_f32_16x16x32_bf16 %0, %2, %1, 0" : "=&v"(c) : "v"(a), "v"(b));
  } else {
    if constexpr (AG) asm volatile("v_mfma_f32_16x16x32_bf16 %0, %2, %1, %0" : "+v"(c) : "v"(a), "a"(b));
    else asm volatile("v_mfma_f32_16x16x32_bf16 %0, %2, %1, %0" : "+v"(c) : "v"(a), "v"(b));
  }
}
DI f32x4 mfc(const u32x4& a, const u32x4& b, const f32x4& c) { f32x4 d; asm volatile("v_mfma_f32_16x16x32_bf16 %0, %1, %2, %3" : "=&v"(d) : "a"(a), "v"(b), "v"(c)); return d; }
DI f32x4 mfz(const u32x4& a, const u32x4& b) { f32x4 d; asm volatile("v_mfma_f32_16x16x32_bf16 %0, %1, %2, 0" : "=&v"(d) : "v"(a), "v"(b)); return d; }
DI float max2(float a, float b) { float r; asm("v_max_f32 %0, %1, %2" : "=v"(r) : "v"(a), "v"(b)); return r; }
DI float max3(float a, float b, float c) { float r; asm("v_max3_f32 %0, %1, %2, %3" : "=v"(r) : "v"(a), "v"(b), "v"(c)); return r; }
DI s16x4 trd(unsigned a) { return __builtin_amdgcn_ds_read_tr16_b64_v4i16((LAS s16x4*)a); }
DI bf16x8 cat(s16x4 lo, s16x4 hi) { return __builtin_bit_cast(bf16x8, __builtin_shufflevector(lo, hi, 0, 1, 2, 3, 4, 5, 6, 7)); }
DI unsigned cvtpk(float lo, float hi) { f32x2 v = {lo, hi}; return __builtin_bit_cast(unsigned, __builtin_convertvector(v, bf16x2)); }
DI float w_mul(float a) { float r; asm volatile("v_mul_f32 %0, 0xbfb8aa3b, %1" : "=v"(r) : "v"(a)); return r; }
DI float w_exp(float a) { float r; asm volatile("v_exp_f32 %0, %1" : "=v"(r) : "v"(a)); return r; }
DI unsigned w_add1(float a) { unsigned r; asm volatile("v_fma_mixlo_f16 %0, %1, 1.0, 1.0" : "=v"(r) : "v"(a)); return r; }
DI unsigned w_rcplo(unsigned a) { unsigned r; asm volatile("v_rcp_f16 %0, %1" : "=v"(r) : "v"(a)); return r; }
DI void w_rcphi(unsigned& d, unsigned a) { asm volatile("v_rcp_f16_sdwa %0, %1 dst_sel:WORD_1 dst_unused:UNUSED_PRESERVE src0_sel:WORD_0" : "+v"(d) : "v"(a)); }
DI unsigned w_pklo(float a, float k) { unsigned r; asm volatile("v_fma_mixlo_f16 %0, %1, %2, 0" : "=v"(r) : "v"(a), "s"(k)); return r; }
DI void w_pkhi(unsigned& d, float a, float k) { asm volatile("v_fma_mixhi_f16 %0, %1, %2, 0" : "+v"(d) : "v"(a), "s"(k)); }
template <int H> DI void w_exph(unsigned& d) {
  if constexpr (H == 0) asm volatile("v_exp_f16_sdwa %0, %0 dst_sel:WORD_0 dst_unused:UNUSED_PRESERVE src0_sel:WORD_0" : "+v"(d));
  else asm volatile("v_exp_f16_sdwa %0, %0 dst_sel:WORD_1 dst_unused:UNUSED_PRESERVE src0_sel:WORD_1" : "+v"(d));
}
template <int H> DI void w_rcph(unsigned& d) {
  if constexpr (H == 0) asm volatile("v_rcp_f16_sdwa %0, %0 dst_sel:WORD_0 dst_unused:UNUSED_PRESERVE src0_sel:WORD_0" : "+v"(d));
  else asm volatile("v_rcp_f16_sdwa %0, %0 dst_sel:WORD_1 dst_unused:UNUSED_PRESERVE src0_sel:WORD_1" : "+v"(d));
}
DI void w_add1h(unsigned& d) { asm volatile("v_pk_add_f16 %0, %0, 1.0 op_sel_hi:[1,0]" : "+v"(d)); }
DI float w_mixlo(float u, unsigned s) { float r; asm volatile("v_fma_mix_f32 %0, %1, %2, 0 op_sel_hi:[0,1,0]" : "=v"(r) : "v"(u), "v"(s)); return r; }
DI float w_mixhi(float u, unsigned s) { float r; asm volatile("v_fma_mix_f32 %0, %1, %2, 0 op_sel:[0,1,0] op_sel_hi:[0,1,0]" : "=v"(r) : "v"(u), "v"(s)); return r; }
DI unsigned w_cvt(float lo, float hi) { unsigned r; asm volatile("v_cvt_pk_bf16_f32 %0, %1, %2" : "=v"(r) : "v"(lo), "v"(hi)); return r; }
DI float bflo(unsigned v) { return __uint_as_float(v << 16); }
DI float bfhi(unsigned v) { return __uint_as_float(v & 0xffff0000u); }
template <class T> DI T ldsr(unsigned a) { return *(const LAS T*)a; }
template <class T> DI void ldsw(unsigned a, T v) { *(LAS T*)a = v; }
DI unsigned lane_id() { unsigned l; asm volatile("v_mbcnt_lo_u32_b32 %0, -1, 0\n\tv_mbcnt_hi_u32_b32 %0, -1, %0" : "=v"(l)); return l; }

template <int N> DI void STUB() { asm volatile(".Lgm_s%c0:\n\ts_cbranch_execz .Lgm_ret" ::"i"(N)); }
template <int N> DI void visit(u64 pc0) {
  asm volatile("s_add_u32 s98, %0, .Lgm_s%c2-.Lgm_code0\n\ts_addc_u32 s99, %1, 0\n\ts_mov_b64 exec, 0\n\ts_swappc_b64 s[100:101], s[98:99]\n\ts_mov_b64 exec, -1"
               ::"s"((unsigned)pc0), "s"((unsigned)(pc0 >> 32)), "i"(N) : "s98", "s99", "s100", "s101", "scc", "memory");
}
template <int B, int CNT> DI void visit_all(u64 pc0, unsigned w) {
  static_for<8>([&](auto W_) { if (w == CI(W_)) static_for<(CNT + 7 - CI(W_)) / 8>([&](auto I_) { visit<B + CI(W_) + 8 * CI(I_)>(pc0); }); });
}

struct Ctx {
  rsrc_t rx, rqkv, rwo, rwg, rwu, rwd, rgw, rws, rout;
  unsigned w, a, T, m;
  unsigned kg[9];
  u64 E, ws, pc0;
};

enum { O_NONE, O_W, O_X, O_HX, O_FIN, O_POLL, O_D2, O_B2, O_GATE, O_ST, O_TW, O_TL, O_PZ };
constexpr int NEVER = 1 << 20;
struct Op { int kind, a, b, need; };
constexpr int PD = 3;
constexpr int MLP_AD = 3, ATT_AD = 3;
constexpr Op NOP_ = {O_NONE, 0, 0, 0};
constexpr int uk(int U, int NT) { const int o = NT == 8 ? 12 : 6; return U < o ? U : U < o + 3 ? U - o + 18 : U - 3; }
constexpr int dep(int U, int NT) { return U == 15 ? 2 : NT == 8 ? (U == 6 ? 1 : U == 12 ? 3 : -1) : (U == 6 ? 3 : U == 9 ? 1 : -1); }
constexpr int FLG2 = 4, FLGA = 1;
constexpr int RS = 19;
constexpr Op p2_op(int N, int R, int NT, int AD, int FB) {
  const int ND = NT == 8 ? 4 : 6, NO = ND + 4, RG = (N + PD) / NT, pos = (N + PD) % NT, FL = NT == 8 ? FLG2 : FLGA;
  if (RG > 20 || pos == 0 || RG == FL - 1) return NOP_;
  if (NT != 8 && R == 3 && pos == 1 && dep(RG + AD + 1, NT) >= 0) return {O_POLL, dep(RG + AD + 1, NT), 0, FB + (RG + 1) * NT - PD};
  int k;
  const bool busy = RG == FL || RG == FL + 1;
  if (busy) { const int d = NT == 8 ? 1 : 2, nb = NT == 8 ? 5 : 4; k = pos < d ? (R == 2 ? pos - 1 : -1) : pos < d + nb ? d - 1 + 2 * (pos - d) + (R - 2) : (R == 2 ? pos - 1 + nb : -1); }
  else if (NT == 8) k = R == 2 ? (pos == 1 ? 0 : pos) : pos == 1 ? 1 : pos == 7 ? 8 : -1;
  else k = R == 2 ? pos - 1 : -1;
  if (k < 0) return NOP_;
  const int n = busy ? (FL - 1 + AD) * NO + (RG - FL) * (3 * NO / 2) + k : (RG + AD) * NO + k, U = n / NO, i = n % NO;
  if (k < (busy ? 3 * NO / 2 : NO)) return U > 20 ? NOP_ : Op{i < ND ? O_D2 : O_B2, U, i < ND ? i : i - ND, FB + U * NT - PD};
  if (k != NO || busy) return NOP_;
  if (NT == 8 && dep(RG + AD + 2, NT) >= 0) return {O_POLL, dep(RG + AD + 2, NT), 0, FB + (RG + 2) * NT - PD};
  if (RG == 14) return {O_POLL, 4, 0, FB + 17 * NT - PD};
  if (RG == 15) return {O_GATE, 0, 0, FB + 18 * NT - PD};
  return NOP_;
}
constexpr int FIN0 = 36, ST0 = 72, FLG0 = 17, FLG1 = 25;
constexpr int PZR = 8;
constexpr int lastfin(int N) { return N == 1 ? 0 : N == 3 ? 1 : N == 7 ? 2 : N == 8 ? 3 : N == 10 ? 4 : N == 11 ? 5 : N == 15 ? 6 : N == 16 ? 7 : -1; }
constexpr int bar1(int n) { return n == 0 ? -1 : 16 * n - PD; }
constexpr Op opW(int n, int i) { return {O_W, n, i, bar1(n)}; }
constexpr int XR = 11;
constexpr Op opX(int c, int i) { return {O_X, c, i, c == 0 ? -1 : 16 * (c - 1) + XR}; }
constexpr Op mlp_op(int F, int R) {
  if (F < 0) {
    const int i = F + 15;
    if (R != 2 || i < 0 || i >= 13) return NOP_;
    return i < 8 ? opX(0, i) : i < 12 ? opW(0, i - 8) : Op{O_TL, 0, 0, bar1(1)};
  }
  if (F >= 440) {
    const int N = F - 440;
    if (N < 5) return R == 2 ? Op{N < 4 ? O_D2 : O_B2, MLP_AD, N & 3, 440 + MLP_AD * 8 - PD} : N == 0 || N == 2 || N == 4 ? Op{O_B2, MLP_AD, N / 2 + 1, 440 + MLP_AD * 8 - PD} : Op{O_FIN, 2, N / 2, 440 + FLG2 * 8 - PD};
    if (R == 3 && N < 20 && lastfin(N) >= 0) return {O_FIN, 2, lastfin(N), 440 + FLG2 * 8 - PD};
    return p2_op(N, R, 8, MLP_AD, 440);
  }
  if (F >= 432) return NOP_;
  const int RG = (F + PD) / 16, pos = (F + PD) % 16, r = R - 2;
  if (pos == 0) return NOP_;
  if (F < 4) return opX(1, 2 * F + r);
  if (F < 6) return opW(1, 2 * (F - 4) + r);
  if (R == 3) return NOP_;
  if (RG <= 5) {
    const int n = F - 6 - RG, c = 2 + n / 12, i = n % 12;
    const int cap = RG + 3;
    if (RG <= 3) { if (c <= cap) return i < 8 ? opX(c, i) : opW(c, i - 8); return NOP_; }
    if (RG == 4 && pos <= 8) return pos <= 4 ? opX(6, pos + 3) : opW(6, pos - 5);
    if (RG == 4) return opX(7, pos - 9);
    if (pos == 1) return opX(7, 7);
    if (pos <= 5) return opW(7, pos - 2);
    return pos <= 13 ? opX(8, pos - 6) : opW(8, pos - 14);
  }
  if (RG == 6 && pos <= 2) return opW(8, pos + 1);
  const int k8 = pos >= 3 && pos < 15 && (pos - 3) % 3 != 2 ? (pos - 3) - (pos - 3) / 3 : -1;
  if (RG == 6 && k8 >= 0) return opW(9 + k8 / 4, k8 % 4);
  if (RG == 7 && pos <= 12) return opW(pos <= 8 ? 11 + (pos - 1) / 4 : 14, (pos - 1) % 4);
  if (RG == 8 && pos <= 12) return opW(pos <= 4 ? 13 : 14 + (pos - 1) / 4, (pos - 1) % 4);
  if ((RG == 7 || RG == 8) && pos >= 13) return {O_PZ, 3 * (RG - 7) + pos - 13, 0, bar1(FLG0)};
  if (F >= 144 + ST0 && F < 144 + ST0 + 32 && F % 4 == 2) return {O_FIN, 0, (F - 144 - ST0) / 4, bar1(FLG0)};
  if (F >= 288 + ST0 && F < 288 + ST0 + 16 && F % 2 == 0) return {O_FIN, 1, (F - 288 - ST0) / 2, bar1(FLG1)};
  if (RG >= 9 && RG <= 12 && (pos == 9 || pos == 15)) return {O_TW, 2 * (RG - 9) + (pos == 15), 0, NEVER};
  if (RG == 9 && pos >= 6 && pos % 2 == 0 && pos < 14) return opW(17, (pos - 6) / 2);
  if (RG >= 10 && RG <= 12 && pos % 4 == 2) return opW(RG + 8, pos / 4);
  if (RG == 13 && pos <= 4) return opW(21, pos - 1);
  if (RG >= 18 && RG <= 19 && k8 >= 0) return opW(22 + 2 * (RG - 18) + k8 / 4, k8 % 4);
  if (RG == 20 && k8 >= 0) return k8 < 4 ? opW(26, k8) : Op{O_D2, 0, k8 - 4, bar1(27)};
  if (RG >= 21 && RG <= 22 && pos <= 4) return {O_D2, RG - 20, pos - 1, 440 + (RG - 20) * 8 - PD};
  if (RG == 22 && pos == 7) return {O_POLL, 0, 0, bar1(25)};
  if (RG >= 25 && RG <= 26) {
    const int k = (RG - 25) * 15 + pos - 1;
    if (k < 24 && k % 2 == 0) { const int U = k / 8; return {O_B2, U, (k / 2) % 4, U == 0 ? bar1(27) : 440 + U * 8 - PD}; }
  }
  return NOP_;
}
constexpr int barA(int n) { return n == 0 ? -1 : n <= 9 ? 16 * n - PD : 144 + 14 * (n - 9) - PD; }
constexpr int rgA(int F) { return F + PD < 144 ? (F + PD) / 16 : 9 + (F + PD - 144) / 14; }
constexpr int posA(int F) { return F + PD < 144 ? (F + PD) % 16 : (F + PD - 144) % 14; }
struct AttSeq {
  Op op[270] = {};
  constexpr AttSeq() {
    constexpr int seq[27][2] = {{O_X, 2}, {O_W, 2}, {O_HX, 1}, {O_X, 3}, {O_W, 3}, {O_HX, 2}, {O_W, 4}, {O_X, 4}, {O_HX, 3}, {O_HX, 4}, {O_W, 5}, {O_X, 5}, {O_W, 6}, {O_X, 6},
                                {O_W, 7}, {O_X, 7}, {O_W, 8}, {O_X, 8}, {O_W, 9}, {O_W, 10}, {O_W, 11}, {O_W, 12}, {O_W, 13}, {O_W, 14}, {O_W, 15}, {O_W, 16}, {O_W, 17}};
    int F = 6;
    for (int e = 0; e < 27; ++e) {
      const int k = seq[e][0], a = seq[e][1], n = k == O_X ? 8 : k == O_W ? 4 : 1, rg = k == O_X ? a - 2 : k == O_W ? a - 3 : 0;
      for (int i = 0; i < n; ++i) {
        while (posA(F) == 0 || rgA(F) < rg || (F >= 130 && F < 148)) ++F;
        op[F++] = k == O_X ? opX(a, i) : k == O_W ? Op{O_W, a, i, barA(a)} : Op{O_HX, a, 0, barA(a < 3 ? 2 * a : 5)};
      }
    }
    int z = 0;
    for (int G = 150; G < 270; ++G) {
      const int RG = rgA(G), pos = posA(G);
      if (RG >= 10 && RG <= 14 && (pos == 7 || pos == 10) && z < 9) op[G] = {O_PZ, z++, 0, barA(16)};
      if (RG == 16 && pos >= 2 && pos < 8) op[G] = {O_D2, 0, pos - 2, NEVER};
    }
  }
};
constexpr AttSeq ATT_SEQ{};
constexpr Op att1_op(int F, int R) {
  if (F < 0) {
    const int i = F + 16;
    if (R != 2 || i < 0 || i >= 14) return NOP_;
    return i < 8 ? opX(0, i) : i < 12 ? Op{O_W, 0, i - 8, -1} : i == 12 ? Op{O_HX, 0, 0, -1} : Op{O_TL, 0, 0, NEVER};
  }
  if (F < 4) return opX(1, 2 * F + R - 2);
  if (F < 6) return {O_W, 1, 2 * (F - 4) + R - 2, barA(1)};
  return R == 2 && F < 270 ? ATT_SEQ.op[F] : NOP_;
}
constexpr Op att2_op(int F, int R) {
  if (F < 0) {
    const int i = F + 50;
    if (R != 2 || i < 0 || i >= 43) return NOP_;
    constexpr int n1 = 12 - PD, n2 = 24 - PD, ns = FLGA * 12 - PD;
    if (i < 6) return {O_D2, 0, i, -1};
    if (i == 6) return {O_POLL, 0, 0, -28};
    if (i < 13) return {O_D2, 1, i - 7, n1};
    if (i < 16) return {O_ST, 0, 0, ns};
    if (i < 22) return {O_D2, 2, i - 16, n2};
    if (i < 25) return {O_ST, 0, 0, ns};
    if (i < 29) return {O_B2, 0, i - 25, -1};
    if (i < 33) return {O_B2, 1, i - 29, n1};
    if (i < 36) return {O_ST, 0, 0, ns};
    if (i < 40) return {O_B2, 2, i - 36, n2};
    return {O_ST, 0, 0, ns};
  }
  return p2_op(F, R, 12, ATT_AD, 0);
}
template <int WHICH> constexpr Op op_at(int F, int R) { return WHICH == 0 ? mlp_op(F, R) : WHICH == 1 ? att1_op(F, R) : att2_op(F, R); }
template <int WHICH> constexpr int vm_at(int tau) {
  int s = 0;
  for (int F = tau - 1; F >= -64; --F)
    for (int R = 3; R >= 2; --R) {
      const Op o = op_at<WHICH>(F, R);
      if (o.kind == O_NONE) continue;
      if (o.need <= tau || s >= 63) return s;
      ++s;
    }
  return s;
}
template <int WHICH, int TAU> constexpr int VM = vm_at<WHICH>(TAU);

DI void load_epoch(Ctx& c) {
  asm volatile("s_load_dwordx2 %0, %1, %2" : "=s"(c.E) : "s"(c.ws), "s"(WS_E + c.T * 128) : "memory");
}
DI void flag_store(const Ctx& c, unsigned off, unsigned val) {
  const u64 v = c.E + val;
  __builtin_amdgcn_raw_buffer_store_b64(u32x2{(unsigned)v, (unsigned)(v >> 32)}, c.rws, 0, off, 16);
}
DI void poll_issue(const Ctx& c, u32x2& f, unsigned voff, unsigned off) {
  asm volatile("buffer_load_dwordx2 %0, %1, %2, %3 offen sc1" : "=v"(f) : "v"(voff), "s"(c.rws), "s"(off) : "memory");
}
DI bool flag_ok(const Ctx& c, u32x2 f, unsigned need) {
  const u64 d = (((u64)f[1] << 32) | f[0]) - c.E - need;
  return __builtin_amdgcn_ballot_w64(d >= 8) == 0;
}
DI void poll_check(const Ctx& c, u32x2& f, unsigned voff, unsigned off, unsigned need) {
  asm volatile("" : "+v"(f));
  if (__builtin_expect(!flag_ok(c, f, need), 0)) {
    for (unsigned spin = 0; spin < (1u << 16); ++spin) {
      __builtin_amdgcn_s_sleep(1);
      asm volatile("buffer_load_dwordx2 %0, %1, %2, %3 offen sc1\n\ts_waitcnt vmcnt(0)" : "=&v"(f) : "v"(voff), "s"(c.rws), "s"(off) : "memory");
      if (flag_ok(c, f, need)) break;
    }
  }
}

constexpr unsigned POISON = 0xffffffffu;
DI unsigned inbox(const Ctx& c) { return WS_PART + ((c.T * 8 + c.m) * 64 + c.w) * 3072; }
DI unsigned flag2(const Ctx& c) { return WS_F2 + ((c.T * 8 + c.m) * 4 + c.w) * 128; }
template <int NI, int J> DI void poison(const Ctx& c, u32x4& pz, unsigned vl16) {
  if constexpr (J == 0) { pz = u32x4{POISON, POISON, POISON, POISON}; PIN(pz); }
  asm volatile("buffer_store_dwordx4 %0, %1, %2, %3 offen offset:%4 sc1" ::"v"(pz), "v"(vl16), "s"(c.rws), "s"(inbox(c) + (c.a * 4 + ((J / NI + 1) ^ c.a)) * 12288), "i"((J % NI) * 1024) : "memory");
}
template <int NT> struct P2 {
  static constexpr int AD = NT == 8 ? MLP_AD : ATT_AD, NS = NT == 8 ? 8 : 4, FB = NT == 8 ? 440 : 0, ROT = NT == 8 ? 4 : 0;
  static constexpr unsigned SS = NT == 8 ? S2 : S3;
  static constexpr int sidx(int u) { return NT == 8 ? (u + ROT) % NS : u % 4 == 0 ? 2 : u % 4 == 3 ? 3 : u % 4 - 1; }
  static constexpr unsigned slot(int u) { return sidx(u) * SS; }
  static constexpr int WN = NT == 8 ? 3 : 2;
  static constexpr unsigned win(int u) { return (sidx(u) / WN * WN) * SS; }
  unsigned v128[2], v64, vh[2], vf;
  unsigned lw, lw64, sd, so, sh[2];
  unsigned f1, cur, curh;
  bf16x8 bf[21][4];
  u32x2 pf;
  f32x4 omg;
  DI void init(const Ctx& c) {
    const unsigned l = lane_id(), colb = NT == 8 ? 256 * (c.m >> 1) : 768, kh = c.w >> 1;
    const unsigned lo = 16 * ((l & 15) ^ ((l >> 3) & 2) ^ (4 * (c.a ^ 3)));
    v128[0] = (l >> 4) * 4608 + lo; v128[1] = (l >> 4) * 4608 + (lo ^ 64);
    v64 = (l >> 3) * 2304 + 16 * ((l & 7) ^ (((l >> 1) & 4) | ((l >> 3) & 2)) ^ (2 * (c.a ^ 3))) + 256;
    vh[0] = l * 16 + kh * 2048; vh[1] = l * 16 + (1 - kh) * 2048;
    vf = 8 * (l & 7);
    lw = c.w * 4608; lw64 = c.w * 2304 + S2;
    sd = (1152 * c.a + 16 * c.w) * 1152 + colb;
    so = (144 * c.a + 72 * kh + 16 * (c.w & 1)) * 1152 + colb;
    sh[0] = WS_H + (((4 * c.T + 2 * (c.m & 1) + (c.w & 1)) * 4 + c.a) * 42 + kh) * 4096; sh[1] = sh[0] + kh * 8192;
    f1 = WS_F1 + (c.T * 4 + c.a) * 128;
  }
  template <int S> DI unsigned pvo() const { return S < 3 ? (vf < 48 ? vf : 0) : S == 3 ? 48 + (vf & 8) : vf & 24; }
  template <int S> DI void poll(const Ctx& c) { poll_issue(c, pf, pvo<S>(), S < 4 ? f1 : flag2(c)); }
  template <int S> DI void check(const Ctx& c) { poll_check(c, pf, pvo<S>(), S < 4 ? f1 : flag2(c), S < 3 ? S + 1 : S == 3 ? 3 : 1); }
  template <int KIND, int U, int I> DI void prep(const Ctx& c) {
    if constexpr (KIND == O_D2) {
      constexpr int K = uk(U, NT);
      constexpr unsigned row = K < 18 ? 64 * K : 32 * (K - 18);
      if constexpr (I == 0) cur = dprep<slot(U), row * 1152>(lw, K < 18 ? sd : so);
      if constexpr (I == 4) dprep_l<slot(U)>(lw64);
    }
    if constexpr (KIND == O_B2 && U < RS && (I == 0 || (NT == 8 && U == AD && I == 1))) { curh = sh[uk(U, NT) >= 18] + (uk(U, NT) < 18 ? 2 * uk(U, NT) : 18 + uk(U, NT)) * 4096; asm volatile("" : "+s"(curh)); }
    if constexpr (KIND == O_B2 && U >= RS && (I & 1) == 0) { curh = sh[0] - (c.w >> 1) * 4096 + (2 * uk(U, NT) + (I >> 1)) * 4096; asm volatile("" : "+s"(curh)); }
  }
  template <int KIND, int U, int I> DI void fire(const Ctx& c) {
    if constexpr (KIND == O_D2) dfire<(I & 3) * 1152>(I < 4 ? v128[I >> 1] : v64, uk(U, NT) < 18 ? c.rwd : c.rwo, cur);
    if constexpr (KIND == O_B2) ldh<(I & 1) * 1024>(bf[U][I], vh[U < RS ? I >> 1 : 0], c.rws, curh);
    if constexpr (KIND == O_POLL) poll<U>(c);
    if constexpr (KIND == O_GATE) ldg(omg, vh[0] & 255, c.rws, WS_G + (c.a * 32 + 4 * c.T + 2 * (c.m & 1) + (c.w & 1)) * 256);
  }
  static constexpr int tp(int t) { return NT == 8 || t < 6 ? t : t < 9 ? t + 2 : t < 11 ? t - 3 : 11; }
  static DI unsigned rbase(const Ctx& c, unsigned l, int t) {
    const unsigned g = l >> 4, q = (l >> 2) & 3;
    return (c.w >> 1) * 9216 + q * 1152 + g * 256 + 32 * (t ^ ((q & 2) | (g & 1))) + 8 * (l & 3);
  }
};

template <int NT, class Extra> DI void phase2(Ctx& c, P2<NT>& p, bf16x8 (&f)[16], Extra&& extra) {
  using P = P2<NT>;
  constexpr int AD = P::AD, WH = NT == 8 ? 0 : 2;
  const unsigned l = lane_id(), g = l >> 4, q = (l >> 2) & 3, kh = c.w >> 1;
  unsigned rb[4], rb64[4];
#pragma unroll
  for (int t = 0; t < 4; ++t) {
    rb[t] = P::win(0) + P::rbase(c, l, t); PIN(rb[t]);
    if (NT != 8) { rb64[t] = P::win(0) + S2 + kh * 4608 + (q & 1) * 1152 + (2 * g + (q >> 1)) * 128 + 32 * (t ^ ((q & 2) | (g & 1))) + 8 * (l & 3); PIN(rb64[t]); }
  }
  f32x4 acc[NT][4], oth[NT][2];
  bf16x8 f2[NT];
  s16x4 flo;
  auto xoff = [](int t) { return NT == 8 ? 20480 + t * 8192 : t < 6 ? t * 8192 : t < 9 ? (t - 6) * 8192 : 28672 + (t - 9) * 8192; };
  unsigned wa[2] = {0, 0}, ra[2] = {0, 0}, l16 = 0;
  const unsigned pb = inbox(c);
  auto rd = [&](auto U_, auto T_, auto H_) {
    constexpr int t = P::tp(CI(T_)), H = CI(H_);
    constexpr unsigned o = P::slot(CI(U_)) - P::win(CI(U_));
    return t < 8 ? trd(rb[t & 3] + o + 128 * (t >> 2) + H * 4608) : trd(rb64[t & 3] + o + H * 2304);
  };
  if constexpr (NT != 8) {
    wait_bar<VM<2, -1>>();
    tieA(p.bf[0][0], p.bf[0][1], p.bf[0][2], p.bf[0][3]);
    static_for<PD>([&](auto T_) { f[CI(T_)] = cat(rd(IC(0), T_, IC(0)), rd(IC(0), T_, IC(1))); });
  }
  auto send = [&](auto T_, auto R_) {
    constexpr int t = CI(T_), R = CI(R_), it = P::tp(t), dq = it < 8 ? (it >> 1) ^ 3 : (it - 8) ^ 3, idx = it < 8 ? it & 1 : 2;
    if constexpr (R < 2) { acc[t][R] += oth[t][R]; PIN(acc[t][R]); }
    if constexpr (R == 2 && dq != 0) {
      const u32x4 v = {cvtpk(acc[t][0][0], acc[t][0][1]), cvtpk(acc[t][0][2], acc[t][0][3]), cvtpk(acc[t][1][0], acc[t][1][1]), cvtpk(acc[t][1][2], acc[t][1][3])};
      st16(__builtin_elementwise_min(v, u32x4{POISON - 1, POISON - 1, POISON - 1, POISON - 1}), c.rws, l16 + idx * 1024, pb + ((dq ^ c.a) * 4 + c.a) * 12288);
    }
  };
  static_for<21>([&](auto U_) {
    static_for<NT>([&](auto T_) {
      constexpr int U = CI(U_), t = CI(T_), N = U * NT + t, UN = (N + PD) / NT, tn = (N + PD) % NT;
      constexpr int RR = tn == 0 ? 2 : 0;
      const bf16x8 a = f[t], a2 = f2[t];
      static_for<4>([&](auto R_) {
        constexpr int R = CI(R_);
        constexpr int rs = (t & 1) ? 3 - R : R, sk = (R == 1 || R == 2);
        if constexpr (U < RS) mf<true, U == 0>(acc[t][rs], a, p.bf[U][rs]); else mf<true, false>(acc[t][R >> 1], sk ? a2 : a, p.bf[U][2 * sk + (R >> 1)]);
        SBAR();
        if constexpr (tn == 0 && UN <= 20) {
          if constexpr (R < 2 && (P::win(UN) != P::win(UN - 1) || UN == RS)) {
            const unsigned d = P::win(UN) - P::win(UN - 1);
#pragma unroll
            for (int i = 2 * R; i < 2 * R + 2; ++i) {
              rb[i] += d - (UN == RS ? kh * 9216 : 0); PIN(rb[i]);
              if (NT != 8) { rb64[i] += d - (UN == RS ? kh * 4608 : 0); PIN(rb64[i]); }
            }
          }
          if constexpr (R == 2) {
            if constexpr (NT == 8 && UN % 5 == 1) STUB<13 + UN / 5>();
            if constexpr (NT == 8 && UN == 20) STUB<17>();
            if constexpr (NT != 8 && UN % 3 == 2) STUB<46 + UN / 3>();
            wait_bar<(UN == (NT == 8 ? FLG2 : FLGA) ? 0 : VM<WH, P::FB + N>)>();
            tieA(p.bf[UN][0], p.bf[UN][1], p.bf[UN][2], p.bf[UN][3]);
          }
        }
        if constexpr (UN < RS || (UN <= 20 && tn != 0)) {
          if constexpr (R == RR) flo = rd(IC(UN), IC(tn), IC(0));
          if constexpr (R == RR + 1) f[tn] = cat(flo, rd(IC(UN), IC(tn), IC(1)));
          if constexpr (UN >= RS && R == 2) flo = rd(IC(UN), IC(tn), IC(2));
          if constexpr (UN >= RS && R == 3) f2[tn] = cat(flo, rd(IC(UN), IC(tn), IC(3)));
        } else if constexpr (UN <= 20) {
          if constexpr (R == 2) f[0] = cat(rd(IC(UN), IC(0), IC(0)), rd(IC(UN), IC(0), IC(1)));
          if constexpr (R == 3) f2[0] = cat(rd(IC(UN), IC(0), IC(2)), rd(IC(UN), IC(0), IC(3)));
        }
        SBAR();
        if constexpr (tn == 0 && R == 3 && UN <= 20) {
          if constexpr (UN == (NT == 8 ? FLG2 : FLGA)) { if (threadIdx.x == 0) flag_store(c, p.f1 + 8 * c.m, 3); }
          if constexpr (dep(UN + AD, NT) >= 0) p.template check<dep(UN + AD, NT)>(c);
          if constexpr (UN == 17) p.template check<4>(c);
          if constexpr (UN == 18) {
            PIN(p.omg);
            l16 = lane_id() * 16; PIN(l16);
            wa[0] = l16 + c.w * 2048 + (NT == 8 ? 0 : 4 * S3); wa[1] = l16 + c.w * 2048 + (NT == 8 ? 32768 : 0); PIN(wa[0]); PIN(wa[1]);
          }
          if constexpr (UN == 20) { const unsigned d = kh ? 0u - 4096 : 4096; ra[0] = wa[0] + d; ra[1] = wa[1] + d; PIN(ra[0]); PIN(ra[1]); }
        }
        extra(IC(N), R_);
        {
          constexpr int gi = N - (RS - 1) * NT - 2, wi = gi < 0 ? -1 : gi < 5 ? (R < 2 ? 2 * gi + R : -1) : (R == 0 ? gi + 5 : -1);
          if constexpr (wi >= 0 && wi < 2 * NT) {
            constexpr int tt = wi / 2, hi = NT == 8 ? tt >= 4 : tt >= 6;
            ldsw<f32x4>(wa[hi] + xoff(tt) - (NT == 8 && hi ? 32768 : 0) + (wi & 1) * 1024, acc[tt][2 + (wi & 1)]);
          }
          constexpr int rt_ = N - (20 * NT - 2);
          if constexpr (rt_ >= 0 && rt_ < NT && R < 2) {
            constexpr int hi = NT == 8 ? rt_ >= 4 : rt_ >= 6;
            oth[rt_][R] = ldsr<f32x4>(ra[hi] + xoff(rt_) - (NT == 8 && hi ? 32768 : 0) + R * 1024);
          }
          if constexpr (U == 20 && t >= 2) send(IC(t - 2), R_);
        }
        if constexpr (R >= 2) {
          constexpr Op o = op_at<WH>(P::FB + N, R), n = op_at<WH>(P::FB + N + 1, 2);
          if constexpr (R == 3 || N == 0) p.template prep<o.kind, o.a, o.b>(c);
          p.template fire<o.kind, o.a, o.b>(c);
          if constexpr (R == 3) p.template prep<n.kind, n.a, n.b>(c);
        }
        SBAR();
      });
    });
  });
  STUB<(NT == 8 ? 18 : 53)>();
  const unsigned l2 = l16 >> 4;
  constexpr int NI = NT / 4;
  u32x4 in[3][NI];
  for (unsigned spin = 0; spin < (1u << 16); ++spin) {
    unsigned mx = 0;
#pragma unroll
    for (int s = 1; s < 4; ++s)
#pragma unroll
      for (int i = 0; i < NI; ++i) in[s - 1][i] = __builtin_amdgcn_raw_buffer_load_b128(c.rws, l16 + i * 1024, pb + (c.a * 4 + (s ^ c.a)) * 12288, 16);
    asm volatile("" ::: "memory");
#pragma unroll
    for (int s = 0; s < 3; ++s)
#pragma unroll
      for (int i = 0; i < NI; ++i) mx = max(max(mx, max(in[s][i][0], in[s][i][1])), max(in[s][i][2], in[s][i][3]));
    if (__builtin_amdgcn_ballot_w64(mx == POISON) == 0) break;
  }
  const unsigned row0 = 256 * c.T + 128 * (c.m & 1) + 64 * (c.w & 1) + 32 * kh;
  const unsigned vo = (l2 & 15) * 2304 + 8 * (l2 >> 4), colb = NT == 8 ? 256 * (c.m >> 1) : 768;
#pragma unroll
  for (int i = 0; i < NI; ++i) {
    const int t = NT - NI + i;
    f32x4 s0 = acc[t][0], s1 = acc[t][1];
    if (t >= NT - 2) { s0 += oth[t][0]; s1 += oth[t][1]; }
#pragma unroll
    for (int s = 0; s < 3; ++s) {
      s0 += f32x4{bflo(in[s][i][0]), bfhi(in[s][i][0]), bflo(in[s][i][1]), bfhi(in[s][i][1])};
      s1 += f32x4{bflo(in[s][i][2]), bfhi(in[s][i][2]), bflo(in[s][i][3]), bfhi(in[s][i][3])};
    }
    s0 *= kh ? p.omg[2] : p.omg[0]; s1 *= kh ? p.omg[3] : p.omg[1];
    const unsigned cb = colb + (i < 2 ? 64 * c.a + 32 * i : 256 + 32 * c.a);
    __builtin_amdgcn_raw_buffer_store_b64(u32x2{cvtpk(s0[0], s0[1]), cvtpk(s0[2], s0[3])}, c.rout, vo, row0 * 1152 + cb, 0);
    __builtin_amdgcn_raw_buffer_store_b64(u32x2{cvtpk(s1[0], s1[1]), cvtpk(s1[2], s1[3])}, c.rout, vo, (row0 + 1) * 1152 + cb, 0);
  }
  if (NT == 8 && c.a == 0 && c.m == 0 && c.w == 0 && l2 == 0) flag_store(c, WS_E + c.T * 128, 16);
  if constexpr (NT == 8) asm volatile(".Lgm_code1:" ::: "memory"); else asm volatile(".Lgm_code2:" ::: "memory");
}

struct XS {
  unsigned v[2], lw, sx, rd[2], cur, sg;
  DI void init(const Ctx& c, unsigned base) {
    const unsigned l = lane_id(), j = l & 15, g = l >> 4;
    v[0] = (l >> 3) * 2304 + 16 * ((l & 7) ^ g); v[1] = (l >> 3) * 2304 + 16 * ((l & 7) ^ (4 + g));
    lw = base + c.w * 8192; sx = (256 * c.T + 64 * c.w) * 1152;
    rd[0] = lw + j * 128 + 16 * (g ^ (j >> 1)); rd[1] = rd[0] ^ 64;
    PIN(rd[0]); PIN(rd[1]);
  }
  template <int NSL, int C, int I> DI void prep(const Ctx& c) {
    if constexpr (I == 0) sg = sx + c.kg[C] * 128;
    cur = dprep<(C % NSL) * 32768 + I * 1024, (32 * (I >> 2) + 16 * (I & 1) + ((I >> 1) & 1)) * 1152>(lw, sg);
  }
  template <int I> DI void fire(const Ctx& c) { dfire<0>(v[I & 1], c.rx, cur); }
};
template <unsigned LA> DI void touch_pages(const Ctx& c) {
  const unsigned l = lane_id(), own = c.m < 6 ? 2 * c.m : 18 + 3 * c.m;
  const unsigned o = l < 4 ? WS_H + (((4 * c.T + l) * 4 + c.a) * 42 + own) * 4096 : l < 8 ? WS_PART + ((c.T * 8 + c.m) * 64 + (l - 4) * 20) * 3072 : l < 9 ? WS_G + 32768 : WS_F1 + 4096;
  touch<LA>(c.w < 2 ? o : (256 * c.T + 64 * (l & 3)) * 1152, c.w < 2 ? c.rws : c.rout, 0);
}
template <int I> DI void touch_w2(const Ctx& c, unsigned v128) {
  const unsigned idx = (c.T & 3) * 6 + c.m;
  const unsigned so = I < 7 ? (1152 * c.a + 48 * idx) * 1152 + c.w * 2048 + I * 8192 : (144 * c.a + 7 * idx) * 1152 + c.w * 2048;
  asm volatile("s_mov_b32 m0, %3\n\ts_mov_b64 exec, 0xffff\n\tbuffer_load_dword %0, %1, %2 offen sc1 lds\n\ts_mov_b64 exec, -1"
               ::"v"(v128), "s"(I < 7 ? c.rwd : c.rwo), "s"(so), "i"(9 * S1) : "memory");
}

constexpr int slot1(int n) { return n < 9 ? n % 4 : n % 9 < 5 ? 4 + n % 9 : n % 9 % 4; }
constexpr int sig_idx(int F) {
  if ((F + PD) % 16 == 0) return -1;
  int e = 0;
  for (int G = F - F % 72; G < F; ++G) if ((G + PD) % 16 != 0) ++e;
  return e;
}
constexpr int w_ks(int n, int I) { return n < 9 ? 2 * n + (I & 1) : ((n % 9) * 4 + I) % 18; }
constexpr int w_up(int n, int I) { return n < 9 ? I >> 1 : (n % 9) * 4 + I >= 18; }
DI void role_mlp(Ctx& c, XS& xs) {
  const unsigned l = lane_id(), g = l >> 4, q = (l & 15) >> 2, pp = l & 3, r8 = l >> 3;
  const unsigned vW = (8 * (r8 >> 2) + (r8 & 3)) * 9216 + 16 * ((l & 7) ^ (((r8 >> 1) & 3) << 1));
  const unsigned lw = c.w * 1024, sW = (16 * (c.w & 1) + 4 * (c.w >> 1)) * 9216 + (1152 * c.a + 64 * c.m) * 2;
  unsigned sWk[9];
#pragma unroll
  for (int i = 0; i < 9; ++i) sWk[i] = sW + c.kg[i] * (64 * 9216);
  const unsigned sH = WS_H + (((4 * c.T + c.w) * 4 + c.a) * 42 + 2 * c.m) * 4096;
  const unsigned f1 = WS_F1 + (c.T * 4 + c.a) * 128;
  bf16x8 xf[18][4];
  f32x4 ag0[4][4], ag[4][4], au[4][4];
  const float nl2e = -1.4426950408889634f;
  unsigned sp[4][4][2];
  P2<8> p2;
  u32x4 hq[8], pz;
  unsigned vl16 = l * 16;
  unsigned cur = 0;
  auto prep = [&](auto F_, auto R_) {
    constexpr Op o = mlp_op(CI(F_), CI(R_));
    if constexpr (o.kind == O_W) {
      constexpr int pn = o.a / 9, k = w_ks(o.a, o.b);
      cur = dprep<slot1(o.a) * S1 + o.b * 4096, (k & 1) * 32 * 9216 + pn * 768>(lw, sWk[k >> 1]);
    } else if constexpr (o.kind == O_X) xs.template prep<3, o.a, o.b>(c);
    else p2.template prep<o.kind, o.a, o.b>(c);
  };
  auto fire = [&](auto F_, auto R_) {
    constexpr Op o = mlp_op(CI(F_), CI(R_));
    if constexpr (o.kind == O_W) dfire<0>(vW, w_up(o.a, o.b) ? c.rwu : c.rwg, cur);
    else if constexpr (o.kind == O_X) {
      if constexpr (o.a >= 3 && o.b == 0) asm volatile("s_waitcnt lgkmcnt(4)" ::: "memory");
      xs.template fire<o.b>(c);
    } else if constexpr (o.kind == O_FIN) {
      __builtin_amdgcn_raw_buffer_store_b128(hq[o.b], c.rws, vl16 + (o.b & 3) * 1024, sH + (12 * o.a + (o.b >> 2)) * 4096, 16);
      asm volatile("" ::: "memory");
    } else if constexpr (o.kind == O_PZ) poison<2, o.a>(c, pz, vl16);
    else if constexpr (o.kind == O_TW) touch_w2<o.a>(c, vl16 * 8);
    else if constexpr (o.kind == O_TL) touch_pages<3 * S1>(c);
    else p2.template fire<o.kind, o.a, o.b>(c);
  };
  static_for<5>([&](auto I_) { prep(IC(CI(I_) - 7), IC(2)); fire(IC(CI(I_) - 7), IC(2)); });
  visit_all<0, 19>(c.pc0, c.w + ((blockIdx.x >> 3) & 4));
  load_epoch(c);
  unsigned rdx2[2] = {xs.rd[0] + 65536, xs.rd[1] + 65536}; PIN(rdx2[0]); PIN(rdx2[1]);
  auto rdx = [&](auto C_, auto I_) {
    constexpr int C = CI(C_), s = CI(I_) >> 2, rt = CI(I_) & 3;
    xf[2 * C + s][rt] = ldsr<bf16x8>((C % 3 == 2 ? rdx2[s] : xs.rd[s] + (C % 3) * 32768) + rt * 2048);
  };
  unsigned rb[4];
#pragma unroll
  for (int t = 0; t < 4; ++t) rb[t] = (4 * g + q) * 128 + 32 * (t ^ ((2 * g + (q >> 1)) & 3)) + 8 * pp;
  float e0 = 0.f, e1 = 0.f, h0 = 0.f, h1 = 0.f;
  unsigned e2 = 0;
  bf16x8 f[16];
  s16x4 flo;
  unsigned rb2[3];
  wait_bar<VM<0, -1>>();
  asm volatile("" : "+s"(c.E));
  rdx(IC(0), IC(0)); rdx(IC(0), IC(4));
  static_for<PD>([&](auto I_) { constexpr unsigned o = (CI(I_) & 1) * 4096; f[CI(I_)] = cat(trd(rb[CI(I_) >> 1] + o), trd(rb[CI(I_) >> 1] + o + 2048)); });
  static_for<6>([&](auto I_) { rdx(IC(0), IC(1 + CI(I_) / 2 + 4 * (CI(I_) & 1))); });
  SBAR();
  static_for<27>([&](auto N_) {
    static_for<16>([&](auto I_) {
      constexpr int N = CI(N_), I = CI(I_), F = N * 16 + I, FP = F % 144, ps = w_up(N, 2 * (I >> 3)), k0 = w_ks(N, 2 * (I >> 3)), t = (I >> 1) & 3;
      constexpr int RG = (F + PD) / 16, pos = (F + PD) % 16, RR = pos == 0 ? 2 : 0;
      constexpr unsigned ro = (slot1(RG) % 4) * S1 + (2 * (pos >> 3) + (pos & 1)) * 4096;
      const bf16x8 a[2] = {f[I & ~1], f[I | 1]};
      static_for<4>([&](auto R_) {
        constexpr int M = CI(R_), R = M == 1 ? 2 : M == 2 ? 1 : M;
        constexpr int rt0 = 2 * (I & 1) + (M >> 1), sk = (M == 1 || M == 2), ks = k0 + sk;
        mf<(ks < 16), (k0 == 0 && (M & 1) == 0)>(ps == 1 ? au[t][rt0] : F < 144 ? ag0[t][rt0] : ag[t][rt0], a[sk], xf[ks][rt0]);
        SBAR();
        if constexpr (R == 0 && N < 8 && I == XR) wait_vm<VM<0, F>>();
        if constexpr (pos == 0) {
          if constexpr (R < 2 && RG < 27 && slot1(RG) / 4 != slot1(RG - 1) / 4) {
#pragma unroll
            for (int i = 2 * R; i < 2 * R + 2; ++i) { rb[i] += (unsigned)(slot1(RG) / 4 - slot1(RG - 1) / 4) * 4 * S1; PIN(rb[i]); }
          }
          if constexpr (R == 0 && RG == 27) {
            const unsigned l4 = lane_id();
#pragma unroll
            for (int i = 0; i < 3; ++i) { rb2[i] = P2<8>::slot(0) + P2<8>::rbase(c, l4, i); PIN(rb2[i]); }
          }
          if constexpr (R == 2) {
            if constexpr (RG % 2 == 0) STUB<RG / 2 - 1>();
            static_assert((RG != FLG0 && RG != FLG1) || VM<0, F> == 0, "flags follow a full drain");
            wait_bar<VM<0, F>>();
            if constexpr (RG == 27) tieA(p2.bf[0][0], p2.bf[0][1], p2.bf[0][2], p2.bf[0][3]);
          }
        }
        if constexpr (R == RR) { if constexpr (RG < 27) flo = trd(rb[(pos >> 1) & 3] + ro); else flo = trd(rb2[pos]); }
        if constexpr (R == RR + 1) { if constexpr (RG < 27) f[pos] = cat(flo, trd(rb[(pos >> 1) & 3] + ro + 2048)); else f[pos] = cat(flo, trd(rb2[pos] + 4608)); }
        if constexpr (N < 8 && (I == XR || I == XR + 1)) rdx(IC(N + 1), IC(4 * (I - XR) + R));
        SBAR();
        if constexpr (pos == 0 && R == 3) {
          if constexpr (RG == FLG0 || RG == FLG1) { if (threadIdx.x == 0) flag_store(c, f1 + 8 * c.m, RG == FLG0 ? 1 : 2); }
          if constexpr (RG == PZR + 1) keep(pz);
          if constexpr (RG == FLG0) { if (vl16 == 0) flag_store(c, flag2(c) + 8 * c.a, 1); }
          if constexpr (RG == 19) p2.init(c);
          if constexpr (RG == 25) p2.template check<0>(c);
        }
        if constexpr (R >= 2) { if constexpr (R == 3 || F == 0) prep(IC(F), IC(R)); fire(IC(F), IC(R)); }
        if constexpr (F >= 144 && ps == 1 && sig_idx(F) >= 0 && sig_idx(F) <= 65) {
          constexpr int e = sig_idx(F), d = e - 2;
          if constexpr (R == 0 && d >= 0) e2 = w_add1(e1);
          if constexpr (R == 1 && e < 64) e0 = w_mul(ag[e / 16][(e / 4) % 4][e % 4]);
          if constexpr (R == 2 && e >= 1 && e <= 64) e1 = w_exp(e0);
          if constexpr (R == 3 && d >= 0) { if constexpr (d % 2 == 0) sp[d / 16][(d / 4) % 4][(d % 4) / 2] = w_rcplo(e2); else w_rcphi(sp[d / 16][(d / 4) % 4][(d % 4) / 2], e2); }
        }
        if constexpr (F >= 134 && F < 142) {
          constexpr int tt = (F - 134) / 2, rt = 2 * (F & 1) + (R >> 1), k = R & 1;
          sp[tt][rt][k] = w_pklo(ag0[tt][rt][2 * k], nl2e); w_pkhi(sp[tt][rt][k], ag0[tt][rt][2 * k + 1], nl2e);
        }
        if constexpr (F >= 144 && F < 216 && sig_idx(F) >= 0) {
          constexpr int e = sig_idx(F), d = e - 2, pa = (e - 2) / 2, pf = (e - 4) / 2;
          if constexpr (R == 0 && e % 2 == 0 && e >= 2 && pa < 32) w_add1h(sp[pa / 8][(pa / 2) % 4][pa % 2]);
          if constexpr (R == 0 && e % 2 == 1 && e >= 5) h1 = w_mixhi(au[pf / 8][(pf / 2) % 4][2 * (pf % 2) + 1], sp[pf / 8][(pf / 2) % 4][pf % 2]);
          if constexpr (R == 1 && e % 2 == 0 && e >= 4) h0 = w_mixlo(au[pf / 8][(pf / 2) % 4][2 * (pf % 2)], sp[pf / 8][(pf / 2) % 4][pf % 2]);
          if constexpr (R == 1 && e % 2 == 1 && e >= 5) hq[(pf / 16) * 4 + (pf / 2) % 4][2 * ((pf / 8) & 1) + pf % 2] = w_cvt(h0, h1);
          if constexpr (R == 2 && e < 64) w_exph<e & 1>(sp[e / 16][(e / 4) % 4][(e % 4) / 2]);
          if constexpr (R == 3 && d >= 0 && d < 64) w_rcph<d & 1>(sp[d / 16][(d / 4) % 4][(d % 4) / 2]);
        }
        if constexpr (F >= 288 && FP >= FIN0 && FP < FIN0 + 32) {
          constexpr int J = (FP - FIN0) / 4, i = FP % 4, e = J >> 2, rt = J & 3, tt = 2 * e + (i >> 1), k = 2 * (i & 1);
          if constexpr (R == 0) h0 = w_mixlo(au[tt][rt][k], sp[tt][rt][i & 1]);
          if constexpr (R == 1) h1 = w_mixhi(au[tt][rt][k + 1], sp[tt][rt][i & 1]);
          if constexpr (R == 3) hq[J][i] = w_cvt(h0, h1);
        }
        if constexpr (R == 3 && mlp_op(F, 2).kind == O_FIN) keep(hq[mlp_op(F, 2).b]);
        if constexpr (R == 3 && F < 431) prep(IC(F + 1), IC(2));
        SBAR();
      });
    });
  });
  phase2<8>(c, p2, f, [&](auto N_, auto R_) {
    constexpr int N = CI(N_), R = CI(R_);
    if constexpr (N < 16 && R < 3) {
      constexpr int J = N / 2, e = J >> 2, rt = J & 3, tt = 2 * e + (N & 1);
      if constexpr (R == 0) { h0 = w_mixlo(au[tt][rt][0], sp[tt][rt][0]); h1 = w_mixhi(au[tt][rt][1], sp[tt][rt][0]); }
      if constexpr (R == 1) { e0 = w_mixlo(au[tt][rt][2], sp[tt][rt][1]); e1 = w_mixhi(au[tt][rt][3], sp[tt][rt][1]); }
      if constexpr (R == 2) { hq[J][2 * (N & 1)] = w_cvt(h0, h1); hq[J][2 * (N & 1) + 1] = w_cvt(e0, e1); }
    }
    if constexpr (R == 3 && N < 20 && lastfin(N) >= 0) {
      constexpr int J = lastfin(N);
      __builtin_amdgcn_raw_buffer_store_b128(hq[J], c.rws, vl16 + (J & 3) * 1024, sH + (24 + (J >> 2)) * 4096, 16);
      asm volatile("" ::: "memory");
    }
    if constexpr (R == 1 && N >= 1 && N < 21 && lastfin(N - 1) >= 0) keep(hq[lastfin(N - 1)]);
  });
}

DI u32x2 pk4(const f32x4& v) { return u32x2{cvtpk(v[0], v[1]), cvtpk(v[2], v[3])}; }
DI void sw16(float& a, float& b) { auto r = __builtin_amdgcn_permlane16_swap(__float_as_uint(a), __float_as_uint(b), false, false); a = __uint_as_float(r[0]); b = __uint_as_float(r[1]); }
DI void sw32(float& a, float& b) { auto r = __builtin_amdgcn_permlane32_swap(__float_as_uint(a), __float_as_uint(b), false, false); a = __uint_as_float(r[0]); b = __uint_as_float(r[1]); }
template <bool MX> DI float xred(float (&v)[4]) {
  sw16(v[0], v[1]); sw16(v[2], v[3]);
  float a = MX ? max2(v[0], v[1]) : v[0] + v[1], b = MX ? max2(v[2], v[3]) : v[2] + v[3];
  sw32(a, b);
  return MX ? max2(a, b) : a + b;
}
DI void xbc(float t, float (&v)[4]) {
  float a = t, b = t;
  sw32(a, b);
  v[0] = a; v[1] = a; v[2] = b; v[3] = b;
  sw16(v[0], v[1]); sw16(v[2], v[3]);
}

DI void role_att(Ctx& c, XS& xs) {
  const unsigned mp = c.m & 1;
  u32x2 vp[4][5], qp[4][5];
  u32x4 kt[6][4];
  f32x4 gate, bprev, bown, bprev0;
  unsigned mpv[2];
  P2<12> p2;
  {
    const unsigned l = lane_id(), g = l >> 4, q = (l & 15) >> 2, pp = l & 3;
    const unsigned lc = (l & 15) ^ (g << 1), hb = 144 * (2 * c.a + mp);
    const unsigned vA = g * 3456 + hb + (lc < 10 ? 1152 + 16 * (lc < 9 ? lc : 8) : 2304 + 16 * (lc - 10));
    const unsigned vB = g * 3456 + hb + (lc < 4 ? 2304 + 16 * (lc < 3 ? 6 + lc : 8) : 16 * (lc - 4));
    const u64 maskB = __builtin_amdgcn_ballot_w64(lc < 13);
    const unsigned lw = c.w * 1152;
    unsigned sWk[9];
#pragma unroll
    for (int i = 0; i < 9; ++i) sWk[i] = (8 * c.w + 64 * c.kg[i]) * 3456;
    const int hr = 256 * (int)c.T + ((c.w & 1) ? -16 : -15) + 2 * (int)(l >> 3);
    const unsigned vHx = (unsigned)(hr < 0 ? 0 : hr) * 1152 + 16 * ((l & 7) ^ (4 * (c.w & 1) + g));
    const unsigned lHx = A_HX + c.w * 1024;
    unsigned rh[2] = {A_HX + (l & 15) * 128 + 16 * (g ^ ((l & 15) >> 1)), 0}; rh[1] = rh[0] ^ 64; PIN(rh[0]); PIN(rh[1]);
    bf16x8 xf[18][4];
    unsigned cur = 0, l8 = 0;
    u32x4 pz;
    auto prep = [&](auto F_, auto R_) {
      constexpr Op o = att1_op(CI(F_), CI(R_));
      if constexpr (o.kind == O_W) cur = dprep<(o.a % 4) * S2 + o.b * 4608, (32 * (o.b >> 1) + 4 * (o.b & 1)) * 3456>(lw, sWk[o.a % 9]);
      else if constexpr (o.kind == O_X) xs.template prep<2, o.a, o.b>(c);
      else if constexpr (o.kind == O_HX) {
        if constexpr (o.a < 4) cur = dprep<o.a * 4096, 0>(lHx, (c.w < 2 ? c.kg[2 * o.a] : c.kg[2 * o.a + 1]) * 128);
        else cur = dprep<16384, 0>(lHx, c.w < 2 ? c.kg[8] * 128 : (c.w & 1) * 1024);
      } else p2.template prep<o.kind, o.a, o.b>(c);
    };
    auto fire = [&](auto F_, auto R_) {
      constexpr Op o = att1_op(CI(F_), CI(R_));
      if constexpr (o.kind == O_W) { if constexpr (o.a < 9) dfire<0>(vA, c.rqkv, cur); else dfire_m(vB, c.rqkv, cur, maskB); }
      else if constexpr (o.kind == O_X) {
        if constexpr (o.a >= 2 && o.b == 0) asm volatile("s_waitcnt lgkmcnt(4)" ::: "memory");
        xs.template fire<o.b>(c);
      } else if constexpr (o.kind == O_HX) {
        if constexpr (o.a < 4) dfire<0>(vHx, c.rx, cur); else dfire<0>(c.w < 2 ? vHx : l * 16, c.w < 2 ? c.rx : c.rgw, cur);
      } else if constexpr (o.kind == O_TL) touch_pages<A_GW + 2048>(c);
      else if constexpr (o.kind == O_PZ) poison<3, o.a>(c, pz, 2 * l8);
      else p2.template fire<o.kind, o.a, o.b>(c);
    };
    auto rdx = [&](auto C_, auto I_) {
      constexpr int C = CI(C_), s = CI(I_) >> 2, rt = CI(I_) & 3;
      xf[2 * C + s][rt] = ldsr<bf16x8>(xs.rd[s] + (C % 2) * 32768 + rt * 2048);
    };
    static_for<6>([&](auto I_) { prep(IC(CI(I_) - 8), IC(2)); fire(IC(CI(I_) - 8), IC(2)); });
    visit_all<32, 22>(c.pc0, c.w + ((blockIdx.x >> 3) & 4));
    load_epoch(c);
    unsigned rb[4], rx1, rx2;
#pragma unroll
    for (int t = 0; t < 4; ++t) rb[t] = g * 1152 + q * 256 + 32 * (t ^ q) + 8 * pp;
    rx1 = g * 1152 + q * 256 + 32 * (c.w ^ q) + 8 * pp;
    rx2 = g * 1152 + q * 256 + 128 + 32 * ((c.w < 2 ? c.w + 1 : 3) ^ q) + 8 * pp;
    const unsigned gsrc = A_GW + 2 * (32 * (l >> 5) + 8 * ((l >> 2) & 3) + 4 * ((l >> 4) & 1) + (l & 3));
    const unsigned gdst = (l >> 2) * 1152 + (l & 3) * 256 + 16 * (13 ^ ((l & 3) << 1));
    u16 gwv = 0;
    f32x4 acc[2][8][4], ah[4];
    u32x2 hp;
    unsigned wk = 0, wv = 0;
    bf16x8 f[16], hx[2], ha[2], hc[2];
    s16x4 flo, hlo;
    wait_bar<VM<1, -1>>();
    asm volatile("" : "+s"(c.E));
    rdx(IC(0), IC(0)); rdx(IC(0), IC(4));
    static_for<PD>([&](auto I_) { constexpr unsigned o = (CI(I_) & 1) * 9216; f[CI(I_)] = cat(trd(rb[CI(I_) >> 1] + o), trd(rb[CI(I_) >> 1] + o + 4608)); });
    static_for<6>([&](auto I_) { rdx(IC(0), IC(1 + CI(I_) / 2 + 4 * (CI(I_) & 1))); });
    SBAR();
    auto pass = [&](auto PS_) {
      constexpr int PS = CI(PS_), NT = PS == 0 ? 8 : 7, FC = 2 * NT;
      static_for<9>([&](auto N_) {
        static_for<FC>([&](auto I_) {
          constexpr int N = CI(N_), I = CI(I_), F = 144 * PS + N * FC + I, t = I >> 1;
          constexpr int RG = rgA(F), pos = posA(F);
          constexpr int tn = pos >> 1, RR = pos == 0 ? 2 : 0;
          constexpr unsigned ro = (RG % 2) * S2 + (pos & 1) * 9216 + 128 * (tn >> 2);
          constexpr unsigned rc = ((9 * PS + N) % 2) * S2 + ((I >> 1) & 1) * 9216 + (I & 1) * 4608;
          constexpr bool SW = PS == 0 ? t >= 5 : t < 2;
          const bf16x8 a[2] = {f[I & ~1], f[I | 1]};
          static_for<5>([&](auto R_) {
            constexpr int M = CI(R_), R = M == 1 ? 2 : M == 2 ? 1 : M;
            if constexpr (M < 4) {
              constexpr int rt0 = 2 * (I & 1) + (M >> 1), sk = (M == 1 || M == 2), ks = 2 * N + sk;
              if constexpr (SW) mfs<(ks < 16), (N == 0 && (M & 1) == 0)>(acc[PS][t][rt0], a[sk], xf[ks][rt0]); else mf<(ks < 16), (N == 0 && (M & 1) == 0)>(acc[PS][t][rt0], a[sk], xf[ks][rt0]);
            } else if constexpr (PS == 0) {
              if constexpr (I == 7) { mf<false, N == 0>(ah[0], ha[1], hx[1]); mf<false, false>(ah[0], ha[0], hx[0]); }
              if constexpr (I == 9) { mf<false, N == 0>(ah[1], a[0], hx[0]); mf<false, false>(ah[1], a[1], hx[1]); }
              if constexpr (I == 12) { mfs<false, N == 0>(ah[2], hc[1], hx[1]); mfs<false, false>(ah[2], hc[0], hx[0]); }
            } else if constexpr (I == 7) { mfs<false, N == 0>(ah[3], ha[1], hx[1]); mfs<false, false>(ah[3], ha[0], hx[0]); }
            SBAR();
            if constexpr (R == 0 && PS == 0 && I == XR && N < 8) wait_vm<VM<1, F>>();
            if constexpr (pos == 0 && RG < 18) {
              if constexpr (R < 2 && RG % 2 == 0) {
                constexpr unsigned d = RG % 4 == 0 ? 0u - 2 * S2 : 2 * S2;
#pragma unroll
                for (int i = 2 * R; i < 2 * R + 2; ++i) { rb[i] += d; PIN(rb[i]); }
                if (R == 0) { rx1 += d; PIN(rx1); } else if (RG < 9) { rx2 += d; PIN(rx2); }
              }
              if constexpr (R == 0 && RG == 9) { rx1 ^= 32 * (c.w & 2); PIN(rx1); }
              if constexpr (R == 2) {
                if constexpr (RG % 2 == 1) STUB<32 + RG / 2>();
                static_assert(RG != 16 || VM<1, F> == 0, "flags follow a full drain");
                wait_bar<VM<1, F>>();
              }
            }
            if constexpr (R == RR && RG < 18) flo = trd(rb[tn & 3] + ro);
            if constexpr (R == RR + 1 && RG < 18) f[pos] = cat(flo, trd(rb[tn & 3] + ro + 4608));
            if constexpr (PS == 0 && N < 8 && (I == XR || I == XR + 1) && R < 4) rdx(IC(N + 1), IC(4 * (I - XR) + R));
            if constexpr (I < 2 && R == 3) hx[I] = ldsr<bf16x8>(rh[I] + N * 2048);
            if constexpr (I >= 2 && I < 6 && R == 3) { if constexpr (I % 2 == 0) hlo = trd(rx1 + rc); else ha[(I >> 1) & 1] = cat(hlo, trd(rx1 + rc)); }
            if constexpr (PS == 0 && I >= 6 && I < 10 && R == 3) { if constexpr (I % 2 == 0) hlo = trd(rx2 + rc); else hc[(I >> 1) & 1] = cat(hlo, trd(rx2 + rc)); }
            if constexpr (RG + 3 >= 9 && RG + 3 <= 17 && R == 1) {
              if constexpr (pos == 6) gwv = ldsr<u16>(gsrc + c.kg[RG + 3 - 9] * 128);
              if constexpr (pos == 9) ldsw<u16>(gdst + ((RG + 3) % 4) * S2, gwv);
            }
            SBAR();
            if constexpr (F == 128 && R == 0) { l8 = lane_id() * 8; PIN(l8); wk = A_KX + (4 * c.w + 2) * 2560 + l8; PIN(wk); }
            if constexpr (F >= 130 && F < 146 && (R == 0 || R == 2)) {
              constexpr int tt = (F - 130) / 2, rt = 2 * (F & 1) + R / 2;
              if constexpr (tt < 5) ldsw<u32x2>(wk + rt * 2560 + tt * 512, pk4(acc[0][tt][rt])); else { vp[rt][tt - 5] = pk4(acc[0][tt][rt]); PIN(vp[rt][tt - 5]); }
            }
            if constexpr (F == 146 && R < 2) { const u32x2 h = pk4(ah[R]); const unsigned o = A_KX + (R == 0 ? c.w : 4) * 512; ldsw<u32x2>(o + l8, h); ldsw<u32x2>(o + 2560 + (l8 ^ 64), h); }
            if constexpr (F == 146 && R == 2) { hp = pk4(ah[2]); PIN(hp); }
            if constexpr (pos == 0 && R == 3) {
              if constexpr (RG == 15) { keep(pz); p2.init(c); wv = A_VX + (c.w < 3 ? (c.w + 1) * 5120 : A_GW + 2304 - A_VX) + (c.w < 3 ? l8 : 0); PIN(wv); }
              if constexpr (RG == 16) { if (l8 == 0) flag_store(c, flag2(c) + 8 * c.a, 1); }
            }
            if constexpr (RG == 15 && pos >= 4 && pos < 10 && R == 2) { if (c.w < 3) ldsw<u32x2>(wv + (((pos - 4) / 3) * 5 + (pos - 4) % 3) * 512, vp[2 + (pos - 4) / 3][(pos - 4) % 3]); }
            if constexpr (RG == 15 && pos == 11 && R == 2) { const unsigned o = A_VX + (c.w < 2 ? c.w : 2) * 512; ldsw<u32x2>(o + l8, hp); ldsw<u32x2>(o + 2560 + (l8 ^ 256), hp); }
            if constexpr (RG == 16 && pos >= 9 && pos < 13 && R == 2) {
              constexpr int r = pos - 9;
              const int key = (int)(l8 >> 7) * 4 + r, jj = (int)(l8 >> 3) & 15;
              bprev[r] = key >= jj + 9 ? 0.f : -1e30f;
              bown[r] = (key <= jj && key >= jj - 7) ? 0.f : -1e30f;
              bprev0[r] = (c.T == 0 && c.w == 0) ? -1e30f : bprev[r];
              if constexpr (r % 2 == 0) mpv[r / 2] = key >= jj + 9 ? 0xffffu : 0u; else { mpv[r / 2] |= key >= jj + 9 ? 0xffff0000u : 0u; PIN(mpv[r / 2]); }
              if constexpr (r == 3) { PIN(bprev); PIN(bown); PIN(bprev0); }
            }
            if constexpr (F == 256 && R == 0) { wk -= 5120; PIN(wk); }
            if constexpr (F >= 257 && F < 265 && (R == 0 || R == 1 || R == 3)) {
              constexpr int n = 3 * (F - 257) + (R == 3 ? 2 : R), i = n / 4, h = n % 4;
              const u32x2 k0 = ldsr<u32x2>(wk + i * 2560 + h * 512), k1 = ldsr<u32x2>(wk + i * 2560 + h * 512 + 512);
              kt[i][h] = u32x4{k0[0], k0[1], k1[0], k1[1]};
              asm volatile("" ::: "memory");
            }
            if constexpr (F >= 258 && R == 2) {
              constexpr int tt = (F - 258) / 2;
#pragma unroll
              for (int rt = 2 * (F & 1); rt < 2 * (F & 1) + 2; ++rt) {
                if constexpr (tt < 2) { vp[rt][3 + tt] = pk4(acc[1][tt][rt]); PIN(vp[rt][3 + tt]); }
                else { qp[rt][tt - 2] = pk4(acc[1][tt][rt]); PIN(qp[rt][tt - 2]); }
              }
              if constexpr (tt < 2 && (F & 1)) { if (c.w < 3) { ldsw<u32x2>(wv + (3 + tt) * 512, vp[2][3 + tt]); ldsw<u32x2>(wv + (8 + tt) * 512, vp[3][3 + tt]); } }
            }
            if constexpr (F == 267 && R == 0) { const u32x2 h = pk4(ah[3]); const unsigned o = A_VX + (3 + (c.w & 1)) * 512; ldsw<u32x2>(o + l8, h); ldsw<u32x2>(o + 2560 + (l8 ^ 256), h); }
            if constexpr (R == 2 || R == 3) { if constexpr (R == 3 || F == 0) prep(IC(F), IC(R)); fire(IC(F), IC(R)); }
            if constexpr (R == 4) prep(IC(F + 1), IC(2));
            SBAR();
          });
        });
      });
    };
    pass(IC(0));
    pass(IC(1));
    STUB<41>();
    asm volatile("s_nop 6\n\ts_waitcnt lgkmcnt(0)\n\ts_barrier" ::: "memory");
#pragma unroll
    for (int rt = 0; rt < 4; ++rt) {
      qp[rt][4] = pk4(acc[1][6][rt]);
      gate[rt] = __builtin_amdgcn_exp2f(fminf(1.4426950408889634f * acc[1][6][rt][0], 80.0f));
    }
  }
  auto pre = [&](auto A_, auto N_) { static_for<CI(N_)>([&](auto I_) { constexpr Op o = att2_op(CI(A_) + CI(I_) - 50, 2); p2.template prep<o.kind, o.a, o.b>(c); p2.template fire<o.kind, o.a, o.b>(c); }); };
  const unsigned l = lane_id(), g = (l >> 4) & 3, j = l & 15;
  {
    u32x6 vt[5][2];
    const unsigned ra = A_VX + c.w * 5120 + l * 8;
#pragma unroll
    for (int pa = 0; pa < 2; ++pa)
#pragma unroll
      for (int t = 0; t < 5; ++t) {
        const u32x2 v2 = ldsr<u32x2>(ra + (pa * 5 + t) * 512);
        vt[t][pa] = u32x6{v2[0], v2[1], vp[pa][t][0], vp[pa][t][1], vp[2 + pa][t][0], vp[2 + pa][t][1]};
      }
    const f32x4 omg = {__builtin_amdgcn_rcpf(1.0f + gate[0]), __builtin_amdgcn_rcpf(1.0f + gate[1]), __builtin_amdgcn_rcpf(1.0f + gate[2]), __builtin_amdgcn_rcpf(1.0f + gate[3])};
    if (mp == 0 && g == 2) st16(__builtin_bit_cast(u32x4, omg), c.rws, j * 16, WS_G + (c.a * 32 + 4 * c.T + c.w) * 256);
#pragma unroll
    for (int rt = 0; rt < 4; ++rt) gate[rt] = __shfl(gate[rt], 32 + j);
    constexpr float SC2 = 0.23570226039551584f * 1.4426950408889634f;
    const unsigned sH = WS_H + (((4 * c.T + c.w) * 4 + c.a) * 42 + 36 + 3 * mp) * 4096;
    u32x4 qf[4], P[4];
    f32x4 sp[4], so[4], hi[4], lo[4];
    auto mkq = [&](auto RT_) {
      constexpr int rt = CI(RT_);
#pragma unroll
      for (int h = 0; h < 4; ++h) {
#pragma unroll
        for (int i = 0; i < 4; ++i) { const int col = 16 * (h + (i >> 1)) + 4 * (int)g + 2 * (i & 1); qf[h][i] = (col >= 18 * h && col < 18 * h + 18) ? qp[rt][h + (i >> 1)][i & 1] : 0u; }
        PIN(qf[h]);
      }
    };
    auto qk = [&](auto RT_, auto H_, auto W_) {
      constexpr int rt = CI(RT_), h = CI(H_);
      if constexpr (CI(W_) == 0) sp[h] = mfc(kt[rt][h], qf[h], rt < 2 ? bprev0 : bprev);
      else so[h] = mfc(kt[rt + 2][h], qf[h], bown);
    };
    auto pv = [&](auto RT_, auto T_, auto W_) {
      constexpr int rt = CI(RT_), t = CI(T_) + CI(W_), pa = rt & 1, sn = rt >> 1;
      const f32x4 r = mfz(__builtin_shufflevector(vt[t][pa], vt[t][pa], 2 * sn, 2 * sn + 1, 2 * sn + 2, 2 * sn + 3), P[CI(T_)]);
      if constexpr (CI(W_) == 0) hi[CI(T_)] = r; else lo[CI(T_)] = r;
    };
    auto outp = [&](auto RT_) {
      constexpr int rt = CI(RT_);
      u32x2 o[5];
      PIN(lo[0]);
#pragma unroll
      for (int t = 0; t < 5; ++t)
#pragma unroll
        for (int i = 0; i < 2; ++i) {
          const unsigned vh = t < 4 ? cvtpk(hi[t < 4 ? t : 0][2 * i], hi[t < 4 ? t : 0][2 * i + 1]) : 0u;
          o[t][i] = (4 * (int)g + 2 * i < 2 * t) ? cvtpk(lo[t > 0 ? t - 1 : 0][2 * i], lo[t > 0 ? t - 1 : 0][2 * i + 1]) : vh;
        }
      st16(u32x4{o[0][0], o[0][1], o[1][0], o[1][1]}, c.rws, l * 16 + rt * 1024, sH);
      st16(u32x4{o[2][0], o[2][1], o[3][0], o[3][1]}, c.rws, l * 16 + rt * 1024, sH + 4096);
      st16(u32x4{o[4][0], o[4][1], 0u, 0u}, c.rws, l * 16 + rt * 1024, sH + 8192);
    };
    mkq(IC(0));
    asm volatile("s_nop 1" ::: "memory");
    static_for<8>([&](auto I_) { qk(IC(0), IC(CI(I_) >> 1), IC(CI(I_) & 1)); });
    SBAR();
    mkq(IC(1));
    static_for<4>([&](auto RT_) {
      constexpr int rt = CI(RT_);
      float s[4][4], red[4], bc[4];
      STUB<42 + rt>();
      SBAR();
      static_for<4>([&](auto H_) {
        constexpr int h = CI(H_);
        if constexpr (rt > 0) pv(IC(rt - 1), H_, IC(0));
        s[h][0] = max2(sp[h][0], so[h][0]); s[h][1] = max2(sp[h][1], so[h][1]); s[h][2] = max2(sp[h][2], so[h][2]);
        SBAR();
        if constexpr (rt > 0) pv(IC(rt - 1), H_, IC(1));
        s[h][3] = max2(sp[h][3], so[h][3]);
        red[h] = max2(max3(s[h][0], s[h][1], s[h][2]), s[h][3]);
        SBAR();
      });
      xbc(-SC2 * xred<true>(red), bc);
      if constexpr (rt == 0) pre(IC(6), IC(7));
      if constexpr (rt == 2) { wait_vm<VM<2, -28>>(); p2.template check<0>(c); }
      if constexpr (rt > 0) outp(IC(rt - 1));
      SBAR();
      static_for<4>([&](auto H_) {
        constexpr int h = CI(H_);
        if constexpr (rt < 3) qk(IC(rt + 1), H_, IC(0));
        s[h][0] = __builtin_amdgcn_exp2f(__builtin_fmaf(s[h][0], SC2, bc[h])); s[h][1] = __builtin_amdgcn_exp2f(__builtin_fmaf(s[h][1], SC2, bc[h]));
        SBAR();
        if constexpr (rt < 3) qk(IC(rt + 1), H_, IC(1));
        s[h][2] = __builtin_amdgcn_exp2f(__builtin_fmaf(s[h][2], SC2, bc[h])); s[h][3] = __builtin_amdgcn_exp2f(__builtin_fmaf(s[h][3], SC2, bc[h]));
        SBAR();
      });
#pragma unroll
      for (int h = 0; h < 4; ++h) red[h] = (s[h][0] + s[h][1]) + (s[h][2] + s[h][3]);
      xbc(gate[rt] * __builtin_amdgcn_rcpf(xred<false>(red)), bc);
      if constexpr (rt == 1) pre(IC(16), IC(6));
      if constexpr (rt == 2) pre(IC(25), IC(8));
      if constexpr (rt == 3) pre(IC(36), IC(4));
      if constexpr (rt == 0) {
#pragma unroll
        for (int i = 0; i < 10; ++i) PIN(vt[i >> 1][i & 1]);
      }
      if constexpr (rt < 2) mkq(IC(rt + 2));
#pragma unroll
      for (int h = 0; h < 4; ++h) {
        const unsigned p0 = cvtpk(s[h][0] * bc[h], s[h][1] * bc[h]), p1 = cvtpk(s[h][2] * bc[h], s[h][3] * bc[h]);
        P[h] = u32x4{p0 & mpv[0], p1 & mpv[1], p0 & ~mpv[0], p1 & ~mpv[1]};
        PIN(P[h]);
      }
      asm volatile("s_nop 1" ::: "memory");
    });
    static_for<8>([&](auto I_) { pv(IC(3), IC(CI(I_) >> 1), IC(CI(I_) & 1)); });
    asm volatile("s_nop 7" ::: "memory");
    SBAR();
    outp(IC(3));
  }
  bf16x8 f[16];
  phase2<12>(c, p2, f, [](auto, auto) {});
}

#endif
__global__ __launch_bounds__(256, 1) void fused_kernel(const u16* x, const u16* wqkv, const u16* wo, const u16* wg, const u16* wu, const u16* wd,
                                                        const u16* gw, char* ws, u16* out) {
#if defined(__HIP_DEVICE_COMPILE__)
  __shared__ __attribute__((aligned(16))) char smem[LDS_BYTES];
  asm volatile("" ::"v"((unsigned)(size_t)smem));
  Ctx c;
  const unsigned bid = blockIdx.x, xcd = bid & 7, idx = bid >> 3;
  c.w = __builtin_amdgcn_readfirstlane(threadIdx.x >> 6);
  {
    u64 pc; unsigned len;
    asm volatile("s_getpc_b64 %0\n.Lgm_code0:\n\ts_mov_b32 %1, .Lgm_code1-.Lgm_code0\n\ts_max_u32 %1, %1, .Lgm_code2-.Lgm_code0" : "=s"(pc), "=s"(len) :: "scc");
    c.pc0 = pc;
    asm volatile("s_branch .Lgm_go\n.Lgm_ret:\n\ts_setpc_b64 s[100:101]\n.Lgm_go:");
    const rsrc_t rc = __builtin_amdgcn_make_buffer_rsrc((void*)pc, (short)0, len, 0x00020000);
    if (idx < 16) touch<3 * S1>((threadIdx.x & 15) * 128, rc, idx * 8192 + c.w * 2048);
  }
  asm volatile("" ::"s"(x), "s"(out));
  c.rx = __builtin_amdgcn_make_buffer_rsrc((void*)x, (short)0, SEQ * DM * 2, 0x00020000);
  c.rqkv = __builtin_amdgcn_make_buffer_rsrc((void*)wqkv, (short)0, DM * 3 * DM * 2, 0x00020000);
  c.rwo = __builtin_amdgcn_make_buffer_rsrc((void*)wo, (short)0, DM * DM * 2, 0x00020000);
  c.rwg = __builtin_amdgcn_make_buffer_rsrc((void*)wg, (short)0, DM * FF * 2, 0x00020000);
  c.rwu = __builtin_amdgcn_make_buffer_rsrc((void*)wu, (short)0, DM * FF * 2, 0x00020000);
  c.rwd = __builtin_amdgcn_make_buffer_rsrc((void*)wd, (short)0, DM * FF * 2, 0x00020000);
  c.rgw = __builtin_amdgcn_make_buffer_rsrc((void*)gw, (short)0, DM * 2, 0x00020000);
  c.rws = __builtin_amdgcn_make_buffer_rsrc((void*)ws, (short)0, WS_BYTES, 0x00020000);
  c.rout = __builtin_amdgcn_make_buffer_rsrc((void*)out, (short)0, SEQ * DM * 2, 0x00020000);
  c.ws = (u64)ws;
  c.a = xcd & 3; c.T = 4 * (xcd >> 2) + (idx >> 3);
  c.m = (idx & 3) == 3 ? 6 + ((idx >> 2) & 1) : 3 * ((idx >> 2) & 1) + (idx & 3);
  const unsigned rot = (2 * (c.a + c.T) + (c.m & 3)) % 9;
  c.kg[0] = rot;
  XS xs; xs.init(c, c.m < 6 ? M_X : A_X);
  static_for<8>([&](auto I_) { xs.template prep<1, 0, CI(I_)>(c); xs.template fire<CI(I_)>(c); });
  SBAR();
#pragma unroll
  for (int i = 1; i < 9; ++i) c.kg[i] = c.kg[i - 1] == 8 ? 0 : c.kg[i - 1] + 1;
  if (__builtin_expect(c.m < 6, 1)) role_mlp(c, xs); else role_att(c, xs);
#endif
}

extern "C" void kernel_launch(void* const* d_in, const int* in_sizes, int n_in, void* d_out, int out_size, void* d_ws, size_t ws_size,
                              hipStream_t stream) {
  fused_kernel<<<256, 256, 0, stream>>>((const u16*)d_in[0], (const u16*)d_in[1], (const u16*)d_in[2], (const u16*)d_in[4], (const u16*)d_in[5],
                                        (const u16*)d_in[6], (const u16*)d_in[7], (char*)d_ws, (u16*)d_out);
}
```

```cpp
#include <hip/hip_runtime.h>
#include <utility>

typedef unsigned short u16;
typedef unsigned long long u64;
typedef short s16x4 __attribute__((ext_vector_type(4)));
typedef __bf16 bf16x8 __attribute__((ext_vector_type(8)));
typedef __bf16 bf16x2 __attribute__((ext_vector_type(2)));
typedef float f32x2 __attribute__((ext_vector_type(2)));
typedef float f32x4 __attribute__((ext_vector_type(4)));
typedef unsigned u32x2 __attribute__((ext_vector_type(2)));
typedef unsigned u32x4 __attribute__((ext_vector_type(4)));
typedef unsigned u32x6 __attribute__((ext_vector_type(6)));
typedef __amdgpu_buffer_rsrc_t rsrc_t;
#define LAS __attribute__((address_space(3)))
#define DI __device__ __forceinline__
#define SBAR() __builtin_amdgcn_sched_barrier(0)
#define IC(x) std::integral_constant<int, (x)>{}
#define CI(x) decltype(x)::value

constexpr int SEQ = 2048, DM = 576, FF = 4608;
constexpr unsigned WS_H = 0;
constexpr unsigned WS_PART = 24u << 20;
constexpr unsigned WS_G = 40u << 20;
constexpr unsigned WS_F1 = 41u << 20;
constexpr unsigned WS_F2 = WS_F1 + 8192;
constexpr unsigned WS_E = WS_F2 + 65536;
constexpr unsigned WS_BYTES = 42u << 20;
constexpr unsigned LDS_BYTES = 163840;
constexpr unsigned S1 = 16384;
constexpr unsigned S2 = 18432;
constexpr unsigned S3 = S2 + 9216;
constexpr unsigned M_X = 65536;
constexpr unsigned A_X = 4 * S2;
constexpr unsigned A_HX = A_X + 65536;
constexpr unsigned A_GW = A_HX + 18432;
constexpr unsigned A_KX = 83968;
constexpr unsigned A_VX = A_KX + 18 * 2560;
static_assert(A_GW + 2048 <= LDS_BYTES && A_VX <= A_HX && A_VX + 20480 <= LDS_BYTES && 3 * S3 <= A_KX, "lds");

#if defined(__HIP_DEVICE_COMPILE__)
template <int... Is> using iseq = std::integer_sequence<int, Is...>;
template <class Fn, int... Is> DI void static_for_(Fn&& f, iseq<Is...>) { (f(std::integral_constant<int, Is>{}), ...); }
template <int N, class Fn> DI void static_for(Fn&& f) { static_for_(f, std::make_integer_sequence<int, N>{}); }

template <int N> DI void wait_bar() { asm volatile("s_waitcnt vmcnt(%0) lgkmcnt(0)\n\ts_barrier" ::"i"(N) : "memory"); }
template <int N> DI void wait_vm() { asm volatile("s_waitcnt vmcnt(%0)" ::"i"(N) : "memory"); }
template <class T> DI void PIN(T& x) { asm volatile("" : "+v"(x)); }
DI void tieA(bf16x8& a, bf16x8& b, bf16x8& c, bf16x8& d) { asm volatile("" : "+a"(a), "+a"(b), "+a"(c), "+a"(d)); }
template <unsigned LO, unsigned SO> DI unsigned dprep(unsigned lb, unsigned sb) {
  unsigned t;
  asm volatile("s_add_u32 m0, %1, %3\n\ts_add_u32 %0, %2, %4" : "=s"(t) : "s"(lb), "s"(sb), "i"(LO), "i"(SO) : "scc");
  return t;
}
template <unsigned LO> DI void dprep_l(unsigned lb) { asm volatile("s_add_u32 m0, %0, %1\n\ts_nop 0" ::"s"(lb), "i"(LO) : "scc"); }
template <unsigned IMM> DI void dfire(unsigned voff, rsrc_t srd, unsigned so) {
  asm volatile("buffer_load_dwordx4 %0, %1, %2 offen offset:%3 lds" ::"v"(voff), "s"(srd), "s"(so), "i"(IMM) : "memory");
}
DI void dfire_m(unsigned voff, rsrc_t srd, unsigned so, u64 mask) {
  asm volatile("s_mov_b64 exec, %3\n\tbuffer_load_dwordx4 %0, %1, %2 offen lds\n\ts_mov_b64 exec, -1" ::"v"(voff), "s"(srd), "s"(so), "s"(mask) : "memory");
}
template <unsigned IMM> DI void ldh(bf16x8& d, unsigned voff, rsrc_t srd, unsigned so) {
  asm volatile("buffer_load_dwordx4 %0, %1, %2, %3 offen offset:%4 sc1" : "=a"(d) : "v"(voff), "s"(srd), "s"(so), "i"(IMM) : "memory");
}
DI void ldg(f32x4& d, unsigned voff, rsrc_t srd, unsigned so) { asm volatile("buffer_load_dwordx4 %0, %1, %2, %3 offen sc1" : "=v"(d) : "v"(voff), "s"(srd), "s"(so) : "memory"); }
template <unsigned LA> DI void touch(unsigned voff, rsrc_t srd, unsigned so) {
  asm volatile("s_mov_b32 m0, %3\n\ts_nop 0\n\tbuffer_load_dword %0, %1, %2 offen sc1 lds" ::"v"(voff), "s"(srd), "s"(so), "i"(LA) : "memory");
}
DI void st16(const u32x4& v, rsrc_t srd, unsigned voff, unsigned so) {
  __builtin_amdgcn_raw_buffer_store_b128(v, srd, voff, so, 16);
  asm volatile("s_nop 1" ::"v"(v) : "memory");
}
DI void keep(const u32x4& v) { asm volatile("" ::"v"(v)); }
template <bool AG, bool ZERO> DI void mf(f32x4& c, const bf16x8& a, const bf16x8& b) {
  if constexpr (ZERO) {
    if constexpr (AG) asm volatile("v_mfma_f32_16x16x32_bf16 %0, %1, %2, 0" : "=&v"(c) : "v"(a), "a"(b));
    else asm volatile("v_mfma_f32_16x16x32_bf16 %0, %1, %2, 0" : "=&v"(c) : "v"(a), "v"(b));
  } else {
    if constexpr (AG) asm volatile("v_mfma_f32_16x16x32_bf16 %0, %1, %2, %0" : "+v"(c) : "v"(a), "a"(b));
    else asm volatile("v_mfma_f32_16x16x32_bf16 %0, %1, %2, %0" : "+v"(c) : "v"(a), "v"(b));
  }
}
template <bool AG, bool ZERO> DI void mfs(f32x4& c, const bf16x8& a, const bf16x8& b) {
  if constexpr (ZERO) {
    if constexpr (AG) asm volatile("v_mfma_f32_16x16x32_bf16 %0, %2, %1, 0" : "=&v"(c) : "v"(a), "a"(b));
    else asm volatile("v_mfma_f32_16x16x32_bf16 %0, %2, %1, 0" : "=&v"(c) : "v"(a), "v"(b));
  } else {
    if constexpr (AG) asm volatile("v_mfma_f32_16x16x32_bf16 %0, %2, %1, %0" : "+v"(c) : "v"(a), "a"(b));
    else asm volatile("v_mfma_f32_16x16x32_bf16 %0, %2, %1, %0" : "+v"(c) : "v"(a), "v"(b));
  }
}
DI f32x4 mfc(const u32x4& a, const u32x4& b, const f32x4& c) { f32x4 d; asm volatile("v_mfma_f32_16x16x32_bf16 %0, %1, %2, %3" : "=&v"(d) : "a"(a), "v"(b), "v"(c)); return d; }
DI f32x4 mfz(const u32x4& a, const u32x4& b) { f32x4 d; asm volatile("v_mfma_f32_16x16x32_bf16 %0, %1, %2, 0" : "=&v"(d) : "v"(a), "v"(b)); return d; }
DI float max2(float a, float b) { float r; asm("v_max_f32 %0, %1, %2" : "=v"(r) : "v"(a), "v"(b)); return r; }
DI float max3(float a, float b, float c) { float r; asm("v_max3_f32 %0, %1, %2, %3" : "=v"(r) : "v"(a), "v"(b), "v"(c)); return r; }
DI s16x4 trd(unsigned a) { return __builtin_amdgcn_ds_read_tr16_b64_v4i16((LAS s16x4*)a); }
DI bf16x8 cat(s16x4 lo, s16x4 hi) { return __builtin_bit_cast(bf16x8, __builtin_shufflevector(lo, hi, 0, 1, 2, 3, 4, 5, 6, 7)); }
DI unsigned cvtpk(float lo, float hi) { f32x2 v = {lo, hi}; return __builtin_bit_cast(unsigned, __builtin_convertvector(v, bf16x2)); }
DI float w_mul(float a) { float r; asm volatile("v_mul_f32 %0, 0xbfb8aa3b, %1" : "=v"(r) : "v"(a)); return r; }
DI float w_exp(float a) { float r; asm volatile("v_exp_f32 %0, %1" : "=v"(r) : "v"(a)); return r; }
DI unsigned w_add1(float a) { unsigned r; asm volatile("v_fma_mixlo_f16 %0, %1, 1.0, 1.0" : "=v"(r) : "v"(a)); return r; }
DI unsigned w_rcplo(unsigned a) { unsigned r; asm volatile("v_rcp_f16 %0, %1" : "=v"(r) : "v"(a)); return r; }
DI void w_rcphi(unsigned& d, unsigned a) { asm volatile("v_rcp_f16_sdwa %0, %1 dst_sel:WORD_1 dst_unused:UNUSED_PRESERVE src0_sel:WORD_0" : "+v"(d) : "v"(a)); }
DI unsigned w_pklo(float a, float k) { unsigned r; asm volatile("v_fma_mixlo_f16 %0, %1, %2, 0" : "=v"(r) : "v"(a), "s"(k)); return r; }
DI void w_pkhi(unsigned& d, float a, float k) { asm volatile("v_fma_mixhi_f16 %0, %1, %2, 0" : "+v"(d) : "v"(a), "s"(k)); }
template <int H> DI void w_exph(unsigned& d) {
  if constexpr (H == 0) asm volatile("v_exp_f16_sdwa %0, %0 dst_sel:WORD_0 dst_unused:UNUSED_PRESERVE src0_sel:WORD_0" : "+v"(d));
  else asm volatile("v_exp_f16_sdwa %0, %0 dst_sel:WORD_1 dst_unused:UNUSED_PRESERVE src0_sel:WORD_1" : "+v"(d));
}
template <int H> DI void w_rcph(unsigned& d) {
  if constexpr (H == 0) asm volatile("v_rcp_f16_sdwa %0, %0 dst_sel:WORD_0 dst_unused:UNUSED_PRESERVE src0_sel:WORD_0" : "+v"(d));
  else asm volatile("v_rcp_f16_sdwa %0, %0 dst_sel:WORD_1 dst_unused:UNUSED_PRESERVE src0_sel:WORD_1" : "+v"(d));
}
DI void w_add1h(unsigned& d) { asm volatile("v_pk_add_f16 %0, %0, 1.0 op_sel_hi:[1,0]" : "+v"(d)); }
DI float w_mixlo(float u, unsigned s) { float r; asm volatile("v_fma_mix_f32 %0, %1, %2, 0 op_sel_hi:[0,1,0]" : "=v"(r) : "v"(u), "v"(s)); return r; }
DI float w_mixhi(float u, unsigned s) { float r; asm volatile("v_fma_mix_f32 %0, %1, %2, 0 op_sel:[0,1,0] op_sel_hi:[0,1,0]" : "=v"(r) : "v"(u), "v"(s)); return r; }
DI unsigned w_cvt(float lo, float hi) { unsigned r; asm volatile("v_cvt_pk_bf16_f32 %0, %1, %2" : "=v"(r) : "v"(lo), "v"(hi)); return r; }
DI float bflo(unsigned v) { return __uint_as_float(v << 16); }
DI float bfhi(unsigned v) { return __uint_as_float(v & 0xffff0000u); }
template <class T> DI T ldsr(unsigned a) { return *(const LAS T*)a; }
template <class T> DI void ldsw(unsigned a, T v) { *(LAS T*)a = v; }
DI unsigned lane_id() { unsigned l; asm volatile("v_mbcnt_lo_u32_b32 %0, -1, 0\n\tv_mbcnt_hi_u32_b32 %0, -1, %0" : "=v"(l)); return l; }

template <int N> DI void STUB() { asm volatile(".Lgm_s%c0:\n\ts_cbranch_execz .Lgm_ret" ::"i"(N)); }
template <int N> DI void visit(u64 pc0) {
  asm volatile("s_add_u32 s98, %0, .Lgm_s%c2-.Lgm_code0\n\ts_addc_u32 s99, %1, 0\n\ts_mov_b64 exec, 0\n\ts_swappc_b64 s[100:101], s[98:99]\n\ts_mov_b64 exec, -1"
               ::"s"((unsigned)pc0), "s"((unsigned)(pc0 >> 32)), "i"(N) : "s98", "s99", "s100", "s101", "scc", "memory");
}
template <int B, int CNT> DI void visit_all(u64 pc0, unsigned w) {
  static_for<4>([&](auto W_) { if (w == CI(W_)) static_for<(CNT + 3 - CI(W_)) / 4>([&](auto I_) { visit<B + CI(W_) + 4 * CI(I_)>(pc0); }); });
}

struct Ctx {
  rsrc_t rx, rqkv, rwo, rwg, rwu, rwd, rgw, rws, rout;
  unsigned w, a, T, m;
  unsigned kg[9];
  u64 E, ws, pc0;
};

enum { O_NONE, O_W, O_X, O_HX, O_FIN, O_POLL, O_D2, O_B2, O_GATE, O_ST, O_TW, O_TL, O_PZ };
constexpr int NEVER = 1 << 20;
struct Op { int kind, a, b, need; };
constexpr int PD = 3;
constexpr int MLP_AD = 3, ATT_AD = 3;
constexpr Op NOP_ = {O_NONE, 0, 0, 0};
constexpr int uk(int U, int NT) { const int o = NT == 8 ? 12 : 6; return U < o ? U : U < o + 3 ? U - o + 18 : U - 3; }
constexpr int dep(int U, int NT) { return U == 15 ? 2 : NT == 8 ? (U == 6 ? 1 : U == 12 ? 3 : -1) : (U == 6 ? 3 : U == 9 ? 1 : -1); }
constexpr int FLG2 = 4, FLGA = 1;
constexpr int RS = 19;
constexpr Op p2_op(int N, int R, int NT, int AD, int FB) {
  const int ND = NT == 8 ? 4 : 6, NO = ND + 4, RG = (N + PD) / NT, pos = (N + PD) % NT, FL = NT == 8 ? FLG2 : FLGA;
  if (RG > 20 || pos == 0 || RG == FL - 1) return NOP_;
  if (NT != 8 && R == 3 && pos == 1 && dep(RG + AD + 1, NT) >= 0) return {O_POLL, dep(RG + AD + 1, NT), 0, FB + (RG + 1) * NT - PD};
  int k;
  const bool busy = RG == FL || RG == FL + 1;
  if (busy) { const int d = NT == 8 ? 1 : 2, nb = NT == 8 ? 5 : 4; k = pos < d ? (R == 2 ? pos - 1 : -1) : pos < d + nb ? d - 1 + 2 * (pos - d) + (R - 2) : (R == 2 ? pos - 1 + nb : -1); }
  else if (NT == 8) k = R == 2 ? (pos == 1 ? 0 : pos) : pos == 1 ? 1 : pos == 7 ? 8 : -1;
  else k = R == 2 ? pos - 1 : -1;
  if (k < 0) return NOP_;
  const int n = busy ? (FL - 1 + AD) * NO + (RG - FL) * (3 * NO / 2) + k : (RG + AD) * NO + k, U = n / NO, i = n % NO;
  if (k < (busy ? 3 * NO / 2 : NO)) return U > 20 ? NOP_ : Op{i < ND ? O_D2 : O_B2, U, i < ND ? i : i - ND, FB + U * NT - PD};
  if (k != NO || busy) return NOP_;
  if (NT == 8 && dep(RG + AD + 2, NT) >= 0) return {O_POLL, dep(RG + AD + 2, NT), 0, FB + (RG + 2) * NT - PD};
  if (RG == 14) return {O_POLL, 4, 0, FB + 17 * NT - PD};
  if (RG == 15) return {O_GATE, 0, 0, FB + 18 * NT - PD};
  return NOP_;
}
constexpr int FIN0 = 36, ST0 = 72, FLG0 = 17, FLG1 = 25;
constexpr int PZR = 8;
constexpr int lastfin(int N) { return N == 1 ? 0 : N == 3 ? 1 : N == 7 ? 2 : N == 8 ? 3 : N == 10 ? 4 : N == 11 ? 5 : N == 15 ? 6 : N == 16 ? 7 : -1; }
constexpr int bar1(int n) { return n == 0 ? -1 : 16 * n - PD; }
constexpr Op opW(int n, int i) { return {O_W, n, i, bar1(n)}; }
constexpr int XR = 11;
constexpr Op opX(int c, int i) { return {O_X, c, i, c == 0 ? -1 : 16 * (c - 1) + XR}; }
constexpr Op mlp_op(int F, int R) {
  if (F < 0) {
    const int i = F + 15;
    if (R != 2 || i < 0 || i >= 13) return NOP_;
    return i < 8 ? opX(0, i) : i < 12 ? opW(0, i - 8) : Op{O_TL, 0, 0, bar1(1)};
  }
  if (F >= 440) {
    const int N = F - 440;
    if (N < 5) return R == 2 ? Op{N < 4 ? O_D2 : O_B2, MLP_AD, N & 3, 440 + MLP_AD * 8 - PD} : N == 0 || N == 2 || N == 4 ? Op{O_B2, MLP_AD, N / 2 + 1, 440 + MLP_AD * 8 - PD} : Op{O_FIN, 2, N / 2, 440 + FLG2 * 8 - PD};
    if (R == 3 && N < 20 && lastfin(N) >= 0) return {O_FIN, 2, lastfin(N), 440 + FLG2 * 8 - PD};
    return p2_op(N, R, 8, MLP_AD, 440);
  }
  if (F >= 432) return NOP_;
  const int RG = (F + PD) / 16, pos = (F + PD) % 16, r = R - 2;
  if (pos == 0) return NOP_;
  if (F < 4) return opX(1, 2 * F + r);
  if (F < 6) return opW(1, 2 * (F - 4) + r);
  if (R == 3) return NOP_;
  if (RG <= 5) {
    const int n = F - 6 - RG, c = 2 + n / 12, i = n % 12;
    const int cap = RG + 3;
    if (RG <= 3) { if (c <= cap) return i < 8 ? opX(c, i) : opW(c, i - 8); return NOP_; }
    if (RG == 4 && pos <= 8) return pos <= 4 ? opX(6, pos + 3) : opW(6, pos - 5);
    if (RG == 4) return opX(7, pos - 9);
    if (pos == 1) return opX(7, 7);
    if (pos <= 5) return opW(7, pos - 2);
    return pos <= 13 ? opX(8, pos - 6) : opW(8, pos - 14);
  }
  if (RG == 6 && pos <= 2) return opW(8, pos + 1);
  const int k8 = pos >= 3 && pos < 15 && (pos - 3) % 3 != 2 ? (pos - 3) - (pos - 3) / 3 : -1;
  if (RG == 6 && k8 >= 0) return opW(9 + k8 / 4, k8 % 4);
  if (RG == 7 && pos <= 12) return opW(pos <= 8 ? 11 + (pos - 1) / 4 : 14, (pos - 1) % 4);
  if (RG == 8 && pos <= 12) return opW(pos <= 4 ? 13 : 14 + (pos - 1) / 4, (pos - 1) % 4);
  if ((RG == 7 || RG == 8) && pos >= 13) return {O_PZ, 3 * (RG - 7) + pos - 13, 0, bar1(FLG0)};
  if (F >= 144 + ST0 && F < 144 + ST0 + 32 && F % 4 == 2) return {O_FIN, 0, (F - 144 - ST0) / 4, bar1(FLG0)};
  if (F >= 288 + ST0 && F < 288 + ST0 + 16 && F % 2 == 0) return {O_FIN, 1, (F - 288 - ST0) / 2, bar1(FLG1)};
  if (RG >= 9 && RG <= 12 && (pos == 9 || pos == 15)) return {O_TW, 2 * (RG - 9) + (pos == 15), 0, NEVER};
  if (RG == 9 && pos >= 6 && pos % 2 == 0 && pos < 14) return opW(17, (pos - 6) / 2);
  if (RG >= 10 && RG <= 12 && pos % 4 == 2) return opW(RG + 8, pos / 4);
  if (RG == 13 && pos <= 4) return opW(21, pos - 1);
  if (RG >= 18 && RG <= 19 && k8 >= 0) return opW(22 + 2 * (RG - 18) + k8 / 4, k8 % 4);
  if (RG == 20 && k8 >= 0) return k8 < 4 ? opW(26, k8) : Op{O_D2, 0, k8 - 4, bar1(27)};
  if (RG >= 21 && RG <= 22 && pos <= 4) return {O_D2, RG - 20, pos - 1, 440 + (RG - 20) * 8 - PD};
  if (RG == 22 && pos == 7) return {O_POLL, 0, 0, bar1(25)};
  if (RG >= 25 && RG <= 26) {
    const int k = (RG - 25) * 15 + pos - 1;
    if (k < 24 && k % 2 == 0) { const int U = k / 8; return {O_B2, U, (k / 2) % 4, U == 0 ? bar1(27) : 440 + U * 8 - PD}; }
  }
  return NOP_;
}
constexpr int barA(int n) { return n == 0 ? -1 : n <= 9 ? 16 * n - PD : 144 + 14 * (n - 9) - PD; }
constexpr int rgA(int F) { return F + PD < 144 ? (F + PD) / 16 : 9 + (F + PD - 144) / 14; }
constexpr int posA(int F) { return F + PD < 144 ? (F + PD) % 16 : (F + PD - 144) % 14; }
struct AttSeq {
  Op op[270] = {};
  constexpr AttSeq() {
    constexpr int seq[27][2] = {{O_X, 2}, {O_W, 2}, {O_HX, 1}, {O_X, 3}, {O_W, 3}, {O_HX, 2}, {O_W, 4}, {O_X, 4}, {O_HX, 3}, {O_HX, 4}, {O_W, 5}, {O_X, 5}, {O_W, 6}, {O_X, 6},
                                {O_W, 7}, {O_X, 7}, {O_W, 8}, {O_X, 8}, {O_W, 9}, {O_W, 10}, {O_W, 11}, {O_W, 12}, {O_W, 13}, {O_W, 14}, {O_W, 15}, {O_W, 16}, {O_W, 17}};
    int F = 6;
    for (int e = 0; e < 27; ++e) {
      const int k = seq[e][0], a = seq[e][1], n = k == O_X ? 8 : k == O_W ? 4 : 1, rg = k == O_X ? a - 2 : k == O_W ? a - 3 : 0;
      for (int i = 0; i < n; ++i) {
        while (posA(F) == 0 || rgA(F) < rg || (F >= 130 && F < 148)) ++F;
        op[F++] = k == O_X ? opX(a, i) : k == O_W ? Op{O_W, a, i, barA(a)} : Op{O_HX, a, 0, barA(a < 3 ? 2 * a : 5)};
      }
    }
    int z = 0;
    for (int G = 150; G < 270; ++G) {
      const int RG = rgA(G), pos = posA(G);
      if (RG >= 10 && RG <= 14 && (pos == 7 || pos == 10) && z < 9) op[G] = {O_PZ, z++, 0, barA(16)};
      if (RG == 16 && pos >= 2 && pos < 8) op[G] = {O_D2, 0, pos - 2, NEVER};
    }
  }
};
constexpr AttSeq ATT_SEQ{};
constexpr Op att1_op(int F, int R) {
  if (F < 0) {
    const int i = F + 16;
    if (R != 2 || i < 0 || i >= 14) return NOP_;
    return i < 8 ? opX(0, i) : i < 12 ? Op{O_W, 0, i - 8, -1} : i == 12 ? Op{O_HX, 0, 0, -1} : Op{O_TL, 0, 0, NEVER};
  }
  if (F < 4) return opX(1, 2 * F + R - 2);
  if (F < 6) return {O_W, 1, 2 * (F - 4) + R - 2, barA(1)};
  return R == 2 && F < 270 ? ATT_SEQ.op[F] : NOP_;
}
constexpr Op att2_op(int F, int R) {
  if (F < 0) {
    const int i = F + 50;
    if (R != 2 || i < 0 || i >= 43) return NOP_;
    constexpr int n1 = 12 - PD, n2 = 24 - PD, ns = FLGA * 12 - PD;
    if (i < 6) return {O_D2, 0, i, -1};
    if (i == 6) return {O_POLL, 0, 0, -28};
    if (i < 13) return {O_D2, 1, i - 7, n1};
    if (i < 16) return {O_ST, 0, 0, ns};
    if (i < 22) return {O_D2, 2, i - 16, n2};
    if (i < 25) return {O_ST, 0, 0, ns};
    if (i < 29) return {O_B2, 0, i - 25, -1};
    if (i < 33) return {O_B2, 1, i - 29, n1};
    if (i < 36) return {O_ST, 0, 0, ns};
    if (i < 40) return {O_B2, 2, i - 36, n2};
    return {O_ST, 0, 0, ns};
  }
  return p2_op(F, R, 12, ATT_AD, 0);
}
template <int WHICH> constexpr Op op_at(int F, int R) { return WHICH == 0 ? mlp_op(F, R) : WHICH == 1 ? att1_op(F, R) : att2_op(F, R); }
template <int WHICH> constexpr int vm_at(int tau) {
  int s = 0;
  for (int F = tau - 1; F >= -64; --F)
    for (int R = 3; R >= 2; --R) {
      const Op o = op_at<WHICH>(F, R);
      if (o.kind == O_NONE) continue;
      if (o.need <= tau || s >= 63) return s;
      ++s;
    }
  return s;
}
template <int WHICH, int TAU> constexpr int VM = vm_at<WHICH>(TAU);

DI void load_epoch(Ctx& c) {
  asm volatile("s_load_dwordx2 %0, %1, %2" : "=s"(c.E) : "s"(c.ws), "s"(WS_E + c.T * 128) : "memory");
}
DI void flag_store(const Ctx& c, unsigned off, unsigned val) {
  const u64 v = c.E + val;
  __builtin_amdgcn_raw_buffer_store_b64(u32x2{(unsigned)v, (unsigned)(v >> 32)}, c.rws, 0, off, 16);
}
DI void poll_issue(const Ctx& c, u32x2& f, unsigned voff, unsigned off) {
  asm volatile("buffer_load_dwordx2 %0, %1, %2, %3 offen sc1" : "=v"(f) : "v"(voff), "s"(c.rws), "s"(off) : "memory");
}
DI bool flag_ok(const Ctx& c, u32x2 f, unsigned need) {
  const u64 d = (((u64)f[1] << 32) | f[0]) - c.E - need;
  return __builtin_amdgcn_ballot_w64(d >= 8) == 0;
}
DI void poll_check(const Ctx& c, u32x2& f, unsigned voff, unsigned off, unsigned need) {
  asm volatile("" : "+v"(f));
  if (__builtin_expect(!flag_ok(c, f, need), 0)) {
    for (unsigned spin = 0; spin < (1u << 16); ++spin) {
      __builtin_amdgcn_s_sleep(1);
      asm volatile("buffer_load_dwordx2 %0, %1, %2, %3 offen sc1\n\ts_waitcnt vmcnt(0)" : "=&v"(f) : "v"(voff), "s"(c.rws), "s"(off) : "memory");
      if (flag_ok(c, f, need)) break;
    }
  }
}

constexpr unsigned POISON = 0xffffffffu;
DI unsigned inbox(const Ctx& c) { return WS_PART + ((c.T * 8 + c.m) * 64 + c.w) * 3072; }
DI unsigned flag2(const Ctx& c) { return WS_F2 + ((c.T * 8 + c.m) * 4 + c.w) * 128; }
template <int NI, int J> DI void poison(const Ctx& c, u32x4& pz, unsigned vl16) {
  if constexpr (J == 0) { pz = u32x4{POISON, POISON, POISON, POISON}; PIN(pz); }
  asm volatile("buffer_store_dwordx4 %0, %1, %2, %3 offen offset:%4 sc1" ::"v"(pz), "v"(vl16), "s"(c.rws), "s"(inbox(c) + (c.a * 4 + ((J / NI + 1) ^ c.a)) * 12288), "i"((J % NI) * 1024) : "memory");
}
template <int NT> struct P2 {
  static constexpr int AD = NT == 8 ? MLP_AD : ATT_AD, NS = NT == 8 ? 8 : 4, FB = NT == 8 ? 440 : 0, ROT = NT == 8 ? 4 : 0;
  static constexpr unsigned SS = NT == 8 ? S2 : S3;
  static constexpr int sidx(int u) { return NT == 8 ? (u + ROT) % NS : u % 4 == 0 ? 2 : u % 4 == 3 ? 3 : u % 4 - 1; }
  static constexpr unsigned slot(int u) { return sidx(u) * SS; }
  static constexpr int WN = NT == 8 ? 3 : 2;
  static constexpr unsigned win(int u) { return (sidx(u) / WN * WN) * SS; }
  unsigned v128[2], v64, vh[2], vf;
  unsigned lw, lw64, sd, so, sh[2];
  unsigned f1, cur, curh;
  bf16x8 bf[21][4];
  u32x2 pf;
  f32x4 omg;
  DI void init(const Ctx& c) {
    const unsigned l = lane_id(), colb = NT == 8 ? 256 * (c.m >> 1) : 768, kh = c.w >> 1;
    const unsigned lo = 16 * ((l & 15) ^ ((l >> 3) & 2) ^ (4 * (c.a ^ 3)));
    v128[0] = (l >> 4) * 4608 + lo; v128[1] = (l >> 4) * 4608 + (lo ^ 64);
    v64 = (l >> 3) * 2304 + 16 * ((l & 7) ^ (((l >> 1) & 4) | ((l >> 3) & 2)) ^ (2 * (c.a ^ 3))) + 256;
    vh[0] = l * 16 + kh * 2048; vh[1] = l * 16 + (1 - kh) * 2048;
    vf = 8 * (l & 7);
    lw = c.w * 4608; lw64 = c.w * 2304 + S2;
    sd = (1152 * c.a + 16 * c.w) * 1152 + colb;
    so = (144 * c.a + 72 * kh + 16 * (c.w & 1)) * 1152 + colb;
    sh[0] = WS_H + (((4 * c.T + 2 * (c.m & 1) + (c.w & 1)) * 4 + c.a) * 42 + kh) * 4096; sh[1] = sh[0] + kh * 8192;
    f1 = WS_F1 + (c.T * 4 + c.a) * 128;
  }
  template <int S> DI unsigned pvo() const { return S < 3 ? (vf < 48 ? vf : 0) : S == 3 ? 48 + (vf & 8) : vf & 24; }
  template <int S> DI void poll(const Ctx& c) { poll_issue(c, pf, pvo<S>(), S < 4 ? f1 : flag2(c)); }
  template <int S> DI void check(const Ctx& c) { poll_check(c, pf, pvo<S>(), S < 4 ? f1 : flag2(c), S < 3 ? S + 1 : S == 3 ? 3 : 1); }
  template <int KIND, int U, int I> DI void prep(const Ctx& c) {
    if constexpr (KIND == O_D2) {
      constexpr int K = uk(U, NT);
      constexpr unsigned row = K < 18 ? 64 * K : 32 * (K - 18);
      if constexpr (I == 0) cur = dprep<slot(U), row * 1152>(lw, K < 18 ? sd : so);
      if constexpr (I == 4) dprep_l<slot(U)>(lw64);
    }
    if constexpr (KIND == O_B2 && U < RS && (I == 0 || (NT == 8 && U == AD && I == 1))) { curh = sh[uk(U, NT) >= 18] + (uk(U, NT) < 18 ? 2 * uk(U, NT) : 18 + uk(U, NT)) * 4096; asm volatile("" : "+s"(curh)); }
    if constexpr (KIND == O_B2 && U >= RS && (I & 1) == 0) { curh = sh[0] - (c.w >> 1) * 4096 + (2 * uk(U, NT) + (I >> 1)) * 4096; asm volatile("" : "+s"(curh)); }
  }
  template <int KIND, int U, int I> DI void fire(const Ctx& c) {
    if constexpr (KIND == O_D2) dfire<(I & 3) * 1152>(I < 4 ? v128[I >> 1] : v64, uk(U, NT) < 18 ? c.rwd : c.rwo, cur);
    if constexpr (KIND == O_B2) ldh<(I & 1) * 1024>(bf[U][I], vh[U < RS ? I >> 1 : 0], c.rws, curh);
    if constexpr (KIND == O_POLL) poll<U>(c);
    if constexpr (KIND == O_GATE) ldg(omg, vh[0] & 255, c.rws, WS_G + (c.a * 32 + 4 * c.T + 2 * (c.m & 1) + (c.w & 1)) * 256);
  }
  static constexpr int tp(int t) { return NT == 8 || t < 6 ? t : t < 9 ? t + 2 : t < 11 ? t - 3 : 11; }
  static DI unsigned rbase(const Ctx& c, unsigned l, int t) {
    const unsigned g = l >> 4, q = (l >> 2) & 3;
    return (c.w >> 1) * 9216 + q * 1152 + g * 256 + 32 * (t ^ ((q & 2) | (g & 1))) + 8 * (l & 3);
  }
};

template <int NT, class Extra> DI void phase2(Ctx& c, P2<NT>& p, bf16x8 (&f)[16], Extra&& extra) {
  using P = P2<NT>;
  constexpr int AD = P::AD, WH = NT == 8 ? 0 : 2;
  const unsigned l = lane_id(), g = l >> 4, q = (l >> 2) & 3, kh = c.w >> 1;
  unsigned rb[4], rb64[4];
#pragma unroll
  for (int t = 0; t < 4; ++t) {
    rb[t] = P::win(0) + P::rbase(c, l, t); PIN(rb[t]);
    if (NT != 8) { rb64[t] = P::win(0) + S2 + kh * 4608 + (q & 1) * 1152 + (2 * g + (q >> 1)) * 128 + 32 * (t ^ ((q & 2) | (g & 1))) + 8 * (l & 3); PIN(rb64[t]); }
  }
  f32x4 acc[NT][4], oth[NT][2];
  bf16x8 f2[NT];
  s16x4 flo;
  auto xoff = [](int t) { return NT == 8 ? 20480 + t * 8192 : t < 6 ? t * 8192 : t < 9 ? (t - 6) * 8192 : 28672 + (t - 9) * 8192; };
  unsigned wa[2] = {0, 0}, ra[2] = {0, 0}, l16 = 0;
  const unsigned pb = inbox(c);
  auto rd = [&](auto U_, auto T_, auto H_) {
    constexpr int t = P::tp(CI(T_)), H = CI(H_);
    constexpr unsigned o = P::slot(CI(U_)) - P::win(CI(U_));
    return t < 8 ? trd(rb[t & 3] + o + 128 * (t >> 2) + H * 4608) : trd(rb64[t & 3] + o + H * 2304);
  };
  if constexpr (NT != 8) {
    wait_bar<VM<2, -1>>();
    tieA(p.bf[0][0], p.bf[0][1], p.bf[0][2], p.bf[0][3]);
    static_for<PD>([&](auto T_) { f[CI(T_)] = cat(rd(IC(0), T_, IC(0)), rd(IC(0), T_, IC(1))); });
  }
  auto send = [&](auto T_, auto R_) {
    constexpr int t = CI(T_), R = CI(R_), it = P::tp(t), dq = it < 8 ? (it >> 1) ^ 3 : (it - 8) ^ 3, idx = it < 8 ? it & 1 : 2;
    if constexpr (R < 2) { acc[t][R] += oth[t][R]; PIN(acc[t][R]); }
    if constexpr (R == 2 && dq != 0) {
      const u32x4 v = {cvtpk(acc[t][0][0], acc[t][0][1]), cvtpk(acc[t][0][2], acc[t][0][3]), cvtpk(acc[t][1][0], acc[t][1][1]), cvtpk(acc[t][1][2], acc[t][1][3])};
      st16(__builtin_elementwise_min(v, u32x4{POISON - 1, POISON - 1, POISON - 1, POISON - 1}), c.rws, l16 + idx * 1024, pb + ((dq ^ c.a) * 4 + c.a) * 12288);
    }
  };
  static_for<21>([&](auto U_) {
    static_for<NT>([&](auto T_) {
      constexpr int U = CI(U_), t = CI(T_), N = U * NT + t, UN = (N + PD) / NT, tn = (N + PD) % NT;
      constexpr int RR = tn == 0 ? 2 : 0;
      const bf16x8 a = f[t], a2 = f2[t];
      static_for<4>([&](auto R_) {
        constexpr int R = CI(R_);
        constexpr int rs = (t & 1) ? 3 - R : R, sk = (R == 1 || R == 2);
        if constexpr (U < RS) mf<true, U == 0>(acc[t][rs], a, p.bf[U][rs]); else mf<true, false>(acc[t][R >> 1], sk ? a2 : a, p.bf[U][2 * sk + (R >> 1)]);
        SBAR();
        if constexpr (tn == 0 && UN <= 20) {
          if constexpr (R < 2 && (P::win(UN) != P::win(UN - 1) || UN == RS)) {
            const unsigned d = P::win(UN) - P::win(UN - 1);
#pragma unroll
            for (int i = 2 * R; i < 2 * R + 2; ++i) {
              rb[i] += d - (UN == RS ? kh * 9216 : 0); PIN(rb[i]);
              if (NT != 8) { rb64[i] += d - (UN == RS ? kh * 4608 : 0); PIN(rb64[i]); }
            }
          }
          if constexpr (R == 2) {
            if constexpr (NT == 8 && UN % 5 == 1) STUB<13 + UN / 5>();
            if constexpr (NT == 8 && UN == 20) STUB<17>();
            if constexpr (NT != 8 && UN % 3 == 2) STUB<46 + UN / 3>();
            wait_bar<(UN == (NT == 8 ? FLG2 : FLGA) ? 0 : VM<WH, P::FB + N>)>();
            tieA(p.bf[UN][0], p.bf[UN][1], p.bf[UN][2], p.bf[UN][3]);
          }
        }
        if constexpr (UN < RS || (UN <= 20 && tn != 0)) {
          if constexpr (R == RR) flo = rd(IC(UN), IC(tn), IC(0));
          if constexpr (R == RR + 1) f[tn] = cat(flo, rd(IC(UN), IC(tn), IC(1)));
          if constexpr (UN >= RS && R == 2) flo = rd(IC(UN), IC(tn), IC(2));
          if constexpr (UN >= RS && R == 3) f2[tn] = cat(flo, rd(IC(UN), IC(tn), IC(3)));
        } else if constexpr (UN <= 20) {
          if constexpr (R == 2) f[0] = cat(rd(IC(UN), IC(0), IC(0)), rd(IC(UN), IC(0), IC(1)));
          if constexpr (R == 3) f2[0] = cat(rd(IC(UN), IC(0), IC(2)), rd(IC(UN), IC(0), IC(3)));
        }
        SBAR();
        if constexpr (tn == 0 && R == 3 && UN <= 20) {
          if constexpr (UN == (NT == 8 ? FLG2 : FLGA)) { if (threadIdx.x == 0) flag_store(c, p.f1 + 8 * c.m, 3); }
          if constexpr (dep(UN + AD, NT) >= 0) p.template check<dep(UN + AD, NT)>(c);
          if constexpr (UN == 17) p.template check<4>(c);
          if constexpr (UN == 18) {
            PIN(p.omg);
            l16 = lane_id() * 16; PIN(l16);
            wa[0] = l16 + c.w * 2048 + (NT == 8 ? 0 : 4 * S3); wa[1] = l16 + c.w * 2048 + (NT == 8 ? 32768 : 0); PIN(wa[0]); PIN(wa[1]);
          }
          if constexpr (UN == 20) { const unsigned d = kh ? 0u - 4096 : 4096; ra[0] = wa[0] + d; ra[1] = wa[1] + d; PIN(ra[0]); PIN(ra[1]); }
        }
        extra(IC(N), R_);
        {
          constexpr int gi = N - (RS - 1) * NT - 2, wi = gi < 0 ? -1 : gi < 5 ? (R < 2 ? 2 * gi + R : -1) : (R == 0 ? gi + 5 : -1);
          if constexpr (wi >= 0 && wi < 2 * NT) {
            constexpr int tt = wi / 2, hi = NT == 8 ? tt >= 4 : tt >= 6;
            ldsw<f32x4>(wa[hi] + xoff(tt) - (NT == 8 && hi ? 32768 : 0) + (wi & 1) * 1024, acc[tt][2 + (wi & 1)]);
          }
          constexpr int rt_ = N - (20 * NT - 2);
          if constexpr (rt_ >= 0 && rt_ < NT && R < 2) {
            constexpr int hi = NT == 8 ? rt_ >= 4 : rt_ >= 6;
            oth[rt_][R] = ldsr<f32x4>(ra[hi] + xoff(rt_) - (NT == 8 && hi ? 32768 : 0) + R * 1024);
          }
          if constexpr (U == 20 && t >= 2) send(IC(t - 2), R_);
        }
        if constexpr (R >= 2) {
          constexpr Op o = op_at<WH>(P::FB + N, R), n = op_at<WH>(P::FB + N + 1, 2);
          if constexpr (R == 3 || N == 0) p.template prep<o.kind, o.a, o.b>(c);
          p.template fire<o.kind, o.a, o.b>(c);
          if constexpr (R == 3) p.template prep<n.kind, n.a, n.b>(c);
        }
        SBAR();
      });
    });
  });
  STUB<(NT == 8 ? 18 : 53)>();
  const unsigned l2 = l16 >> 4;
  constexpr int NI = NT / 4;
  u32x4 in[3][NI];
  for (unsigned spin = 0; spin < (1u << 16); ++spin) {
    unsigned mx = 0;
#pragma unroll
    for (int s = 1; s < 4; ++s)
#pragma unroll
      for (int i = 0; i < NI; ++i) in[s - 1][i] = __builtin_amdgcn_raw_buffer_load_b128(c.rws, l16 + i * 1024, pb + (c.a * 4 + (s ^ c.a)) * 12288, 16);
    asm volatile("" ::: "memory");
#pragma unroll
    for (int s = 0; s < 3; ++s)
#pragma unroll
      for (int i = 0; i < NI; ++i) mx = max(max(mx, max(in[s][i][0], in[s][i][1])), max(in[s][i][2], in[s][i][3]));
    if (__builtin_amdgcn_ballot_w64(mx == POISON) == 0) break;
  }
  const unsigned row0 = 256 * c.T + 128 * (c.m & 1) + 64 * (c.w & 1) + 32 * kh;
  const unsigned vo = (l2 & 15) * 2304 + 8 * (l2 >> 4), colb = NT == 8 ? 256 * (c.m >> 1) : 768;
#pragma unroll
  for (int i = 0; i < NI; ++i) {
    const int t = NT - NI + i;
    f32x4 s0 = acc[t][0], s1 = acc[t][1];
    if (t >= NT - 2) { s0 += oth[t][0]; s1 += oth[t][1]; }
#pragma unroll
    for (int s = 0; s < 3; ++s) {
      s0 += f32x4{bflo(in[s][i][0]), bfhi(in[s][i][0]), bflo(in[s][i][1]), bfhi(in[s][i][1])};
      s1 += f32x4{bflo(in[s][i][2]), bfhi(in[s][i][2]), bflo(in[s][i][3]), bfhi(in[s][i][3])};
    }
    s0 *= kh ? p.omg[2] : p.omg[0]; s1 *= kh ? p.omg[3] : p.omg[1];
    const unsigned cb = colb + (i < 2 ? 64 * c.a + 32 * i : 256 + 32 * c.a);
    __builtin_amdgcn_raw_buffer_store_b64(u32x2{cvtpk(s0[0], s0[1]), cvtpk(s0[2], s0[3])}, c.rout, vo, row0 * 1152 + cb, 0);
    __builtin_amdgcn_raw_buffer_store_b64(u32x2{cvtpk(s1[0], s1[1]), cvtpk(s1[2], s1[3])}, c.rout, vo, (row0 + 1) * 1152 + cb, 0);
  }
  if (NT == 8 && c.a == 0 && c.m == 0 && c.w == 0 && l2 == 0) flag_store(c, WS_E + c.T * 128, 16);
  if constexpr (NT == 8) asm volatile(".Lgm_code1:" ::: "memory"); else asm volatile(".Lgm_code2:" ::: "memory");
}

struct XS {
  unsigned v[2], lw, sx, rd[2], cur, sg;
  DI void init(const Ctx& c, unsigned base) {
    const unsigned l = lane_id(), j = l & 15, g = l >> 4;
    v[0] = (l >> 3) * 2304 + 16 * ((l & 7) ^ g); v[1] = (l >> 3) * 2304 + 16 * ((l & 7) ^ (4 + g));
    lw = base + c.w * 8192; sx = (256 * c.T + 64 * c.w) * 1152;
    rd[0] = lw + j * 128 + 16 * (g ^ (j >> 1)); rd[1] = rd[0] ^ 64;
    PIN(rd[0]); PIN(rd[1]);
  }
  template <int NSL, int C, int I> DI void prep(const Ctx& c) {
    if constexpr (I == 0) sg = sx + c.kg[C] * 128;
    cur = dprep<(C % NSL) * 32768 + I * 1024, (32 * (I >> 2) + 16 * (I & 1) + ((I >> 1) & 1)) * 1152>(lw, sg);
  }
  template <int I> DI void fire(const Ctx& c) { dfire<0>(v[I & 1], c.rx, cur); }
};
template <unsigned LA> DI void touch_pages(const Ctx& c) {
  const unsigned l = lane_id(), own = c.m < 6 ? 2 * c.m : 18 + 3 * c.m;
  const unsigned o = l < 4 ? WS_H + (((4 * c.T + l) * 4 + c.a) * 42 + own) * 4096 : l < 8 ? WS_PART + ((c.T * 8 + c.m) * 64 + (l - 4) * 20) * 3072 : l < 9 ? WS_G + 32768 : WS_F1 + 4096;
  touch<LA>(c.w < 2 ? o : (256 * c.T + 64 * (l & 3)) * 1152, c.w < 2 ? c.rws : c.rout, 0);
}
template <int I> DI void touch_w2(const Ctx& c, unsigned v128) {
  const unsigned idx = (c.T & 3) * 6 + c.m;
  const unsigned so = I < 7 ? (1152 * c.a + 48 * idx) * 1152 + c.w * 2048 + I * 8192 : (144 * c.a + 7 * idx) * 1152 + c.w * 2048;
  asm volatile("s_mov_b32 m0, %3\n\ts_mov_b64 exec, 0xffff\n\tbuffer_load_dword %0, %1, %2 offen sc1 lds\n\ts_mov_b64 exec, -1"
               ::"v"(v128), "s"(I < 7 ? c.rwd : c.rwo), "s"(so), "i"(9 * S1) : "memory");
}

constexpr int slot1(int n) { return n < 9 ? n % 4 : n % 9 < 5 ? 4 + n % 9 : n % 9 % 4; }
constexpr int sig_idx(int F) {
  if ((F + PD) % 16 == 0) return -1;
  int e = 0;
  for (int G = F - F % 72; G < F; ++G) if ((G + PD) % 16 != 0) ++e;
  return e;
}
constexpr int w_ks(int n, int I) { return n < 9 ? 2 * n + (I & 1) : ((n % 9) * 4 + I) % 18; }
constexpr int w_up(int n, int I) { return n < 9 ? I >> 1 : (n % 9) * 4 + I >= 18; }
DI void role_mlp(Ctx& c, XS& xs) {
  const unsigned l = lane_id(), g = l >> 4, q = (l & 15) >> 2, pp = l & 3, r8 = l >> 3;
  const unsigned vW = (8 * (r8 >> 2) + (r8 & 3)) * 9216 + 16 * ((l & 7) ^ (((r8 >> 1) & 3) << 1));
  const unsigned lw = c.w * 1024, sW = (16 * (c.w & 1) + 4 * (c.w >> 1)) * 9216 + (1152 * c.a + 64 * c.m) * 2;
  unsigned sWk[9];
#pragma unroll
  for (int i = 0; i < 9; ++i) sWk[i] = sW + c.kg[i] * (64 * 9216);
  const unsigned sH = WS_H + (((4 * c.T + c.w) * 4 + c.a) * 42 + 2 * c.m) * 4096;
  const unsigned f1 = WS_F1 + (c.T * 4 + c.a) * 128;
  bf16x8 xf[18][4];
  f32x4 ag0[4][4], ag[4][4], au[4][4];
  const float nl2e = -1.4426950408889634f;
  unsigned sp[4][4][2];
  P2<8> p2;
  u32x4 hq[8], pz;
  unsigned vl16 = l * 16;
  unsigned cur = 0;
  auto prep = [&](auto F_, auto R_) {
    constexpr Op o = mlp_op(CI(F_), CI(R_));
    if constexpr (o.kind == O_W) {
      constexpr int pn = o.a / 9, k = w_ks(o.a, o.b);
      cur = dprep<slot1(o.a) * S1 + o.b * 4096, (k & 1) * 32 * 9216 + pn * 768>(lw, sWk[k >> 1]);
    } else if constexpr (o.kind == O_X) xs.template prep<3, o.a, o.b>(c);
    else p2.template prep<o.kind, o.a, o.b>(c);
  };
  auto fire = [&](auto F_, auto R_) {
    constexpr Op o = mlp_op(CI(F_), CI(R_));
    if constexpr (o.kind == O_W) dfire<0>(vW, w_up(o.a, o.b) ? c.rwu : c.rwg, cur);
    else if constexpr (o.kind == O_X) {
      if constexpr (o.a >= 3 && o.b == 0) asm volatile("s_waitcnt lgkmcnt(4)" ::: "memory");
      xs.template fire<o.b>(c);
    } else if constexpr (o.kind == O_FIN) {
      __builtin_amdgcn_raw_buffer_store_b128(hq[o.b], c.rws, vl16 + (o.b & 3) * 1024, sH + (12 * o.a + (o.b >> 2)) * 4096, 16);
      asm volatile("" ::: "memory");
    } else if constexpr (o.kind == O_PZ) poison<2, o.a>(c, pz, vl16);
    else if constexpr (o.kind == O_TW) touch_w2<o.a>(c, vl16 * 8);
    else if constexpr (o.kind == O_TL) touch_pages<3 * S1>(c);
    else p2.template fire<o.kind, o.a, o.b>(c);
  };
  static_for<5>([&](auto I_) { prep(IC(CI(I_) - 7), IC(2)); fire(IC(CI(I_) - 7), IC(2)); });
  visit_all<0, 19>(c.pc0, c.w);
  load_epoch(c);
  unsigned rdx2[2] = {xs.rd[0] + 65536, xs.rd[1] + 65536}; PIN(rdx2[0]); PIN(rdx2[1]);
  auto rdx = [&](auto C_, auto I_) {
    constexpr int C = CI(C_), s = CI(I_) >> 2, rt = CI(I_) & 3;
    xf[2 * C + s][rt] = ldsr<bf16x8>((C % 3 == 2 ? rdx2[s] : xs.rd[s] + (C % 3) * 32768) + rt * 2048);
  };
  unsigned rb[4];
#pragma unroll
  for (int t = 0; t < 4; ++t) rb[t] = (4 * g + q) * 128 + 32 * (t ^ ((2 * g + (q >> 1)) & 3)) + 8 * pp;
  float e0 = 0.f, e1 = 0.f, h0 = 0.f, h1 = 0.f;
  unsigned e2 = 0;
  bf16x8 f[16];
  s16x4 flo;
  unsigned rb2[3];
  wait_bar<VM<0, -1>>();
  asm volatile("" : "+s"(c.E));
  rdx(IC(0), IC(0)); rdx(IC(0), IC(4));
  static_for<PD>([&](auto I_) { constexpr unsigned o = (CI(I_) & 1) * 4096; f[CI(I_)] = cat(trd(rb[CI(I_) >> 1] + o), trd(rb[CI(I_) >> 1] + o + 2048)); });
  static_for<6>([&](auto I_) { rdx(IC(0), IC(1 + CI(I_) / 2 + 4 * (CI(I_) & 1))); });
  SBAR();
  static_for<27>([&](auto N_) {
    static_for<16>([&](auto I_) {
      constexpr int N = CI(N_), I = CI(I_), F = N * 16 + I, FP = F % 144, ps = w_up(N, 2 * (I >> 3)), k0 = w_ks(N, 2 * (I >> 3)), t = (I >> 1) & 3;
      constexpr int RG = (F + PD) / 16, pos = (F + PD) % 16, RR = pos == 0 ? 2 : 0;
      constexpr unsigned ro = (slot1(RG) % 4) * S1 + (2 * (pos >> 3) + (pos & 1)) * 4096;
      const bf16x8 a[2] = {f[I & ~1], f[I | 1]};
      static_for<4>([&](auto R_) {
        constexpr int M = CI(R_), R = M == 1 ? 2 : M == 2 ? 1 : M;
        constexpr int rt0 = 2 * (I & 1) + (M >> 1), sk = (M == 1 || M == 2), ks = k0 + sk;
        mf<(ks < 16), (k0 == 0 && (M & 1) == 0)>(ps == 1 ? au[t][rt0] : F < 144 ? ag0[t][rt0] : ag[t][rt0], a[sk], xf[ks][rt0]);
        SBAR();
        if constexpr (R == 0 && N < 8 && I == XR) wait_vm<VM<0, F>>();
        if constexpr (pos == 0) {
          if constexpr (R < 2 && RG < 27 && slot1(RG) / 4 != slot1(RG - 1) / 4) {
#pragma unroll
            for (int i = 2 * R; i < 2 * R + 2; ++i) { rb[i] += (unsigned)(slot1(RG) / 4 - slot1(RG - 1) / 4) * 4 * S1; PIN(rb[i]); }
          }
          if constexpr (R == 0 && RG == 27) {
            const unsigned l4 = lane_id();
#pragma unroll
            for (int i = 0; i < 3; ++i) { rb2[i] = P2<8>::slot(0) + P2<8>::rbase(c, l4, i); PIN(rb2[i]); }
          }
          if constexpr (R == 2) {
            if constexpr (RG % 2 == 0) STUB<RG / 2 - 1>();
            static_assert((RG != FLG0 && RG != FLG1) || VM<0, F> == 0, "flags follow a full drain");
            wait_bar<VM<0, F>>();
            if constexpr (RG == 27) tieA(p2.bf[0][0], p2.bf[0][1], p2.bf[0][2], p2.bf[0][3]);
          }
        }
        if constexpr (R == RR) { if constexpr (RG < 27) flo = trd(rb[(pos >> 1) & 3] + ro); else flo = trd(rb2[pos]); }
        if constexpr (R == RR + 1) { if constexpr (RG < 27) f[pos] = cat(flo, trd(rb[(pos >> 1) & 3] + ro + 2048)); else f[pos] = cat(flo, trd(rb2[pos] + 4608)); }
        if constexpr (N < 8 && (I == XR || I == XR + 1)) rdx(IC(N + 1), IC(4 * (I - XR) + R));
        SBAR();
        if constexpr (pos == 0 && R == 3) {
          if constexpr (RG == FLG0 || RG == FLG1) { if (threadIdx.x == 0) flag_store(c, f1 + 8 * c.m, RG == FLG0 ? 1 : 2); }
          if constexpr (RG == PZR + 1) keep(pz);
          if constexpr (RG == FLG0) { if (vl16 == 0) flag_store(c, flag2(c) + 8 * c.a, 1); }
          if constexpr (RG == 19) p2.init(c);
          if constexpr (RG == 25) p2.template check<0>(c);
        }
        if constexpr (R >= 2) { if constexpr (R == 3 || F == 0) prep(IC(F), IC(R)); fire(IC(F), IC(R)); }
        if constexpr (F >= 144 && ps == 1 && sig_idx(F) >= 0 && sig_idx(F) <= 65) {
          constexpr int e = sig_idx(F), d = e - 2;
          if constexpr (R == 0 && d >= 0) e2 = w_add1(e1);
          if constexpr (R == 1 && e < 64) e0 = w_mul(ag[e / 16][(e / 4) % 4][e % 4]);
          if constexpr (R == 2 && e >= 1 && e <= 64) e1 = w_exp(e0);
          if constexpr (R == 3 && d >= 0) { if constexpr (d % 2 == 0) sp[d / 16][(d / 4) % 4][(d % 4) / 2] = w_rcplo(e2); else w_rcphi(sp[d / 16][(d / 4) % 4][(d % 4) / 2], e2); }
        }
        if constexpr (F >= 134 && F < 142) {
          constexpr int tt = (F - 134) / 2, rt = 2 * (F & 1) + (R >> 1), k = R & 1;
          sp[tt][rt][k] = w_pklo(ag0[tt][rt][2 * k], nl2e); w_pkhi(sp[tt][rt][k], ag0[tt][rt][2 * k + 1], nl2e);
        }
        if constexpr (F >= 144 && F < 216 && sig_idx(F) >= 0) {
          constexpr int e = sig_idx(F), d = e - 2, pa = (e - 2) / 2, pf = (e - 4) / 2;
          if constexpr (R == 0 && e % 2 == 0 && e >= 2 && pa < 32) w_add1h(sp[pa / 8][(pa / 2) % 4][pa % 2]);
          if constexpr (R == 0 && e % 2 == 1 && e >= 5) h1 = w_mixhi(au[pf / 8][(pf / 2) % 4][2 * (pf % 2) + 1], sp[pf / 8][(pf / 2) % 4][pf % 2]);
          if constexpr (R == 1 && e % 2 == 0 && e >= 4) h0 = w_mixlo(au[pf / 8][(pf / 2) % 4][2 * (pf % 2)], sp[pf / 8][(pf / 2) % 4][pf % 2]);
          if constexpr (R == 1 && e % 2 == 1 && e >= 5) hq[(pf / 16) * 4 + (pf / 2) % 4][2 * ((pf / 8) & 1) + pf % 2] = w_cvt(h0, h1);
          if constexpr (R == 2 && e < 64) w_exph<e & 1>(sp[e / 16][(e / 4) % 4][(e % 4) / 2]);
          if constexpr (R == 3 && d >= 0 && d < 64) w_rcph<d & 1>(sp[d / 16][(d / 4) % 4][(d % 4) / 2]);
        }
        if constexpr (F >= 288 && FP >= FIN0 && FP < FIN0 + 32) {
          constexpr int J = (FP - FIN0) / 4, i = FP % 4, e = J >> 2, rt = J & 3, tt = 2 * e + (i >> 1), k = 2 * (i & 1);
          if constexpr (R == 0) h0 = w_mixlo(au[tt][rt][k], sp[tt][rt][i & 1]);
          if constexpr (R == 1) h1 = w_mixhi(au[tt][rt][k + 1], sp[tt][rt][i & 1]);
          if constexpr (R == 3) hq[J][i] = w_cvt(h0, h1);
        }
        if constexpr (R == 3 && mlp_op(F, 2).kind == O_FIN) keep(hq[mlp_op(F, 2).b]);
        if constexpr (R == 3 && F < 431) prep(IC(F + 1), IC(2));
        SBAR();
      });
    });
  });
  phase2<8>(c, p2, f, [&](auto N_, auto R_) {
    constexpr int N = CI(N_), R = CI(R_);
    if constexpr (N < 16 && R < 3) {
      constexpr int J = N / 2, e = J >> 2, rt = J & 3, tt = 2 * e + (N & 1);
      if constexpr (R == 0) { h0 = w_mixlo(au[tt][rt][0], sp[tt][rt][0]); h1 = w_mixhi(au[tt][rt][1], sp[tt][rt][0]); }
      if constexpr (R == 1) { e0 = w_mixlo(au[tt][rt][2], sp[tt][rt][1]); e1 = w_mixhi(au[tt][rt][3], sp[tt][rt][1]); }
      if constexpr (R == 2) { hq[J][2 * (N & 1)] = w_cvt(h0, h1); hq[J][2 * (N & 1) + 1] = w_cvt(e0, e1); }
    }
    if constexpr (R == 3 && N < 20 && lastfin(N) >= 0) {
      constexpr int J = lastfin(N);
      __builtin_amdgcn_raw_buffer_store_b128(hq[J], c.rws, vl16 + (J & 3) * 1024, sH + (24 + (J >> 2)) * 4096, 16);
      asm volatile("" ::: "memory");
    }
    if constexpr (R == 1 && N >= 1 && N < 21 && lastfin(N - 1) >= 0) keep(hq[lastfin(N - 1)]);
  });
}

DI u32x2 pk4(const f32x4& v) { return u32x2{cvtpk(v[0], v[1]), cvtpk(v[2], v[3])}; }
DI void sw16(float& a, float& b) { auto r = __builtin_amdgcn_permlane16_swap(__float_as_uint(a), __float_as_uint(b), false, false); a = __uint_as_float(r[0]); b = __uint_as_float(r[1]); }
DI void sw32(float& a, float& b) { auto r = __builtin_amdgcn_permlane32_swap(__float_as_uint(a), __float_as_uint(b), false, false); a = __uint_as_float(r[0]); b = __uint_as_float(r[1]); }
template <bool MX> DI float xred(float (&v)[4]) {
  sw16(v[0], v[1]); sw16(v[2], v[3]);
  float a = MX ? max2(v[0], v[1]) : v[0] + v[1], b = MX ? max2(v[2], v[3]) : v[2] + v[3];
  sw32(a, b);
  return MX ? max2(a, b) : a + b;
}
DI void xbc(float t, float (&v)[4]) {
  float a = t, b = t;
  sw32(a, b);
  v[0] = a; v[1] = a; v[2] = b; v[3] = b;
  sw16(v[0], v[1]); sw16(v[2], v[3]);
}

DI void role_att(Ctx& c, XS& xs) {
  const unsigned mp = c.m & 1;
  u32x2 vp[4][5], qp[4][5];
  u32x4 kt[6][4];
  f32x4 gate, bprev, bown, bprev0;
  unsigned mpv[2];
  P2<12> p2;
  {
    const unsigned l = lane_id(), g = l >> 4, q = (l & 15) >> 2, pp = l & 3;
    const unsigned lc = (l & 15) ^ (g << 1), hb = 144 * (2 * c.a + mp);
    const unsigned vA = g * 3456 + hb + (lc < 10 ? 1152 + 16 * (lc < 9 ? lc : 8) : 2304 + 16 * (lc - 10));
    const unsigned vB = g * 3456 + hb + (lc < 4 ? 2304 + 16 * (lc < 3 ? 6 + lc : 8) : 16 * (lc - 4));
    const u64 maskB = __builtin_amdgcn_ballot_w64(lc < 13);
    const unsigned lw = c.w * 1152;
    unsigned sWk[9];
#pragma unroll
    for (int i = 0; i < 9; ++i) sWk[i] = (8 * c.w + 64 * c.kg[i]) * 3456;
    const int hr = 256 * (int)c.T + ((c.w & 1) ? -16 : -15) + 2 * (int)(l >> 3);
    const unsigned vHx = (unsigned)(hr < 0 ? 0 : hr) * 1152 + 16 * ((l & 7) ^ (4 * (c.w & 1) + g));
    const unsigned lHx = A_HX + c.w * 1024;
    unsigned rh[2] = {A_HX + (l & 15) * 128 + 16 * (g ^ ((l & 15) >> 1)), 0}; rh[1] = rh[0] ^ 64; PIN(rh[0]); PIN(rh[1]);
    bf16x8 xf[18][4];
    unsigned cur = 0, l8 = 0;
    u32x4 pz;
    auto prep = [&](auto F_, auto R_) {
      constexpr Op o = att1_op(CI(F_), CI(R_));
      if constexpr (o.kind == O_W) cur = dprep<(o.a % 4) * S2 + o.b * 4608, (32 * (o.b >> 1) + 4 * (o.b & 1)) * 3456>(lw, sWk[o.a % 9]);
      else if constexpr (o.kind == O_X) xs.template prep<2, o.a, o.b>(c);
      else if constexpr (o.kind == O_HX) {
        if constexpr (o.a < 4) cur = dprep<o.a * 4096, 0>(lHx, (c.w < 2 ? c.kg[2 * o.a] : c.kg[2 * o.a + 1]) * 128);
        else cur = dprep<16384, 0>(lHx, c.w < 2 ? c.kg[8] * 128 : (c.w & 1) * 1024);
      } else p2.template prep<o.kind, o.a, o.b>(c);
    };
    auto fire = [&](auto F_, auto R_) {
      constexpr Op o = att1_op(CI(F_), CI(R_));
      if constexpr (o.kind == O_W) { if constexpr (o.a < 9) dfire<0>(vA, c.rqkv, cur); else dfire_m(vB, c.rqkv, cur, maskB); }
      else if constexpr (o.kind == O_X) {
        if constexpr (o.a >= 2 && o.b == 0) asm volatile("s_waitcnt lgkmcnt(4)" ::: "memory");
        xs.template fire<o.b>(c);
      } else if constexpr (o.kind == O_HX) {
        if constexpr (o.a < 4) dfire<0>(vHx, c.rx, cur); else dfire<0>(c.w < 2 ? vHx : l * 16, c.w < 2 ? c.rx : c.rgw, cur);
      } else if constexpr (o.kind == O_TL) touch_pages<A_GW + 2048>(c);
      else if constexpr (o.kind == O_PZ) poison<3, o.a>(c, pz, 2 * l8);
      else p2.template fire<o.kind, o.a, o.b>(c);
    };
    auto rdx = [&](auto C_, auto I_) {
      constexpr int C = CI(C_), s = CI(I_) >> 2, rt = CI(I_) & 3;
      xf[2 * C + s][rt] = ldsr<bf16x8>(xs.rd[s] + (C % 2) * 32768 + rt * 2048);
    };
    static_for<6>([&](auto I_) { prep(IC(CI(I_) - 8), IC(2)); fire(IC(CI(I_) - 8), IC(2)); });
    visit_all<32, 22>(c.pc0, c.w);
    load_epoch(c);
    unsigned rb[4], rx1, rx2;
#pragma unroll
    for (int t = 0; t < 4; ++t) rb[t] = g * 1152 + q * 256 + 32 * (t ^ q) + 8 * pp;
    rx1 = g * 1152 + q * 256 + 32 * (c.w ^ q) + 8 * pp;
    rx2 = g * 1152 + q * 256 + 128 + 32 * ((c.w < 2 ? c.w + 1 : 3) ^ q) + 8 * pp;
    const unsigned gsrc = A_GW + 2 * (32 * (l >> 5) + 8 * ((l >> 2) & 3) + 4 * ((l >> 4) & 1) + (l & 3));
    const unsigned gdst = (l >> 2) * 1152 + (l & 3) * 256 + 16 * (13 ^ ((l & 3) << 1));
    u16 gwv = 0;
    f32x4 acc[2][8][4], ah[4];
    u32x2 hp;
    unsigned wk = 0, wv = 0;
    bf16x8 f[16], hx[2], ha[2], hc[2];
    s16x4 flo, hlo;
    wait_bar<VM<1, -1>>();
    asm volatile("" : "+s"(c.E));
    rdx(IC(0), IC(0)); rdx(IC(0), IC(4));
    static_for<PD>([&](auto I_) { constexpr unsigned o = (CI(I_) & 1) * 9216; f[CI(I_)] = cat(trd(rb[CI(I_) >> 1] + o), trd(rb[CI(I_) >> 1] + o + 4608)); });
    static_for<6>([&](auto I_) { rdx(IC(0), IC(1 + CI(I_) / 2 + 4 * (CI(I_) & 1))); });
    SBAR();
    auto pass = [&](auto PS_) {
      constexpr int PS = CI(PS_), NT = PS == 0 ? 8 : 7, FC = 2 * NT;
      static_for<9>([&](auto N_) {
        static_for<FC>([&](auto I_) {
          constexpr int N = CI(N_), I = CI(I_), F = 144 * PS + N * FC + I, t = I >> 1;
          constexpr int RG = rgA(F), pos = posA(F);
          constexpr int tn = pos >> 1, RR = pos == 0 ? 2 : 0;
          constexpr unsigned ro = (RG % 2) * S2 + (pos & 1) * 9216 + 128 * (tn >> 2);
          constexpr unsigned rc = ((9 * PS + N) % 2) * S2 + ((I >> 1) & 1) * 9216 + (I & 1) * 4608;
          constexpr bool SW = PS == 0 ? t >= 5 : t < 2;
          const bf16x8 a[2] = {f[I & ~1], f[I | 1]};
          static_for<5>([&](auto R_) {
            constexpr int M = CI(R_), R = M == 1 ? 2 : M == 2 ? 1 : M;
            if constexpr (M < 4) {
              constexpr int rt0 = 2 * (I & 1) + (M >> 1), sk = (M == 1 || M == 2), ks = 2 * N + sk;
              if constexpr (SW) mfs<(ks < 16), (N == 0 && (M & 1) == 0)>(acc[PS][t][rt0], a[sk], xf[ks][rt0]); else mf<(ks < 16), (N == 0 && (M & 1) == 0)>(acc[PS][t][rt0], a[sk], xf[ks][rt0]);
            } else if constexpr (PS == 0) {
              if constexpr (I == 7) { mf<false, N == 0>(ah[0], ha[1], hx[1]); mf<false, false>(ah[0], ha[0], hx[0]); }
              if constexpr (I == 9) { mf<false, N == 0>(ah[1], a[0], hx[0]); mf<false, false>(ah[1], a[1], hx[1]); }
              if constexpr (I == 12) { mfs<false, N == 0>(ah[2], hc[1], hx[1]); mfs<false, false>(ah[2], hc[0], hx[0]); }
            } else if constexpr (I == 7) { mfs<false, N == 0>(ah[3], ha[1], hx[1]); mfs<false, false>(ah[3], ha[0], hx[0]); }
            SBAR();
            if constexpr (R == 0 && PS == 0 && I == XR && N < 8) wait_vm<VM<1, F>>();
            if constexpr (pos == 0 && RG < 18) {
              if constexpr (R < 2 && RG % 2 == 0) {
                constexpr unsigned d = RG % 4 == 0 ? 0u - 2 * S2 : 2 * S2;
#pragma unroll
                for (int i = 2 * R; i < 2 * R + 2; ++i) { rb[i] += d; PIN(rb[i]); }
                if (R == 0) { rx1 += d; PIN(rx1); } else if (RG < 9) { rx2 += d; PIN(rx2); }
              }
              if constexpr (R == 0 && RG == 9) { rx1 ^= 32 * (c.w & 2); PIN(rx1); }
              if constexpr (R == 2) {
                if constexpr (RG % 2 == 1) STUB<32 + RG / 2>();
                static_assert(RG != 16 || VM<1, F> == 0, "flags follow a full drain");
                wait_bar<VM<1, F>>();
              }
            }
            if constexpr (R == RR && RG < 18) flo = trd(rb[tn & 3] + ro);
            if constexpr (R == RR + 1 && RG < 18) f[pos] = cat(flo, trd(rb[tn & 3] + ro + 4608));
            if constexpr (PS == 0 && N < 8 && (I == XR || I == XR + 1) && R < 4) rdx(IC(N + 1), IC(4 * (I - XR) + R));
            if constexpr (I < 2 && R == 3) hx[I] = ldsr<bf16x8>(rh[I] + N * 2048);
            if constexpr (I >= 2 && I < 6 && R == 3) { if constexpr (I % 2 == 0) hlo = trd(rx1 + rc); else ha[(I >> 1) & 1] = cat(hlo, trd(rx1 + rc)); }
            if constexpr (PS == 0 && I >= 6 && I < 10 && R == 3) { if constexpr (I % 2 == 0) hlo = trd(rx2 + rc); else hc[(I >> 1) & 1] = cat(hlo, trd(rx2 + rc)); }
            if constexpr (RG + 3 >= 9 && RG + 3 <= 17 && R == 1) {
              if constexpr (pos == 6) gwv = ldsr<u16>(gsrc + c.kg[RG + 3 - 9] * 128);
              if constexpr (pos == 9) ldsw<u16>(gdst + ((RG + 3) % 4) * S2, gwv);
            }
            SBAR();
            if constexpr (F == 128 && R == 0) { l8 = lane_id() * 8; PIN(l8); wk = A_KX + (4 * c.w + 2) * 2560 + l8; PIN(wk); }
            if constexpr (F >= 130 && F < 146 && (R == 0 || R == 2)) {
              constexpr int tt = (F - 130) / 2, rt = 2 * (F & 1) + R / 2;
              if constexpr (tt < 5) ldsw<u32x2>(wk + rt * 2560 + tt * 512, pk4(acc[0][tt][rt])); else { vp[rt][tt - 5] = pk4(acc[0][tt][rt]); PIN(vp[rt][tt - 5]); }
            }
            if constexpr (F == 146 && R < 2) { const u32x2 h = pk4(ah[R]); const unsigned o = A_KX + (R == 0 ? c.w : 4) * 512; ldsw<u32x2>(o + l8, h); ldsw<u32x2>(o + 2560 + (l8 ^ 64), h); }
            if constexpr (F == 146 && R == 2) { hp = pk4(ah[2]); PIN(hp); }
            if constexpr (pos == 0 && R == 3) {
              if constexpr (RG == 15) { keep(pz); p2.init(c); wv = A_VX + (c.w < 3 ? (c.w + 1) * 5120 : A_GW + 2304 - A_VX) + (c.w < 3 ? l8 : 0); PIN(wv); }
              if constexpr (RG == 16) { if (l8 == 0) flag_store(c, flag2(c) + 8 * c.a, 1); }
            }
            if constexpr (RG == 15 && pos >= 4 && pos < 10 && R == 2) { if (c.w < 3) ldsw<u32x2>(wv + (((pos - 4) / 3) * 5 + (pos - 4) % 3) * 512, vp[2 + (pos - 4) / 3][(pos - 4) % 3]); }
            if constexpr (RG == 15 && pos == 11 && R == 2) { const unsigned o = A_VX + (c.w < 2 ? c.w : 2) * 512; ldsw<u32x2>(o + l8, hp); ldsw<u32x2>(o + 2560 + (l8 ^ 256), hp); }
            if constexpr (RG == 16 && pos >= 9 && pos < 13 && R == 2) {
              constexpr int r = pos - 9;
              const int key = (int)(l8 >> 7) * 4 + r, jj = (int)(l8 >> 3) & 15;
              bprev[r] = key >= jj + 9 ? 0.f : -1e30f;
              bown[r] = (key <= jj && key >= jj - 7) ? 0.f : -1e30f;
              bprev0[r] = (c.T == 0 && c.w == 0) ? -1e30f : bprev[r];
              if constexpr (r % 2 == 0) mpv[r / 2] = key >= jj + 9 ? 0xffffu : 0u; else { mpv[r / 2] |= key >= jj + 9 ? 0xffff0000u : 0u; PIN(mpv[r / 2]); }
              if constexpr (r == 3) { PIN(bprev); PIN(bown); PIN(bprev0); }
            }
            if constexpr (F == 256 && R == 0) { wk -= 5120; PIN(wk); }
            if constexpr (F >= 257 && F < 265 && (R == 0 || R == 1 || R == 3)) {
              constexpr int n = 3 * (F - 257) + (R == 3 ? 2 : R), i = n / 4, h = n % 4;
              const u32x2 k0 = ldsr<u32x2>(wk + i * 2560 + h * 512), k1 = ldsr<u32x2>(wk + i * 2560 + h * 512 + 512);
              kt[i][h] = u32x4{k0[0], k0[1], k1[0], k1[1]};
              asm volatile("" ::: "memory");
            }
            if constexpr (F >= 258 && R == 2) {
              constexpr int tt = (F - 258) / 2;
#pragma unroll
              for (int rt = 2 * (F & 1); rt < 2 * (F & 1) + 2; ++rt) {
                if constexpr (tt < 2) { vp[rt][3 + tt] = pk4(acc[1][tt][rt]); PIN(vp[rt][3 + tt]); }
                else { qp[rt][tt - 2] = pk4(acc[1][tt][rt]); PIN(qp[rt][tt - 2]); }
              }
              if constexpr (tt < 2 && (F & 1)) { if (c.w < 3) { ldsw<u32x2>(wv + (3 + tt) * 512, vp[2][3 + tt]); ldsw<u32x2>(wv + (8 + tt) * 512, vp[3][3 + tt]); } }
            }
            if constexpr (F == 267 && R == 0) { const u32x2 h = pk4(ah[3]); const unsigned o = A_VX + (3 + (c.w & 1)) * 512; ldsw<u32x2>(o + l8, h); ldsw<u32x2>(o + 2560 + (l8 ^ 256), h); }
            if constexpr (R == 2 || R == 3) { if constexpr (R == 3 || F == 0) prep(IC(F), IC(R)); fire(IC(F), IC(R)); }
            if constexpr (R == 4) prep(IC(F + 1), IC(2));
            SBAR();
          });
        });
      });
    };
    pass(IC(0));
    pass(IC(1));
    STUB<41>();
    asm volatile("s_nop 6\n\ts_waitcnt lgkmcnt(0)\n\ts_barrier" ::: "memory");
#pragma unroll
    for (int rt = 0; rt < 4; ++rt) {
      qp[rt][4] = pk4(acc[1][6][rt]);
      gate[rt] = __builtin_amdgcn_exp2f(fminf(1.4426950408889634f * acc[1][6][rt][0], 80.0f));
    }
  }
  auto pre = [&](auto A_, auto N_) { static_for<CI(N_)>([&](auto I_) { constexpr Op o = att2_op(CI(A_) + CI(I_) - 50, 2); p2.template prep<o.kind, o.a, o.b>(c); p2.template fire<o.kind, o.a, o.b>(c); }); };
  const unsigned l = lane_id(), g = (l >> 4) & 3, j = l & 15;
  {
    u32x6 vt[5][2];
    const unsigned ra = A_VX + c.w * 5120 + l * 8;
#pragma unroll
    for (int pa = 0; pa < 2; ++pa)
#pragma unroll
      for (int t = 0; t < 5; ++t) {
        const u32x2 v2 = ldsr<u32x2>(ra + (pa * 5 + t) * 512);
        vt[t][pa] = u32x6{v2[0], v2[1], vp[pa][t][0], vp[pa][t][1], vp[2 + pa][t][0], vp[2 + pa][t][1]};
      }
    const f32x4 omg = {__builtin_amdgcn_rcpf(1.0f + gate[0]), __builtin_amdgcn_rcpf(1.0f + gate[1]), __builtin_amdgcn_rcpf(1.0f + gate[2]), __builtin_amdgcn_rcpf(1.0f + gate[3])};
    if (mp == 0 && g == 2) st16(__builtin_bit_cast(u32x4, omg), c.rws, j * 16, WS_G + (c.a * 32 + 4 * c.T + c.w) * 256);
#pragma unroll
    for (int rt = 0; rt < 4; ++rt) gate[rt] = __shfl(gate[rt], 32 + j);
    constexpr float SC2 = 0.23570226039551584f * 1.4426950408889634f;
    const unsigned sH = WS_H + (((4 * c.T + c.w) * 4 + c.a) * 42 + 36 + 3 * mp) * 4096;
    u32x4 qf[4], P[4];
    f32x4 sp[4], so[4], hi[4], lo[4];
    auto mkq = [&](auto RT_) {
      constexpr int rt = CI(RT_);
#pragma unroll
      for (int h = 0; h < 4; ++h) {
#pragma unroll
        for (int i = 0; i < 4; ++i) { const int col = 16 * (h + (i >> 1)) + 4 * (int)g + 2 * (i & 1); qf[h][i] = (col >= 18 * h && col < 18 * h + 18) ? qp[rt][h + (i >> 1)][i & 1] : 0u; }
        PIN(qf[h]);
      }
    };
    auto qk = [&](auto RT_, auto H_, auto W_) {
      constexpr int rt = CI(RT_), h = CI(H_);
      if constexpr (CI(W_) == 0) sp[h] = mfc(kt[rt][h], qf[h], rt < 2 ? bprev0 : bprev);
      else so[h] = mfc(kt[rt + 2][h], qf[h], bown);
    };
    auto pv = [&](auto RT_, auto T_, auto W_) {
      constexpr int rt = CI(RT_), t = CI(T_) + CI(W_), pa = rt & 1, sn = rt >> 1;
      const f32x4 r = mfz(__builtin_shufflevector(vt[t][pa], vt[t][pa], 2 * sn, 2 * sn + 1, 2 * sn + 2, 2 * sn + 3), P[CI(T_)]);
      if constexpr (CI(W_) == 0) hi[CI(T_)] = r; else lo[CI(T_)] = r;
    };
    auto outp = [&](auto RT_) {
      constexpr int rt = CI(RT_);
      u32x2 o[5];
      PIN(lo[0]);
#pragma unroll
      for (int t = 0; t < 5; ++t)
#pragma unroll
        for (int i = 0; i < 2; ++i) {
          const unsigned vh = t < 4 ? cvtpk(hi[t < 4 ? t : 0][2 * i], hi[t < 4 ? t : 0][2 * i + 1]) : 0u;
          o[t][i] = (4 * (int)g + 2 * i < 2 * t) ? cvtpk(lo[t > 0 ? t - 1 : 0][2 * i], lo[t > 0 ? t - 1 : 0][2 * i + 1]) : vh;
        }
      st16(u32x4{o[0][0], o[0][1], o[1][0], o[1][1]}, c.rws, l * 16 + rt * 1024, sH);
      st16(u32x4{o[2][0], o[2][1], o[3][0], o[3][1]}, c.rws, l * 16 + rt * 1024, sH + 4096);
      st16(u32x4{o[4][0], o[4][1], 0u, 0u}, c.rws, l * 16 + rt * 1024, sH + 8192);
    };
    mkq(IC(0));
    asm volatile("s_nop 1" ::: "memory");
    static_for<8>([&](auto I_) { qk(IC(0), IC(CI(I_) >> 1), IC(CI(I_) & 1)); });
    SBAR();
    mkq(IC(1));
    static_for<4>([&](auto RT_) {
      constexpr int rt = CI(RT_);
      float s[4][4], red[4], bc[4];
      STUB<42 + rt>();
      SBAR();
      static_for<4>([&](auto H_) {
        constexpr int h = CI(H_);
        if constexpr (rt > 0) pv(IC(rt - 1), H_, IC(0));
        s[h][0] = max2(sp[h][0], so[h][0]); s[h][1] = max2(sp[h][1], so[h][1]); s[h][2] = max2(sp[h][2], so[h][2]);
        SBAR();
        if constexpr (rt > 0) pv(IC(rt - 1), H_, IC(1));
        s[h][3] = max2(sp[h][3], so[h][3]);
        red[h] = max2(max3(s[h][0], s[h][1], s[h][2]), s[h][3]);
        SBAR();
      });
      xbc(-SC2 * xred<true>(red), bc);
      if constexpr (rt == 0) pre(IC(6), IC(7));
      if constexpr (rt == 2) { wait_vm<VM<2, -28>>(); p2.template check<0>(c); }
      if constexpr (rt > 0) outp(IC(rt - 1));
      SBAR();
      static_for<4>([&](auto H_) {
        constexpr int h = CI(H_);
        if constexpr (rt < 3) qk(IC(rt + 1), H_, IC(0));
        s[h][0] = __builtin_amdgcn_exp2f(__builtin_fmaf(s[h][0], SC2, bc[h])); s[h][1] = __builtin_amdgcn_exp2f(__builtin_fmaf(s[h][1], SC2, bc[h]));
        SBAR();
        if constexpr (rt < 3) qk(IC(rt + 1), H_, IC(1));
        s[h][2] = __builtin_amdgcn_exp2f(__builtin_fmaf(s[h][2], SC2, bc[h])); s[h][3] = __builtin_amdgcn_exp2f(__builtin_fmaf(s[h][3], SC2, bc[h]));
        SBAR();
      });
#pragma unroll
      for (int h = 0; h < 4; ++h) red[h] = (s[h][0] + s[h][1]) + (s[h][2] + s[h][3]);
      xbc(gate[rt] * __builtin_amdgcn_rcpf(xred<false>(red)), bc);
      if constexpr (rt == 1) pre(IC(16), IC(6));
      if constexpr (rt == 2) pre(IC(25), IC(8));
      if constexpr (rt == 3) pre(IC(36), IC(4));
      if constexpr (rt == 0) {
#pragma unroll
        for (int i = 0; i < 10; ++i) PIN(vt[i >> 1][i & 1]);
      }
      if constexpr (rt < 2) mkq(IC(rt + 2));
#pragma unroll
      for (int h = 0; h < 4; ++h) {
        const unsigned p0 = cvtpk(s[h][0] * bc[h], s[h][1] * bc[h]), p1 = cvtpk(s[h][2] * bc[h], s[h][3] * bc[h]);
        P[h] = u32x4{p0 & mpv[0], p1 & mpv[1], p0 & ~mpv[0], p1 & ~mpv[1]};
        PIN(P[h]);
      }
      asm volatile("s_nop 1" ::: "memory");
    });
    static_for<8>([&](auto I_) { pv(IC(3), IC(CI(I_) >> 1), IC(CI(I_) & 1)); });
    asm volatile("s_nop 7" ::: "memory");
    SBAR();
    outp(IC(3));
  }
  bf16x8 f[16];
  phase2<12>(c, p2, f, [](auto, auto) {});
}

#endif
__global__ __launch_bounds__(256, 1) void fused_kernel(const u16* x, const u16* wqkv, const u16* wo, const u16* wg, const u16* wu, const u16* wd,
                                                        const u16* gw, char* ws, u16* out) {
#if defined(__HIP_DEVICE_COMPILE__)
  __shared__ __attribute__((aligned(16))) char smem[LDS_BYTES];
  asm volatile("" ::"v"((unsigned)(size_t)smem));
  Ctx c;
  const unsigned bid = blockIdx.x, xcd = bid & 7, idx = bid >> 3;
  c.w = __builtin_amdgcn_readfirstlane(threadIdx.x >> 6);
  {
    u64 pc; unsigned len;
    asm volatile("s_getpc_b64 %0\n.Lgm_code0:\n\ts_mov_b32 %1, .Lgm_code1-.Lgm_code0\n\ts_max_u32 %1, %1, .Lgm_code2-.Lgm_code0" : "=s"(pc), "=s"(len) :: "scc");
    c.pc0 = pc;
    asm volatile("s_branch .Lgm_go\n.Lgm_ret:\n\ts_setpc_b64 s[100:101]\n.Lgm_go:");
    const rsrc_t rc = __builtin_amdgcn_make_buffer_rsrc((void*)pc, (short)0, len, 0x00020000);
    if (idx < 16) touch<3 * S1>((threadIdx.x & 15) * 128, rc, idx * 8192 + c.w * 2048);
  }
  asm volatile("" ::"s"(x), "s"(out));
  c.rx = __builtin_amdgcn_make_buffer_rsrc((void*)x, (short)0, SEQ * DM * 2, 0x00020000);
  c.rqkv = __builtin_amdgcn_make_buffer_rsrc((void*)wqkv, (short)0, DM * 3 * DM * 2, 0x00020000);
  c.rwo = __builtin_amdgcn_make_buffer_rsrc((void*)wo, (short)0, DM * DM * 2, 0x00020000);
  c.rwg = __builtin_amdgcn_make_buffer_rsrc((void*)wg, (short)0, DM * FF * 2, 0x00020000);
  c.rwu = __builtin_amdgcn_make_buffer_rsrc((void*)wu, (short)0, DM * FF * 2, 0x00020000);
  c.rwd = __builtin_amdgcn_make_buffer_rsrc((void*)wd, (short)0, DM * FF * 2, 0x00020000);
  c.rgw = __builtin_amdgcn_make_buffer_rsrc((void*)gw, (short)0, DM * 2, 0x00020000);
  c.rws = __builtin_amdgcn_make_buffer_rsrc((void*)ws, (short)0, WS_BYTES, 0x00020000);
  c.rout = __builtin_amdgcn_make_buffer_rsrc((void*)out, (short)0, SEQ * DM * 2, 0x00020000);
  c.ws = (u64)ws;
  c.a = xcd & 3; c.T = 4 * (xcd >> 2) + (idx >> 3);
  c.m = (idx & 3) == 3 ? 6 + ((idx >> 2) & 1) : 3 * ((idx >> 2) & 1) + (idx & 3);
  const unsigned rot = (2 * (c.a + c.T) + (c.m & 3)) % 9;
  c.kg[0] = rot;
  XS xs; xs.init(c, c.m < 6 ? M_X : A_X);
  static_for<8>([&](auto I_) { xs.template prep<1, 0, CI(I_)>(c); xs.template fire<CI(I_)>(c); });
  SBAR();
#pragma unroll
  for (int i = 1; i < 9; ++i) c.kg[i] = c.kg[i - 1] == 8 ? 0 : c.kg[i - 1] + 1;
  if (__builtin_expect(c.m < 6, 1)) role_mlp(c, xs); else role_att(c, xs);
#endif
}

extern "C" void kernel_launch(void* const* d_in, const int* in_sizes, int n_in, void* d_out, int out_size, void* d_ws, size_t ws_size,
                              hipStream_t stream) {
  fused_kernel<<<256, 256, 0, stream>>>((const u16*)d_in[0], (const u16*)d_in[1], (const u16*)d_in[2], (const u16*)d_in[4], (const u16*)d_in[5],
                                        (const u16*)d_in[6], (const u16*)d_in[7], (char*)d_ws, (u16*)d_out);
}
```

```cpp
#include <hip/hip_runtime.h>
#include <utility>

typedef unsigned short u16;
typedef unsigned long long u64;
typedef short s16x4 __attribute__((ext_vector_type(4)));
typedef __bf16 bf16x8 __attribute__((ext_vector_type(8)));
typedef __bf16 bf16x2 __attribute__((ext_vector_type(2)));
typedef float f32x2 __attribute__((ext_vector_type(2)));
typedef float f32x4 __attribute__((ext_vector_type(4)));
typedef unsigned u32x2 __attribute__((ext_vector_type(2)));
typedef unsigned u32x4 __attribute__((ext_vector_type(4)));
typedef unsigned u32x6 __attribute__((ext_vector_type(6)));
typedef __amdgpu_buffer_rsrc_t rsrc_t;
#define LAS __attribute__((address_space(3)))
#define DI __device__ __forceinline__
#define SBAR() __builtin_amdgcn_sched_barrier(0)
#define IC(x) std::integral_constant<int, (x)>{}
#define CI(x) decltype(x)::value

constexpr int SEQ = 2048, DM = 576, FF = 4608;
constexpr unsigned WS_H = 0;
constexpr unsigned WS_PART = 24u << 20;
constexpr unsigned WS_G = 40u << 20;
constexpr unsigned WS_F1 = 41u << 20;
constexpr unsigned WS_F2 = WS_F1 + 8192;
constexpr unsigned WS_E = WS_F2 + 65536;
constexpr unsigned WS_BYTES = 42u << 20;
constexpr unsigned LDS_BYTES = 163840;
constexpr unsigned S1 = 16384;
constexpr unsigned S2 = 18432;
constexpr unsigned S3 = S2 + 9216;
constexpr unsigned M_X = 65536;
constexpr unsigned A_X = 4 * S2;
constexpr unsigned A_HX = A_X + 65536;
constexpr unsigned A_GW = A_HX + 18432;
constexpr unsigned A_KX = 83968;
constexpr unsigned A_VX = A_KX + 18 * 2560;
static_assert(A_GW + 2048 <= LDS_BYTES && A_VX <= A_HX && A_VX + 20480 <= LDS_BYTES && 3 * S3 <= A_KX, "lds");

#if defined(__HIP_DEVICE_COMPILE__)
template <int... Is> using iseq = std::integer_sequence<int, Is...>;
template <class Fn, int... Is> DI void static_for_(Fn&& f, iseq<Is...>) { (f(std::integral_constant<int, Is>{}), ...); }
template <int N, class Fn> DI void static_for(Fn&& f) { static_for_(f, std::make_integer_sequence<int, N>{}); }

template <int N> DI void wait_bar() { asm volatile("s_waitcnt vmcnt(%0) lgkmcnt(0)\n\ts_barrier" ::"i"(N) : "memory"); }
template <int N> DI void wait_vm() { asm volatile("s_waitcnt vmcnt(%0)" ::"i"(N) : "memory"); }
template <class T> DI void PIN(T& x) { asm volatile("" : "+v"(x)); }
DI void tieA(bf16x8& a, bf16x8& b, bf16x8& c, bf16x8& d) { asm volatile("" : "+a"(a), "+a"(b), "+a"(c), "+a"(d)); }
template <unsigned LO, unsigned SO> DI unsigned dprep(unsigned lb, unsigned sb) {
  unsigned t;
  asm volatile("s_add_u32 m0, %1, %3\n\ts_add_u32 %0, %2, %4" : "=s"(t) : "s"(lb), "s"(sb), "i"(LO), "i"(SO) : "scc");
  return t;
}
template <unsigned LO> DI void dprep_l(unsigned lb) { asm volatile("s_add_u32 m0, %0, %1\n\ts_nop 0" ::"s"(lb), "i"(LO) : "scc"); }
template <unsigned IMM> DI void dfire(unsigned voff, rsrc_t srd, unsigned so) {
  asm volatile("buffer_load_dwordx4 %0, %1, %2 offen offset:%3 lds" ::"v"(voff), "s"(srd), "s"(so), "i"(IMM) : "memory");
}
DI void dfire_m(unsigned voff, rsrc_t srd, unsigned so, u64 mask) {
  asm volatile("s_mov_b64 exec, %3\n\tbuffer_load_dwordx4 %0, %1, %2 offen lds\n\ts_mov_b64 exec, -1" ::"v"(voff), "s"(srd), "s"(so), "s"(mask) : "memory");
}
template <unsigned IMM> DI void ldh(bf16x8& d, unsigned voff, rsrc_t srd, unsigned so) {
  asm volatile("buffer_load_dwordx4 %0, %1, %2, %3 offen offset:%4 sc1" : "=a"(d) : "v"(voff), "s"(srd), "s"(so), "i"(IMM) : "memory");
}
DI void ldg(f32x4& d, unsigned voff, rsrc_t srd, unsigned so) { asm volatile("buffer_load_dwordx4 %0, %1, %2, %3 offen sc1" : "=v"(d) : "v"(voff), "s"(srd), "s"(so) : "memory"); }
template <unsigned LA> DI void touch(unsigned voff, rsrc_t srd, unsigned so) {
  asm volatile("s_mov_b32 m0, %3\n\ts_nop 0\n\tbuffer_load_dword %0, %1, %2 offen sc1 lds" ::"v"(voff), "s"(srd), "s"(so), "i"(LA) : "memory");
}
DI void st16(const u32x4& v, rsrc_t srd, unsigned voff, unsigned so) {
  __builtin_amdgcn_raw_buffer_store_b128(v, srd, voff, so, 16);
  asm volatile("s_nop 1" ::"v"(v) : "memory");
}
DI void keep(const u32x4& v) { asm volatile("" ::"v"(v)); }
template <bool AG, bool ZERO> DI void mf(f32x4& c, const bf16x8& a, const bf16x8& b) {
  if constexpr (ZERO) {
    if constexpr (AG) asm volatile("v_mfma_f32_16x16x32_bf16 %0, %1, %2, 0" : "=&v"(c) : "v"(a), "a"(b));
    else asm volatile("v_mfma_f32_16x16x32_bf16 %0, %1, %2, 0" : "=&v"(c) : "v"(a), "v"(b));
  } else {
    if constexpr (AG) asm volatile("v_mfma_f32_16x16x32_bf16 %0, %1, %2, %0" : "+v"(c) : "v"(a), "a"(b));
    else asm volatile("v_mfma_f32_16x16x32_bf16 %0, %1, %2, %0" : "+v"(c) : "v"(a), "v"(b));
  }
}
template <bool AG, bool ZERO> DI void mfs(f32x4& c, const bf16x8& a, const bf16x8& b) {
  if constexpr (ZERO) {
    if constexpr (AG) asm volatile("v_mfma_f32_16x16x32_bf16 %0, %2, %1, 0" : "=&v"(c) : "v"(a), "a"(b));
    else asm volatile("v_mfma_f32_16x16x32_bf16 %0, %2, %1, 0" : "=&v"(c) : "v"(a), "v"(b));
  } else {
    if constexpr (AG) asm volatile("v_mfma_f32_16x16x32_bf16 %0, %2, %1, %0" : "+v"(c) : "v"(a), "a"(b));
    else asm volatile("v_mfma_f32_16x16x32_bf16 %0, %2, %1, %0" : "+v"(c) : "v"(a), "v"(b));
  }
}
DI f32x4 mfc(const u32x4& a, const u32x4& b, const f32x4& c) { f32x4 d; asm volatile("v_mfma_f32_16x16x32_bf16 %0, %1, %2, %3" : "=&v"(d) : "a"(a), "v"(b), "v"(c)); return d; }
DI f32x4 mfz(const u32x4& a, const u32x4& b) { f32x4 d; asm volatile("v_mfma_f32_16x16x32_bf16 %0, %1, %2, 0" : "=&v"(d) : "v"(a), "v"(b)); return d; }
DI float max2(float a, float b) { float r; asm("v_max_f32 %0, %1, %2" : "=v"(r) : "v"(a), "v"(b)); return r; }
DI float max3(float a, float b, float c) { float r; asm("v_max3_f32 %0, %1, %2, %3" : "=v"(r) : "v"(a), "v"(b), "v"(c)); return r; }
DI s16x4 trd(unsigned a) { return __builtin_amdgcn_ds_read_tr16_b64_v4i16((LAS s16x4*)a); }
DI bf16x8 cat(s16x4 lo, s16x4 hi) { return __builtin_bit_cast(bf16x8, __builtin_shufflevector(lo, hi, 0, 1, 2, 3, 4, 5, 6, 7)); }
DI unsigned cvtpk(float lo, float hi) { f32x2 v = {lo, hi}; return __builtin_bit_cast(unsigned, __builtin_convertvector(v, bf16x2)); }
DI float w_mul(float a) { float r; asm volatile("v_mul_f32 %0, 0xbfb8aa3b, %1" : "=v"(r) : "v"(a)); return r; }
DI float w_exp(float a) { float r; asm volatile("v_exp_f32 %0, %1" : "=v"(r) : "v"(a)); return r; }
DI unsigned w_add1(float a) { unsigned r; asm volatile("v_fma_mixlo_f16 %0, %1, 1.0, 1.0" : "=v"(r) : "v"(a)); return r; }
DI unsigned w_rcplo(unsigned a) { unsigned r; asm volatile("v_rcp_f16 %0, %1" : "=v"(r) : "v"(a)); return r; }
DI void w_rcphi(unsigned& d, unsigned a) { asm volatile("v_rcp_f16_sdwa %0, %1 dst_sel:WORD_1 dst_unused:UNUSED_PRESERVE src0_sel:WORD_0" : "+v"(d) : "v"(a)); }
DI unsigned w_pklo(float a, float k) { unsigned r; asm volatile("v_fma_mixlo_f16 %0, %1, %2, 0" : "=v"(r) : "v"(a), "s"(k)); return r; }
DI void w_pkhi(unsigned& d, float a, float k) { asm volatile("v_fma_mixhi_f16 %0, %1, %2, 0" : "+v"(d) : "v"(a), "s"(k)); }
template <int H> DI void w_exph(unsigned& d) {
  if constexpr (H == 0) asm volatile("v_exp_f16_sdwa %0, %0 dst_sel:WORD_0 dst_unused:UNUSED_PRESERVE src0_sel:WORD_0" : "+v"(d));
  else asm volatile("v_exp_f16_sdwa %0, %0 dst_sel:WORD_1 dst_unused:UNUSED_PRESERVE src0_sel:WORD_1" : "+v"(d));
}
template <int H> DI void w_rcph(unsigned& d) {
  if constexpr (H == 0) asm volatile("v_rcp_f16_sdwa %0, %0 dst_sel:WORD_0 dst_unused:UNUSED_PRESERVE src0_sel:WORD_0" : "+v"(d));
  else asm volatile("v_rcp_f16_sdwa %0, %0 dst_sel:WORD_1 dst_unused:UNUSED_PRESERVE src0_sel:WORD_1" : "+v"(d));
}
DI void w_add1h(unsigned& d) { asm volatile("v_pk_add_f16 %0, %0, 1.0 op_sel_hi:[1,0]" : "+v"(d)); }
DI float w_mixlo(float u, unsigned s) { float r; asm volatile("v_fma_mix_f32 %0, %1, %2, 0 op_sel_hi:[0,1,0]" : "=v"(r) : "v"(u), "v"(s)); return r; }
DI float w_mixhi(float u, unsigned s) { float r; asm volatile("v_fma_mix_f32 %0, %1, %2, 0 op_sel:[0,1,0] op_sel_hi:[0,1,0]" : "=v"(r) : "v"(u), "v"(s)); return r; }
DI unsigned w_cvt(float lo, float hi) { unsigned r; asm volatile("v_cvt_pk_bf16_f32 %0, %1, %2" : "=v"(r) : "v"(lo), "v"(hi)); return r; }
DI float bflo(unsigned v) { return __uint_as_float(v << 16); }
DI float bfhi(unsigned v) { return __uint_as_float(v & 0xffff0000u); }
template <class T> DI T ldsr(unsigned a) { return *(const LAS T*)a; }
template <class T> DI void ldsw(unsigned a, T v) { *(LAS T*)a = v; }
DI unsigned lane_id() { unsigned l; asm volatile("v_mbcnt_lo_u32_b32 %0, -1, 0\n\tv_mbcnt_hi_u32_b32 %0, -1, %0" : "=v"(l)); return l; }

template <int N> DI void STUB() { asm volatile(".Lgm_s%c0:\n\ts_cbranch_execz .Lgm_ret" ::"i"(N)); }
template <int N> DI void visit(u64 pc0) {
  asm volatile("s_add_u32 s98, %0, .Lgm_s%c2-.Lgm_code0\n\ts_addc_u32 s99, %1, 0\n\ts_mov_b64 exec, 0\n\ts_swappc_b64 s[100:101], s[98:99]\n\ts_mov_b64 exec, -1"
               ::"s"((unsigned)pc0), "s"((unsigned)(pc0 >> 32)), "i"(N) : "s98", "s99", "s100", "s101", "scc", "memory");
}
template <int B, int CNT> DI void visit_all(u64 pc0, unsigned w) {
  static_for<4>([&](auto W_) { if (w == CI(W_)) static_for<(CNT + 3 - CI(W_)) / 4>([&](auto I_) { visit<B + CI(W_) + 4 * CI(I_)>(pc0); }); });
}

struct Ctx {
  rsrc_t rx, rqkv, rwo, rwg, rwu, rwd, rgw, rws, rout;
  unsigned w, a, T, m;
  unsigned kg[9];
  u64 E, ws, pc0;
};

enum { O_NONE, O_W, O_X, O_HX, O_FIN, O_POLL, O_D2, O_B2, O_GATE, O_ST, O_TW, O_TL, O_PZ };
constexpr int NEVER = 1 << 20;
struct Op { int kind, a, b, need; };
constexpr int PD = 3;
constexpr int MLP_AD = 3, ATT_AD = 3;
constexpr Op NOP_ = {O_NONE, 0, 0, 0};
constexpr int uk(int U, int NT) { const int o = NT == 8 ? 12 : 6; return U < o ? U : U < o + 3 ? U - o + 18 : U - 3; }
constexpr int dep(int U, int NT) { return U == 15 ? 2 : NT == 8 ? (U == 6 ? 1 : U == 12 ? 3 : -1) : (U == 6 ? 3 : U == 9 ? 1 : -1); }
constexpr int FLG2 = 4, FLGA = 1;
constexpr int RS = 19;
constexpr Op p2_op(int N, int R, int NT, int AD, int FB) {
  const int ND = NT == 8 ? 4 : 6, NO = ND + 4, RG = (N + PD) / NT, pos = (N + PD) % NT, FL = NT == 8 ? FLG2 : FLGA;
  if (RG > 20 || pos == 0 || RG == FL - 1) return NOP_;
  if (NT != 8 && R == 3 && pos == 1 && dep(RG + AD + 1, NT) >= 0) return {O_POLL, dep(RG + AD + 1, NT), 0, FB + (RG + 1) * NT - PD};
  int k;
  const bool busy = RG == FL || RG == FL + 1;
  if (busy) { const int d = NT == 8 ? 1 : 2, nb = NT == 8 ? 5 : 4; k = pos < d ? (R == 2 ? pos - 1 : -1) : pos < d + nb ? d - 1 + 2 * (pos - d) + (R - 2) : (R == 2 ? pos - 1 + nb : -1); }
  else if (NT == 8) k = R == 2 ? (pos == 1 ? 0 : pos) : pos == 1 ? 1 : pos == 7 ? 8 : -1;
  else k = R == 2 ? pos - 1 : -1;
  if (k < 0) return NOP_;
  const int n = busy ? (FL - 1 + AD) * NO + (RG - FL) * (3 * NO / 2) + k : (RG + AD) * NO + k, U = n / NO, i = n % NO;
  if (k < (busy ? 3 * NO / 2 : NO)) return U > 20 ? NOP_ : Op{i < ND ? O_D2 : O_B2, U, i < ND ? i : i - ND, FB + U * NT - PD};
  if (k != NO || busy) return NOP_;
  if (NT == 8 && dep(RG + AD + 2, NT) >= 0) return {O_POLL, dep(RG + AD + 2, NT), 0, FB + (RG + 2) * NT - PD};
  if (RG == 14) return {O_POLL, 4, 0, FB + 17 * NT - PD};
  if (RG == 15) return {O_GATE, 0, 0, FB + 18 * NT - PD};
  return NOP_;
}
constexpr int FIN0 = 36, ST0 = 72, FLG0 = 17, FLG1 = 25;
constexpr int PZR = 8;
constexpr int lastfin(int N) { return N == 1 ? 0 : N == 3 ? 1 : N == 7 ? 2 : N == 8 ? 3 : N == 10 ? 4 : N == 11 ? 5 : N == 15 ? 6 : N == 16 ? 7 : -1; }
constexpr int bar1(int n) { return n == 0 ? -1 : 16 * n - PD; }
constexpr Op opW(int n, int i) { return {O_W, n, i, bar1(n)}; }
constexpr int XR = 11;
constexpr Op opX(int c, int i) { return {O_X, c, i, c == 0 ? -1 : 16 * (c - 1) + XR}; }
constexpr Op mlp_op(int F, int R) {
  if (F < 0) {
    const int i = F + 15;
    if (R != 2 || i < 0 || i >= 13) return NOP_;
    return i < 8 ? opX(0, i) : i < 12 ? opW(0, i - 8) : Op{O_TL, 0, 0, bar1(1)};
  }
  if (F >= 440) {
    const int N = F - 440;
    if (N < 5) return R == 2 ? Op{N < 4 ? O_D2 : O_B2, MLP_AD, N & 3, 440 + MLP_AD * 8 - PD} : N == 0 || N == 2 || N == 4 ? Op{O_B2, MLP_AD, N / 2 + 1, 440 + MLP_AD * 8 - PD} : Op{O_FIN, 2, N / 2, 440 + FLG2 * 8 - PD};
    if (R == 3 && N < 20 && lastfin(N) >= 0) return {O_FIN, 2, lastfin(N), 440 + FLG2 * 8 - PD};
    return p2_op(N, R, 8, MLP_AD, 440);
  }
  if (F >= 432) return NOP_;
  const int RG = (F + PD) / 16, pos = (F + PD) % 16, r = R - 2;
  if (pos == 0) return NOP_;
  if (F < 4) return opX(1, 2 * F + r);
  if (F < 6) return opW(1, 2 * (F - 4) + r);
  if (R == 3) return NOP_;
  if (RG <= 5) {
    const int n = F - 6 - RG, c = 2 + n / 12, i = n % 12;
    const int cap = RG + 3;
    if (RG <= 3) { if (c <= cap) return i < 8 ? opX(c, i) : opW(c, i - 8); return NOP_; }
    if (RG == 4 && pos <= 8) return pos <= 4 ? opX(6, pos + 3) : opW(6, pos - 5);
    if (RG == 4) return opX(7, pos - 9);
    if (pos == 1) return opX(7, 7);
    if (pos <= 5) return opW(7, pos - 2);
    return pos <= 13 ? opX(8, pos - 6) : opW(8, pos - 14);
  }
  if (RG == 6 && pos <= 2) return opW(8, pos + 1);
  const int k8 = pos >= 3 && pos < 15 && (pos - 3) % 3 != 2 ? (pos - 3) - (pos - 3) / 3 : -1;
  if (RG == 6 && k8 >= 0) return opW(9 + k8 / 4, k8 % 4);
  if (RG == 7 && pos <= 12) return opW(pos <= 8 ? 11 + (pos - 1) / 4 : 14, (pos - 1) % 4);
  if (RG == 8 && pos <= 12) return opW(pos <= 4 ? 13 : 14 + (pos - 1) / 4, (pos - 1) % 4);
  if ((RG == 7 || RG == 8) && pos >= 13) return {O_PZ, 3 * (RG - 7) + pos - 13, 0, bar1(FLG0)};
  if (F >= 144 + ST0 && F < 144 + ST0 + 32 && F % 4 == 2) return {O_FIN, 0, (F - 144 - ST0) / 4, bar1(FLG0)};
  if (F >= 288 + ST0 && F < 288 + ST0 + 16 && F % 2 == 0) return {O_FIN, 1, (F - 288 - ST0) / 2, bar1(FLG1)};
  if (RG >= 9 && RG <= 12 && (pos == 9 || pos == 15)) return {O_TW, 2 * (RG - 9) + (pos == 15), 0, NEVER};
  if (RG == 9 && pos >= 6 && pos % 2 == 0 && pos < 14) return opW(17, (pos - 6) / 2);
  if (RG >= 10 && RG <= 12 && pos % 4 == 2) return opW(RG + 8, pos / 4);
  if (RG == 13 && pos <= 4) return opW(21, pos - 1);
  if (RG >= 18 && RG <= 19 && k8 >= 0) return opW(22 + 2 * (RG - 18) + k8 / 4, k8 % 4);
  if (RG == 20 && k8 >= 0) return k8 < 4 ? opW(26, k8) : Op{O_D2, 0, k8 - 4, bar1(27)};
  if (RG >= 21 && RG <= 22 && pos <= 4) return {O_D2, RG - 20, pos - 1, 440 + (RG - 20) * 8 - PD};
  if (RG == 22 && pos == 7) return {O_POLL, 0, 0, bar1(25)};
  if (RG >= 25 && RG <= 26) {
    const int k = (RG - 25) * 15 + pos - 1;
    if (k < 24 && k % 2 == 0) { const int U = k / 8; return {O_B2, U, (k / 2) % 4, U == 0 ? bar1(27) : 440 + U * 8 - PD}; }
  }
  return NOP_;
}
constexpr int barA(int n) { return n == 0 ? -1 : n <= 9 ? 16 * n - PD : 144 + 14 * (n - 9) - PD; }
constexpr int rgA(int F) { return F + PD < 144 ? (F + PD) / 16 : 9 + (F + PD - 144) / 14; }
constexpr int posA(int F) { return F + PD < 144 ? (F + PD) % 16 : (F + PD - 144) % 14; }
struct AttSeq {
  Op op[270] = {};
  constexpr AttSeq() {
    constexpr int seq[27][2] = {{O_X, 2}, {O_W, 2}, {O_HX, 1}, {O_X, 3}, {O_W, 3}, {O_HX, 2}, {O_W, 4}, {O_X, 4}, {O_HX, 3}, {O_HX, 4}, {O_W, 5}, {O_X, 5}, {O_W, 6}, {O_X, 6},
                                {O_W, 7}, {O_X, 7}, {O_W, 8}, {O_X, 8}, {O_W, 9}, {O_W, 10}, {O_W, 11}, {O_W, 12}, {O_W, 13}, {O_W, 14}, {O_W, 15}, {O_W, 16}, {O_W, 17}};
    int F = 6;
    for (int e = 0; e < 27; ++e) {
      const int k = seq[e][0], a = seq[e][1], n = k == O_X ? 8 : k == O_W ? 4 : 1, rg = k == O_X ? a - 2 : k == O_W ? a - 3 : 0;
      for (int i = 0; i < n; ++i) {
        while (posA(F) == 0 || rgA(F) < rg || (F >= 130 && F < 148)) ++F;
        op[F++] = k == O_X ? opX(a, i) : k == O_W ? Op{O_W, a, i, barA(a)} : Op{O_HX, a, 0, barA(a < 3 ? 2 * a : 5)};
      }
    }
    int z = 0;
    for (int G = 150; G < 270; ++G) {
      const int RG = rgA(G), pos = posA(G);
      if (RG >= 10 && RG <= 14 && (pos == 7 || pos == 10) && z < 9) op[G] = {O_PZ, z++, 0, barA(16)};
      if (RG == 16 && pos >= 2 && pos < 8) op[G] = {O_D2, 0, pos - 2, NEVER};
    }
  }
};
constexpr AttSeq ATT_SEQ{};
constexpr Op att1_op(int F, int R) {
  if (F < 0) {
    const int i = F + 16;
    if (R != 2 || i < 0 || i >= 14) return NOP_;
    return i < 8 ? opX(0, i) : i < 12 ? Op{O_W, 0, i - 8, -1} : i == 12 ? Op{O_HX, 0, 0, -1} : Op{O_TL, 0, 0, NEVER};
  }
  if (F < 4) return opX(1, 2 * F + R - 2);
  if (F < 6) return {O_W, 1, 2 * (F - 4) + R - 2, barA(1)};
  return R == 2 && F < 270 ? ATT_SEQ.op[F] : NOP_;
}
constexpr Op att2_op(int F, int R) {
  if (F < 0) {
    const int i = F + 50;
    if (R != 2 || i < 0 || i >= 43) return NOP_;
    constexpr int n1 = 12 - PD, n2 = 24 - PD, ns = FLGA * 12 - PD;
    if (i < 6) return {O_D2, 0, i, -1};
    if (i == 6) return {O_POLL, 0, 0, -28};
    if (i < 13) return {O_D2, 1, i - 7, n1};
    if (i < 16) return {O_ST, 0, 0, ns};
    if (i < 22) return {O_D2, 2, i - 16, n2};
    if (i < 25) return {O_ST, 0, 0, ns};
    if (i < 29) return {O_B2, 0, i - 25, -1};
    if (i < 33) return {O_B2, 1, i - 29, n1};
    if (i < 36) return {O_ST, 0, 0, ns};
    if (i < 40) return {O_B2, 2, i - 36, n2};
    return {O_ST, 0, 0, ns};
  }
  return p2_op(F, R, 12, ATT_AD, 0);
}
template <int WHICH> constexpr Op op_at(int F, int R) { return WHICH == 0 ? mlp_op(F, R) : WHICH == 1 ? att1_op(F, R) : att2_op(F, R); }
template <int WHICH> constexpr int vm_at(int tau) {
  int s = 0;
  for (int F = tau - 1; F >= -64; --F)
    for (int R = 3; R >= 2; --R) {
      const Op o = op_at<WHICH>(F, R);
      if (o.kind == O_NONE) continue;
      if (o.need <= tau || s >= 63) return s;
      ++s;
    }
  return s;
}
template <int WHICH, int TAU> constexpr int VM = vm_at<WHICH>(TAU);

DI void load_epoch(Ctx& c) {
  asm volatile("s_load_dwordx2 %0, %1, %2" : "=s"(c.E) : "s"(c.ws), "s"(WS_E + c.T * 128) : "memory");
}
DI void flag_store(const Ctx& c, unsigned off, unsigned val) {
  const u64 v = c.E + val;
  __builtin_amdgcn_raw_buffer_store_b64(u32x2{(unsigned)v, (unsigned)(v >> 32)}, c.rws, 0, off, 16);
}
DI void poll_issue(const Ctx& c, u32x2& f, unsigned voff, unsigned off) {
  asm volatile("buffer_load_dwordx2 %0, %1, %2, %3 offen sc1" : "=v"(f) : "v"(voff), "s"(c.rws), "s"(off) : "memory");
}
DI bool flag_ok(const Ctx& c, u32x2 f, unsigned need) {
  const u64 d = (((u64)f[1] << 32) | f[0]) - c.E - need;
  return __builtin_amdgcn_ballot_w64(d >= 8) == 0;
}
DI void poll_check(const Ctx& c, u32x2& f, unsigned voff, unsigned off, unsigned need) {
  asm volatile("" : "+v"(f));
  if (__builtin_expect(!flag_ok(c, f, need), 0)) {
    for (unsigned spin = 0; spin < (1u << 16); ++spin) {
      __builtin_amdgcn_s_sleep(1);
      asm volatile("buffer_load_dwordx2 %0, %1, %2, %3 offen sc1\n\ts_waitcnt vmcnt(0)" : "=&v"(f) : "v"(voff), "s"(c.rws), "s"(off) : "memory");
      if (flag_ok(c, f, need)) break;
    }
  }
}

constexpr unsigned POISON = 0xffffffffu;
DI unsigned inbox(const Ctx& c) { return WS_PART + ((c.T * 8 + c.m) * 64 + c.w) * 3072; }
DI unsigned flag2(const Ctx& c) { return WS_F2 + ((c.T * 8 + c.m) * 4 + c.w) * 128; }
template <int NI, int J> DI void poison(const Ctx& c, u32x4& pz, unsigned vl16) {
  if constexpr (J == 0) { pz = u32x4{POISON, POISON, POISON, POISON}; PIN(pz); }
  asm volatile("buffer_store_dwordx4 %0, %1, %2, %3 offen offset:%4 sc1" ::"v"(pz), "v"(vl16), "s"(c.rws), "s"(inbox(c) + (c.a * 4 + ((J / NI + 1) ^ c.a)) * 12288), "i"((J % NI) * 1024) : "memory");
}
template <int NT> struct P2 {
  static constexpr int AD = NT == 8 ? MLP_AD : ATT_AD, NS = NT == 8 ? 8 : 4, FB = NT == 8 ? 440 : 0, ROT = NT == 8 ? 4 : 0;
  static constexpr unsigned SS = NT == 8 ? S2 : S3;
  static constexpr int sidx(int u) { return NT == 8 ? (u + ROT) % NS : u % 4 == 0 ? 2 : u % 4 == 3 ? 3 : u % 4 - 1; }
  static constexpr unsigned slot(int u) { return sidx(u) * SS; }
  static constexpr int WN = NT == 8 ? 3 : 2;
  static constexpr unsigned win(int u) { return (sidx(u) / WN * WN) * SS; }
  unsigned v128[2], v64, vh[2], vf;
  unsigned lw, lw64, sd, so, sh[2];
  unsigned f1, cur, curh;
  bf16x8 bf[21][4];
  u32x2 pf;
  f32x4 omg;
  DI void init(const Ctx& c) {
    const unsigned l = lane_id(), colb = NT == 8 ? 256 * (c.m >> 1) : 768, kh = c.w >> 1;
    const unsigned lo = 16 * ((l & 15) ^ ((l >> 3) & 2) ^ (4 * (c.a ^ 3)));
    v128[0] = (l >> 4) * 4608 + lo; v128[1] = (l >> 4) * 4608 + (lo ^ 64);
    v64 = (l >> 3) * 2304 + 16 * ((l & 7) ^ (((l >> 1) & 4) | ((l >> 3) & 2)) ^ (2 * (c.a ^ 3))) + 256;
    vh[0] = l * 16 + kh * 2048; vh[1] = l * 16 + (1 - kh) * 2048;
    vf = 8 * (l & 7);
    lw = c.w * 4608; lw64 = c.w * 2304 + S2;
    sd = (1152 * c.a + 16 * c.w) * 1152 + colb;
    so = (144 * c.a + 72 * kh + 16 * (c.w & 1)) * 1152 + colb;
    sh[0] = WS_H + (((4 * c.T + 2 * (c.m & 1) + (c.w & 1)) * 4 + c.a) * 42 + kh) * 4096; sh[1] = sh[0] + kh * 8192;
    f1 = WS_F1 + (c.T * 4 + c.a) * 128;
  }
  template <int S> DI unsigned pvo() const { return S < 3 ? (vf < 48 ? vf : 0) : S == 3 ? 48 + (vf & 8) : vf & 24; }
  template <int S> DI void poll(const Ctx& c) { poll_issue(c, pf, pvo<S>(), S < 4 ? f1 : flag2(c)); }
  template <int S> DI void check(const Ctx& c) { poll_check(c, pf, pvo<S>(), S < 4 ? f1 : flag2(c), S < 3 ? S + 1 : S == 3 ? 3 : 1); }
  template <int KIND, int U, int I> DI void prep(const Ctx& c) {
    if constexpr (KIND == O_D2) {
      constexpr int K = uk(U, NT);
      constexpr unsigned row = K < 18 ? 64 * K : 32 * (K - 18);
      if constexpr (I == 0) cur = dprep<slot(U), row * 1152>(lw, K < 18 ? sd : so);
      if constexpr (I == 4) dprep_l<slot(U)>(lw64);
    }
    if constexpr (KIND == O_B2 && U < RS && (I == 0 || (NT == 8 && U == AD && I == 1))) { curh = sh[uk(U, NT) >= 18] + (uk(U, NT) < 18 ? 2 * uk(U, NT) : 18 + uk(U, NT)) * 4096; asm volatile("" : "+s"(curh)); }
    if constexpr (KIND == O_B2 && U >= RS && (I & 1) == 0) { curh = sh[0] - (c.w >> 1) * 4096 + (2 * uk(U, NT) + (I >> 1)) * 4096; asm volatile("" : "+s"(curh)); }
  }
  template <int KIND, int U, int I> DI void fire(const Ctx& c) {
    if constexpr (KIND == O_D2) dfire<(I & 3) * 1152>(I < 4 ? v128[I >> 1] : v64, uk(U, NT) < 18 ? c.rwd : c.rwo, cur);
    if constexpr (KIND == O_B2) ldh<(I & 1) * 1024>(bf[U][I], vh[U < RS ? I >> 1 : 0], c.rws, curh);
    if constexpr (KIND == O_POLL) poll<U>(c);
    if constexpr (KIND == O_GATE) ldg(omg, vh[0] & 255, c.rws, WS_G + (c.a * 32 + 4 * c.T + 2 * (c.m & 1) + (c.w & 1)) * 256);
  }
  static constexpr int tp(int t) { return NT == 8 || t < 6 ? t : t < 9 ? t + 2 : t < 11 ? t - 3 : 11; }
  static DI unsigned rbase(const Ctx& c, unsigned l, int t) {
    const unsigned g = l >> 4, q = (l >> 2) & 3;
    return (c.w >> 1) * 9216 + q * 1152 + g * 256 + 32 * (t ^ ((q & 2) | (g & 1))) + 8 * (l & 3);
  }
};

template <int NT, class Extra> DI void phase2(Ctx& c, P2<NT>& p, bf16x8 (&f)[16], Extra&& extra) {
  using P = P2<NT>;
  constexpr int AD = P::AD, WH = NT == 8 ? 0 : 2;
  const unsigned l = lane_id(), g = l >> 4, q = (l >> 2) & 3, kh = c.w >> 1;
  unsigned rb[4], rb64[4];
#pragma unroll
  for (int t = 0; t < 4; ++t) {
    rb[t] = P::win(0) + P::rbase(c, l, t); PIN(rb[t]);
    if (NT != 8) { rb64[t] = P::win(0) + S2 + kh * 4608 + (q & 1) * 1152 + (2 * g + (q >> 1)) * 128 + 32 * (t ^ ((q & 2) | (g & 1))) + 8 * (l & 3); PIN(rb64[t]); }
  }
  f32x4 acc[NT][4], oth[NT][2];
  bf16x8 f2[NT];
  s16x4 flo;
  auto xoff = [](int t) { return NT == 8 ? 20480 + t * 8192 : t < 6 ? t * 8192 : t < 9 ? (t - 6) * 8192 : 28672 + (t - 9) * 8192; };
  unsigned wa[2] = {0, 0}, ra[2] = {0, 0}, l16 = 0;
  const unsigned pb = inbox(c);
  auto rd = [&](auto U_, auto T_, auto H_) {
    constexpr int t = P::tp(CI(T_)), H = CI(H_);
    constexpr unsigned o = P::slot(CI(U_)) - P::win(CI(U_));
    return t < 8 ? trd(rb[t & 3] + o + 128 * (t >> 2) + H * 4608) : trd(rb64[t & 3] + o + H * 2304);
  };
  if constexpr (NT != 8) {
    wait_bar<VM<2, -1>>();
    tieA(p.bf[0][0], p.bf[0][1], p.bf[0][2], p.bf[0][3]);
    static_for<PD>([&](auto T_) { f[CI(T_)] = cat(rd(IC(0), T_, IC(0)), rd(IC(0), T_, IC(1))); });
  }
  auto send = [&](auto T_, auto R_) {
    constexpr int t = CI(T_), R = CI(R_), it = P::tp(t), dq = it < 8 ? (it >> 1) ^ 3 : (it - 8) ^ 3, idx = it < 8 ? it & 1 : 2;
    if constexpr (R < 2) { acc[t][R] += oth[t][R]; PIN(acc[t][R]); }
    if constexpr (R == 2 && dq != 0) {
      const u32x4 v = {cvtpk(acc[t][0][0], acc[t][0][1]), cvtpk(acc[t][0][2], acc[t][0][3]), cvtpk(acc[t][1][0], acc[t][1][1]), cvtpk(acc[t][1][2], acc[t][1][3])};
      st16(__builtin_elementwise_min(v, u32x4{POISON - 1, POISON - 1, POISON - 1, POISON - 1}), c.rws, l16 + idx * 1024, pb + ((dq ^ c.a) * 4 + c.a) * 12288);
    }
  };
  static_for<21>([&](auto U_) {
    static_for<NT>([&](auto T_) {
      constexpr int U = CI(U_), t = CI(T_), N = U * NT + t, UN = (N + PD) / NT, tn = (N + PD) % NT;
      constexpr int RR = tn == 0 ? 2 : 0;
      const bf16x8 a = f[t], a2 = f2[t];
      static_for<4>([&](auto R_) {
        constexpr int R = CI(R_);
        constexpr int rs = (t & 1) ? 3 - R : R, sk = (R == 1 || R == 2);
        if constexpr (U < RS) mf<true, U == 0>(acc[t][rs], a, p.bf[U][rs]); else mf<true, false>(acc[t][R >> 1], sk ? a2 : a, p.bf[U][2 * sk + (R >> 1)]);
        SBAR();
        if constexpr (tn == 0 && UN <= 20) {
          if constexpr (R < 2 && (P::win(UN) != P::win(UN - 1) || UN == RS)) {
            const unsigned d = P::win(UN) - P::win(UN - 1);
#pragma unroll
            for (int i = 2 * R; i < 2 * R + 2; ++i) {
              rb[i] += d - (UN == RS ? kh * 9216 : 0); PIN(rb[i]);
              if (NT != 8) { rb64[i] += d - (UN == RS ? kh * 4608 : 0); PIN(rb64[i]); }
            }
          }
          if constexpr (R == 2) {
            if constexpr (NT == 8 && UN % 5 == 1) STUB<13 + UN / 5>();
            if constexpr (NT == 8 && UN == 20) STUB<17>();
            if constexpr (NT != 8 && UN % 3 == 2) STUB<46 + UN / 3>();
            wait_bar<(UN == (NT == 8 ? FLG2 : FLGA) ? 0 : VM<WH, P::FB + N>)>();
            tieA(p.bf[UN][0], p.bf[UN][1], p.bf[UN][2], p.bf[UN][3]);
          }
        }
        if constexpr (UN < RS || (UN <= 20 && tn != 0)) {
          if constexpr (R == RR) flo = rd(IC(UN), IC(tn), IC(0));
          if constexpr (R == RR + 1) f[tn] = cat(flo, rd(IC(UN), IC(tn), IC(1)));
          if constexpr (UN >= RS && R == 2) flo = rd(IC(UN), IC(tn), IC(2));
          if constexpr (UN >= RS && R == 3) f2[tn] = cat(flo, rd(IC(UN), IC(tn), IC(3)));
        } else if constexpr (UN <= 20) {
          if constexpr (R == 2) f[0] = cat(rd(IC(UN), IC(0), IC(0)), rd(IC(UN), IC(0), IC(1)));
          if constexpr (R == 3) f2[0] = cat(rd(IC(UN), IC(0), IC(2)), rd(IC(UN), IC(0), IC(3)));
        }
        SBAR();
        if constexpr (tn == 0 && R == 3 && UN <= 20) {
          if constexpr (UN == (NT == 8 ? FLG2 : FLGA)) { if (threadIdx.x == 0) flag_store(c, p.f1 + 8 * c.m, 3); }
          if constexpr (dep(UN + AD, NT) >= 0) p.template check<dep(UN + AD, NT)>(c);
          if constexpr (UN == 17) p.template check<4>(c);
          if constexpr (UN == 18) {
            PIN(p.omg);
            l16 = lane_id() * 16; PIN(l16);
            wa[0] = l16 + c.w * 2048 + (NT == 8 ? 0 : 4 * S3); wa[1] = l16 + c.w * 2048 + (NT == 8 ? 32768 : 0); PIN(wa[0]); PIN(wa[1]);
          }
          if constexpr (UN == 20) { const unsigned d = kh ? 0u - 4096 : 4096; ra[0] = wa[0] + d; ra[1] = wa[1] + d; PIN(ra[0]); PIN(ra[1]); }
        }
        extra(IC(N), R_);
        {
          constexpr int gi = N - (RS - 1) * NT - 2, wi = gi < 0 ? -1 : gi < 5 ? (R < 2 ? 2 * gi + R : -1) : (R == 0 ? gi + 5 : -1);
          if constexpr (wi >= 0 && wi < 2 * NT) {
            constexpr int tt = wi / 2, hi = NT == 8 ? tt >= 4 : tt >= 6;
            ldsw<f32x4>(wa[hi] + xoff(tt) - (NT == 8 && hi ? 32768 : 0) + (wi & 1) * 1024, acc[tt][2 + (wi & 1)]);
          }
          constexpr int rt_ = N - (20 * NT - 2);
          if constexpr (rt_ >= 0 && rt_ < NT && R < 2) {
            constexpr int hi = NT == 8 ? rt_ >= 4 : rt_ >= 6;
            oth[rt_][R] = ldsr<f32x4>(ra[hi] + xoff(rt_) - (NT == 8 && hi ? 32768 : 0) + R * 1024);
          }
          if constexpr (U == 20 && t >= 2) send(IC(t - 2), R_);
        }
        if constexpr (R >= 2) {
          constexpr Op o = op_at<WH>(P::FB + N, R), n = op_at<WH>(P::FB + N + 1, 2);
          if constexpr (R == 3 || N == 0) p.template prep<o.kind, o.a, o.b>(c);
          p.template fire<o.kind, o.a, o.b>(c);
          if constexpr (R == 3) p.template prep<n.kind, n.a, n.b>(c);
        }
        SBAR();
      });
    });
  });
  STUB<(NT == 8 ? 18 : 53)>();
  const unsigned l2 = l16 >> 4;
  constexpr int NI = NT / 4;
  u32x4 in[3][NI];
  for (unsigned spin = 0; spin < (1u << 16); ++spin) {
    unsigned mx = 0;
#pragma unroll
    for (int s = 1; s < 4; ++s)
#pragma unroll
      for (int i = 0; i < NI; ++i) in[s - 1][i] = __builtin_amdgcn_raw_buffer_load_b128(c.rws, l16 + i * 1024, pb + (c.a * 4 + (s ^ c.a)) * 12288, 16);
    asm volatile("" ::: "memory");
#pragma unroll
    for (int s = 0; s < 3; ++s)
#pragma unroll
      for (int i = 0; i < NI; ++i) mx = max(max(mx, max(in[s][i][0], in[s][i][1])), max(in[s][i][2], in[s][i][3]));
    if (__builtin_amdgcn_ballot_w64(mx == POISON) == 0) break;
  }
  const unsigned row0 = 256 * c.T + 128 * (c.m & 1) + 64 * (c.w & 1) + 32 * kh;
  const unsigned vo = (l2 & 15) * 2304 + 8 * (l2 >> 4), colb = NT == 8 ? 256 * (c.m >> 1) : 768;
#pragma unroll
  for (int i = 0; i < NI; ++i) {
    const int t = NT - NI + i;
    f32x4 s0 = acc[t][0], s1 = acc[t][1];
    if (t >= NT - 2) { s0 += oth[t][0]; s1 += oth[t][1]; }
#pragma unroll
    for (int s = 0; s < 3; ++s) {
      s0 += f32x4{bflo(in[s][i][0]), bfhi(in[s][i][0]), bflo(in[s][i][1]), bfhi(in[s][i][1])};
      s1 += f32x4{bflo(in[s][i][2]), bfhi(in[s][i][2]), bflo(in[s][i][3]), bfhi(in[s][i][3])};
    }
    s0 *= kh ? p.omg[2] : p.omg[0]; s1 *= kh ? p.omg[3] : p.omg[1];
    const unsigned cb = colb + (i < 2 ? 64 * c.a + 32 * i : 256 + 32 * c.a);
    __builtin_amdgcn_raw_buffer_store_b64(u32x2{cvtpk(s0[0], s0[1]), cvtpk(s0[2], s0[3])}, c.rout, vo, row0 * 1152 + cb, 0);
    __builtin_amdgcn_raw_buffer_store_b64(u32x2{cvtpk(s1[0], s1[1]), cvtpk(s1[2], s1[3])}, c.rout, vo, (row0 + 1) * 1152 + cb, 0);
  }
  if (NT == 8 && c.a == 0 && c.m == 0 && c.w == 0 && l2 == 0) flag_store(c, WS_E + c.T * 128, 16);
  if constexpr (NT == 8) asm volatile(".Lgm_code1:" ::: "memory"); else asm volatile(".Lgm_code2:" ::: "memory");
}

struct XS {
  unsigned v[2], lw, sx, rd[2], cur, sg;
  DI void init(const Ctx& c, unsigned base) {
    const unsigned l = lane_id(), j = l & 15, g = l >> 4;
    v[0] = (l >> 3) * 2304 + 16 * ((l & 7) ^ g); v[1] = (l >> 3) * 2304 + 16 * ((l & 7) ^ (4 + g));
    lw = base + c.w * 8192; sx = (256 * c.T + 64 * c.w) * 1152;
    rd[0] = lw + j * 128 + 16 * (g ^ (j >> 1)); rd[1] = rd[0] ^ 64;
    PIN(rd[0]); PIN(rd[1]);
  }
  template <int NSL, int C, int I> DI void prep(const Ctx& c) {
    if constexpr (I == 0) sg = sx + c.kg[C] * 128;
    cur = dprep<(C % NSL) * 32768 + I * 1024, (32 * (I >> 2) + 16 * (I & 1) + ((I >> 1) & 1)) * 1152>(lw, sg);
  }
  template <int I> DI void fire(const Ctx& c) { dfire<0>(v[I & 1], c.rx, cur); }
};
template <unsigned LA> DI void touch_pages(const Ctx& c) {
  const unsigned l = lane_id(), own = c.m < 6 ? 2 * c.m : 18 + 3 * c.m;
  const unsigned o = l < 4 ? WS_H + (((4 * c.T + l) * 4 + c.a) * 42 + own) * 4096 : l < 8 ? WS_PART + ((c.T * 8 + c.m) * 64 + (l - 4) * 20) * 3072 : l < 9 ? WS_G + 32768 : WS_F1 + 4096;
  touch<LA>(c.w < 2 ? o : (256 * c.T + 64 * (l & 3)) * 1152, c.w < 2 ? c.rws : c.rout, 0);
}
template <int I> DI void touch_w2(const Ctx& c, unsigned v128) {
  const unsigned idx = (c.T & 3) * 6 + c.m;
  const unsigned so = I < 7 ? (1152 * c.a + 48 * idx) * 1152 + c.w * 2048 + I * 8192 : (144 * c.a + 7 * idx) * 1152 + c.w * 2048;
  asm volatile("s_mov_b32 m0, %3\n\ts_mov_b64 exec, 0xffff\n\tbuffer_load_dword %0, %1, %2 offen sc1 lds\n\ts_mov_b64 exec, -1"
               ::"v"(v128), "s"(I < 7 ? c.rwd : c.rwo), "s"(so), "i"(9 * S1) : "memory");
}

DI unsigned vw_mlp(unsigned l) { const unsigned r8 = l >> 3; return (8 * (r8 >> 2) + (r8 & 3)) * 9216 + 16 * ((l & 7) ^ (((r8 >> 1) & 3) << 1)); }
DI unsigned sw_mlp(const Ctx& c) { return (16 * (c.w & 1) + 4 * (c.w >> 1)) * 9216 + (1152 * c.a + 64 * c.m) * 2; }
DI unsigned va_att(const Ctx& c, unsigned l) {
  const unsigned g = l >> 4, lc = (l & 15) ^ (g << 1);
  return g * 3456 + 144 * (2 * c.a + (c.m & 1)) + (lc < 10 ? 1152 + 16 * (lc < 9 ? lc : 8) : 2304 + 16 * (lc - 10));
}
constexpr int slot1(int n) { return n < 9 ? n % 4 : n % 9 < 5 ? 4 + n % 9 : n % 9 % 4; }
constexpr int sig_idx(int F) {
  if ((F + PD) % 16 == 0) return -1;
  int e = 0;
  for (int G = F - F % 72; G < F; ++G) if ((G + PD) % 16 != 0) ++e;
  return e;
}
constexpr int w_ks(int n, int I) { return n < 9 ? 2 * n + (I & 1) : ((n % 9) * 4 + I) % 18; }
constexpr int w_up(int n, int I) { return n < 9 ? I >> 1 : (n % 9) * 4 + I >= 18; }
DI void role_mlp(Ctx& c, XS& xs, const unsigned vW) {
  const unsigned l = lane_id(), g = l >> 4, q = (l & 15) >> 2, pp = l & 3;
  const unsigned lw = c.w * 1024, sW = sw_mlp(c);
  unsigned sWk[9];
#pragma unroll
  for (int i = 0; i < 9; ++i) sWk[i] = sW + c.kg[i] * (64 * 9216);
  const unsigned sH = WS_H + (((4 * c.T + c.w) * 4 + c.a) * 42 + 2 * c.m) * 4096;
  const unsigned f1 = WS_F1 + (c.T * 4 + c.a) * 128;
  bf16x8 xf[18][4];
  f32x4 ag0[4][4], ag[4][4], au[4][4];
  const float nl2e = -1.4426950408889634f;
  unsigned sp[4][4][2];
  P2<8> p2;
  u32x4 hq[8], pz;
  unsigned vl16 = l * 16;
  unsigned cur = 0;
  auto prep = [&](auto F_, auto R_) {
    constexpr Op o = mlp_op(CI(F_), CI(R_));
    if constexpr (o.kind == O_W) {
      constexpr int pn = o.a / 9, k = w_ks(o.a, o.b);
      cur = dprep<slot1(o.a) * S1 + o.b * 4096, (k & 1) * 32 * 9216 + pn * 768>(lw, sWk[k >> 1]);
    } else if constexpr (o.kind == O_X) xs.template prep<3, o.a, o.b>(c);
    else p2.template prep<o.kind, o.a, o.b>(c);
  };
  auto fire = [&](auto F_, auto R_) {
    constexpr Op o = mlp_op(CI(F_), CI(R_));
    if constexpr (o.kind == O_W) dfire<0>(vW, w_up(o.a, o.b) ? c.rwu : c.rwg, cur);
    else if constexpr (o.kind == O_X) {
      if constexpr (o.a >= 3 && o.b == 0) asm volatile("s_waitcnt lgkmcnt(4)" ::: "memory");
      xs.template fire<o.b>(c);
    } else if constexpr (o.kind == O_FIN) {
      __builtin_amdgcn_raw_buffer_store_b128(hq[o.b], c.rws, vl16 + (o.b & 3) * 1024, sH + (12 * o.a + (o.b >> 2)) * 4096, 16);
      asm volatile("" ::: "memory");
    } else if constexpr (o.kind == O_PZ) poison<2, o.a>(c, pz, vl16);
    else if constexpr (o.kind == O_TW) touch_w2<o.a>(c, vl16 * 8);
    else if constexpr (o.kind == O_TL) touch_pages<3 * S1>(c);
    else p2.template fire<o.kind, o.a, o.b>(c);
  };
  fire(IC(-3), IC(2));
  visit_all<0, 19>(c.pc0, c.w);
  load_epoch(c);
  unsigned rdx2[2] = {xs.rd[0] + 65536, xs.rd[1] + 65536}; PIN(rdx2[0]); PIN(rdx2[1]);
  auto rdx = [&](auto C_, auto I_) {
    constexpr int C = CI(C_), s = CI(I_) >> 2, rt = CI(I_) & 3;
    xf[2 * C + s][rt] = ldsr<bf16x8>((C % 3 == 2 ? rdx2[s] : xs.rd[s] + (C % 3) * 32768) + rt * 2048);
  };
  unsigned rb[4];
#pragma unroll
  for (int t = 0; t < 4; ++t) rb[t] = (4 * g + q) * 128 + 32 * (t ^ ((2 * g + (q >> 1)) & 3)) + 8 * pp;
  float e0 = 0.f, e1 = 0.f, h0 = 0.f, h1 = 0.f;
  unsigned e2 = 0;
  bf16x8 f[16];
  s16x4 flo;
  unsigned rb2[3];
  wait_bar<VM<0, -1>>();
  asm volatile("" : "+s"(c.E));
  rdx(IC(0), IC(0)); rdx(IC(0), IC(4));
  static_for<PD>([&](auto I_) { constexpr unsigned o = (CI(I_) & 1) * 4096; f[CI(I_)] = cat(trd(rb[CI(I_) >> 1] + o), trd(rb[CI(I_) >> 1] + o + 2048)); });
  static_for<6>([&](auto I_) { rdx(IC(0), IC(1 + CI(I_) / 2 + 4 * (CI(I_) & 1))); });
  SBAR();
  static_for<27>([&](auto N_) {
    static_for<16>([&](auto I_) {
      constexpr int N = CI(N_), I = CI(I_), F = N * 16 + I, FP = F % 144, ps = w_up(N, 2 * (I >> 3)), k0 = w_ks(N, 2 * (I >> 3)), t = (I >> 1) & 3;
      constexpr int RG = (F + PD) / 16, pos = (F + PD) % 16, RR = pos == 0 ? 2 : 0;
      constexpr unsigned ro = (slot1(RG) % 4) * S1 + (2 * (pos >> 3) + (pos & 1)) * 4096;
      const bf16x8 a[2] = {f[I & ~1], f[I | 1]};
      static_for<4>([&](auto R_) {
        constexpr int M = CI(R_), R = M == 1 ? 2 : M == 2 ? 1 : M;
        constexpr int rt0 = 2 * (I & 1) + (M >> 1), sk = (M == 1 || M == 2), ks = k0 + sk;
        mf<(ks < 16), (k0 == 0 && (M & 1) == 0)>(ps == 1 ? au[t][rt0] : F < 144 ? ag0[t][rt0] : ag[t][rt0], a[sk], xf[ks][rt0]);
        SBAR();
        if constexpr (R == 0 && N < 8 && I == XR) wait_vm<VM<0, F>>();
        if constexpr (pos == 0) {
          if constexpr (R < 2 && RG < 27 && slot1(RG) / 4 != slot1(RG - 1) / 4) {
#pragma unroll
            for (int i = 2 * R; i < 2 * R + 2; ++i) { rb[i] += (unsigned)(slot1(RG) / 4 - slot1(RG - 1) / 4) * 4 * S1; PIN(rb[i]); }
          }
          if constexpr (R == 0 && RG == 27) {
            const unsigned l4 = lane_id();
#pragma unroll
            for (int i = 0; i < 3; ++i) { rb2[i] = P2<8>::slot(0) + P2<8>::rbase(c, l4, i); PIN(rb2[i]); }
          }
          if constexpr (R == 2) {
            if constexpr (RG % 2 == 0) STUB<RG / 2 - 1>();
            static_assert((RG != FLG0 && RG != FLG1) || VM<0, F> == 0, "flags follow a full drain");
            wait_bar<VM<0, F>>();
            if constexpr (RG == 27) tieA(p2.bf[0][0], p2.bf[0][1], p2.bf[0][2], p2.bf[0][3]);
          }
        }
        if constexpr (R == RR) { if constexpr (RG < 27) flo = trd(rb[(pos >> 1) & 3] + ro); else flo = trd(rb2[pos]); }
        if constexpr (R == RR + 1) { if constexpr (RG < 27) f[pos] = cat(flo, trd(rb[(pos >> 1) & 3] + ro + 2048)); else f[pos] = cat(flo, trd(rb2[pos] + 4608)); }
        if constexpr (N < 8 && (I == XR || I == XR + 1)) rdx(IC(N + 1), IC(4 * (I - XR) + R));
        SBAR();
        if constexpr (pos == 0 && R == 3) {
          if constexpr (RG == FLG0 || RG == FLG1) { if (threadIdx.x == 0) flag_store(c, f1 + 8 * c.m, RG == FLG0 ? 1 : 2); }
          if constexpr (RG == PZR + 1) keep(pz);
          if constexpr (RG == FLG0) { if (vl16 == 0) flag_store(c, flag2(c) + 8 * c.a, 1); }
          if constexpr (RG == 19) p2.init(c);
          if constexpr (RG == 25) p2.template check<0>(c);
        }
        if constexpr (R >= 2) { if constexpr (R == 3 || F == 0) prep(IC(F), IC(R)); fire(IC(F), IC(R)); }
        if constexpr (F >= 144 && ps == 1 && sig_idx(F) >= 0 && sig_idx(F) <= 65) {
          constexpr int e = sig_idx(F), d = e - 2;
          if constexpr (R == 0 && d >= 0) e2 = w_add1(e1);
          if constexpr (R == 1 && e < 64) e0 = w_mul(ag[e / 16][(e / 4) % 4][e % 4]);
          if constexpr (R == 2 && e >= 1 && e <= 64) e1 = w_exp(e0);
          if constexpr (R == 3 && d >= 0) { if constexpr (d % 2 == 0) sp[d / 16][(d / 4) % 4][(d % 4) / 2] = w_rcplo(e2); else w_rcphi(sp[d / 16][(d / 4) % 4][(d % 4) / 2], e2); }
        }
        if constexpr (F >= 131 && F < 147) {
          constexpr int tt = (F - 131) / 4, rt = (F - 131) % 4, k = M & 1;
          if constexpr (M < 2) sp[tt][rt][k] = w_pklo(ag0[tt][rt][2 * k], nl2e); else w_pkhi(sp[tt][rt][k], ag0[tt][rt][2 * k + 1], nl2e);
        }
        if constexpr (F >= 144 && F < 216 && sig_idx(F) >= 0) {
          constexpr int e = sig_idx(F), d = e - 2, pa = (e - 2) / 2, pf = (e - 4) / 2;
          if constexpr (R == 0 && e % 2 == 0 && e >= 2 && pa < 32) w_add1h(sp[pa / 8][(pa / 2) % 4][pa % 2]);
          if constexpr (R == 0 && e % 2 == 1 && e >= 5) h1 = w_mixhi(au[pf / 8][(pf / 2) % 4][2 * (pf % 2) + 1], sp[pf / 8][(pf / 2) % 4][pf % 2]);
          if constexpr (R == 1 && e % 2 == 0 && e >= 4) h0 = w_mixlo(au[pf / 8][(pf / 2) % 4][2 * (pf % 2)], sp[pf / 8][(pf / 2) % 4][pf % 2]);
          if constexpr (R == 1 && e % 2 == 1 && e >= 5) hq[(pf / 16) * 4 + (pf / 2) % 4][2 * ((pf / 8) & 1) + pf % 2] = w_cvt(h0, h1);
          if constexpr (R == 2 && e < 64) w_exph<e & 1>(sp[e / 16][(e / 4) % 4][(e % 4) / 2]);
          if constexpr (R == 3 && d >= 0 && d < 64) w_rcph<d & 1>(sp[d / 16][(d / 4) % 4][(d % 4) / 2]);
        }
        if constexpr (F >= 288 && FP >= FIN0 && FP < FIN0 + 32) {
          constexpr int J = (FP - FIN0) / 4, i = FP % 4, e = J >> 2, rt = J & 3, tt = 2 * e + (i >> 1), k = 2 * (i & 1);
          if constexpr (R == 0) h0 = w_mixlo(au[tt][rt][k], sp[tt][rt][i & 1]);
          if constexpr (R == 1) h1 = w_mixhi(au[tt][rt][k + 1], sp[tt][rt][i & 1]);
          if constexpr (R == 3) hq[J][i] = w_cvt(h0, h1);
        }
        if constexpr (R == 3 && mlp_op(F, 2).kind == O_FIN) keep(hq[mlp_op(F, 2).b]);
        if constexpr (R == 3 && F < 431) prep(IC(F + 1), IC(2));
        SBAR();
      });
    });
  });
  phase2<8>(c, p2, f, [&](auto N_, auto R_) {
    constexpr int N = CI(N_), R = CI(R_);
    if constexpr (N < 16 && R < 3) {
      constexpr int J = N / 2, e = J >> 2, rt = J & 3, tt = 2 * e + (N & 1);
      if constexpr (R == 0) { h0 = w_mixlo(au[tt][rt][0], sp[tt][rt][0]); h1 = w_mixhi(au[tt][rt][1], sp[tt][rt][0]); }
      if constexpr (R == 1) { e0 = w_mixlo(au[tt][rt][2], sp[tt][rt][1]); e1 = w_mixhi(au[tt][rt][3], sp[tt][rt][1]); }
      if constexpr (R == 2) { hq[J][2 * (N & 1)] = w_cvt(h0, h1); hq[J][2 * (N & 1) + 1] = w_cvt(e0, e1); }
    }
    if constexpr (R == 3 && N < 20 && lastfin(N) >= 0) {
      constexpr int J = lastfin(N);
      __builtin_amdgcn_raw_buffer_store_b128(hq[J], c.rws, vl16 + (J & 3) * 1024, sH + (24 + (J >> 2)) * 4096, 16);
      asm volatile("" ::: "memory");
    }
    if constexpr (R == 1 && N >= 1 && N < 21 && lastfin(N - 1) >= 0) keep(hq[lastfin(N - 1)]);
  });
}

DI u32x2 pk4(const f32x4& v) { return u32x2{cvtpk(v[0], v[1]), cvtpk(v[2], v[3])}; }
DI void sw16(float& a, float& b) { auto r = __builtin_amdgcn_permlane16_swap(__float_as_uint(a), __float_as_uint(b), false, false); a = __uint_as_float(r[0]); b = __uint_as_float(r[1]); }
DI void sw32(float& a, float& b) { auto r = __builtin_amdgcn_permlane32_swap(__float_as_uint(a), __float_as_uint(b), false, false); a = __uint_as_float(r[0]); b = __uint_as_float(r[1]); }
template <bool MX> DI float xred(float (&v)[4]) {
  sw16(v[0], v[1]); sw16(v[2], v[3]);
  float a = MX ? max2(v[0], v[1]) : v[0] + v[1], b = MX ? max2(v[2], v[3]) : v[2] + v[3];
  sw32(a, b);
  return MX ? max2(a, b) : a + b;
}
DI void xbc(float t, float (&v)[4]) {
  float a = t, b = t;
  sw32(a, b);
  v[0] = a; v[1] = a; v[2] = b; v[3] = b;
  sw16(v[0], v[1]); sw16(v[2], v[3]);
}

DI void role_att(Ctx& c, XS& xs, const unsigned vA) {
  const unsigned mp = c.m & 1;
  u32x2 vp[4][5], qp[4][5];
  u32x4 kt[6][4];
  f32x4 gate, bprev, bown, bprev0;
  unsigned mpv[2];
  P2<12> p2;
  {
    const unsigned l = lane_id(), g = l >> 4, q = (l & 15) >> 2, pp = l & 3;
    const unsigned lc = (l & 15) ^ (g << 1), hb = 144 * (2 * c.a + mp);
    const unsigned vB = g * 3456 + hb + (lc < 4 ? 2304 + 16 * (lc < 3 ? 6 + lc : 8) : 16 * (lc - 4));
    const u64 maskB = __builtin_amdgcn_ballot_w64(lc < 13);
    const unsigned lw = c.w * 1152;
    unsigned sWk[9];
#pragma unroll
    for (int i = 0; i < 9; ++i) sWk[i] = (8 * c.w + 64 * c.kg[i]) * 3456;
    const int hr = 256 * (int)c.T + ((c.w & 1) ? -16 : -15) + 2 * (int)(l >> 3);
    const unsigned vHx = (unsigned)(hr < 0 ? 0 : hr) * 1152 + 16 * ((l & 7) ^ (4 * (c.w & 1) + g));
    const unsigned lHx = A_HX + c.w * 1024;
    unsigned rh[2] = {A_HX + (l & 15) * 128 + 16 * (g ^ ((l & 15) >> 1)), 0}; rh[1] = rh[0] ^ 64; PIN(rh[0]); PIN(rh[1]);
    bf16x8 xf[18][4];
    unsigned cur = 0, l8 = 0;
    u32x4 pz;
    auto prep = [&](auto F_, auto R_) {
      constexpr Op o = att1_op(CI(F_), CI(R_));
      if constexpr (o.kind == O_W) cur = dprep<(o.a % 4) * S2 + o.b * 4608, (32 * (o.b >> 1) + 4 * (o.b & 1)) * 3456>(lw, sWk[o.a % 9]);
      else if constexpr (o.kind == O_X) xs.template prep<2, o.a, o.b>(c);
      else if constexpr (o.kind == O_HX) {
        if constexpr (o.a < 4) cur = dprep<o.a * 4096, 0>(lHx, (c.w < 2 ? c.kg[2 * o.a] : c.kg[2 * o.a + 1]) * 128);
        else cur = dprep<16384, 0>(lHx, c.w < 2 ? c.kg[8] * 128 : (c.w & 1) * 1024);
      } else p2.template prep<o.kind, o.a, o.b>(c);
    };
    auto fire = [&](auto F_, auto R_) {
      constexpr Op o = att1_op(CI(F_), CI(R_));
      if constexpr (o.kind == O_W) { if constexpr (o.a < 9) dfire<0>(vA, c.rqkv, cur); else dfire_m(vB, c.rqkv, cur, maskB); }
      else if constexpr (o.kind == O_X) {
        if constexpr (o.a >= 2 && o.b == 0) asm volatile("s_waitcnt lgkmcnt(4)" ::: "memory");
        xs.template fire<o.b>(c);
      } else if constexpr (o.kind == O_HX) {
        if constexpr (o.a < 4) dfire<0>(vHx, c.rx, cur); else dfire<0>(c.w < 2 ? vHx : l * 16, c.w < 2 ? c.rx : c.rgw, cur);
      } else if constexpr (o.kind == O_TL) touch_pages<A_GW + 2048>(c);
      else if constexpr (o.kind == O_PZ) poison<3, o.a>(c, pz, 2 * l8);
      else p2.template fire<o.kind, o.a, o.b>(c);
    };
    auto rdx = [&](auto C_, auto I_) {
      constexpr int C = CI(C_), s = CI(I_) >> 2, rt = CI(I_) & 3;
      xf[2 * C + s][rt] = ldsr<bf16x8>(xs.rd[s] + (C % 2) * 32768 + rt * 2048);
    };
    static_for<2>([&](auto I_) { prep(IC(CI(I_) - 4), IC(2)); fire(IC(CI(I_) - 4), IC(2)); });
    visit_all<32, 22>(c.pc0, c.w);
    load_epoch(c);
    unsigned rb[4], rx1, rx2;
#pragma unroll
    for (int t = 0; t < 4; ++t) rb[t] = g * 1152 + q * 256 + 32 * (t ^ q) + 8 * pp;
    rx1 = g * 1152 + q * 256 + 32 * (c.w ^ q) + 8 * pp;
    rx2 = g * 1152 + q * 256 + 128 + 32 * ((c.w < 2 ? c.w + 1 : 3) ^ q) + 8 * pp;
    const unsigned gsrc = A_GW + 2 * (32 * (l >> 5) + 8 * ((l >> 2) & 3) + 4 * ((l >> 4) & 1) + (l & 3));
    const unsigned gdst = (l >> 2) * 1152 + (l & 3) * 256 + 16 * (13 ^ ((l & 3) << 1));
    u16 gwv = 0;
    f32x4 acc[2][8][4], ah[4];
    u32x2 hp;
    unsigned wk = 0, wv = 0;
    bf16x8 f[16], hx[2], ha[2], hc[2];
    s16x4 flo, hlo;
    wait_bar<VM<1, -1>>();
    asm volatile("" : "+s"(c.E));
    rdx(IC(0), IC(0)); rdx(IC(0), IC(4));
    static_for<PD>([&](auto I_) { constexpr unsigned o = (CI(I_) & 1) * 9216; f[CI(I_)] = cat(trd(rb[CI(I_) >> 1] + o), trd(rb[CI(I_) >> 1] + o + 4608)); });
    static_for<6>([&](auto I_) { rdx(IC(0), IC(1 + CI(I_) / 2 + 4 * (CI(I_) & 1))); });
    SBAR();
    auto pass = [&](auto PS_) {
      constexpr int PS = CI(PS_), NT = PS == 0 ? 8 : 7, FC = 2 * NT;
      static_for<9>([&](auto N_) {
        static_for<FC>([&](auto I_) {
          constexpr int N = CI(N_), I = CI(I_), F = 144 * PS + N * FC + I, t = I >> 1;
          constexpr int RG = rgA(F), pos = posA(F);
          constexpr int tn = pos >> 1, RR = pos == 0 ? 2 : 0;
          constexpr unsigned ro = (RG % 2) * S2 + (pos & 1) * 9216 + 128 * (tn >> 2);
          constexpr unsigned rc = ((9 * PS + N) % 2) * S2 + ((I >> 1) & 1) * 9216 + (I & 1) * 4608;
          constexpr bool SW = PS == 0 ? t >= 5 : t < 2;
          const bf16x8 a[2] = {f[I & ~1], f[I | 1]};
          static_for<5>([&](auto R_) {
            constexpr int M = CI(R_), R = M == 1 ? 2 : M == 2 ? 1 : M;
            if constexpr (M < 4) {
              constexpr int rt0 = 2 * (I & 1) + (M >> 1), sk = (M == 1 || M == 2), ks = 2 * N + sk;
              if constexpr (SW) mfs<(ks < 16), (N == 0 && (M & 1) == 0)>(acc[PS][t][rt0], a[sk], xf[ks][rt0]); else mf<(ks < 16), (N == 0 && (M & 1) == 0)>(acc[PS][t][rt0], a[sk], xf[ks][rt0]);
            } else if constexpr (PS == 0) {
              if constexpr (I == 7) { mf<false, N == 0>(ah[0], ha[1], hx[1]); mf<false, false>(ah[0], ha[0], hx[0]); }
              if constexpr (I == 9) { mf<false, N == 0>(ah[1], a[0], hx[0]); mf<false, false>(ah[1], a[1], hx[1]); }
              if constexpr (I == 12) { mfs<false, N == 0>(ah[2], hc[1], hx[1]); mfs<false, false>(ah[2], hc[0], hx[0]); }
            } else if constexpr (I == 7) { mfs<false, N == 0>(ah[3], ha[1], hx[1]); mfs<false, false>(ah[3], ha[0], hx[0]); }
            SBAR();
            if constexpr (R == 0 && PS == 0 && I == XR && N < 8) wait_vm<VM<1, F>>();
            if constexpr (pos == 0 && RG < 18) {
              if constexpr (R < 2 && RG % 2 == 0) {
                constexpr unsigned d = RG % 4 == 0 ? 0u - 2 * S2 : 2 * S2;
#pragma unroll
                for (int i = 2 * R; i < 2 * R + 2; ++i) { rb[i] += d; PIN(rb[i]); }
                if (R == 0) { rx1 += d; PIN(rx1); } else if (RG < 9) { rx2 += d; PIN(rx2); }
              }
              if constexpr (R == 0 && RG == 9) { rx1 ^= 32 * (c.w & 2); PIN(rx1); }
              if constexpr (R == 2) {
                if constexpr (RG % 2 == 1) STUB<32 + RG / 2>();
                static_assert(RG != 16 || VM<1, F> == 0, "flags follow a full drain");
                wait_bar<VM<1, F>>();
              }
            }
            if constexpr (R == RR && RG < 18) flo = trd(rb[tn & 3] + ro);
            if constexpr (R == RR + 1 && RG < 18) f[pos] = cat(flo, trd(rb[tn & 3] + ro + 4608));
            if constexpr (PS == 0 && N < 8 && (I == XR || I == XR + 1) && R < 4) rdx(IC(N + 1), IC(4 * (I - XR) + R));
            if constexpr (I < 2 && R == 3) hx[I] = ldsr<bf16x8>(rh[I] + N * 2048);
            if constexpr (I >= 2 && I < 6 && R == 3) { if constexpr (I % 2 == 0) hlo = trd(rx1 + rc); else ha[(I >> 1) & 1] = cat(hlo, trd(rx1 + rc)); }
            if constexpr (PS == 0 && I >= 6 && I < 10 && R == 3) { if constexpr (I % 2 == 0) hlo = trd(rx2 + rc); else hc[(I >> 1) & 1] = cat(hlo, trd(rx2 + rc)); }
            if constexpr (RG + 3 >= 9 && RG + 3 <= 17 && R == 1) {
              if constexpr (pos == 6) gwv = ldsr<u16>(gsrc + c.kg[RG + 3 - 9] * 128);
              if constexpr (pos == 9) ldsw<u16>(gdst + ((RG + 3) % 4) * S2, gwv);
            }
            SBAR();
            if constexpr (F == 128 && R == 0) { l8 = lane_id() * 8; PIN(l8); wk = A_KX + (4 * c.w + 2) * 2560 + l8; PIN(wk); }
            if constexpr (F >= 130 && F < 146 && (R == 0 || R == 2)) {
              constexpr int tt = (F - 130) / 2, rt = 2 * (F & 1) + R / 2;
              if constexpr (tt < 5) ldsw<u32x2>(wk + rt * 2560 + tt * 512, pk4(acc[0][tt][rt])); else { vp[rt][tt - 5] = pk4(acc[0][tt][rt]); PIN(vp[rt][tt - 5]); }
            }
            if constexpr (F == 146 && R < 2) { const u32x2 h = pk4(ah[R]); const unsigned o = A_KX + (R == 0 ? c.w : 4) * 512; ldsw<u32x2>(o + l8, h); ldsw<u32x2>(o + 2560 + (l8 ^ 64), h); }
            if constexpr (F == 146 && R == 2) { hp = pk4(ah[2]); PIN(hp); }
            if constexpr (pos == 0 && R == 3) {
              if constexpr (RG == 15) { keep(pz); p2.init(c); wv = A_VX + (c.w < 3 ? (c.w + 1) * 5120 : A_GW + 2304 - A_VX) + (c.w < 3 ? l8 : 0); PIN(wv); }
              if constexpr (RG == 16) { if (l8 == 0) flag_store(c, flag2(c) + 8 * c.a, 1); }
            }
            if constexpr (RG == 15 && pos >= 4 && pos < 10 && R == 2) { if (c.w < 3) ldsw<u32x2>(wv + (((pos - 4) / 3) * 5 + (pos - 4) % 3) * 512, vp[2 + (pos - 4) / 3][(pos - 4) % 3]); }
            if constexpr (RG == 15 && pos == 11 && R == 2) { const unsigned o = A_VX + (c.w < 2 ? c.w : 2) * 512; ldsw<u32x2>(o + l8, hp); ldsw<u32x2>(o + 2560 + (l8 ^ 256), hp); }
            if constexpr (RG == 16 && pos >= 9 && pos < 13 && R == 2) {
              constexpr int r = pos - 9;
              const int key = (int)(l8 >> 7) * 4 + r, jj = (int)(l8 >> 3) & 15;
              bprev[r] = key >= jj + 9 ? 0.f : -1e30f;
              bown[r] = (key <= jj && key >= jj - 7) ? 0.f : -1e30f;
              bprev0[r] = (c.T == 0 && c.w == 0) ? -1e30f : bprev[r];
              if constexpr (r % 2 == 0) mpv[r / 2] = key >= jj + 9 ? 0xffffu : 0u; else { mpv[r / 2] |= key >= jj + 9 ? 0xffff0000u : 0u; PIN(mpv[r / 2]); }
              if constexpr (r == 3) { PIN(bprev); PIN(bown); PIN(bprev0); }
            }
            if constexpr (F == 256 && R == 0) { wk -= 5120; PIN(wk); }
            if constexpr (F >= 257 && F < 265 && (R == 0 || R == 1 || R == 3)) {
              constexpr int n = 3 * (F - 257) + (R == 3 ? 2 : R), i = n / 4, h = n % 4;
              const u32x2 k0 = ldsr<u32x2>(wk + i * 2560 + h * 512), k1 = ldsr<u32x2>(wk + i * 2560 + h * 512 + 512);
              kt[i][h] = u32x4{k0[0], k0[1], k1[0], k1[1]};
              asm volatile("" ::: "memory");
            }
            if constexpr (F >= 258 && R == 2) {
              constexpr int tt = (F - 258) / 2;
#pragma unroll
              for (int rt = 2 * (F & 1); rt < 2 * (F & 1) + 2; ++rt) {
                if constexpr (tt < 2) { vp[rt][3 + tt] = pk4(acc[1][tt][rt]); PIN(vp[rt][3 + tt]); }
                else { qp[rt][tt - 2] = pk4(acc[1][tt][rt]); PIN(qp[rt][tt - 2]); }
              }
              if constexpr (tt < 2 && (F & 1)) { if (c.w < 3) { ldsw<u32x2>(wv + (3 + tt) * 512, vp[2][3 + tt]); ldsw<u32x2>(wv + (8 + tt) * 512, vp[3][3 + tt]); } }
            }
            if constexpr (F == 267 && R == 0) { const u32x2 h = pk4(ah[3]); const unsigned o = A_VX + (3 + (c.w & 1)) * 512; ldsw<u32x2>(o + l8, h); ldsw<u32x2>(o + 2560 + (l8 ^ 256), h); }
            if constexpr (R == 2 || R == 3) { if constexpr (R == 3 || F == 0) prep(IC(F), IC(R)); fire(IC(F), IC(R)); }
            if constexpr (R == 4) prep(IC(F + 1), IC(2));
            SBAR();
          });
        });
      });
    };
    pass(IC(0));
    pass(IC(1));
    STUB<41>();
    asm volatile("s_nop 6\n\ts_waitcnt lgkmcnt(0)\n\ts_barrier" ::: "memory");
#pragma unroll
    for (int rt = 0; rt < 4; ++rt) {
      qp[rt][4] = pk4(acc[1][6][rt]);
      gate[rt] = __builtin_amdgcn_exp2f(fminf(1.4426950408889634f * acc[1][6][rt][0], 80.0f));
    }
  }
  auto pre = [&](auto A_, auto N_) { static_for<CI(N_)>([&](auto I_) { constexpr Op o = att2_op(CI(A_) + CI(I_) - 50, 2); p2.template prep<o.kind, o.a, o.b>(c); p2.template fire<o.kind, o.a, o.b>(c); }); };
  const unsigned l = lane_id(), g = (l >> 4) & 3, j = l & 15;
  {
    u32x6 vt[5][2];
    const unsigned ra = A_VX + c.w * 5120 + l * 8;
#pragma unroll
    for (int pa = 0; pa < 2; ++pa)
#pragma unroll
      for (int t = 0; t < 5; ++t) {
        const u32x2 v2 = ldsr<u32x2>(ra + (pa * 5 + t) * 512);
        vt[t][pa] = u32x6{v2[0], v2[1], vp[pa][t][0], vp[pa][t][1], vp[2 + pa][t][0], vp[2 + pa][t][1]};
      }
    const f32x4 omg = {__builtin_amdgcn_rcpf(1.0f + gate[0]), __builtin_amdgcn_rcpf(1.0f + gate[1]), __builtin_amdgcn_rcpf(1.0f + gate[2]), __builtin_amdgcn_rcpf(1.0f + gate[3])};
    if (mp == 0 && g == 2) st16(__builtin_bit_cast(u32x4, omg), c.rws, j * 16, WS_G + (c.a * 32 + 4 * c.T + c.w) * 256);
#pragma unroll
    for (int rt = 0; rt < 4; ++rt) gate[rt] = __shfl(gate[rt], 32 + j);
    constexpr float SC2 = 0.23570226039551584f * 1.4426950408889634f;
    const unsigned sH = WS_H + (((4 * c.T + c.w) * 4 + c.a) * 42 + 36 + 3 * mp) * 4096;
    u32x4 qf[4], P[4];
    f32x4 sp[4], so[4], hi[4], lo[4];
    auto mkq = [&](auto RT_) {
      constexpr int rt = CI(RT_);
#pragma unroll
      for (int h = 0; h < 4; ++h) {
#pragma unroll
        for (int i = 0; i < 4; ++i) { const int col = 16 * (h + (i >> 1)) + 4 * (int)g + 2 * (i & 1); qf[h][i] = (col >= 18 * h && col < 18 * h + 18) ? qp[rt][h + (i >> 1)][i & 1] : 0u; }
        PIN(qf[h]);
      }
    };
    auto qk = [&](auto RT_, auto H_, auto W_) {
      constexpr int rt = CI(RT_), h = CI(H_);
      if constexpr (CI(W_) == 0) sp[h] = mfc(kt[rt][h], qf[h], rt < 2 ? bprev0 : bprev);
      else so[h] = mfc(kt[rt + 2][h], qf[h], bown);
    };
    auto pv = [&](auto RT_, auto T_, auto W_) {
      constexpr int rt = CI(RT_), t = CI(T_) + CI(W_), pa = rt & 1, sn = rt >> 1;
      const f32x4 r = mfz(__builtin_shufflevector(vt[t][pa], vt[t][pa], 2 * sn, 2 * sn + 1, 2 * sn + 2, 2 * sn + 3), P[CI(T_)]);
      if constexpr (CI(W_) == 0) hi[CI(T_)] = r; else lo[CI(T_)] = r;
    };
    auto outp = [&](auto RT_) {
      constexpr int rt = CI(RT_);
      u32x2 o[5];
      PIN(lo[0]);
#pragma unroll
      for (int t = 0; t < 5; ++t)
#pragma unroll
        for (int i = 0; i < 2; ++i) {
          const unsigned vh = t < 4 ? cvtpk(hi[t < 4 ? t : 0][2 * i], hi[t < 4 ? t : 0][2 * i + 1]) : 0u;
          o[t][i] = (4 * (int)g + 2 * i < 2 * t) ? cvtpk(lo[t > 0 ? t - 1 : 0][2 * i], lo[t > 0 ? t - 1 : 0][2 * i + 1]) : vh;
        }
      st16(u32x4{o[0][0], o[0][1], o[1][0], o[1][1]}, c.rws, l * 16 + rt * 1024, sH);
      st16(u32x4{o[2][0], o[2][1], o[3][0], o[3][1]}, c.rws, l * 16 + rt * 1024, sH + 4096);
      st16(u32x4{o[4][0], o[4][1], 0u, 0u}, c.rws, l * 16 + rt * 1024, sH + 8192);
    };
    mkq(IC(0));
    asm volatile("s_nop 1" ::: "memory");
    static_for<8>([&](auto I_) { qk(IC(0), IC(CI(I_) >> 1), IC(CI(I_) & 1)); });
    SBAR();
    mkq(IC(1));
    static_for<4>([&](auto RT_) {
      constexpr int rt = CI(RT_);
      float s[4][4], red[4], bc[4];
      STUB<42 + rt>();
      SBAR();
      static_for<4>([&](auto H_) {
        constexpr int h = CI(H_);
        if constexpr (rt > 0) pv(IC(rt - 1), H_, IC(0));
        s[h][0] = max2(sp[h][0], so[h][0]); s[h][1] = max2(sp[h][1], so[h][1]); s[h][2] = max2(sp[h][2], so[h][2]);
        SBAR();
        if constexpr (rt > 0) pv(IC(rt - 1), H_, IC(1));
        s[h][3] = max2(sp[h][3], so[h][3]);
        red[h] = max2(max3(s[h][0], s[h][1], s[h][2]), s[h][3]);
        SBAR();
      });
      xbc(-SC2 * xred<true>(red), bc);
      if constexpr (rt == 0) pre(IC(6), IC(7));
      if constexpr (rt == 2) { wait_vm<VM<2, -28>>(); p2.template check<0>(c); }
      if constexpr (rt > 0) outp(IC(rt - 1));
      SBAR();
      static_for<4>([&](auto H_) {
        constexpr int h = CI(H_);
        if constexpr (rt < 3) qk(IC(rt + 1), H_, IC(0));
        s[h][0] = __builtin_amdgcn_exp2f(__builtin_fmaf(s[h][0], SC2, bc[h])); s[h][1] = __builtin_amdgcn_exp2f(__builtin_fmaf(s[h][1], SC2, bc[h]));
        SBAR();
        if constexpr (rt < 3) qk(IC(rt + 1), H_, IC(1));
        s[h][2] = __builtin_amdgcn_exp2f(__builtin_fmaf(s[h][2], SC2, bc[h])); s[h][3] = __builtin_amdgcn_exp2f(__builtin_fmaf(s[h][3], SC2, bc[h]));
        SBAR();
      });
#pragma unroll
      for (int h = 0; h < 4; ++h) red[h] = (s[h][0] + s[h][1]) + (s[h][2] + s[h][3]);
      xbc(gate[rt] * __builtin_amdgcn_rcpf(xred<false>(red)), bc);
      if constexpr (rt == 1) pre(IC(16), IC(6));
      if constexpr (rt == 2) pre(IC(25), IC(8));
      if constexpr (rt == 3) pre(IC(36), IC(4));
      if constexpr (rt == 0) {
#pragma unroll
        for (int i = 0; i < 10; ++i) PIN(vt[i >> 1][i & 1]);
      }
      if constexpr (rt < 2) mkq(IC(rt + 2));
#pragma unroll
      for (int h = 0; h < 4; ++h) {
        const unsigned p0 = cvtpk(s[h][0] * bc[h], s[h][1] * bc[h]), p1 = cvtpk(s[h][2] * bc[h], s[h][3] * bc[h]);
        P[h] = u32x4{p0 & mpv[0], p1 & mpv[1], p0 & ~mpv[0], p1 & ~mpv[1]};
        PIN(P[h]);
      }
      asm volatile("s_nop 1" ::: "memory");
    });
    static_for<8>([&](auto I_) { pv(IC(3), IC(CI(I_) >> 1), IC(CI(I_) & 1)); });
    asm volatile("s_nop 7" ::: "memory");
    SBAR();
    outp(IC(3));
  }
  bf16x8 f[16];
  phase2<12>(c, p2, f, [](auto, auto) {});
}

#endif
__global__ __launch_bounds__(256, 1) void fused_kernel(const u16* x, const u16* wqkv, const u16* wo, const u16* wg, const u16* wu, const u16* wd,
                                                        const u16* gw, char* ws, u16* out) {
#if defined(__HIP_DEVICE_COMPILE__)
  __shared__ __attribute__((aligned(16))) char smem[LDS_BYTES];
  asm volatile("" ::"v"((unsigned)(size_t)smem));
  Ctx c;
  const unsigned bid = blockIdx.x, xcd = bid & 7, idx = bid >> 3;
  c.w = __builtin_amdgcn_readfirstlane(threadIdx.x >> 6);
  {
    u64 pc; unsigned len;
    asm volatile("s_getpc_b64 %0\n.Lgm_code0:\n\ts_mov_b32 %1, .Lgm_code1-.Lgm_code0\n\ts_max_u32 %1, %1, .Lgm_code2-.Lgm_code0" : "=s"(pc), "=s"(len) :: "scc");
    c.pc0 = pc;
    touch<3 * S1>((threadIdx.x & 15) * 128, __builtin_amdgcn_make_buffer_rsrc((void*)pc, (short)0, len, 0x00020000), idx * 8192 + c.w * 2048);
  }
  c.a = xcd & 3; c.T = 4 * (xcd >> 2) + (idx >> 3);
  c.m = (0x75436210u >> (4 * (idx & 7))) & 7;
  c.kg[0] = (2 * (c.a + c.T) + (c.m & 3)) % 9;
  XS xs; xs.init(c, c.m < 6 ? M_X : A_X);
  PIN(xs.v[0]); PIN(xs.v[1]);
  xs.prep<1, 0, 0>(c);
  asm volatile("" ::"s"(x), "s"(out));
  c.rx = __builtin_amdgcn_make_buffer_rsrc((void*)x, (short)0, SEQ * DM * 2, 0x00020000);
  static_for<8>([&](auto I_) { if constexpr (CI(I_) > 0) xs.template prep<1, 0, CI(I_)>(c); xs.template fire<CI(I_)>(c); });
  const bool mlp = c.m < 6;
  const unsigned l0 = lane_id();
  unsigned vw0 = mlp ? vw_mlp(l0) : va_att(c, l0); PIN(vw0);
  {
    const unsigned sb = mlp ? sw_mlp(c) + c.kg[0] * (64 * 9216) : (8 * c.w + 64 * c.kg[0]) * 3456, lb = c.w * (mlp ? 1024 : 1152);
    static_for<4>([&](auto I_) {
      constexpr int I = CI(I_);
      const rsrc_t rs = __builtin_amdgcn_make_buffer_rsrc((void*)(mlp ? (I < 2 ? wg : wu) : wqkv), (short)0, mlp ? DM * FF * 2 : DM * 3 * DM * 2, 0x00020000);
      dfire<0>(vw0, rs, dprep<0, 0>(lb + I * (mlp ? 4096 : 4608), sb + (mlp ? (I & 1) * 32 * 9216 : (32 * (I >> 1) + 4 * (I & 1)) * 3456)));
    });
  }
  asm volatile("s_branch .Lgm_go\n.Lgm_ret:\n\ts_setpc_b64 s[100:101]\n.Lgm_go:");
  SBAR();
  c.rqkv = __builtin_amdgcn_make_buffer_rsrc((void*)wqkv, (short)0, DM * 3 * DM * 2, 0x00020000);
  c.rwo = __builtin_amdgcn_make_buffer_rsrc((void*)wo, (short)0, DM * DM * 2, 0x00020000);
  c.rwg = __builtin_amdgcn_make_buffer_rsrc((void*)wg, (short)0, DM * FF * 2, 0x00020000);
  c.rwu = __builtin_amdgcn_make_buffer_rsrc((void*)wu, (short)0, DM * FF * 2, 0x00020000);
  c.rwd = __builtin_amdgcn_make_buffer_rsrc((void*)wd, (short)0, DM * FF * 2, 0x00020000);
  c.rgw = __builtin_amdgcn_make_buffer_rsrc((void*)gw, (short)0, DM * 2, 0x00020000);
  c.rws = __builtin_amdgcn_make_buffer_rsrc((void*)ws, (short)0, WS_BYTES, 0x00020000);
  c.rout = __builtin_amdgcn_make_buffer_rsrc((void*)out, (short)0, SEQ * DM * 2, 0x00020000);
  c.ws = (u64)ws;
#pragma unroll
  for (int i = 1; i < 9; ++i) c.kg[i] = c.kg[i - 1] == 8 ? 0 : c.kg[i - 1] + 1;
  if (mlp) role_mlp(c, xs, vw0); else role_att(c, xs, vw0);
#endif
}

extern "C" void kernel_launch(void* const* d_in, const int* in_sizes, int n_in, void* d_out, int out_size, void* d_ws, size_t ws_size,
                              hipStream_t stream) {
  fused_kernel<<<256, 256, 0, stream>>>((const u16*)d_in[0], (const u16*)d_in[1], (const u16*)d_in[2], (const u16*)d_in[4], (const u16*)d_in[5],
                                        (const u16*)d_in[6], (const u16*)d_in[7], (char*)d_ws, (u16*)d_out);
}
```
